```python
import torch

def _softmax(x, axis=-1):
    return torch.softmax(x, dim=axis)

def _gelu(x):
    return torch.nn.functional.gelu(x, approximate='tanh')

def _silu(x):
    return torch.nn.functional.silu(x)

def _sigmoid(x):
    return torch.sigmoid(x)

def _layernorm(x, axis=-1, eps=1e-05):
    m = x.mean(dim=axis, keepdim=True)
    v = x.var(dim=axis, keepdim=True, unbiased=False)
    return (x - m) / torch.sqrt(v + eps)

def _scatter_add(tgt, idx, src, axis=0):
    return tgt.clone().index_add_(axis, idx, src)

def _segment_sum(src, seg, n):
    out = torch.zeros((n,) + src.shape[1:], dtype=src.dtype)
    return out.index_add_(0, seg, src)

def _pad(x, pw):
    flat = tuple((v for pair in reversed(pw) for v in pair))
    return torch.nn.functional.pad(x, flat)
kernel_name = 'adaLN_cond__sliding_block_cached__dense_activation_squared_relu__prompt'

def setup_inputs():

    _w = lambda a: (lambda t: t.to(torch.bfloat16) if t.is_floating_point() else t)(torch.as_tensor(a))

    def _A_setup():
        B, Sq, D = (1, 2048, 512)
        Hq, Hkv, Sk, Dh = (32, 8, 2048, 128)
        assert Hq % Hkv == 0, (Hq, Hkv)
        return {'x': _w((lambda *s: torch.randn(*s, dtype=torch.bfloat16) * 0.05)(B, Sq, D)), 'k_cache': _w((lambda *s: torch.randn(*s, dtype=torch.bfloat16) * 3.2)(B, Hkv, Sk, Dh)), 'v_cache': _w((lambda *s: torch.randn(*s, dtype=torch.bfloat16) * 0.4)(B, Hkv, Sk, Dh)), 'wq': _w((lambda *s: torch.randn(*s, dtype=torch.bfloat16) * 3.2)(D, Hq * Dh) * D ** (-0.5)), 'wo': _w((lambda *s: torch.randn(*s, dtype=torch.bfloat16) * 0.4)(Hq * Dh, D) * (Hq * Dh) ** (-0.5))}
    _A_in = _A_setup()

    def _B_setup():
        B, S, D, F = (1, 2048, 512, 1792)
        return {'x': _w((lambda *s: torch.randn(*s, dtype=torch.bfloat16) * 0.05)(B, S, D)), 'w1': _w((lambda *s: torch.randn(*s, dtype=torch.bfloat16) * 0.05)(D, F)), 'b1': _w((lambda *s: torch.randn(*s, dtype=torch.bfloat16) * 0.4)(F)), 'w2': _w((lambda *s: torch.randn(*s, dtype=torch.bfloat16) * 0.05)(F, D)), 'b2': _w((lambda *s: torch.randn(*s, dtype=torch.bfloat16) * 6.4)(D))}
    _B_in = _B_setup()
    return {'A_x': _A_in['x'], 'A_k_cache': _A_in['k_cache'], 'A_v_cache': _A_in['v_cache'], 'A_wq': _A_in['wq'], 'A_wo': _A_in['wo'], 'B_x': _B_in['x'], 'B_w1': _B_in['w1'], 'B_b1': _B_in['b1'], 'B_w2': _B_in['w2'], 'B_b2': _B_in['b2'], 'c': _w((lambda *s: torch.randn(*s, dtype=torch.bfloat16) * 0.1)(1, 512) * 8.0), 'w_mod': _w((lambda *s: torch.randn(*s, dtype=torch.bfloat16) * 0.1)(512, 6 * 512) * 512 ** (-0.5) * 8.0)}

def reference(A_x, A_k_cache, A_v_cache, A_wq, A_wo, B_x, B_w1, B_b1, B_w2, B_b2, c, w_mod):

    def _A_ref(x, k_cache, v_cache, wq, wo):
        B, Sq, D = x.shape
        Hq, Dh = (32, 128)
        Hkv, Sk = (k_cache.shape[1], k_cache.shape[2])
        W = min(4096, max(256, Sk // 4))
        rep = Hq // Hkv
        q = (lambda x, *d: x.permute(*d))((x @ wq).reshape(B, Sq, Hq, Dh), 0, 2, 1, 3)
        k = (lambda x, n, axis: torch.repeat_interleave(x, n, dim=axis))(k_cache[:, :, Sk - W:, :], rep, axis=1)
        v = (lambda x, n, axis: torch.repeat_interleave(x, n, dim=axis))(v_cache[:, :, Sk - W:, :], rep, axis=1)
        scores = torch.einsum('bhqd,bhkd->bhqk', q, k) * Dh ** (-0.5)
        p = _softmax(scores, axis=-1)
        o = torch.einsum('bhqk,bhkd->bhqd', p, v)
        return (lambda x, *d: x.permute(*d))(o, 0, 2, 1, 3).reshape(B, Sq, Hq * Dh) @ wo

    def _B_ref(x, w1, b1, w2, b2):
        h = torch.einsum('bsd,df->bsf', x, w1) + b1
        zero = torch.zeros_like(h)
        r = torch.where(h > zero, h, zero)
        h = r * r
        out = torch.einsum('bsf,fd->bsd', h, w2) + b2
        return out
    x = A_x
    mod = (_silu(c) @ w_mod).reshape(c.shape[0], 6, 1, -1)
    sa, ca, ga, sb, cb, gb = (mod[:, 0].clone(), mod[:, 1].clone(), mod[:, 2].clone(), mod[:, 3].clone(), mod[:, 4].clone(), mod[:, 5].clone())
    h = _layernorm(x, axis=-1) * (1 + ca) + sa
    x = x + ga * _A_ref(h, A_k_cache, A_v_cache, A_wq, A_wo)
    h = _layernorm(x, axis=-1) * (1 + cb) + sb
    x = x + gb * _B_ref(h, B_w1, B_b1, B_w2, B_b2)
    return x
```

```cpp
#include <hip/hip_runtime.h>
#include <hip/hip_bf16.h>
#include <stdint.h>

typedef unsigned short u16;
typedef __attribute__((ext_vector_type(8))) short bf16x8;
typedef __attribute__((ext_vector_type(4))) short bf16x4;
typedef __attribute__((ext_vector_type(16))) float f32x16;
typedef __attribute__((ext_vector_type(4))) float f32x4;
typedef __attribute__((ext_vector_type(2))) float f32x2;
typedef __attribute__((ext_vector_type(2))) __bf16 bf16x2_t;
typedef __attribute__((ext_vector_type(4))) unsigned u32x4;
typedef __attribute__((ext_vector_type(2))) unsigned u32x2;
#define DI __device__ __forceinline__

DI float bf2f(u16 v) { return __uint_as_float(((unsigned)v) << 16); }
DI unsigned cvtpk(float lo, float hi) { f32x2 v = {lo, hi}; bf16x2_t b = __builtin_convertvector(v, bf16x2_t); return __builtin_bit_cast(unsigned, b); }
DI u16 f2bf(float f) { return (u16)(cvtpk(f, 0.f) & 0xffffu); }
DI float rbf(float f) { return bf2f(f2bf(f)); }

constexpr int S = 2048, D = 512, HQ = 32, HKV = 8, SK = 2048, DH = 128, W = 512, F = 1792;

__global__ __launch_bounds__(256) void k_mod(const u16* __restrict__ c, const u16* __restrict__ wmod, float* __restrict__ mod) {
  __shared__ float sc[512];
  __shared__ float red[4][64];
  int t = threadIdx.x;
  for (int i = t; i < 512; i += 256) { float v = bf2f(c[i]); sc[i] = rbf(v / (1.f + __expf(-v))); }
  __syncthreads();
  int col = blockIdx.x * 64 + (t & 63), g = t >> 6;
  float acc = 0.f;
  for (int i = g * 128; i < g * 128 + 128; ++i) acc += sc[i] * bf2f(wmod[i * 3072 + col]);
  red[g][t & 63] = acc;
  __syncthreads();
  if (t < 64) mod[blockIdx.x * 64 + t] = rbf(red[0][t] + red[1][t] + red[2][t] + red[3][t]);
}

__global__ __launch_bounds__(256) void k_ln(const u16* __restrict__ x, const float* __restrict__ sh, const float* __restrict__ scl, u16* __restrict__ h) {
  int row = blockIdx.x * 4 + (threadIdx.x >> 6), l = threadIdx.x & 63;
  bf16x8 v = *(const bf16x8*)(x + row * 512 + l * 8);
  float f[8], s = 0.f;
  for (int j = 0; j < 8; ++j) { f[j] = bf2f((u16)v[j]); s += f[j]; }
  for (int o = 32; o; o >>= 1) s += __shfl_xor(s, o);
  float m = s * (1.f / 512.f), q = 0.f;
  for (int j = 0; j < 8; ++j) { f[j] -= m; q += f[j] * f[j]; }
  for (int o = 32; o; o >>= 1) q += __shfl_xor(q, o);
  float r = rsqrtf(q * (1.f / 512.f) + 1e-5f);
  bf16x8 o8;
  for (int j = 0; j < 8; ++j) { int cidx = l * 8 + j; o8[j] = (short)f2bf(f[j] * r * rbf(1.f + scl[cidx]) + sh[cidx]); }
  *(bf16x8*)(h + row * 512 + l * 8) = o8;
}

template <int EPI>
__global__ __launch_bounds__(256) void k_gemm(const u16* __restrict__ A, const u16* __restrict__ B, u16* __restrict__ C, int M, int N, int K,
                                              const u16* __restrict__ bias, const u16* __restrict__ res, const float* __restrict__ g) {
  __shared__ __attribute__((aligned(16))) u16 As[64][40];
  __shared__ __attribute__((aligned(16))) u16 Bt[64][40];
  int t = threadIdx.x, l = t & 63, w = t >> 6, wr = w >> 1, wc = w & 1;
  int m0 = blockIdx.y * 64, n0 = blockIdx.x * 64;
  f32x16 acc = {};
  for (int k0 = 0; k0 < K; k0 += 32) {
    { int r = t >> 2, c8 = (t & 3) * 8; *(bf16x8*)&As[r][c8] = *(const bf16x8*)(A + (size_t)(m0 + r) * K + k0 + c8); }
    { int kr = t >> 3, c8 = (t & 7) * 8; bf16x8 v = *(const bf16x8*)(B + (size_t)(k0 + kr) * N + n0 + c8);
      for (int j = 0; j < 8; ++j) Bt[c8 + j][kr] = (u16)v[j]; }
    __syncthreads();
    for (int s = 0; s < 2; ++s) {
      bf16x8 a = *(const bf16x8*)&As[wr * 32 + (l & 31)][s * 16 + (l >> 5) * 8];
      bf16x8 b = *(const bf16x8*)&Bt[wc * 32 + (l & 31)][s * 16 + (l >> 5) * 8];
      acc = __builtin_amdgcn_mfma_f32_32x32x16_bf16(a, b, acc, 0, 0, 0);
    }
    __syncthreads();
  }
  int col = n0 + wc * 32 + (l & 31);
  for (int r = 0; r < 16; ++r) {
    int row = m0 + wr * 32 + (r & 3) + 8 * (r >> 2) + 4 * (l >> 5);
    float v = acc[r];
    if (EPI == 1) v = bf2f(res[(size_t)row * N + col]) + rbf(g[col] * rbf(v));
    if (EPI == 2) { v = rbf(rbf(v) + bf2f(bias[col])); v = v > 0.f ? v : 0.f; v = v * v; }
    if (EPI == 3) v = bf2f(res[(size_t)row * N + col]) + rbf(g[col] * rbf(rbf(v) + bf2f(bias[col])));
    C[(size_t)row * N + col] = f2bf(v);
  }
}

__global__ __launch_bounds__(256) void k_attn(const u16* __restrict__ q, const u16* __restrict__ kc, const u16* __restrict__ vc, u16* __restrict__ o) {
  extern __shared__ __attribute__((aligned(16))) u16 Vt[];
  int t = threadIdx.x, l = t & 63, w = t >> 6, r32 = l & 31, h = l >> 5;
  int head = blockIdx.y, kvh = head >> 2, tok = blockIdx.x * 128 + w * 32 + r32;
  const u16* Kp = kc + ((size_t)kvh * SK + (SK - W)) * DH;
  const u16* Vp = vc + ((size_t)kvh * SK + (SK - W)) * DH;
  for (int i = t; i < W * 16; i += 256) { int key = i >> 4, c8 = (i & 15) * 8; bf16x8 v = *(const bf16x8*)(Vp + key * DH + c8);
    for (int j = 0; j < 8; ++j) Vt[(c8 + j) * 520 + key] = (u16)v[j]; }
  bf16x8 qf[8];
  for (int s = 0; s < 8; ++s) qf[s] = *(const bf16x8*)(q + (size_t)tok * 4096 + head * 128 + s * 16 + h * 8);
  __syncthreads();
  f32x16 oa[4] = {};
  float m = -1e30f, lsum = 0.f;
  const float C = 0.08838834764831845f * 1.4426950408889634f;
  for (int kb = 0; kb < 16; ++kb) {
    f32x16 s16 = {};
    for (int s = 0; s < 8; ++s) {
      bf16x8 kf = *(const bf16x8*)(Kp + (kb * 32 + r32) * DH + s * 16 + h * 8);
      s16 = __builtin_amdgcn_mfma_f32_32x32x16_bf16(kf, qf[s], s16, 0, 0, 0);
    }
    float mx = s16[0];
    for (int r = 1; r < 16; ++r) mx = fmaxf(mx, s16[r]);
    mx = fmaxf(mx, __shfl_xor(mx, 32));
    float mn = fmaxf(m, mx), alpha = exp2f((m - mn) * C);
    m = mn;
    float ps = 0.f;
    for (int r = 0; r < 16; ++r) { s16[r] = exp2f((s16[r] - mn) * C); ps += s16[r]; }
    ps += __shfl_xor(ps, 32);
    lsum = lsum * alpha + ps;
    for (int d = 0; d < 4; ++d) for (int r = 0; r < 16; ++r) oa[d][r] *= alpha;
    for (int s = 0; s < 2; ++s) {
      u32x4 pw = {cvtpk(s16[8 * s], s16[8 * s + 1]), cvtpk(s16[8 * s + 2], s16[8 * s + 3]), cvtpk(s16[8 * s + 4], s16[8 * s + 5]), cvtpk(s16[8 * s + 6], s16[8 * s + 7])};
      bf16x8 pf = __builtin_bit_cast(bf16x8, pw);
      for (int d = 0; d < 4; ++d) {
        const u16* vr = Vt + (d * 32 + r32) * 520 + kb * 32 + 16 * s + 4 * h;
        bf16x4 lo = *(const bf16x4*)vr, hi = *(const bf16x4*)(vr + 8);
        bf16x8 vf = __builtin_shufflevector(lo, hi, 0, 1, 2, 3, 4, 5, 6, 7);
        oa[d] = __builtin_amdgcn_mfma_f32_32x32x16_bf16(vf, pf, oa[d], 0, 0, 0);
      }
    }
  }
  float il = 1.f / lsum;
  for (int d = 0; d < 4; ++d) for (int g = 0; g < 4; ++g) {
    u32x2 wv = {cvtpk(oa[d][4 * g] * il, oa[d][4 * g + 1] * il), cvtpk(oa[d][4 * g + 2] * il, oa[d][4 * g + 3] * il)};
    *(u32x2*)(o + (size_t)tok * 4096 + head * 128 + d * 32 + 8 * g + 4 * h) = wv;
  }
}

extern "C" void kernel_launch(void* const* d_in, const int* in_sizes, int n_in, void* d_out, int out_size, void* d_ws, size_t ws_size, hipStream_t stream) {
  const u16* x = (const u16*)d_in[0]; const u16* kc = (const u16*)d_in[1]; const u16* vc = (const u16*)d_in[2];
  const u16* wq = (const u16*)d_in[3]; const u16* wo = (const u16*)d_in[4];
  const u16* w1 = (const u16*)d_in[6]; const u16* b1 = (const u16*)d_in[7]; const u16* w2 = (const u16*)d_in[8]; const u16* b2 = (const u16*)d_in[9];
  const u16* c = (const u16*)d_in[10]; const u16* wmod = (const u16*)d_in[11];
  char* ws = (char*)d_ws;
  float* mod = (float*)ws;
  u16* h = (u16*)(ws + (1 << 20));
  u16* q = (u16*)(ws + (4 << 20));
  u16* o = (u16*)(ws + (20 << 20));
  u16* x1 = (u16*)(ws + (36 << 20));
  u16* h2 = (u16*)(ws + (38 << 20));
  u16* hid = (u16*)(ws + (40 << 20));
  u16* out = (u16*)d_out;
  (void)hipFuncSetAttribute((const void*)k_attn, hipFuncAttributeMaxDynamicSharedMemorySize, 128 * 520 * 2);
  k_mod<<<48, 256, 0, stream>>>(c, wmod, mod);
  k_ln<<<S / 4, 256, 0, stream>>>(x, mod + 0, mod + 512, h);
  k_gemm<0><<<dim3(4096 / 64, S / 64), 256, 0, stream>>>(h, wq, q, S, 4096, 512, nullptr, nullptr, nullptr);
  k_attn<<<dim3(S / 128, HQ), 256, 128 * 520 * 2, stream>>>(q, kc, vc, o);
  k_gemm<1><<<dim3(512 / 64, S / 64), 256, 0, stream>>>(o, wo, x1, S, 512, 4096, nullptr, x, mod + 1024);
  k_ln<<<S / 4, 256, 0, stream>>>(x1, mod + 1536, mod + 2048, h2);
  k_gemm<2><<<dim3(F / 64, S / 64), 256, 0, stream>>>(h2, w1, hid, S, F, 512, b1, nullptr, nullptr);
  k_gemm<3><<<dim3(512 / 64, S / 64), 256, 0, stream>>>(hid, w2, out, S, 512, F, b2, x1, mod + 2560);
}
```

```cpp
#include <hip/hip_runtime.h>

__global__ __launch_bounds__(256, 1) __attribute__((aligned(4096)))
void k_main(const void* x, const void* kc, const void* vc, const void* wq, const void* wo, void* ws, const void* w1, const void* b1,
            const void* w2, const void* b2, void* out, const void* c, const void* wmod) {
  unsigned tid = threadIdx.x, bid = blockIdx.x;
  unsigned long long kp = (unsigned long long)__builtin_amdgcn_kernarg_segment_ptr();
  asm volatile(
"s_load_dwordx16 s[8:23], s[4:5], 0x0\n"
"s_load_dwordx8 s[24:31], s[4:5], 0x40\n"
"s_load_dwordx2 s[34:35], s[4:5], 0x60\n"
"s_getpc_b64 s[70:71]\n"
"Lpc0_%=:\n"
"s_lshr_b32 s74, s6, 3\n"
"s_mul_i32 s74, s74, 2560\n"
"v_mul_u32_u24 v241, 12, v0\n"
"v_add_u32 v241, s74, v241\n"
"v_min_u32 v241, Lfin_%=-Lpc0_%=, v241\n"
"global_load_dword v255, v241, s[70:71]\n"
"v_and_b32 v192, 63, v0\n"
"v_lshrrev_b32 v240, 6, v0\n"
"s_nop 0\n"
"v_readfirstlane_b32 s64, v240\n"
"s_mul_i32 s37, s6, 171\n"
"s_lshr_b32 s37, s37, 13\n"
"s_mul_i32 s38, s37, 48\n"
"s_sub_u32 s38, s6, s38\n"
"s_lshl_b32 s68, s37, 2\n"
"s_add_u32 s68, s68, s64\n"
"s_lshl_b32 s73, s68, 6\n"
"s_mul_i32 s68, s68, 196608\n"
"s_lshl_b32 s74, s38, 7\n"
"s_add_u32 s68, s68, s74\n"
"v_lshrrev_b32 v100, 3, v192\n"
"v_and_b32 v101, 7, v192\n"
"v_mul_u32_u24 v102, 6144, v100\n"
"v_lshl_add_u32 v102, v101, 4, v102\n"
"v_lshlrev_b32 v103, 1, v100\n"
"v_mov_b32 v104, 0\n"
"s_and_b32 s74, s6, 7\n"
"s_lshr_b32 s39, s6, 3\n"
"s_and_b32 s65, s74, 3\n"
"s_and_b32 s69, s39, 1\n"
"s_lshl_b32 s65, s65, 1\n"
"s_or_b32 s65, s65, s69\n"
"s_lshr_b32 s77, s39, 1\n"
"s_lshr_b32 s66, s74, 2\n"
"s_lshl_b32 s66, s66, 4\n"
"s_add_u32 s66, s66, s77\n"
"s_lshl_b32 s78, s74, 2\n"
"s_lshl_b32 s67, s64, 12\n"
"v_and_b32 v224, 15, v192\n"
"v_lshrrev_b32 v225, 4, v192\n"
"s_and_b32 s74, s77, 3\n"
"s_lshl_b32 s74, s74, 8\n"
"v_lshl_add_u32 v240, v224, 4, s74\n"
"v_add_u32 v220, 0, v240\n"
"v_and_b32 v220, 0x3ff, v220\n"
"v_lshl_add_u32 v216, v225, 10, v220\n"
"v_add_u32 v221, 256, v240\n"
"v_and_b32 v221, 0x3ff, v221\n"
"v_lshl_add_u32 v217, v225, 10, v221\n"
"v_add_u32 v222, 512, v240\n"
"v_and_b32 v222, 0x3ff, v222\n"
"v_lshl_add_u32 v218, v225, 10, v222\n"
"v_add_u32 v223, 768, v240\n"
"v_and_b32 v223, 0x3ff, v223\n"
"v_lshl_add_u32 v219, v225, 10, v223\n"
"s_lshl_b32 s75, s66, 16\n"
"s_lshl_b32 s74, s64, 14\n"
"s_add_u32 s75, s75, s74\n"
"s_lshl_b32 s76, s69, 13\n"
"s_and_b32 s76, s76, 0x3000\n"
"s_waitcnt lgkmcnt(0)\n"
"s_mov_b32 s48, s34\n"
"s_and_b32 s49, s35, 0xffff\n"
"s_mov_b32 s50, 0x300000\n"
"s_mov_b32 s51, 0x20000\n"
"buffer_load_dwordx4 v[64:67], v102, s[48:51], s68 offen\n"
"s_add_u32 s68, s68, 49152\n"
"buffer_load_dwordx4 v[68:71], v102, s[48:51], s68 offen\n"
"s_add_u32 s68, s68, 49152\n"
"buffer_load_dwordx4 v[72:75], v102, s[48:51], s68 offen\n"
"s_add_u32 s68, s68, 49152\n"
"buffer_load_dwordx4 v[76:79], v102, s[48:51], s68 offen\n"
"s_mov_b32 s52, s30\n"
"s_and_b32 s53, s31, 0xffff\n"
"s_mov_b32 s54, 0x400\n"
"s_mov_b32 s55, 0x20000\n"
"buffer_load_ushort v80, v103, s[52:55], s73 offen offset:0\n"
"buffer_load_ushort v81, v103, s[52:55], s73 offen offset:16\n"
"buffer_load_ushort v82, v103, s[52:55], s73 offen offset:32\n"
"buffer_load_ushort v83, v103, s[52:55], s73 offen offset:48\n"
"s_mov_b32 s60, s18\n"
"s_and_b32 s61, s19, 0xffff\n"
"s_mov_b32 s62, 0x4000000\n"
"s_mov_b32 s63, 0x20000\n"
"buffer_load_dword v105, v104, s[60:63], 0 offen offset:0 sc1\n"
"s_mov_b32 s40, s8\n"
"s_and_b32 s41, s9, 0xffff\n"
"s_mov_b32 s42, 0x200000\n"
"s_mov_b32 s43, 0x20000\n"
"s_xor_b32 s74, s76, 0\n"
"s_add_u32 s74, s74, s75\n"
"buffer_load_dwordx4 v[128:131], v216, s[40:43], s74 offen\n"
"buffer_load_dwordx4 v[132:135], v217, s[40:43], s74 offen\n"
"buffer_load_dwordx4 v[136:139], v218, s[40:43], s74 offen\n"
"buffer_load_dwordx4 v[140:143], v219, s[40:43], s74 offen\n"
"s_lshl_b32 s74, s65, 19\n"
"v_mov_b32 v109, s74\n"
"v_add_u32 v110, 393216, v109\n"
"s_lshl_b32 s74, s78, 16\n"
"v_mov_b32 v111, s74\n"
"global_load_dword v255, v104, s[14:15]\n"
"global_load_dword v255, v110, s[10:11]\n"
"global_load_dword v255, v110, s[12:13]\n"
"global_load_dword v255, v109, s[16:17]\n"
"global_load_dword v255, v104, s[20:21]\n"
"global_load_dword v255, v104, s[24:25]\n"
"global_load_dword v255, v104, s[22:23]\n"
"global_load_dword v255, v104, s[26:27]\n"
"global_load_dword v255, v111, s[28:29]\n"
"s_mov_b64 s[18:19], s[70:71]\n"
"s_mov_b32 s31, s39\n"
"s_and_b32 s79, s78, 4\n"
"s_and_b32 s78, s78, 24\n"
"s_lshr_b32 s74, s31, 2\n"
"s_add_u32 s78, s78, s74\n"
"s_and_b32 s74, s31, 3\n"
"s_add_u32 s79, s79, s74\n"
"s_mov_b32 s70, 0x47800080\n"
"s_mov_b32 s71, 0xc7800000\n"
"s_mov_b32 s72, 0x3f803f80\n"
"s_mov_b32 s73, 0x3e0293ee\n"
"s_mov_b32 s39, 0x3f80\n"
"s_mov_b32 s44, s14\n"
"s_and_b32 s45, s15, 0xffff\n"
"s_mov_b32 s46, 0x400000\n"
"s_mov_b32 s47, 0x20000\n"
"s_mov_b32 s56, s16\n"
"s_and_b32 s57, s17, 0xffff\n"
"s_mov_b32 s58, 0x400000\n"
"s_mov_b32 s59, 0x20000\n"
"v_and_b32 v240, 7, v224\n"
"v_xor_b32 v240, v240, v225\n"
"v_lshlrev_b32 v240, 4, v240\n"
"v_lshl_add_u32 v240, v224, 9, v240\n"
"s_lshr_b32 s74, s64, 1\n"
"s_lshl_b32 s74, s74, 15\n"
"s_and_b32 s69, s64, 1\n"
"s_lshl_b32 s69, s69, 8\n"
"s_add_u32 s74, s74, s69\n"
"s_add_u32 s74, s74, 98304\n"
"v_add_u32 v240, s74, v240\n"
"s_lshr_b32 s74, s76, 6\n"
"v_xor_b32 v214, s74, v240\n"
"s_xor_b32 s74, s76, 4096\n"
"s_add_u32 s74, s74, s75\n"
"buffer_load_dwordx4 v[144:147], v216, s[40:43], s74 offen\n"
"buffer_load_dwordx4 v[148:151], v217, s[40:43], s74 offen\n"
"buffer_load_dwordx4 v[152:155], v218, s[40:43], s74 offen\n"
"buffer_load_dwordx4 v[156:159], v219, s[40:43], s74 offen\n"
"v_bfe_u32 v242, v192, 2, 2\n"
"v_xor_b32 v244, v242, v225\n"
"v_lshlrev_b32 v244, 6, v244\n"
"v_and_b32 v243, 3, v192\n"
"v_lshl_add_u32 v244, v243, 4, v244\n"
"v_lshl_add_u32 v213, v225, 8, v244\n"
"v_lshl_add_u32 v205, v225, 13, v244\n"
"s_lshl_b32 s74, s65, 2\n"
"s_add_u32 s74, s74, s64\n"
"s_lshl_b32 s74, s74, 8\n"
"v_add_u32 v205, s74, v205\n"
"v_add_u32 v206, 31744, v205\n"
"v_add_u32 v207, 63488, v205\n"
"v_add_u32 v208, 95232, v205\n"
"v_xor_b32 v244, v224, v225\n"
"v_xor_b32 v245, 0, v244\n"
"v_lshlrev_b32 v245, 4, v245\n"
"v_lshl_add_u32 v209, v225, 8, v245\n"
"v_xor_b32 v245, 4, v244\n"
"v_lshlrev_b32 v245, 4, v245\n"
"v_lshl_add_u32 v210, v225, 8, v245\n"
"v_xor_b32 v245, 8, v244\n"
"v_lshlrev_b32 v245, 4, v245\n"
"v_lshl_add_u32 v211, v225, 8, v245\n"
"v_xor_b32 v245, 12, v244\n"
"v_lshlrev_b32 v245, 4, v245\n"
"v_lshl_add_u32 v212, v225, 8, v245\n"
"v_lshrrev_b32 v246, 5, v192\n"
"v_bfe_u32 v245, v192, 4, 1\n"
"v_and_b32 v215, 1, v192\n"
"v_lshlrev_b32 v215, 4, v215\n"
"v_lshl_add_u32 v215, v245, 5, v215\n"
"v_bfe_u32 v245, v192, 1, 1\n"
"v_lshl_add_u32 v215, v245, 3, v215\n"
"v_and_b32 v244, 24, v215\n"
"v_lshlrev_b32 v244, 1, v244\n"
"v_bfe_u32 v245, v192, 4, 1\n"
"v_lshl_add_u32 v244, v245, 14, v244\n"
"v_lshl_add_u32 v244, v242, 8, v244\n"
"v_lshl_add_u32 v244, v246, 11, v244\n"
"v_add_u32 v244, s67, v244\n"
"v_xor_b32 v245, 0, v242\n"
"v_lshl_add_u32 v197, v245, 6, v244\n"
"v_add_u32 v201, 0x10000, v197\n"
"v_xor_b32 v245, 1, v242\n"
"v_lshl_add_u32 v198, v245, 6, v244\n"
"v_add_u32 v202, 0x10000, v198\n"
"v_xor_b32 v245, 2, v242\n"
"v_lshl_add_u32 v199, v245, 6, v244\n"
"v_add_u32 v203, 0x10000, v199\n"
"v_xor_b32 v245, 3, v242\n"
"v_lshl_add_u32 v200, v245, 6, v244\n"
"v_add_u32 v204, 0x10000, v200\n"
"v_bfe_u32 v245, v192, 4, 1\n"
"v_lshl_add_u32 v244, v245, 1, v246\n"
"v_xor_b32 v247, v224, v244\n"
"v_lshlrev_b32 v247, 4, v247\n"
"v_lshl_add_u32 v247, v244, 9, v247\n"
"v_add_u32 v193, 98304, v247\n"
"v_xor_b32 v194, 64, v193\n"
"v_bfe_u32 v244, v192, 3, 1\n"
"v_bfe_u32 v245, v192, 4, 1\n"
"v_lshl_add_u32 v244, v245, 1, v244\n"
"v_lshl_add_u32 v244, v246, 2, v244\n"
"v_lshl_add_u32 v244, v101, 3, v244\n"
"s_lshl_b32 s74, s64, 8\n"
"s_add_u32 s74, s74, 81920\n"
"v_lshl_add_u32 v106, v244, 2, s74\n"
"v_lshlrev_b32 v107, 2, v192\n"
"v_add_u32 v107, 81920, v107\n"
"v_lshlrev_b32 v108, 3, v192\n"
"s_cmp_eq_u32 s64, 0\n"
"s_cselect_b32 s7, 0, 0x7000000\n"
"s_mul_i32 s35, s37, 24576\n"
"s_lshl_b32 s74, s38, 9\n"
"s_add_u32 s35, s35, s74\n"
"s_add_u32 s35, s35, 262144\n"
"s_add_u32 s35, s35, s7\n"
"s_mov_b32 s38, 0x3f800000\n"
"v_lshlrev_b32 v226, 4, v192\n"
"v_mul_u32_u24 v227, 24576, v242\n"
"v_lshl_add_u32 v227, v243, 4, v227\n"
"v_lshlrev_b32 v228, 3, v243\n"
"s_lshl_b32 s37, s64, 11\n"
"s_add_u32 s37, s37, 262144\n"
"s_lshl_b32 s33, s6, 6\n"
"s_add_u32 s33, s33, 270336\n"
"v_and_b32 v244, 8, v192\n"
"v_cmp_ne_u32 vcc, 0, v244\n"
"v_add_u32 v220, 82944, v220\n"
"v_add_u32 v221, 82944, v221\n"
"v_add_u32 v222, 82944, v222\n"
"v_add_u32 v223, 82944, v223\n"
"v_lshrrev_b32 v229, 2, v192\n"
"v_lshlrev_b32 v229, 4, v229\n"
"v_bfe_u32 v244, v192, 1, 1\n"
"v_lshl_add_u32 v229, v244, 10, v229\n"
"v_and_b32 v244, 1, v192\n"
"v_lshl_add_u32 v229, v244, 3, v229\n"
"s_lshr_b32 s74, s64, 1\n"
"s_mul_i32 s74, s74, 1024\n"
"s_lshl_b32 s69, s64, 9\n"
"s_add_u32 s74, s74, s69\n"
"s_add_u32 s74, s74, 82944\n"
"v_add_u32 v229, s74, v229\n"
"s_mov_b64 exec, 0\n"
"s_cmp_eq_u32 s64, 0\n"
"s_cbranch_scc1 Lst0_%=\n"
"s_cmp_eq_u32 s64, 1\n"
"s_cbranch_scc1 Lst1_%=\n"
"s_cmp_eq_u32 s64, 2\n"
"s_cbranch_scc1 Lst2_%=\n"
"s_branch Lst3_%=\n"
"Lback0_%=:\n"
"s_mov_b64 exec, -1\n"
"s_nop 1\n"
"s_waitcnt vmcnt(21)\n"
"v_lshlrev_b32 v80, 16, v80\n"
"s_waitcnt vmcnt(20)\n"
"v_lshlrev_b32 v81, 16, v81\n"
"s_waitcnt vmcnt(19)\n"
"v_lshlrev_b32 v82, 16, v82\n"
"s_waitcnt vmcnt(18)\n"
"v_lshlrev_b32 v83, 16, v83\n"
"v_mul_f32 v84, 0xbfb8aa3b, v80\n"
"v_mul_f32 v85, 0xbfb8aa3b, v81\n"
"v_mul_f32 v86, 0xbfb8aa3b, v82\n"
"v_mul_f32 v87, 0xbfb8aa3b, v83\n"
"v_exp_f32 v84, v84\n"
"v_exp_f32 v85, v85\n"
"v_exp_f32 v86, v86\n"
"v_exp_f32 v87, v87\n"
"v_add_f32 v84, 1.0, v84\n"
"v_add_f32 v85, 1.0, v85\n"
"v_add_f32 v86, 1.0, v86\n"
"v_add_f32 v87, 1.0, v87\n"
"v_rcp_f32 v84, v84\n"
"v_rcp_f32 v85, v85\n"
"v_rcp_f32 v86, v86\n"
"v_rcp_f32 v87, v87\n"
"v_mul_f32 v84, v84, v80\n"
"v_mul_f32 v85, v85, v81\n"
"v_mul_f32 v86, v86, v82\n"
"v_mul_f32 v87, v87, v83\n"
"v_cvt_pk_bf16_f32 v84, v84, v104\n"
"v_cvt_pk_bf16_f32 v85, v85, v104\n"
"v_cvt_pk_bf16_f32 v86, v86, v104\n"
"v_cvt_pk_bf16_f32 v87, v87, v104\n"
"v_lshlrev_b32 v88, 16, v84\n"
"v_lshlrev_b32 v89, 16, v85\n"
"v_lshlrev_b32 v90, 16, v86\n"
"v_lshlrev_b32 v91, 16, v87\n"
"v_dot2_f32_bf16 v92, v64, v84, 0\n"
"v_dot2_f32_bf16 v93, v64, v88, 0\n"
"v_dot2_f32_bf16 v94, v65, v84, 0\n"
"v_dot2_f32_bf16 v95, v65, v88, 0\n"
"v_dot2_f32_bf16 v96, v66, v84, 0\n"
"v_dot2_f32_bf16 v97, v66, v88, 0\n"
"v_dot2_f32_bf16 v98, v67, v84, 0\n"
"v_dot2_f32_bf16 v99, v67, v88, 0\n"
"v_dot2_f32_bf16 v92, v68, v85, v92\n"
"v_dot2_f32_bf16 v93, v68, v89, v93\n"
"v_dot2_f32_bf16 v94, v69, v85, v94\n"
"v_dot2_f32_bf16 v95, v69, v89, v95\n"
"v_dot2_f32_bf16 v96, v70, v85, v96\n"
"v_dot2_f32_bf16 v97, v70, v89, v97\n"
"v_dot2_f32_bf16 v98, v71, v85, v98\n"
"v_dot2_f32_bf16 v99, v71, v89, v99\n"
"v_dot2_f32_bf16 v92, v72, v86, v92\n"
"v_dot2_f32_bf16 v93, v72, v90, v93\n"
"v_dot2_f32_bf16 v94, v73, v86, v94\n"
"v_dot2_f32_bf16 v95, v73, v90, v95\n"
"v_dot2_f32_bf16 v96, v74, v86, v96\n"
"v_dot2_f32_bf16 v97, v74, v90, v97\n"
"v_dot2_f32_bf16 v98, v75, v86, v98\n"
"v_dot2_f32_bf16 v99, v75, v90, v99\n"
"v_dot2_f32_bf16 v92, v76, v87, v92\n"
"v_dot2_f32_bf16 v93, v76, v91, v93\n"
"v_dot2_f32_bf16 v94, v77, v87, v94\n"
"v_dot2_f32_bf16 v95, v77, v91, v95\n"
"v_dot2_f32_bf16 v96, v78, v87, v96\n"
"v_dot2_f32_bf16 v97, v78, v91, v97\n"
"v_dot2_f32_bf16 v98, v79, v87, v98\n"
"v_dot2_f32_bf16 v99, v79, v91, v99\n"
"s_waitcnt vmcnt(17)\n"
"v_readfirstlane_b32 s36, v105\n"
"v_permlane32_swap_b32 v92, v96\n"
"v_permlane32_swap_b32 v93, v97\n"
"v_permlane32_swap_b32 v94, v98\n"
"v_permlane32_swap_b32 v95, v99\n"
"v_add_f32 v92, v92, v96\n"
"v_add_f32 v93, v93, v97\n"
"v_add_f32 v94, v94, v98\n"
"v_add_f32 v95, v95, v99\n"
"s_add_u32 s36, s36, 1\n"
"v_permlane16_swap_b32 v92, v94\n"
"v_permlane16_swap_b32 v93, v95\n"
"v_add_f32 v92, v92, v94\n"
"v_add_f32 v93, v93, v95\n"
"s_nop 0\n"
"v_add_f32_dpp v92, v92, v92 row_ror:8 row_mask:0xf bank_mask:0xf\n"
"v_add_f32_dpp v93, v93, v93 row_ror:8 row_mask:0xf bank_mask:0xf\n"
"v_cndmask_b32 v92, v92, v93, vcc\n"
"ds_write_b32 v106, v92\n"
"s_waitcnt lgkmcnt(0)\n"
"s_barrier\n"
"ds_read2_b32 v[92:93], v107 offset0:0 offset1:64\n"
"ds_read2_b32 v[94:95], v107 offset0:128 offset1:192\n"
"s_lshl_b32 s74, s65, 19\n"
"s_add_u32 s74, s74, 393216\n"
"s_add_u32 s48, s10, s74\n"
"s_addc_u32 s49, s11, 0\n"
"s_and_b32 s49, s49, 0xffff\n"
"s_mov_b32 s50, 0x20000\n"
"s_mov_b32 s51, 0x20000\n"
"s_add_u32 s52, s12, s74\n"
"s_addc_u32 s53, s13, 0\n"
"s_and_b32 s53, s53, 0xffff\n"
"s_mov_b32 s54, 0x20000\n"
"s_mov_b32 s55, 0x20000\n"
"s_waitcnt lgkmcnt(0)\n"
"v_pk_add_f32 v[92:93], v[92:93], v[94:95]\n"
"v_add_f32 v92, v92, v93\n"
"v_mov_b32 v93, s36\n"
"buffer_store_dwordx2 v[92:93], v108, s[60:63], s35 offen sc1\n"
"s_xor_b32 s74, s76, 8192\n"
"s_add_u32 s74, s74, s75\n"
"buffer_load_dwordx4 v[160:163], v216, s[40:43], s74 offen\n"
"buffer_load_dwordx4 v[164:167], v217, s[40:43], s74 offen\n"
"buffer_load_dwordx4 v[168:171], v218, s[40:43], s74 offen\n"
"buffer_load_dwordx4 v[172:175], v219, s[40:43], s74 offen\n"
"s_waitcnt vmcnt(21)\n"
"v_dot2_f32_bf16 v230, v128, s72, 0\n"
"v_dot2_f32_bf16 v231, v128, v128, 0\n"
"v_dot2_f32_bf16 v230, v129, s72, v230\n"
"v_dot2_f32_bf16 v231, v129, v129, v231\n"
"v_dot2_f32_bf16 v230, v130, s72, v230\n"
"v_dot2_f32_bf16 v231, v130, v130, v231\n"
"v_dot2_f32_bf16 v230, v131, s72, v230\n"
"v_dot2_f32_bf16 v231, v131, v131, v231\n"
"s_waitcnt vmcnt(20)\n"
"v_dot2_f32_bf16 v230, v132, s72, v230\n"
"v_dot2_f32_bf16 v231, v132, v132, v231\n"
"v_dot2_f32_bf16 v230, v133, s72, v230\n"
"v_dot2_f32_bf16 v231, v133, v133, v231\n"
"v_dot2_f32_bf16 v230, v134, s72, v230\n"
"v_dot2_f32_bf16 v231, v134, v134, v231\n"
"v_dot2_f32_bf16 v230, v135, s72, v230\n"
"v_dot2_f32_bf16 v231, v135, v135, v231\n"
"s_waitcnt vmcnt(19)\n"
"v_dot2_f32_bf16 v230, v136, s72, v230\n"
"v_dot2_f32_bf16 v231, v136, v136, v231\n"
"v_dot2_f32_bf16 v230, v137, s72, v230\n"
"v_dot2_f32_bf16 v231, v137, v137, v231\n"
"v_dot2_f32_bf16 v230, v138, s72, v230\n"
"v_dot2_f32_bf16 v231, v138, v138, v231\n"
"v_dot2_f32_bf16 v230, v139, s72, v230\n"
"v_dot2_f32_bf16 v231, v139, v139, v231\n"
"s_waitcnt vmcnt(18)\n"
"v_dot2_f32_bf16 v230, v140, s72, v230\n"
"v_dot2_f32_bf16 v231, v140, v140, v231\n"
"v_dot2_f32_bf16 v230, v141, s72, v230\n"
"v_dot2_f32_bf16 v231, v141, v141, v231\n"
"v_dot2_f32_bf16 v230, v142, s72, v230\n"
"v_dot2_f32_bf16 v231, v142, v142, v231\n"
"v_dot2_f32_bf16 v230, v143, s72, v230\n"
"v_dot2_f32_bf16 v231, v143, v143, v231\n"
"s_xor_b32 s74, s76, 12288\n"
"s_add_u32 s74, s74, s75\n"
"buffer_load_dwordx4 v[176:179], v216, s[40:43], s74 offen\n"
"buffer_load_dwordx4 v[180:183], v217, s[40:43], s74 offen\n"
"buffer_load_dwordx4 v[184:187], v218, s[40:43], s74 offen\n"
"buffer_load_dwordx4 v[188:191], v219, s[40:43], s74 offen\n"
"s_and_b32 s68, s77, 7\n"
"s_mov_b32 s75, 0\n"
"s_and_b32 s68, s68, 3\n"
"s_lshl_b32 s68, s68, 20\n"
"s_add_u32 s75, s75, s67\n"
"s_sub_u32 s76, s75, 0x2000\n"
"s_and_b32 s76, s76, 0x1ffff\n"
"s_lshr_b32 s16, s77, 2\n"
"s_lshl_b32 s17, s16, 17\n"
"s_lshl_b32 s16, s16, 14\n"
"s_waitcnt vmcnt(12)\n"
"v_dot2_f32_bf16 v232, v144, s72, 0\n"
"v_add_f32_dpp v230, v230, v230 row_ror:8 row_mask:0xf bank_mask:0xf\n"
"v_dot2_f32_bf16 v233, v144, v144, 0\n"
"v_add_f32_dpp v231, v231, v231 row_ror:8 row_mask:0xf bank_mask:0xf\n"
"v_dot2_f32_bf16 v232, v145, s72, v232\n"
"v_add_f32_dpp v230, v230, v230 row_ror:4 row_mask:0xf bank_mask:0xf\n"
"v_dot2_f32_bf16 v233, v145, v145, v233\n"
"v_add_f32_dpp v231, v231, v231 row_ror:4 row_mask:0xf bank_mask:0xf\n"
"v_dot2_f32_bf16 v232, v146, s72, v232\n"
"v_add_f32_dpp v230, v230, v230 row_ror:2 row_mask:0xf bank_mask:0xf\n"
"v_dot2_f32_bf16 v233, v146, v146, v233\n"
"v_add_f32_dpp v231, v231, v231 row_ror:2 row_mask:0xf bank_mask:0xf\n"
"v_dot2_f32_bf16 v232, v147, s72, v232\n"
"v_add_f32_dpp v230, v230, v230 row_ror:1 row_mask:0xf bank_mask:0xf\n"
"v_dot2_f32_bf16 v233, v147, v147, v233\n"
"v_add_f32_dpp v231, v231, v231 row_ror:1 row_mask:0xf bank_mask:0xf\n"
"s_waitcnt vmcnt(11)\n"
"v_dot2_f32_bf16 v232, v148, s72, v232\n"
"v_mul_f32 v230, 0x3b000000, v230\n"
"v_dot2_f32_bf16 v233, v148, v148, v233\n"
"v_mul_f32 v231, 0x3b000000, v231\n"
"v_dot2_f32_bf16 v232, v149, s72, v232\n"
"v_fma_f32 v231, -v230, v230, v231\n"
"v_dot2_f32_bf16 v233, v149, v149, v233\n"
"v_max_f32 v231, 0, v231\n"
"v_dot2_f32_bf16 v232, v150, s72, v232\n"
"v_cvt_pk_bf16_f32 v230, 0, v230\n"
"v_dot2_f32_bf16 v233, v150, v150, v233\n"
"v_cvt_pk_bf16_f32 v231, 0, v231\n"
"v_dot2_f32_bf16 v232, v151, s72, v232\n"
"v_add_f32 v231, 0x3727c5ac, v231\n"
"v_dot2_f32_bf16 v233, v151, v151, v233\n"
"v_cvt_pk_bf16_f32 v231, 0, v231\n"
"s_waitcnt vmcnt(10)\n"
"v_dot2_f32_bf16 v232, v152, s72, v232\n"
"v_sqrt_f32 v231, v231\n"
"v_dot2_f32_bf16 v233, v152, v152, v233\n"
"v_cvt_pk_bf16_f32 v231, 0, v231\n"
"v_dot2_f32_bf16 v232, v153, s72, v232\n"
"v_rcp_f32 v231, v231\n"
"v_dot2_f32_bf16 v233, v153, v153, v233\n"
"v_sub_f32 v230, 0, v230\n"
"v_dot2_f32_bf16 v232, v154, s72, v232\n"
"v_dot2_f32_bf16 v233, v154, v154, v233\n"
"v_dot2_f32_bf16 v232, v155, s72, v232\n"
"v_dot2_f32_bf16 v233, v155, v155, v233\n"
"s_waitcnt vmcnt(9)\n"
"v_dot2_f32_bf16 v232, v156, s72, v232\n"
"v_dot2_f32_bf16 v233, v156, v156, v233\n"
"v_dot2_f32_bf16 v232, v157, s72, v232\n"
"v_dot2_f32_bf16 v233, v157, v157, v233\n"
"v_dot2_f32_bf16 v232, v158, s72, v232\n"
"v_dot2_f32_bf16 v233, v158, v158, v233\n"
"v_dot2_f32_bf16 v232, v159, s72, v232\n"
"v_dot2_f32_bf16 v233, v159, v159, v233\n"
"v_dot2_f32_bf16 v0, v128, s39, v230\n"
"s_nop 0\n"
"v_add_f32_dpp v232, v232, v232 row_ror:8 row_mask:0xf bank_mask:0xf\n"
"v_dot2_f32_bf16 v1, v128, s38, v230\n"
"v_add_f32_dpp v233, v233, v233 row_ror:8 row_mask:0xf bank_mask:0xf\n"
"v_dot2_f32_bf16 v2, v129, s39, v230\n"
"v_add_f32_dpp v232, v232, v232 row_ror:4 row_mask:0xf bank_mask:0xf\n"
"v_dot2_f32_bf16 v3, v129, s38, v230\n"
"v_add_f32_dpp v233, v233, v233 row_ror:4 row_mask:0xf bank_mask:0xf\n"
"v_dot2_f32_bf16 v4, v130, s39, v230\n"
"v_add_f32_dpp v232, v232, v232 row_ror:2 row_mask:0xf bank_mask:0xf\n"
"v_dot2_f32_bf16 v5, v130, s38, v230\n"
"v_add_f32_dpp v233, v233, v233 row_ror:2 row_mask:0xf bank_mask:0xf\n"
"v_dot2_f32_bf16 v6, v131, s39, v230\n"
"v_add_f32_dpp v232, v232, v232 row_ror:1 row_mask:0xf bank_mask:0xf\n"
"v_dot2_f32_bf16 v7, v131, s38, v230\n"
"v_add_f32_dpp v233, v233, v233 row_ror:1 row_mask:0xf bank_mask:0xf\n"
"v_dot2_f32_bf16 v8, v132, s39, v230\n"
"v_mul_f32 v232, 0x3b000000, v232\n"
"v_dot2_f32_bf16 v9, v132, s38, v230\n"
"v_mul_f32 v233, 0x3b000000, v233\n"
"v_dot2_f32_bf16 v10, v133, s39, v230\n"
"v_fma_f32 v233, -v232, v232, v233\n"
"v_dot2_f32_bf16 v11, v133, s38, v230\n"
"v_max_f32 v233, 0, v233\n"
"s_xor_b32 s14, s16, 0\n"
"s_add_u32 m0, s67, s14\n"
"s_xor_b32 s14, s17, 0\n"
"s_add_u32 s14, s14, s68\n"
"buffer_load_dwordx4 v205, s[44:47], s14 offen offset:0 lds\n"
"v_dot2_f32_bf16 v12, v134, s39, v230\n"
"v_cvt_pk_bf16_f32 v232, 0, v232\n"
"v_dot2_f32_bf16 v13, v134, s38, v230\n"
"v_cvt_pk_bf16_f32 v233, 0, v233\n"
"v_dot2_f32_bf16 v14, v135, s39, v230\n"
"v_add_f32 v233, 0x3727c5ac, v233\n"
"v_dot2_f32_bf16 v15, v135, s38, v230\n"
"v_cvt_pk_bf16_f32 v233, 0, v233\n"
"v_pk_mul_f32 v[240:241], v[0:1], s[70:71] op_sel_hi:[1,0]\n"
"v_sqrt_f32 v233, v233\n"
"v_pk_mul_f32 v[242:243], v[2:3], s[70:71] op_sel_hi:[1,0]\n"
"v_cvt_pk_bf16_f32 v233, 0, v233\n"
"v_pk_mul_f32 v[244:245], v[4:5], s[70:71] op_sel_hi:[1,0]\n"
"v_rcp_f32 v233, v233\n"
"v_pk_mul_f32 v[246:247], v[6:7], s[70:71] op_sel_hi:[1,0]\n"
"v_sub_f32 v232, 0, v232\n"
"v_pk_mul_f32 v[248:249], v[8:9], s[70:71] op_sel_hi:[1,0]\n"
"v_pk_mul_f32 v[250:251], v[10:11], s[70:71] op_sel_hi:[1,0]\n"
"v_pk_mul_f32 v[252:253], v[12:13], s[70:71] op_sel_hi:[1,0]\n"
"v_pk_mul_f32 v[238:239], v[14:15], s[70:71] op_sel_hi:[1,0]\n"
"v_pk_fma_f32 v[0:1], v[0:1], s[70:71], v[240:241] op_sel:[0,1,0] op_sel_hi:[1,1,1]\n"
"v_pk_fma_f32 v[2:3], v[2:3], s[70:71], v[242:243] op_sel:[0,1,0] op_sel_hi:[1,1,1]\n"
"v_pk_fma_f32 v[4:5], v[4:5], s[70:71], v[244:245] op_sel:[0,1,0] op_sel_hi:[1,1,1]\n"
"v_pk_fma_f32 v[6:7], v[6:7], s[70:71], v[246:247] op_sel:[0,1,0] op_sel_hi:[1,1,1]\n"
"buffer_load_dwordx4 v206, s[44:47], s14 offen offset:1024 lds\n"
"v_pk_fma_f32 v[8:9], v[8:9], s[70:71], v[248:249] op_sel:[0,1,0] op_sel_hi:[1,1,1]\n"
"v_pk_fma_f32 v[10:11], v[10:11], s[70:71], v[250:251] op_sel:[0,1,0] op_sel_hi:[1,1,1]\n"
"v_pk_fma_f32 v[12:13], v[12:13], s[70:71], v[252:253] op_sel:[0,1,0] op_sel_hi:[1,1,1]\n"
"v_pk_fma_f32 v[14:15], v[14:15], s[70:71], v[238:239] op_sel:[0,1,0] op_sel_hi:[1,1,1]\n"
"v_pk_mul_f32 v[0:1], v[0:1], v[230:231] op_sel:[0,1] op_sel_hi:[1,1]\n"
"v_pk_mul_f32 v[2:3], v[2:3], v[230:231] op_sel:[0,1] op_sel_hi:[1,1]\n"
"v_pk_mul_f32 v[4:5], v[4:5], v[230:231] op_sel:[0,1] op_sel_hi:[1,1]\n"
"v_pk_mul_f32 v[6:7], v[6:7], v[230:231] op_sel:[0,1] op_sel_hi:[1,1]\n"
"v_pk_mul_f32 v[8:9], v[8:9], v[230:231] op_sel:[0,1] op_sel_hi:[1,1]\n"
"v_pk_mul_f32 v[10:11], v[10:11], v[230:231] op_sel:[0,1] op_sel_hi:[1,1]\n"
"v_pk_mul_f32 v[12:13], v[12:13], v[230:231] op_sel:[0,1] op_sel_hi:[1,1]\n"
"v_pk_mul_f32 v[14:15], v[14:15], v[230:231] op_sel:[0,1] op_sel_hi:[1,1]\n"
"v_pk_mul_f32 v[240:241], v[0:1], s[70:71] op_sel_hi:[1,0]\n"
"v_pk_mul_f32 v[242:243], v[2:3], s[70:71] op_sel_hi:[1,0]\n"
"v_pk_mul_f32 v[244:245], v[4:5], s[70:71] op_sel_hi:[1,0]\n"
"v_pk_mul_f32 v[246:247], v[6:7], s[70:71] op_sel_hi:[1,0]\n"
"v_pk_mul_f32 v[248:249], v[8:9], s[70:71] op_sel_hi:[1,0]\n"
"v_pk_mul_f32 v[250:251], v[10:11], s[70:71] op_sel_hi:[1,0]\n"
"v_pk_mul_f32 v[252:253], v[12:13], s[70:71] op_sel_hi:[1,0]\n"
"v_pk_mul_f32 v[238:239], v[14:15], s[70:71] op_sel_hi:[1,0]\n"
"v_pk_fma_f32 v[0:1], v[0:1], s[70:71], v[240:241] op_sel:[0,1,0] op_sel_hi:[1,1,1]\n"
"v_pk_fma_f32 v[2:3], v[2:3], s[70:71], v[242:243] op_sel:[0,1,0] op_sel_hi:[1,1,1]\n"
"v_pk_fma_f32 v[4:5], v[4:5], s[70:71], v[244:245] op_sel:[0,1,0] op_sel_hi:[1,1,1]\n"
"v_pk_fma_f32 v[6:7], v[6:7], s[70:71], v[246:247] op_sel:[0,1,0] op_sel_hi:[1,1,1]\n"
"buffer_load_dwordx4 v207, s[44:47], s14 offen offset:2048 lds\n"
"v_pk_fma_f32 v[8:9], v[8:9], s[70:71], v[248:249] op_sel:[0,1,0] op_sel_hi:[1,1,1]\n"
"v_pk_fma_f32 v[10:11], v[10:11], s[70:71], v[250:251] op_sel:[0,1,0] op_sel_hi:[1,1,1]\n"
"v_pk_fma_f32 v[12:13], v[12:13], s[70:71], v[252:253] op_sel:[0,1,0] op_sel_hi:[1,1,1]\n"
"v_pk_fma_f32 v[14:15], v[14:15], s[70:71], v[238:239] op_sel:[0,1,0] op_sel_hi:[1,1,1]\n"
"v_dot2_f32_bf16 v16, v136, s39, v230\n"
"v_dot2_f32_bf16 v17, v136, s38, v230\n"
"v_dot2_f32_bf16 v18, v137, s39, v230\n"
"v_dot2_f32_bf16 v19, v137, s38, v230\n"
"v_dot2_f32_bf16 v20, v138, s39, v230\n"
"v_dot2_f32_bf16 v21, v138, s38, v230\n"
"v_dot2_f32_bf16 v22, v139, s39, v230\n"
"v_dot2_f32_bf16 v23, v139, s38, v230\n"
"v_dot2_f32_bf16 v24, v140, s39, v230\n"
"v_dot2_f32_bf16 v25, v140, s38, v230\n"
"v_dot2_f32_bf16 v26, v141, s39, v230\n"
"v_dot2_f32_bf16 v27, v141, s38, v230\n"
"v_dot2_f32_bf16 v28, v142, s39, v230\n"
"v_dot2_f32_bf16 v29, v142, s38, v230\n"
"v_dot2_f32_bf16 v30, v143, s39, v230\n"
"v_dot2_f32_bf16 v31, v143, s38, v230\n"
"v_pk_mul_f32 v[240:241], v[16:17], s[70:71] op_sel_hi:[1,0]\n"
"v_pk_mul_f32 v[242:243], v[18:19], s[70:71] op_sel_hi:[1,0]\n"
"v_pk_mul_f32 v[244:245], v[20:21], s[70:71] op_sel_hi:[1,0]\n"
"v_pk_mul_f32 v[246:247], v[22:23], s[70:71] op_sel_hi:[1,0]\n"
"buffer_load_dwordx4 v208, s[44:47], s14 offen offset:3072 lds\n"
"v_pk_mul_f32 v[248:249], v[24:25], s[70:71] op_sel_hi:[1,0]\n"
"v_pk_mul_f32 v[250:251], v[26:27], s[70:71] op_sel_hi:[1,0]\n"
"v_pk_mul_f32 v[252:253], v[28:29], s[70:71] op_sel_hi:[1,0]\n"
"v_pk_mul_f32 v[238:239], v[30:31], s[70:71] op_sel_hi:[1,0]\n"
"v_pk_fma_f32 v[16:17], v[16:17], s[70:71], v[240:241] op_sel:[0,1,0] op_sel_hi:[1,1,1]\n"
"v_pk_fma_f32 v[18:19], v[18:19], s[70:71], v[242:243] op_sel:[0,1,0] op_sel_hi:[1,1,1]\n"
"v_pk_fma_f32 v[20:21], v[20:21], s[70:71], v[244:245] op_sel:[0,1,0] op_sel_hi:[1,1,1]\n"
"v_pk_fma_f32 v[22:23], v[22:23], s[70:71], v[246:247] op_sel:[0,1,0] op_sel_hi:[1,1,1]\n"
"v_pk_fma_f32 v[24:25], v[24:25], s[70:71], v[248:249] op_sel:[0,1,0] op_sel_hi:[1,1,1]\n"
"v_pk_fma_f32 v[26:27], v[26:27], s[70:71], v[250:251] op_sel:[0,1,0] op_sel_hi:[1,1,1]\n"
"v_pk_fma_f32 v[28:29], v[28:29], s[70:71], v[252:253] op_sel:[0,1,0] op_sel_hi:[1,1,1]\n"
"v_pk_fma_f32 v[30:31], v[30:31], s[70:71], v[238:239] op_sel:[0,1,0] op_sel_hi:[1,1,1]\n"
"v_pk_mul_f32 v[16:17], v[16:17], v[230:231] op_sel:[0,1] op_sel_hi:[1,1]\n"
"v_pk_mul_f32 v[18:19], v[18:19], v[230:231] op_sel:[0,1] op_sel_hi:[1,1]\n"
"v_pk_mul_f32 v[20:21], v[20:21], v[230:231] op_sel:[0,1] op_sel_hi:[1,1]\n"
"v_pk_mul_f32 v[22:23], v[22:23], v[230:231] op_sel:[0,1] op_sel_hi:[1,1]\n"
"v_pk_mul_f32 v[24:25], v[24:25], v[230:231] op_sel:[0,1] op_sel_hi:[1,1]\n"
"v_pk_mul_f32 v[26:27], v[26:27], v[230:231] op_sel:[0,1] op_sel_hi:[1,1]\n"
"v_pk_mul_f32 v[28:29], v[28:29], v[230:231] op_sel:[0,1] op_sel_hi:[1,1]\n"
"v_pk_mul_f32 v[30:31], v[30:31], v[230:231] op_sel:[0,1] op_sel_hi:[1,1]\n"
"v_pk_mul_f32 v[240:241], v[16:17], s[70:71] op_sel_hi:[1,0]\n"
"v_pk_mul_f32 v[242:243], v[18:19], s[70:71] op_sel_hi:[1,0]\n"
"v_pk_mul_f32 v[244:245], v[20:21], s[70:71] op_sel_hi:[1,0]\n"
"v_pk_mul_f32 v[246:247], v[22:23], s[70:71] op_sel_hi:[1,0]\n"
"s_xor_b32 s14, s16, 16384\n"
"Lst0_%=:\n"
"s_cbranch_execz Lst4_%=\n"
"s_add_u32 m0, s67, s14\n"
"s_xor_b32 s14, s17, 131072\n"
"s_add_u32 s14, s14, s68\n"
"buffer_load_dwordx4 v205, s[44:47], s14 offen offset:0 lds\n"
"v_pk_mul_f32 v[248:249], v[24:25], s[70:71] op_sel_hi:[1,0]\n"
"v_pk_mul_f32 v[250:251], v[26:27], s[70:71] op_sel_hi:[1,0]\n"
"v_pk_mul_f32 v[252:253], v[28:29], s[70:71] op_sel_hi:[1,0]\n"
"v_pk_mul_f32 v[238:239], v[30:31], s[70:71] op_sel_hi:[1,0]\n"
"v_pk_fma_f32 v[16:17], v[16:17], s[70:71], v[240:241] op_sel:[0,1,0] op_sel_hi:[1,1,1]\n"
"v_pk_fma_f32 v[18:19], v[18:19], s[70:71], v[242:243] op_sel:[0,1,0] op_sel_hi:[1,1,1]\n"
"v_pk_fma_f32 v[20:21], v[20:21], s[70:71], v[244:245] op_sel:[0,1,0] op_sel_hi:[1,1,1]\n"
"v_pk_fma_f32 v[22:23], v[22:23], s[70:71], v[246:247] op_sel:[0,1,0] op_sel_hi:[1,1,1]\n"
"v_pk_fma_f32 v[24:25], v[24:25], s[70:71], v[248:249] op_sel:[0,1,0] op_sel_hi:[1,1,1]\n"
"v_pk_fma_f32 v[26:27], v[26:27], s[70:71], v[250:251] op_sel:[0,1,0] op_sel_hi:[1,1,1]\n"
"v_pk_fma_f32 v[28:29], v[28:29], s[70:71], v[252:253] op_sel:[0,1,0] op_sel_hi:[1,1,1]\n"
"v_pk_fma_f32 v[30:31], v[30:31], s[70:71], v[238:239] op_sel:[0,1,0] op_sel_hi:[1,1,1]\n"
"v_dot2_f32_bf16 v32, v144, s39, v232\n"
"v_dot2_f32_bf16 v33, v144, s38, v232\n"
"v_dot2_f32_bf16 v34, v145, s39, v232\n"
"v_dot2_f32_bf16 v35, v145, s38, v232\n"
"v_dot2_f32_bf16 v36, v146, s39, v232\n"
"v_dot2_f32_bf16 v37, v146, s38, v232\n"
"v_dot2_f32_bf16 v38, v147, s39, v232\n"
"v_dot2_f32_bf16 v39, v147, s38, v232\n"
"v_dot2_f32_bf16 v40, v148, s39, v232\n"
"v_dot2_f32_bf16 v41, v148, s38, v232\n"
"v_dot2_f32_bf16 v42, v149, s39, v232\n"
"v_dot2_f32_bf16 v43, v149, s38, v232\n"
"buffer_load_dwordx4 v206, s[44:47], s14 offen offset:1024 lds\n"
"v_dot2_f32_bf16 v44, v150, s39, v232\n"
"v_dot2_f32_bf16 v45, v150, s38, v232\n"
"v_dot2_f32_bf16 v46, v151, s39, v232\n"
"v_dot2_f32_bf16 v47, v151, s38, v232\n"
"v_pk_mul_f32 v[240:241], v[32:33], s[70:71] op_sel_hi:[1,0]\n"
"v_pk_mul_f32 v[242:243], v[34:35], s[70:71] op_sel_hi:[1,0]\n"
"v_pk_mul_f32 v[244:245], v[36:37], s[70:71] op_sel_hi:[1,0]\n"
"v_pk_mul_f32 v[246:247], v[38:39], s[70:71] op_sel_hi:[1,0]\n"
"v_pk_mul_f32 v[248:249], v[40:41], s[70:71] op_sel_hi:[1,0]\n"
"v_pk_mul_f32 v[250:251], v[42:43], s[70:71] op_sel_hi:[1,0]\n"
"v_pk_mul_f32 v[252:253], v[44:45], s[70:71] op_sel_hi:[1,0]\n"
"v_pk_mul_f32 v[238:239], v[46:47], s[70:71] op_sel_hi:[1,0]\n"
"v_pk_fma_f32 v[32:33], v[32:33], s[70:71], v[240:241] op_sel:[0,1,0] op_sel_hi:[1,1,1]\n"
"v_pk_fma_f32 v[34:35], v[34:35], s[70:71], v[242:243] op_sel:[0,1,0] op_sel_hi:[1,1,1]\n"
"v_pk_fma_f32 v[36:37], v[36:37], s[70:71], v[244:245] op_sel:[0,1,0] op_sel_hi:[1,1,1]\n"
"v_pk_fma_f32 v[38:39], v[38:39], s[70:71], v[246:247] op_sel:[0,1,0] op_sel_hi:[1,1,1]\n"
"v_pk_fma_f32 v[40:41], v[40:41], s[70:71], v[248:249] op_sel:[0,1,0] op_sel_hi:[1,1,1]\n"
"v_pk_fma_f32 v[42:43], v[42:43], s[70:71], v[250:251] op_sel:[0,1,0] op_sel_hi:[1,1,1]\n"
"v_pk_fma_f32 v[44:45], v[44:45], s[70:71], v[252:253] op_sel:[0,1,0] op_sel_hi:[1,1,1]\n"
"v_pk_fma_f32 v[46:47], v[46:47], s[70:71], v[238:239] op_sel:[0,1,0] op_sel_hi:[1,1,1]\n"
"v_pk_mul_f32 v[32:33], v[32:33], v[232:233] op_sel:[0,1] op_sel_hi:[1,1]\n"
"v_pk_mul_f32 v[34:35], v[34:35], v[232:233] op_sel:[0,1] op_sel_hi:[1,1]\n"
"v_pk_mul_f32 v[36:37], v[36:37], v[232:233] op_sel:[0,1] op_sel_hi:[1,1]\n"
"v_pk_mul_f32 v[38:39], v[38:39], v[232:233] op_sel:[0,1] op_sel_hi:[1,1]\n"
"buffer_load_dwordx4 v207, s[44:47], s14 offen offset:2048 lds\n"
"v_pk_mul_f32 v[40:41], v[40:41], v[232:233] op_sel:[0,1] op_sel_hi:[1,1]\n"
"v_pk_mul_f32 v[42:43], v[42:43], v[232:233] op_sel:[0,1] op_sel_hi:[1,1]\n"
"v_pk_mul_f32 v[44:45], v[44:45], v[232:233] op_sel:[0,1] op_sel_hi:[1,1]\n"
"v_pk_mul_f32 v[46:47], v[46:47], v[232:233] op_sel:[0,1] op_sel_hi:[1,1]\n"
"v_pk_mul_f32 v[240:241], v[32:33], s[70:71] op_sel_hi:[1,0]\n"
"v_pk_mul_f32 v[242:243], v[34:35], s[70:71] op_sel_hi:[1,0]\n"
"v_pk_mul_f32 v[244:245], v[36:37], s[70:71] op_sel_hi:[1,0]\n"
"v_pk_mul_f32 v[246:247], v[38:39], s[70:71] op_sel_hi:[1,0]\n"
"v_pk_mul_f32 v[248:249], v[40:41], s[70:71] op_sel_hi:[1,0]\n"
"v_pk_mul_f32 v[250:251], v[42:43], s[70:71] op_sel_hi:[1,0]\n"
"v_pk_mul_f32 v[252:253], v[44:45], s[70:71] op_sel_hi:[1,0]\n"
"v_pk_mul_f32 v[238:239], v[46:47], s[70:71] op_sel_hi:[1,0]\n"
"v_pk_fma_f32 v[32:33], v[32:33], s[70:71], v[240:241] op_sel:[0,1,0] op_sel_hi:[1,1,1]\n"
"v_pk_fma_f32 v[34:35], v[34:35], s[70:71], v[242:243] op_sel:[0,1,0] op_sel_hi:[1,1,1]\n"
"v_pk_fma_f32 v[36:37], v[36:37], s[70:71], v[244:245] op_sel:[0,1,0] op_sel_hi:[1,1,1]\n"
"v_pk_fma_f32 v[38:39], v[38:39], s[70:71], v[246:247] op_sel:[0,1,0] op_sel_hi:[1,1,1]\n"
"v_pk_fma_f32 v[40:41], v[40:41], s[70:71], v[248:249] op_sel:[0,1,0] op_sel_hi:[1,1,1]\n"
"v_pk_fma_f32 v[42:43], v[42:43], s[70:71], v[250:251] op_sel:[0,1,0] op_sel_hi:[1,1,1]\n"
"v_pk_fma_f32 v[44:45], v[44:45], s[70:71], v[252:253] op_sel:[0,1,0] op_sel_hi:[1,1,1]\n"
"v_pk_fma_f32 v[46:47], v[46:47], s[70:71], v[238:239] op_sel:[0,1,0] op_sel_hi:[1,1,1]\n"
"v_dot2_f32_bf16 v48, v152, s39, v232\n"
"v_dot2_f32_bf16 v49, v152, s38, v232\n"
"v_dot2_f32_bf16 v50, v153, s39, v232\n"
"v_dot2_f32_bf16 v51, v153, s38, v232\n"
"buffer_load_dwordx4 v208, s[44:47], s14 offen offset:3072 lds\n"
"v_dot2_f32_bf16 v52, v154, s39, v232\n"
"v_dot2_f32_bf16 v53, v154, s38, v232\n"
"v_dot2_f32_bf16 v54, v155, s39, v232\n"
"v_dot2_f32_bf16 v55, v155, s38, v232\n"
"v_dot2_f32_bf16 v56, v156, s39, v232\n"
"v_dot2_f32_bf16 v57, v156, s38, v232\n"
"v_dot2_f32_bf16 v58, v157, s39, v232\n"
"v_dot2_f32_bf16 v59, v157, s38, v232\n"
"v_dot2_f32_bf16 v60, v158, s39, v232\n"
"v_dot2_f32_bf16 v61, v158, s38, v232\n"
"v_dot2_f32_bf16 v62, v159, s39, v232\n"
"v_dot2_f32_bf16 v63, v159, s38, v232\n"
"v_pk_mul_f32 v[240:241], v[48:49], s[70:71] op_sel_hi:[1,0]\n"
"v_pk_mul_f32 v[242:243], v[50:51], s[70:71] op_sel_hi:[1,0]\n"
"v_pk_mul_f32 v[244:245], v[52:53], s[70:71] op_sel_hi:[1,0]\n"
"v_pk_mul_f32 v[246:247], v[54:55], s[70:71] op_sel_hi:[1,0]\n"
"v_pk_mul_f32 v[248:249], v[56:57], s[70:71] op_sel_hi:[1,0]\n"
"v_pk_mul_f32 v[250:251], v[58:59], s[70:71] op_sel_hi:[1,0]\n"
"v_pk_mul_f32 v[252:253], v[60:61], s[70:71] op_sel_hi:[1,0]\n"
"v_pk_mul_f32 v[238:239], v[62:63], s[70:71] op_sel_hi:[1,0]\n"
"v_pk_fma_f32 v[48:49], v[48:49], s[70:71], v[240:241] op_sel:[0,1,0] op_sel_hi:[1,1,1]\n"
"v_pk_fma_f32 v[50:51], v[50:51], s[70:71], v[242:243] op_sel:[0,1,0] op_sel_hi:[1,1,1]\n"
"v_pk_fma_f32 v[52:53], v[52:53], s[70:71], v[244:245] op_sel:[0,1,0] op_sel_hi:[1,1,1]\n"
"v_pk_fma_f32 v[54:55], v[54:55], s[70:71], v[246:247] op_sel:[0,1,0] op_sel_hi:[1,1,1]\n"
"s_xor_b32 s14, s16, 32768\n"
"s_add_u32 m0, s67, s14\n"
"s_xor_b32 s14, s17, 262144\n"
"s_add_u32 s14, s14, s68\n"
"buffer_load_dwordx4 v205, s[44:47], s14 offen offset:0 lds\n"
"v_pk_fma_f32 v[56:57], v[56:57], s[70:71], v[248:249] op_sel:[0,1,0] op_sel_hi:[1,1,1]\n"
"v_pk_fma_f32 v[58:59], v[58:59], s[70:71], v[250:251] op_sel:[0,1,0] op_sel_hi:[1,1,1]\n"
"v_pk_fma_f32 v[60:61], v[60:61], s[70:71], v[252:253] op_sel:[0,1,0] op_sel_hi:[1,1,1]\n"
"v_pk_fma_f32 v[62:63], v[62:63], s[70:71], v[238:239] op_sel:[0,1,0] op_sel_hi:[1,1,1]\n"
"v_pk_mul_f32 v[48:49], v[48:49], v[232:233] op_sel:[0,1] op_sel_hi:[1,1]\n"
"v_pk_mul_f32 v[50:51], v[50:51], v[232:233] op_sel:[0,1] op_sel_hi:[1,1]\n"
"v_pk_mul_f32 v[52:53], v[52:53], v[232:233] op_sel:[0,1] op_sel_hi:[1,1]\n"
"v_pk_mul_f32 v[54:55], v[54:55], v[232:233] op_sel:[0,1] op_sel_hi:[1,1]\n"
"v_pk_mul_f32 v[56:57], v[56:57], v[232:233] op_sel:[0,1] op_sel_hi:[1,1]\n"
"v_pk_mul_f32 v[58:59], v[58:59], v[232:233] op_sel:[0,1] op_sel_hi:[1,1]\n"
"v_pk_mul_f32 v[60:61], v[60:61], v[232:233] op_sel:[0,1] op_sel_hi:[1,1]\n"
"v_pk_mul_f32 v[62:63], v[62:63], v[232:233] op_sel:[0,1] op_sel_hi:[1,1]\n"
"v_pk_mul_f32 v[240:241], v[48:49], s[70:71] op_sel_hi:[1,0]\n"
"v_pk_mul_f32 v[242:243], v[50:51], s[70:71] op_sel_hi:[1,0]\n"
"v_pk_mul_f32 v[244:245], v[52:53], s[70:71] op_sel_hi:[1,0]\n"
"v_pk_mul_f32 v[246:247], v[54:55], s[70:71] op_sel_hi:[1,0]\n"
"v_pk_mul_f32 v[248:249], v[56:57], s[70:71] op_sel_hi:[1,0]\n"
"v_pk_mul_f32 v[250:251], v[58:59], s[70:71] op_sel_hi:[1,0]\n"
"v_pk_mul_f32 v[252:253], v[60:61], s[70:71] op_sel_hi:[1,0]\n"
"v_pk_mul_f32 v[238:239], v[62:63], s[70:71] op_sel_hi:[1,0]\n"
"v_pk_fma_f32 v[48:49], v[48:49], s[70:71], v[240:241] op_sel:[0,1,0] op_sel_hi:[1,1,1]\n"
"v_pk_fma_f32 v[50:51], v[50:51], s[70:71], v[242:243] op_sel:[0,1,0] op_sel_hi:[1,1,1]\n"
"v_pk_fma_f32 v[52:53], v[52:53], s[70:71], v[244:245] op_sel:[0,1,0] op_sel_hi:[1,1,1]\n"
"v_pk_fma_f32 v[54:55], v[54:55], s[70:71], v[246:247] op_sel:[0,1,0] op_sel_hi:[1,1,1]\n"
"buffer_load_dwordx4 v206, s[44:47], s14 offen offset:1024 lds\n"
"v_pk_fma_f32 v[56:57], v[56:57], s[70:71], v[248:249] op_sel:[0,1,0] op_sel_hi:[1,1,1]\n"
"v_pk_fma_f32 v[58:59], v[58:59], s[70:71], v[250:251] op_sel:[0,1,0] op_sel_hi:[1,1,1]\n"
"v_pk_fma_f32 v[60:61], v[60:61], s[70:71], v[252:253] op_sel:[0,1,0] op_sel_hi:[1,1,1]\n"
"v_pk_fma_f32 v[62:63], v[62:63], s[70:71], v[238:239] op_sel:[0,1,0] op_sel_hi:[1,1,1]\n"
"s_waitcnt vmcnt(17)\n"
"v_dot2_f32_bf16 v234, v160, s72, 0\n"
"v_dot2_f32_bf16 v235, v160, v160, 0\n"
"v_dot2_f32_bf16 v234, v161, s72, v234\n"
"v_dot2_f32_bf16 v235, v161, v161, v235\n"
"v_dot2_f32_bf16 v234, v162, s72, v234\n"
"v_dot2_f32_bf16 v235, v162, v162, v235\n"
"v_dot2_f32_bf16 v234, v163, s72, v234\n"
"v_dot2_f32_bf16 v235, v163, v163, v235\n"
"s_waitcnt vmcnt(16)\n"
"v_dot2_f32_bf16 v234, v164, s72, v234\n"
"v_dot2_f32_bf16 v235, v164, v164, v235\n"
"v_dot2_f32_bf16 v234, v165, s72, v234\n"
"v_dot2_f32_bf16 v235, v165, v165, v235\n"
"v_dot2_f32_bf16 v234, v166, s72, v234\n"
"v_dot2_f32_bf16 v235, v166, v166, v235\n"
"v_dot2_f32_bf16 v234, v167, s72, v234\n"
"v_dot2_f32_bf16 v235, v167, v167, v235\n"
"s_waitcnt vmcnt(15)\n"
"v_dot2_f32_bf16 v234, v168, s72, v234\n"
"v_dot2_f32_bf16 v235, v168, v168, v235\n"
"v_dot2_f32_bf16 v234, v169, s72, v234\n"
"v_dot2_f32_bf16 v235, v169, v169, v235\n"
"v_dot2_f32_bf16 v234, v170, s72, v234\n"
"v_dot2_f32_bf16 v235, v170, v170, v235\n"
"v_dot2_f32_bf16 v234, v171, s72, v234\n"
"v_dot2_f32_bf16 v235, v171, v171, v235\n"
"s_waitcnt vmcnt(14)\n"
"v_dot2_f32_bf16 v234, v172, s72, v234\n"
"v_dot2_f32_bf16 v235, v172, v172, v235\n"
"v_dot2_f32_bf16 v234, v173, s72, v234\n"
"v_dot2_f32_bf16 v235, v173, v173, v235\n"
"v_dot2_f32_bf16 v234, v174, s72, v234\n"
"v_dot2_f32_bf16 v235, v174, v174, v235\n"
"v_dot2_f32_bf16 v234, v175, s72, v234\n"
"v_dot2_f32_bf16 v235, v175, v175, v235\n"
"s_waitcnt vmcnt(13)\n"
"v_dot2_f32_bf16 v236, v176, s72, 0\n"
"v_add_f32_dpp v234, v234, v234 row_ror:8 row_mask:0xf bank_mask:0xf\n"
"v_dot2_f32_bf16 v237, v176, v176, 0\n"
"v_add_f32_dpp v235, v235, v235 row_ror:8 row_mask:0xf bank_mask:0xf\n"
"v_dot2_f32_bf16 v236, v177, s72, v236\n"
"v_add_f32_dpp v234, v234, v234 row_ror:4 row_mask:0xf bank_mask:0xf\n"
"v_dot2_f32_bf16 v237, v177, v177, v237\n"
"v_add_f32_dpp v235, v235, v235 row_ror:4 row_mask:0xf bank_mask:0xf\n"
"v_dot2_f32_bf16 v236, v178, s72, v236\n"
"v_add_f32_dpp v234, v234, v234 row_ror:2 row_mask:0xf bank_mask:0xf\n"
"v_dot2_f32_bf16 v237, v178, v178, v237\n"
"v_add_f32_dpp v235, v235, v235 row_ror:2 row_mask:0xf bank_mask:0xf\n"
"v_dot2_f32_bf16 v236, v179, s72, v236\n"
"v_add_f32_dpp v234, v234, v234 row_ror:1 row_mask:0xf bank_mask:0xf\n"
"v_dot2_f32_bf16 v237, v179, v179, v237\n"
"v_add_f32_dpp v235, v235, v235 row_ror:1 row_mask:0xf bank_mask:0xf\n"
"s_waitcnt vmcnt(12)\n"
"v_dot2_f32_bf16 v236, v180, s72, v236\n"
"v_mul_f32 v234, 0x3b000000, v234\n"
"v_dot2_f32_bf16 v237, v180, v180, v237\n"
"v_mul_f32 v235, 0x3b000000, v235\n"
"v_dot2_f32_bf16 v236, v181, s72, v236\n"
"v_fma_f32 v235, -v234, v234, v235\n"
"v_dot2_f32_bf16 v237, v181, v181, v237\n"
"v_max_f32 v235, 0, v235\n"
"v_dot2_f32_bf16 v236, v182, s72, v236\n"
"v_cvt_pk_bf16_f32 v234, 0, v234\n"
"v_dot2_f32_bf16 v237, v182, v182, v237\n"
"v_cvt_pk_bf16_f32 v235, 0, v235\n"
"v_dot2_f32_bf16 v236, v183, s72, v236\n"
"v_add_f32 v235, 0x3727c5ac, v235\n"
"v_dot2_f32_bf16 v237, v183, v183, v237\n"
"v_cvt_pk_bf16_f32 v235, 0, v235\n"
"s_waitcnt vmcnt(11)\n"
"v_dot2_f32_bf16 v236, v184, s72, v236\n"
"v_sqrt_f32 v235, v235\n"
"v_dot2_f32_bf16 v237, v184, v184, v237\n"
"v_cvt_pk_bf16_f32 v235, 0, v235\n"
"v_dot2_f32_bf16 v236, v185, s72, v236\n"
"v_rcp_f32 v235, v235\n"
"v_dot2_f32_bf16 v237, v185, v185, v237\n"
"v_sub_f32 v234, 0, v234\n"
"v_dot2_f32_bf16 v236, v186, s72, v236\n"
"v_dot2_f32_bf16 v237, v186, v186, v237\n"
"v_dot2_f32_bf16 v236, v187, s72, v236\n"
"v_dot2_f32_bf16 v237, v187, v187, v237\n"
"s_waitcnt vmcnt(10)\n"
"v_dot2_f32_bf16 v236, v188, s72, v236\n"
"v_dot2_f32_bf16 v237, v188, v188, v237\n"
"v_dot2_f32_bf16 v236, v189, s72, v236\n"
"v_dot2_f32_bf16 v237, v189, v189, v237\n"
"v_dot2_f32_bf16 v236, v190, s72, v236\n"
"v_dot2_f32_bf16 v237, v190, v190, v237\n"
"v_dot2_f32_bf16 v236, v191, s72, v236\n"
"v_dot2_f32_bf16 v237, v191, v191, v237\n"
"v_dot2_f32_bf16 v64, v160, s39, v234\n"
"s_nop 0\n"
"v_add_f32_dpp v236, v236, v236 row_ror:8 row_mask:0xf bank_mask:0xf\n"
"v_dot2_f32_bf16 v65, v160, s38, v234\n"
"v_add_f32_dpp v237, v237, v237 row_ror:8 row_mask:0xf bank_mask:0xf\n"
"v_dot2_f32_bf16 v66, v161, s39, v234\n"
"v_add_f32_dpp v236, v236, v236 row_ror:4 row_mask:0xf bank_mask:0xf\n"
"v_dot2_f32_bf16 v67, v161, s38, v234\n"
"v_add_f32_dpp v237, v237, v237 row_ror:4 row_mask:0xf bank_mask:0xf\n"
"v_dot2_f32_bf16 v68, v162, s39, v234\n"
"v_add_f32_dpp v236, v236, v236 row_ror:2 row_mask:0xf bank_mask:0xf\n"
"v_dot2_f32_bf16 v69, v162, s38, v234\n"
"v_add_f32_dpp v237, v237, v237 row_ror:2 row_mask:0xf bank_mask:0xf\n"
"v_dot2_f32_bf16 v70, v163, s39, v234\n"
"v_add_f32_dpp v236, v236, v236 row_ror:1 row_mask:0xf bank_mask:0xf\n"
"v_dot2_f32_bf16 v71, v163, s38, v234\n"
"v_add_f32_dpp v237, v237, v237 row_ror:1 row_mask:0xf bank_mask:0xf\n"
"buffer_load_dwordx4 v207, s[44:47], s14 offen offset:2048 lds\n"
"v_dot2_f32_bf16 v72, v164, s39, v234\n"
"v_mul_f32 v236, 0x3b000000, v236\n"
"v_dot2_f32_bf16 v73, v164, s38, v234\n"
"v_mul_f32 v237, 0x3b000000, v237\n"
"v_dot2_f32_bf16 v74, v165, s39, v234\n"
"v_fma_f32 v237, -v236, v236, v237\n"
"v_dot2_f32_bf16 v75, v165, s38, v234\n"
"v_max_f32 v237, 0, v237\n"
"v_dot2_f32_bf16 v76, v166, s39, v234\n"
"v_cvt_pk_bf16_f32 v236, 0, v236\n"
"v_dot2_f32_bf16 v77, v166, s38, v234\n"
"v_cvt_pk_bf16_f32 v237, 0, v237\n"
"v_dot2_f32_bf16 v78, v167, s39, v234\n"
"v_add_f32 v237, 0x3727c5ac, v237\n"
"v_dot2_f32_bf16 v79, v167, s38, v234\n"
"v_cvt_pk_bf16_f32 v237, 0, v237\n"
"buffer_load_dwordx4 v208, s[44:47], s14 offen offset:3072 lds\n"
"v_pk_mul_f32 v[240:241], v[64:65], s[70:71] op_sel_hi:[1,0]\n"
"v_sqrt_f32 v237, v237\n"
"v_pk_mul_f32 v[242:243], v[66:67], s[70:71] op_sel_hi:[1,0]\n"
"v_cvt_pk_bf16_f32 v237, 0, v237\n"
"v_pk_mul_f32 v[244:245], v[68:69], s[70:71] op_sel_hi:[1,0]\n"
"v_rcp_f32 v237, v237\n"
"v_pk_mul_f32 v[246:247], v[70:71], s[70:71] op_sel_hi:[1,0]\n"
"v_sub_f32 v236, 0, v236\n"
"v_pk_mul_f32 v[248:249], v[72:73], s[70:71] op_sel_hi:[1,0]\n"
"v_pk_mul_f32 v[250:251], v[74:75], s[70:71] op_sel_hi:[1,0]\n"
"v_pk_mul_f32 v[252:253], v[76:77], s[70:71] op_sel_hi:[1,0]\n"
"v_pk_mul_f32 v[238:239], v[78:79], s[70:71] op_sel_hi:[1,0]\n"
"v_pk_fma_f32 v[64:65], v[64:65], s[70:71], v[240:241] op_sel:[0,1,0] op_sel_hi:[1,1,1]\n"
"v_pk_fma_f32 v[66:67], v[66:67], s[70:71], v[242:243] op_sel:[0,1,0] op_sel_hi:[1,1,1]\n"
"v_pk_fma_f32 v[68:69], v[68:69], s[70:71], v[244:245] op_sel:[0,1,0] op_sel_hi:[1,1,1]\n"
"v_pk_fma_f32 v[70:71], v[70:71], s[70:71], v[246:247] op_sel:[0,1,0] op_sel_hi:[1,1,1]\n"
"s_xor_b32 s14, s16, 49152\n"
"s_add_u32 m0, s67, s14\n"
"s_xor_b32 s14, s17, 393216\n"
"s_add_u32 s14, s14, s68\n"
"buffer_load_dwordx4 v205, s[44:47], s14 offen offset:0 lds\n"
"v_pk_fma_f32 v[72:73], v[72:73], s[70:71], v[248:249] op_sel:[0,1,0] op_sel_hi:[1,1,1]\n"
"v_pk_fma_f32 v[74:75], v[74:75], s[70:71], v[250:251] op_sel:[0,1,0] op_sel_hi:[1,1,1]\n"
"v_pk_fma_f32 v[76:77], v[76:77], s[70:71], v[252:253] op_sel:[0,1,0] op_sel_hi:[1,1,1]\n"
"v_pk_fma_f32 v[78:79], v[78:79], s[70:71], v[238:239] op_sel:[0,1,0] op_sel_hi:[1,1,1]\n"
"v_pk_mul_f32 v[64:65], v[64:65], v[234:235] op_sel:[0,1] op_sel_hi:[1,1]\n"
"v_pk_mul_f32 v[66:67], v[66:67], v[234:235] op_sel:[0,1] op_sel_hi:[1,1]\n"
"v_pk_mul_f32 v[68:69], v[68:69], v[234:235] op_sel:[0,1] op_sel_hi:[1,1]\n"
"v_pk_mul_f32 v[70:71], v[70:71], v[234:235] op_sel:[0,1] op_sel_hi:[1,1]\n"
"v_pk_mul_f32 v[72:73], v[72:73], v[234:235] op_sel:[0,1] op_sel_hi:[1,1]\n"
"v_pk_mul_f32 v[74:75], v[74:75], v[234:235] op_sel:[0,1] op_sel_hi:[1,1]\n"
"v_pk_mul_f32 v[76:77], v[76:77], v[234:235] op_sel:[0,1] op_sel_hi:[1,1]\n"
"v_pk_mul_f32 v[78:79], v[78:79], v[234:235] op_sel:[0,1] op_sel_hi:[1,1]\n"
"v_pk_mul_f32 v[240:241], v[64:65], s[70:71] op_sel_hi:[1,0]\n"
"v_pk_mul_f32 v[242:243], v[66:67], s[70:71] op_sel_hi:[1,0]\n"
"v_pk_mul_f32 v[244:245], v[68:69], s[70:71] op_sel_hi:[1,0]\n"
"v_pk_mul_f32 v[246:247], v[70:71], s[70:71] op_sel_hi:[1,0]\n"
"buffer_load_dwordx4 v206, s[44:47], s14 offen offset:1024 lds\n"
"v_pk_mul_f32 v[248:249], v[72:73], s[70:71] op_sel_hi:[1,0]\n"
"v_pk_mul_f32 v[250:251], v[74:75], s[70:71] op_sel_hi:[1,0]\n"
"v_pk_mul_f32 v[252:253], v[76:77], s[70:71] op_sel_hi:[1,0]\n"
"v_pk_mul_f32 v[238:239], v[78:79], s[70:71] op_sel_hi:[1,0]\n"
"v_pk_fma_f32 v[64:65], v[64:65], s[70:71], v[240:241] op_sel:[0,1,0] op_sel_hi:[1,1,1]\n"
"v_pk_fma_f32 v[66:67], v[66:67], s[70:71], v[242:243] op_sel:[0,1,0] op_sel_hi:[1,1,1]\n"
"v_pk_fma_f32 v[68:69], v[68:69], s[70:71], v[244:245] op_sel:[0,1,0] op_sel_hi:[1,1,1]\n"
"v_pk_fma_f32 v[70:71], v[70:71], s[70:71], v[246:247] op_sel:[0,1,0] op_sel_hi:[1,1,1]\n"
"v_pk_fma_f32 v[72:73], v[72:73], s[70:71], v[248:249] op_sel:[0,1,0] op_sel_hi:[1,1,1]\n"
"v_pk_fma_f32 v[74:75], v[74:75], s[70:71], v[250:251] op_sel:[0,1,0] op_sel_hi:[1,1,1]\n"
"v_pk_fma_f32 v[76:77], v[76:77], s[70:71], v[252:253] op_sel:[0,1,0] op_sel_hi:[1,1,1]\n"
"v_pk_fma_f32 v[78:79], v[78:79], s[70:71], v[238:239] op_sel:[0,1,0] op_sel_hi:[1,1,1]\n"
"v_dot2_f32_bf16 v80, v168, s39, v234\n"
"v_dot2_f32_bf16 v81, v168, s38, v234\n"
"v_dot2_f32_bf16 v82, v169, s39, v234\n"
"v_dot2_f32_bf16 v83, v169, s38, v234\n"
"buffer_load_dwordx4 v207, s[44:47], s14 offen offset:2048 lds\n"
"v_dot2_f32_bf16 v84, v170, s39, v234\n"
"v_dot2_f32_bf16 v85, v170, s38, v234\n"
"v_dot2_f32_bf16 v86, v171, s39, v234\n"
"v_dot2_f32_bf16 v87, v171, s38, v234\n"
"v_dot2_f32_bf16 v88, v172, s39, v234\n"
"v_dot2_f32_bf16 v89, v172, s38, v234\n"
"v_dot2_f32_bf16 v90, v173, s39, v234\n"
"v_dot2_f32_bf16 v91, v173, s38, v234\n"
"v_dot2_f32_bf16 v92, v174, s39, v234\n"
"v_dot2_f32_bf16 v93, v174, s38, v234\n"
"v_dot2_f32_bf16 v94, v175, s39, v234\n"
"v_dot2_f32_bf16 v95, v175, s38, v234\n"
"v_pk_mul_f32 v[240:241], v[80:81], s[70:71] op_sel_hi:[1,0]\n"
"v_pk_mul_f32 v[242:243], v[82:83], s[70:71] op_sel_hi:[1,0]\n"
"v_pk_mul_f32 v[244:245], v[84:85], s[70:71] op_sel_hi:[1,0]\n"
"v_pk_mul_f32 v[246:247], v[86:87], s[70:71] op_sel_hi:[1,0]\n"
"buffer_load_dwordx4 v208, s[44:47], s14 offen offset:3072 lds\n"
"v_pk_mul_f32 v[248:249], v[88:89], s[70:71] op_sel_hi:[1,0]\n"
"v_pk_mul_f32 v[250:251], v[90:91], s[70:71] op_sel_hi:[1,0]\n"
"v_pk_mul_f32 v[252:253], v[92:93], s[70:71] op_sel_hi:[1,0]\n"
"v_pk_mul_f32 v[238:239], v[94:95], s[70:71] op_sel_hi:[1,0]\n"
"v_pk_fma_f32 v[80:81], v[80:81], s[70:71], v[240:241] op_sel:[0,1,0] op_sel_hi:[1,1,1]\n"
"v_pk_fma_f32 v[82:83], v[82:83], s[70:71], v[242:243] op_sel:[0,1,0] op_sel_hi:[1,1,1]\n"
"v_pk_fma_f32 v[84:85], v[84:85], s[70:71], v[244:245] op_sel:[0,1,0] op_sel_hi:[1,1,1]\n"
"v_pk_fma_f32 v[86:87], v[86:87], s[70:71], v[246:247] op_sel:[0,1,0] op_sel_hi:[1,1,1]\n"
"v_pk_fma_f32 v[88:89], v[88:89], s[70:71], v[248:249] op_sel:[0,1,0] op_sel_hi:[1,1,1]\n"
"v_pk_fma_f32 v[90:91], v[90:91], s[70:71], v[250:251] op_sel:[0,1,0] op_sel_hi:[1,1,1]\n"
"v_pk_fma_f32 v[92:93], v[92:93], s[70:71], v[252:253] op_sel:[0,1,0] op_sel_hi:[1,1,1]\n"
"v_pk_fma_f32 v[94:95], v[94:95], s[70:71], v[238:239] op_sel:[0,1,0] op_sel_hi:[1,1,1]\n"
"v_pk_mul_f32 v[80:81], v[80:81], v[234:235] op_sel:[0,1] op_sel_hi:[1,1]\n"
"v_pk_mul_f32 v[82:83], v[82:83], v[234:235] op_sel:[0,1] op_sel_hi:[1,1]\n"
"v_pk_mul_f32 v[84:85], v[84:85], v[234:235] op_sel:[0,1] op_sel_hi:[1,1]\n"
"v_pk_mul_f32 v[86:87], v[86:87], v[234:235] op_sel:[0,1] op_sel_hi:[1,1]\n"
"s_add_u32 s68, s68, 0x80000\n"
"s_and_b32 s68, s68, 0x3fffff\n"
"v_pk_mul_f32 v[88:89], v[88:89], v[234:235] op_sel:[0,1] op_sel_hi:[1,1]\n"
"v_pk_mul_f32 v[90:91], v[90:91], v[234:235] op_sel:[0,1] op_sel_hi:[1,1]\n"
"v_pk_mul_f32 v[92:93], v[92:93], v[234:235] op_sel:[0,1] op_sel_hi:[1,1]\n"
"v_pk_mul_f32 v[94:95], v[94:95], v[234:235] op_sel:[0,1] op_sel_hi:[1,1]\n"
"v_pk_mul_f32 v[240:241], v[80:81], s[70:71] op_sel_hi:[1,0]\n"
"v_pk_mul_f32 v[242:243], v[82:83], s[70:71] op_sel_hi:[1,0]\n"
"v_pk_mul_f32 v[244:245], v[84:85], s[70:71] op_sel_hi:[1,0]\n"
"v_pk_mul_f32 v[246:247], v[86:87], s[70:71] op_sel_hi:[1,0]\n"
"v_pk_mul_f32 v[248:249], v[88:89], s[70:71] op_sel_hi:[1,0]\n"
"v_pk_mul_f32 v[250:251], v[90:91], s[70:71] op_sel_hi:[1,0]\n"
"v_pk_mul_f32 v[252:253], v[92:93], s[70:71] op_sel_hi:[1,0]\n"
"v_pk_mul_f32 v[238:239], v[94:95], s[70:71] op_sel_hi:[1,0]\n"
"v_pk_fma_f32 v[80:81], v[80:81], s[70:71], v[240:241] op_sel:[0,1,0] op_sel_hi:[1,1,1]\n"
"v_pk_fma_f32 v[82:83], v[82:83], s[70:71], v[242:243] op_sel:[0,1,0] op_sel_hi:[1,1,1]\n"
"v_pk_fma_f32 v[84:85], v[84:85], s[70:71], v[244:245] op_sel:[0,1,0] op_sel_hi:[1,1,1]\n"
"v_pk_fma_f32 v[86:87], v[86:87], s[70:71], v[246:247] op_sel:[0,1,0] op_sel_hi:[1,1,1]\n"
"v_pk_fma_f32 v[88:89], v[88:89], s[70:71], v[248:249] op_sel:[0,1,0] op_sel_hi:[1,1,1]\n"
"v_pk_fma_f32 v[90:91], v[90:91], s[70:71], v[250:251] op_sel:[0,1,0] op_sel_hi:[1,1,1]\n"
"v_pk_fma_f32 v[92:93], v[92:93], s[70:71], v[252:253] op_sel:[0,1,0] op_sel_hi:[1,1,1]\n"
"v_pk_fma_f32 v[94:95], v[94:95], s[70:71], v[238:239] op_sel:[0,1,0] op_sel_hi:[1,1,1]\n"
"s_add_u32 s35, s37, 0\n"
"buffer_load_dwordx4 v[128:131], v226, s[60:63], s35 offen offset:0 sc1\n"
"buffer_load_dwordx4 v[132:135], v226, s[60:63], s35 offen offset:1024 sc1\n"
"s_add_u32 s35, s37, 24576\n"
"buffer_load_dwordx4 v[136:139], v226, s[60:63], s35 offen offset:0 sc1\n"
"buffer_load_dwordx4 v[140:143], v226, s[60:63], s35 offen offset:1024 sc1\n"
"s_add_u32 s35, s37, 49152\n"
"buffer_load_dwordx4 v[144:147], v226, s[60:63], s35 offen offset:0 sc1\n"
"buffer_load_dwordx4 v[148:151], v226, s[60:63], s35 offen offset:1024 sc1\n"
"s_add_u32 s35, s37, 73728\n"
"buffer_load_dwordx4 v[152:155], v226, s[60:63], s35 offen offset:0 sc1\n"
"buffer_load_dwordx4 v[156:159], v226, s[60:63], s35 offen offset:1024 sc1\n"
"buffer_load_dwordx4 v[216:219], v227, s[60:63], s33 offen sc1\n"
"v_dot2_f32_bf16 v96, v176, s39, v236\n"
"v_dot2_f32_bf16 v97, v176, s38, v236\n"
"v_dot2_f32_bf16 v98, v177, s39, v236\n"
"v_dot2_f32_bf16 v99, v177, s38, v236\n"
"v_dot2_f32_bf16 v100, v178, s39, v236\n"
"v_dot2_f32_bf16 v101, v178, s38, v236\n"
"v_dot2_f32_bf16 v102, v179, s39, v236\n"
"v_dot2_f32_bf16 v103, v179, s38, v236\n"
"v_dot2_f32_bf16 v104, v180, s39, v236\n"
"v_dot2_f32_bf16 v105, v180, s38, v236\n"
"v_dot2_f32_bf16 v106, v181, s39, v236\n"
"v_dot2_f32_bf16 v107, v181, s38, v236\n"
"v_dot2_f32_bf16 v108, v182, s39, v236\n"
"v_dot2_f32_bf16 v109, v182, s38, v236\n"
"v_dot2_f32_bf16 v110, v183, s39, v236\n"
"v_dot2_f32_bf16 v111, v183, s38, v236\n"
"v_pk_mul_f32 v[240:241], v[96:97], s[70:71] op_sel_hi:[1,0]\n"
"v_pk_mul_f32 v[242:243], v[98:99], s[70:71] op_sel_hi:[1,0]\n"
"v_pk_mul_f32 v[244:245], v[100:101], s[70:71] op_sel_hi:[1,0]\n"
"v_pk_mul_f32 v[246:247], v[102:103], s[70:71] op_sel_hi:[1,0]\n"
"v_pk_mul_f32 v[248:249], v[104:105], s[70:71] op_sel_hi:[1,0]\n"
"v_pk_mul_f32 v[250:251], v[106:107], s[70:71] op_sel_hi:[1,0]\n"
"v_pk_mul_f32 v[252:253], v[108:109], s[70:71] op_sel_hi:[1,0]\n"
"v_pk_mul_f32 v[238:239], v[110:111], s[70:71] op_sel_hi:[1,0]\n"
"v_pk_fma_f32 v[96:97], v[96:97], s[70:71], v[240:241] op_sel:[0,1,0] op_sel_hi:[1,1,1]\n"
"v_pk_fma_f32 v[98:99], v[98:99], s[70:71], v[242:243] op_sel:[0,1,0] op_sel_hi:[1,1,1]\n"
"v_pk_fma_f32 v[100:101], v[100:101], s[70:71], v[244:245] op_sel:[0,1,0] op_sel_hi:[1,1,1]\n"
"v_pk_fma_f32 v[102:103], v[102:103], s[70:71], v[246:247] op_sel:[0,1,0] op_sel_hi:[1,1,1]\n"
"v_pk_fma_f32 v[104:105], v[104:105], s[70:71], v[248:249] op_sel:[0,1,0] op_sel_hi:[1,1,1]\n"
"v_pk_fma_f32 v[106:107], v[106:107], s[70:71], v[250:251] op_sel:[0,1,0] op_sel_hi:[1,1,1]\n"
"v_pk_fma_f32 v[108:109], v[108:109], s[70:71], v[252:253] op_sel:[0,1,0] op_sel_hi:[1,1,1]\n"
"v_pk_fma_f32 v[110:111], v[110:111], s[70:71], v[238:239] op_sel:[0,1,0] op_sel_hi:[1,1,1]\n"
"v_pk_mul_f32 v[96:97], v[96:97], v[236:237] op_sel:[0,1] op_sel_hi:[1,1]\n"
"v_pk_mul_f32 v[98:99], v[98:99], v[236:237] op_sel:[0,1] op_sel_hi:[1,1]\n"
"v_pk_mul_f32 v[100:101], v[100:101], v[236:237] op_sel:[0,1] op_sel_hi:[1,1]\n"
"v_pk_mul_f32 v[102:103], v[102:103], v[236:237] op_sel:[0,1] op_sel_hi:[1,1]\n"
"v_pk_mul_f32 v[104:105], v[104:105], v[236:237] op_sel:[0,1] op_sel_hi:[1,1]\n"
"v_pk_mul_f32 v[106:107], v[106:107], v[236:237] op_sel:[0,1] op_sel_hi:[1,1]\n"
"v_pk_mul_f32 v[108:109], v[108:109], v[236:237] op_sel:[0,1] op_sel_hi:[1,1]\n"
"v_pk_mul_f32 v[110:111], v[110:111], v[236:237] op_sel:[0,1] op_sel_hi:[1,1]\n"
"v_pk_mul_f32 v[240:241], v[96:97], s[70:71] op_sel_hi:[1,0]\n"
"v_pk_mul_f32 v[242:243], v[98:99], s[70:71] op_sel_hi:[1,0]\n"
"v_pk_mul_f32 v[244:245], v[100:101], s[70:71] op_sel_hi:[1,0]\n"
"v_pk_mul_f32 v[246:247], v[102:103], s[70:71] op_sel_hi:[1,0]\n"
"v_pk_mul_f32 v[248:249], v[104:105], s[70:71] op_sel_hi:[1,0]\n"
"v_pk_mul_f32 v[250:251], v[106:107], s[70:71] op_sel_hi:[1,0]\n"
"v_pk_mul_f32 v[252:253], v[108:109], s[70:71] op_sel_hi:[1,0]\n"
"v_pk_mul_f32 v[238:239], v[110:111], s[70:71] op_sel_hi:[1,0]\n"
"v_pk_fma_f32 v[96:97], v[96:97], s[70:71], v[240:241] op_sel:[0,1,0] op_sel_hi:[1,1,1]\n"
"v_pk_fma_f32 v[98:99], v[98:99], s[70:71], v[242:243] op_sel:[0,1,0] op_sel_hi:[1,1,1]\n"
"v_pk_fma_f32 v[100:101], v[100:101], s[70:71], v[244:245] op_sel:[0,1,0] op_sel_hi:[1,1,1]\n"
"v_pk_fma_f32 v[102:103], v[102:103], s[70:71], v[246:247] op_sel:[0,1,0] op_sel_hi:[1,1,1]\n"
"v_pk_fma_f32 v[104:105], v[104:105], s[70:71], v[248:249] op_sel:[0,1,0] op_sel_hi:[1,1,1]\n"
"v_pk_fma_f32 v[106:107], v[106:107], s[70:71], v[250:251] op_sel:[0,1,0] op_sel_hi:[1,1,1]\n"
"v_pk_fma_f32 v[108:109], v[108:109], s[70:71], v[252:253] op_sel:[0,1,0] op_sel_hi:[1,1,1]\n"
"v_pk_fma_f32 v[110:111], v[110:111], s[70:71], v[238:239] op_sel:[0,1,0] op_sel_hi:[1,1,1]\n"
"v_dot2_f32_bf16 v112, v184, s39, v236\n"
"v_dot2_f32_bf16 v113, v184, s38, v236\n"
"v_dot2_f32_bf16 v114, v185, s39, v236\n"
"v_dot2_f32_bf16 v115, v185, s38, v236\n"
"v_dot2_f32_bf16 v116, v186, s39, v236\n"
"v_dot2_f32_bf16 v117, v186, s38, v236\n"
"v_dot2_f32_bf16 v118, v187, s39, v236\n"
"v_dot2_f32_bf16 v119, v187, s38, v236\n"
"v_dot2_f32_bf16 v120, v188, s39, v236\n"
"v_dot2_f32_bf16 v121, v188, s38, v236\n"
"v_dot2_f32_bf16 v122, v189, s39, v236\n"
"v_dot2_f32_bf16 v123, v189, s38, v236\n"
"v_dot2_f32_bf16 v124, v190, s39, v236\n"
"v_dot2_f32_bf16 v125, v190, s38, v236\n"
"v_dot2_f32_bf16 v126, v191, s39, v236\n"
"v_dot2_f32_bf16 v127, v191, s38, v236\n"
"v_pk_mul_f32 v[240:241], v[112:113], s[70:71] op_sel_hi:[1,0]\n"
"v_pk_mul_f32 v[242:243], v[114:115], s[70:71] op_sel_hi:[1,0]\n"
"v_pk_mul_f32 v[244:245], v[116:117], s[70:71] op_sel_hi:[1,0]\n"
"v_pk_mul_f32 v[246:247], v[118:119], s[70:71] op_sel_hi:[1,0]\n"
"v_pk_mul_f32 v[248:249], v[120:121], s[70:71] op_sel_hi:[1,0]\n"
"v_pk_mul_f32 v[250:251], v[122:123], s[70:71] op_sel_hi:[1,0]\n"
"v_pk_mul_f32 v[252:253], v[124:125], s[70:71] op_sel_hi:[1,0]\n"
"v_pk_mul_f32 v[238:239], v[126:127], s[70:71] op_sel_hi:[1,0]\n"
"v_pk_fma_f32 v[112:113], v[112:113], s[70:71], v[240:241] op_sel:[0,1,0] op_sel_hi:[1,1,1]\n"
"v_pk_fma_f32 v[114:115], v[114:115], s[70:71], v[242:243] op_sel:[0,1,0] op_sel_hi:[1,1,1]\n"
"v_pk_fma_f32 v[116:117], v[116:117], s[70:71], v[244:245] op_sel:[0,1,0] op_sel_hi:[1,1,1]\n"
"v_pk_fma_f32 v[118:119], v[118:119], s[70:71], v[246:247] op_sel:[0,1,0] op_sel_hi:[1,1,1]\n"
"v_pk_fma_f32 v[120:121], v[120:121], s[70:71], v[248:249] op_sel:[0,1,0] op_sel_hi:[1,1,1]\n"
"v_pk_fma_f32 v[122:123], v[122:123], s[70:71], v[250:251] op_sel:[0,1,0] op_sel_hi:[1,1,1]\n"
"v_pk_fma_f32 v[124:125], v[124:125], s[70:71], v[252:253] op_sel:[0,1,0] op_sel_hi:[1,1,1]\n"
"v_pk_fma_f32 v[126:127], v[126:127], s[70:71], v[238:239] op_sel:[0,1,0] op_sel_hi:[1,1,1]\n"
"v_pk_mul_f32 v[112:113], v[112:113], v[236:237] op_sel:[0,1] op_sel_hi:[1,1]\n"
"v_pk_mul_f32 v[114:115], v[114:115], v[236:237] op_sel:[0,1] op_sel_hi:[1,1]\n"
"v_pk_mul_f32 v[116:117], v[116:117], v[236:237] op_sel:[0,1] op_sel_hi:[1,1]\n"
"v_pk_mul_f32 v[118:119], v[118:119], v[236:237] op_sel:[0,1] op_sel_hi:[1,1]\n"
"v_pk_mul_f32 v[120:121], v[120:121], v[236:237] op_sel:[0,1] op_sel_hi:[1,1]\n"
"v_pk_mul_f32 v[122:123], v[122:123], v[236:237] op_sel:[0,1] op_sel_hi:[1,1]\n"
"v_pk_mul_f32 v[124:125], v[124:125], v[236:237] op_sel:[0,1] op_sel_hi:[1,1]\n"
"v_pk_mul_f32 v[126:127], v[126:127], v[236:237] op_sel:[0,1] op_sel_hi:[1,1]\n"
"v_pk_mul_f32 v[240:241], v[112:113], s[70:71] op_sel_hi:[1,0]\n"
"v_pk_mul_f32 v[242:243], v[114:115], s[70:71] op_sel_hi:[1,0]\n"
"v_pk_mul_f32 v[244:245], v[116:117], s[70:71] op_sel_hi:[1,0]\n"
"v_pk_mul_f32 v[246:247], v[118:119], s[70:71] op_sel_hi:[1,0]\n"
"v_pk_mul_f32 v[248:249], v[120:121], s[70:71] op_sel_hi:[1,0]\n"
"v_pk_mul_f32 v[250:251], v[122:123], s[70:71] op_sel_hi:[1,0]\n"
"v_pk_mul_f32 v[252:253], v[124:125], s[70:71] op_sel_hi:[1,0]\n"
"v_pk_mul_f32 v[238:239], v[126:127], s[70:71] op_sel_hi:[1,0]\n"
"v_pk_fma_f32 v[112:113], v[112:113], s[70:71], v[240:241] op_sel:[0,1,0] op_sel_hi:[1,1,1]\n"
"v_pk_fma_f32 v[114:115], v[114:115], s[70:71], v[242:243] op_sel:[0,1,0] op_sel_hi:[1,1,1]\n"
"v_pk_fma_f32 v[116:117], v[116:117], s[70:71], v[244:245] op_sel:[0,1,0] op_sel_hi:[1,1,1]\n"
"v_pk_fma_f32 v[118:119], v[118:119], s[70:71], v[246:247] op_sel:[0,1,0] op_sel_hi:[1,1,1]\n"
"v_pk_fma_f32 v[120:121], v[120:121], s[70:71], v[248:249] op_sel:[0,1,0] op_sel_hi:[1,1,1]\n"
"v_pk_fma_f32 v[122:123], v[122:123], s[70:71], v[250:251] op_sel:[0,1,0] op_sel_hi:[1,1,1]\n"
"v_pk_fma_f32 v[124:125], v[124:125], s[70:71], v[252:253] op_sel:[0,1,0] op_sel_hi:[1,1,1]\n"
"v_pk_fma_f32 v[126:127], v[126:127], s[70:71], v[238:239] op_sel:[0,1,0] op_sel_hi:[1,1,1]\n"
"s_waitcnt vmcnt(7)\n"
"v_min3_u32 v240, v129, v131, v133\n"
"v_max3_u32 v241, v129, v131, v133\n"
"s_waitcnt vmcnt(6)\n"
"v_min3_u32 v240, v240, v135, v137\n"
"v_max3_u32 v241, v241, v135, v137\n"
"s_waitcnt vmcnt(5)\n"
"v_min3_u32 v240, v240, v139, v141\n"
"v_max3_u32 v241, v241, v139, v141\n"
"s_waitcnt vmcnt(4)\n"
"v_min3_u32 v240, v240, v143, v145\n"
"v_max3_u32 v241, v241, v143, v145\n"
"s_waitcnt vmcnt(3)\n"
"v_min3_u32 v240, v240, v147, v149\n"
"v_max3_u32 v241, v241, v147, v149\n"
"s_waitcnt vmcnt(2)\n"
"v_min3_u32 v240, v240, v151, v153\n"
"v_max3_u32 v241, v241, v151, v153\n"
"s_waitcnt vmcnt(1)\n"
"v_min3_u32 v240, v240, v155, v157\n"
"v_max3_u32 v241, v241, v155, v157\n"
"s_waitcnt vmcnt(0)\n"
"v_min3_u32 v240, v240, v159, v217\n"
"v_max3_u32 v241, v241, v159, v217\n"
"v_min3_u32 v240, v240, v219, v219\n"
"v_max3_u32 v241, v241, v219, v219\n"
"v_xor_b32 v240, s36, v240\n"
"v_xor_b32 v241, s36, v241\n"
"v_or_b32 v240, v240, v241\n"
"v_cmp_ne_u32 vcc, 0, v240\n"
"s_nop 1\n"
"Lst1_%=:\n"
"s_cbranch_execz Lst5_%=\n"
"s_cbranch_vccnz Lg0s_1_%=\n"
"Lg0r_2_%=:\n"
"v_add_f32 v128, v128, v136\n"
"v_add_f32 v144, v144, v152\n"
"v_add_f32 v130, v130, v138\n"
"v_add_f32 v146, v146, v154\n"
"v_add_f32 v132, v132, v140\n"
"v_add_f32 v148, v148, v156\n"
"v_add_f32 v134, v134, v142\n"
"v_add_f32 v150, v150, v158\n"
"v_add_f32_dpp v216, v216, v216 row_ror:4 row_mask:0xf bank_mask:0xf\n"
"v_add_f32_dpp v218, v218, v218 row_ror:4 row_mask:0xf bank_mask:0xf\n"
"v_add_f32 v128, v128, v144\n"
"v_add_f32 v130, v130, v146\n"
"v_add_f32 v132, v132, v148\n"
"v_add_f32 v134, v134, v150\n"
"v_add_f32_dpp v216, v216, v216 row_ror:8 row_mask:0xf bank_mask:0xf\n"
"v_add_f32_dpp v218, v218, v218 row_ror:8 row_mask:0xf bank_mask:0xf\n"
"s_cmp_ge_u32 s64, 2\n"
"s_cselect_b32 s35, 1.0, 0\n"
"s_cselect_b32 s14, s70, 1.0\n"
"s_cselect_b32 s15, 1, 3\n"
"s_lshl_b32 s15, s15, 11\n"
"s_lshr_b32 s74, s6, 6\n"
"s_cmp_eq_u32 s74, 2\n"
"s_cselect_b32 s37, 1.0, 0\n"
"v_cvt_pk_bf16_f32 v128, 0, v128\n"
"v_cvt_pk_bf16_f32 v130, 0, v130\n"
"v_cvt_pk_bf16_f32 v132, 0, v132\n"
"v_cvt_pk_bf16_f32 v134, 0, v134\n"
"v_cvt_pk_bf16_f32 v216, 0, v216\n"
"v_cvt_pk_bf16_f32 v218, 0, v218\n"
"v_add_f32 v128, s35, v128\n"
"v_add_f32 v130, s35, v130\n"
"v_add_f32 v132, s35, v132\n"
"v_add_f32 v134, s35, v134\n"
"v_add_f32 v216, s37, v216\n"
"v_add_f32 v218, s37, v218\n"
"v_cvt_pk_bf16_f32 v128, 0, v128\n"
"v_cvt_pk_bf16_f32 v130, 0, v130\n"
"v_cvt_pk_bf16_f32 v132, 0, v132\n"
"v_cvt_pk_bf16_f32 v134, 0, v134\n"
"v_add_u32 v241, s15, v229\n"
"v_mul_f32 v136, s14, v128\n"
"v_mul_f32 v140, s71, v128\n"
"v_mul_f32 v137, s14, v130\n"
"v_mul_f32 v141, s71, v130\n"
"v_mul_f32 v138, s14, v132\n"
"v_mul_f32 v142, s71, v132\n"
"v_mul_f32 v139, s14, v134\n"
"v_mul_f32 v143, s71, v134\n"
"ds_write_b64 v229, v[136:137]\n"
"ds_write_b64 v229, v[138:139] offset:256\n"
"ds_write_b64 v241, v[140:141]\n"
"ds_write_b64 v241, v[142:143] offset:256\n"
"v_cvt_pk_bf16_f32 v216, v216, v218\n"
"s_lshl_b32 s74, s6, 5\n"
"s_add_u32 s74, s74, 40960\n"
"s_add_u32 s74, s74, s7\n"
"buffer_store_dwordx2 v[216:217], v228, s[60:63], s74 offen sc1\n"
"v_xor_b32 v230, 0, v214\n"
"v_xor_b32 v231, 64, v214\n"
"v_xor_b32 v232, 128, v214\n"
"v_xor_b32 v233, 192, v214\n"
"s_waitcnt lgkmcnt(0)\n"
"s_barrier\n"
"ds_read_b128 v[128:131], v220 offset:2048\n"
"ds_read_b128 v[132:135], v220 offset:3072\n"
"ds_read_b128 v[136:139], v220 offset:4096\n"
"ds_read_b128 v[140:143], v220 offset:5120\n"
"ds_read_b128 v[144:147], v220 offset:0\n"
"ds_read_b128 v[148:151], v220 offset:1024\n"
"ds_read_b128 v[152:155], v221 offset:2048\n"
"ds_read_b128 v[156:159], v221 offset:3072\n"
"ds_read_b128 v[160:163], v221 offset:4096\n"
"ds_read_b128 v[164:167], v221 offset:5120\n"
"ds_read_b128 v[168:171], v221 offset:0\n"
"ds_read_b128 v[172:175], v221 offset:1024\n"
"v_bfe_u32 v254, v192, 3, 2\n"
"v_lshlrev_b32 v254, 13, v254\n"
"v_and_b32 v253, 7, v192\n"
"v_lshl_add_u32 v254, v253, 7, v254\n"
"s_lshr_b32 s30, s77, 2\n"
"s_lshl_b32 s30, s30, 2\n"
"s_add_u32 s30, s30, s77\n"
"s_add_u32 s30, s30, 4\n"
"s_lshl_b32 s30, s30, 2\n"
"s_add_u32 s30, s30, s64\n"
"s_lshl_b32 s30, s30, 5\n"
"s_add_u32 s30, s30, s65\n"
"s_lshl_b32 s30, s30, 10\n"
"buffer_load_dword v255, v254, s[44:47], s30 offen\n"
"v_and_b32 v254, 7, v192\n"
"v_lshlrev_b32 v254, 6, v254\n"
"s_lshl_b32 s30, s77, 2\n"
"s_add_u32 s30, s30, s64\n"
"s_lshl_b32 s30, s30, 9\n"
"buffer_load_dword v255, v254, s[48:51], s30 offen\n"
"s_sub_u32 s30, s30, 0x2000\n"
"s_and_b32 s30, s30, 0x1ffff\n"
"buffer_load_dword v255, v254, s[52:55], s30 offen\n"
"s_waitcnt lgkmcnt(11)\n"
"v_pk_mul_f32 v[240:241], v[0:1], v[128:129]\n"
"v_pk_mul_f32 v[176:177], v[32:33], v[128:129]\n"
"v_pk_mul_f32 v[242:243], v[2:3], v[130:131]\n"
"v_pk_mul_f32 v[178:179], v[34:35], v[130:131]\n"
"s_waitcnt lgkmcnt(10)\n"
"v_pk_mul_f32 v[244:245], v[4:5], v[132:133]\n"
"v_pk_mul_f32 v[180:181], v[36:37], v[132:133]\n"
"v_pk_mul_f32 v[246:247], v[6:7], v[134:135]\n"
"v_pk_mul_f32 v[182:183], v[38:39], v[134:135]\n"
"s_waitcnt lgkmcnt(9)\n"
"v_pk_fma_f32 v[0:1], v[0:1], v[136:137], v[240:241]\n"
"v_pk_fma_f32 v[32:33], v[32:33], v[136:137], v[176:177]\n"
"v_pk_fma_f32 v[2:3], v[2:3], v[138:139], v[242:243]\n"
"v_pk_fma_f32 v[34:35], v[34:35], v[138:139], v[178:179]\n"
"s_waitcnt lgkmcnt(8)\n"
"v_pk_fma_f32 v[4:5], v[4:5], v[140:141], v[244:245]\n"
"v_pk_fma_f32 v[36:37], v[36:37], v[140:141], v[180:181]\n"
"v_pk_fma_f32 v[6:7], v[6:7], v[142:143], v[246:247]\n"
"v_pk_fma_f32 v[38:39], v[38:39], v[142:143], v[182:183]\n"
"s_waitcnt lgkmcnt(7)\n"
"v_pk_add_f32 v[0:1], v[0:1], v[144:145]\n"
"v_pk_add_f32 v[32:33], v[32:33], v[144:145]\n"
"v_pk_add_f32 v[2:3], v[2:3], v[146:147]\n"
"v_pk_add_f32 v[34:35], v[34:35], v[146:147]\n"
"s_waitcnt lgkmcnt(6)\n"
"v_pk_add_f32 v[4:5], v[4:5], v[148:149]\n"
"v_pk_add_f32 v[36:37], v[36:37], v[148:149]\n"
"v_pk_add_f32 v[6:7], v[6:7], v[150:151]\n"
"v_pk_add_f32 v[38:39], v[38:39], v[150:151]\n"
"v_cvt_pk_bf16_f32 v0, v0, v1\n"
"v_cvt_pk_bf16_f32 v32, v32, v33\n"
"v_cvt_pk_bf16_f32 v1, v2, v3\n"
"v_cvt_pk_bf16_f32 v33, v34, v35\n"
"v_cvt_pk_bf16_f32 v2, v4, v5\n"
"v_cvt_pk_bf16_f32 v34, v36, v37\n"
"v_cvt_pk_bf16_f32 v3, v6, v7\n"
"v_cvt_pk_bf16_f32 v35, v38, v39\n"
"v_pk_mul_f32 v[240:241], v[64:65], v[128:129]\n"
"v_pk_mul_f32 v[176:177], v[96:97], v[128:129]\n"
"v_pk_mul_f32 v[242:243], v[66:67], v[130:131]\n"
"v_pk_mul_f32 v[178:179], v[98:99], v[130:131]\n"
"ds_write_b128 v230, v[0:3] offset:0\n"
"v_pk_mul_f32 v[244:245], v[68:69], v[132:133]\n"
"v_pk_mul_f32 v[180:181], v[100:101], v[132:133]\n"
"v_pk_mul_f32 v[246:247], v[70:71], v[134:135]\n"
"v_pk_mul_f32 v[182:183], v[102:103], v[134:135]\n"
"v_pk_fma_f32 v[64:65], v[64:65], v[136:137], v[240:241]\n"
"v_pk_fma_f32 v[96:97], v[96:97], v[136:137], v[176:177]\n"
"v_pk_fma_f32 v[66:67], v[66:67], v[138:139], v[242:243]\n"
"v_pk_fma_f32 v[98:99], v[98:99], v[138:139], v[178:179]\n"
"v_pk_fma_f32 v[68:69], v[68:69], v[140:141], v[244:245]\n"
"v_pk_fma_f32 v[100:101], v[100:101], v[140:141], v[180:181]\n"
"v_pk_fma_f32 v[70:71], v[70:71], v[142:143], v[246:247]\n"
"v_pk_fma_f32 v[102:103], v[102:103], v[142:143], v[182:183]\n"
"v_pk_add_f32 v[64:65], v[64:65], v[144:145]\n"
"v_pk_add_f32 v[96:97], v[96:97], v[144:145]\n"
"v_pk_add_f32 v[66:67], v[66:67], v[146:147]\n"
"v_pk_add_f32 v[98:99], v[98:99], v[146:147]\n"
"ds_write_b128 v231, v[32:35] offset:0\n"
"v_pk_add_f32 v[68:69], v[68:69], v[148:149]\n"
"v_pk_add_f32 v[100:101], v[100:101], v[148:149]\n"
"v_pk_add_f32 v[70:71], v[70:71], v[150:151]\n"
"v_pk_add_f32 v[102:103], v[102:103], v[150:151]\n"
"v_cvt_pk_bf16_f32 v64, v64, v65\n"
"v_cvt_pk_bf16_f32 v96, v96, v97\n"
"v_cvt_pk_bf16_f32 v65, v66, v67\n"
"v_cvt_pk_bf16_f32 v97, v98, v99\n"
"v_cvt_pk_bf16_f32 v66, v68, v69\n"
"v_cvt_pk_bf16_f32 v98, v100, v101\n"
"v_cvt_pk_bf16_f32 v67, v70, v71\n"
"v_cvt_pk_bf16_f32 v99, v102, v103\n"
"ds_read_b128 v[128:131], v222 offset:2048\n"
"ds_read_b128 v[132:135], v222 offset:3072\n"
"ds_read_b128 v[136:139], v222 offset:4096\n"
"ds_read_b128 v[140:143], v222 offset:5120\n"
"ds_read_b128 v[144:147], v222 offset:0\n"
"ds_read_b128 v[148:151], v222 offset:1024\n"
"s_waitcnt lgkmcnt(13)\n"
"v_pk_mul_f32 v[240:241], v[8:9], v[152:153]\n"
"v_pk_mul_f32 v[176:177], v[40:41], v[152:153]\n"
"v_pk_mul_f32 v[242:243], v[10:11], v[154:155]\n"
"v_pk_mul_f32 v[178:179], v[42:43], v[154:155]\n"
"ds_write_b128 v232, v[64:67] offset:0\n"
"s_waitcnt lgkmcnt(13)\n"
"v_pk_mul_f32 v[244:245], v[12:13], v[156:157]\n"
"v_pk_mul_f32 v[180:181], v[44:45], v[156:157]\n"
"v_pk_mul_f32 v[246:247], v[14:15], v[158:159]\n"
"v_pk_mul_f32 v[182:183], v[46:47], v[158:159]\n"
"s_waitcnt lgkmcnt(12)\n"
"v_pk_fma_f32 v[8:9], v[8:9], v[160:161], v[240:241]\n"
"v_pk_fma_f32 v[40:41], v[40:41], v[160:161], v[176:177]\n"
"v_pk_fma_f32 v[10:11], v[10:11], v[162:163], v[242:243]\n"
"v_pk_fma_f32 v[42:43], v[42:43], v[162:163], v[178:179]\n"
"s_waitcnt lgkmcnt(11)\n"
"v_pk_fma_f32 v[12:13], v[12:13], v[164:165], v[244:245]\n"
"v_pk_fma_f32 v[44:45], v[44:45], v[164:165], v[180:181]\n"
"v_pk_fma_f32 v[14:15], v[14:15], v[166:167], v[246:247]\n"
"v_pk_fma_f32 v[46:47], v[46:47], v[166:167], v[182:183]\n"
"s_waitcnt lgkmcnt(10)\n"
"v_pk_add_f32 v[8:9], v[8:9], v[168:169]\n"
"v_pk_add_f32 v[40:41], v[40:41], v[168:169]\n"
"v_pk_add_f32 v[10:11], v[10:11], v[170:171]\n"
"v_pk_add_f32 v[42:43], v[42:43], v[170:171]\n"
"ds_write_b128 v233, v[96:99] offset:0\n"
"s_waitcnt lgkmcnt(10)\n"
"v_pk_add_f32 v[12:13], v[12:13], v[172:173]\n"
"v_pk_add_f32 v[44:45], v[44:45], v[172:173]\n"
"v_pk_add_f32 v[14:15], v[14:15], v[174:175]\n"
"v_pk_add_f32 v[46:47], v[46:47], v[174:175]\n"
"v_cvt_pk_bf16_f32 v8, v8, v9\n"
"v_cvt_pk_bf16_f32 v40, v40, v41\n"
"v_cvt_pk_bf16_f32 v9, v10, v11\n"
"v_cvt_pk_bf16_f32 v41, v42, v43\n"
"v_cvt_pk_bf16_f32 v10, v12, v13\n"
"v_cvt_pk_bf16_f32 v42, v44, v45\n"
"v_cvt_pk_bf16_f32 v11, v14, v15\n"
"v_cvt_pk_bf16_f32 v43, v46, v47\n"
"v_pk_mul_f32 v[240:241], v[72:73], v[152:153]\n"
"v_pk_mul_f32 v[176:177], v[104:105], v[152:153]\n"
"v_pk_mul_f32 v[242:243], v[74:75], v[154:155]\n"
"v_pk_mul_f32 v[178:179], v[106:107], v[154:155]\n"
"ds_write_b128 v230, v[8:11] offset:8192\n"
"v_pk_mul_f32 v[244:245], v[76:77], v[156:157]\n"
"v_pk_mul_f32 v[180:181], v[108:109], v[156:157]\n"
"v_pk_mul_f32 v[246:247], v[78:79], v[158:159]\n"
"v_pk_mul_f32 v[182:183], v[110:111], v[158:159]\n"
"v_pk_fma_f32 v[72:73], v[72:73], v[160:161], v[240:241]\n"
"v_pk_fma_f32 v[104:105], v[104:105], v[160:161], v[176:177]\n"
"v_pk_fma_f32 v[74:75], v[74:75], v[162:163], v[242:243]\n"
"v_pk_fma_f32 v[106:107], v[106:107], v[162:163], v[178:179]\n"
"v_pk_fma_f32 v[76:77], v[76:77], v[164:165], v[244:245]\n"
"v_pk_fma_f32 v[108:109], v[108:109], v[164:165], v[180:181]\n"
"v_pk_fma_f32 v[78:79], v[78:79], v[166:167], v[246:247]\n"
"v_pk_fma_f32 v[110:111], v[110:111], v[166:167], v[182:183]\n"
"v_pk_add_f32 v[72:73], v[72:73], v[168:169]\n"
"v_pk_add_f32 v[104:105], v[104:105], v[168:169]\n"
"v_pk_add_f32 v[74:75], v[74:75], v[170:171]\n"
"v_pk_add_f32 v[106:107], v[106:107], v[170:171]\n"
"ds_write_b128 v231, v[40:43] offset:8192\n"
"v_pk_add_f32 v[76:77], v[76:77], v[172:173]\n"
"v_pk_add_f32 v[108:109], v[108:109], v[172:173]\n"
"v_pk_add_f32 v[78:79], v[78:79], v[174:175]\n"
"v_pk_add_f32 v[110:111], v[110:111], v[174:175]\n"
"v_cvt_pk_bf16_f32 v72, v72, v73\n"
"v_cvt_pk_bf16_f32 v104, v104, v105\n"
"v_cvt_pk_bf16_f32 v73, v74, v75\n"
"v_cvt_pk_bf16_f32 v105, v106, v107\n"
"v_cvt_pk_bf16_f32 v74, v76, v77\n"
"v_cvt_pk_bf16_f32 v106, v108, v109\n"
"v_cvt_pk_bf16_f32 v75, v78, v79\n"
"v_cvt_pk_bf16_f32 v107, v110, v111\n"
"ds_read_b128 v[152:155], v223 offset:2048\n"
"ds_read_b128 v[156:159], v223 offset:3072\n"
"ds_read_b128 v[160:163], v223 offset:4096\n"
"ds_read_b128 v[164:167], v223 offset:5120\n"
"ds_read_b128 v[168:171], v223 offset:0\n"
"ds_read_b128 v[172:175], v223 offset:1024\n"
"s_waitcnt lgkmcnt(15)\n"
"v_pk_mul_f32 v[240:241], v[16:17], v[128:129]\n"
"v_pk_mul_f32 v[176:177], v[48:49], v[128:129]\n"
"v_pk_mul_f32 v[242:243], v[18:19], v[130:131]\n"
"v_pk_mul_f32 v[178:179], v[50:51], v[130:131]\n"
"ds_write_b128 v232, v[72:75] offset:8192\n"
"s_waitcnt lgkmcnt(15)\n"
"v_pk_mul_f32 v[244:245], v[20:21], v[132:133]\n"
"v_pk_mul_f32 v[180:181], v[52:53], v[132:133]\n"
"v_pk_mul_f32 v[246:247], v[22:23], v[134:135]\n"
"v_pk_mul_f32 v[182:183], v[54:55], v[134:135]\n"
"s_waitcnt lgkmcnt(14)\n"
"v_pk_fma_f32 v[16:17], v[16:17], v[136:137], v[240:241]\n"
"v_pk_fma_f32 v[48:49], v[48:49], v[136:137], v[176:177]\n"
"v_pk_fma_f32 v[18:19], v[18:19], v[138:139], v[242:243]\n"
"v_pk_fma_f32 v[50:51], v[50:51], v[138:139], v[178:179]\n"
"s_waitcnt lgkmcnt(13)\n"
"v_pk_fma_f32 v[20:21], v[20:21], v[140:141], v[244:245]\n"
"v_pk_fma_f32 v[52:53], v[52:53], v[140:141], v[180:181]\n"
"v_pk_fma_f32 v[22:23], v[22:23], v[142:143], v[246:247]\n"
"v_pk_fma_f32 v[54:55], v[54:55], v[142:143], v[182:183]\n"
"s_waitcnt lgkmcnt(12)\n"
"v_pk_add_f32 v[16:17], v[16:17], v[144:145]\n"
"v_pk_add_f32 v[48:49], v[48:49], v[144:145]\n"
"v_pk_add_f32 v[18:19], v[18:19], v[146:147]\n"
"v_pk_add_f32 v[50:51], v[50:51], v[146:147]\n"
"ds_write_b128 v233, v[104:107] offset:8192\n"
"s_waitcnt lgkmcnt(12)\n"
"v_pk_add_f32 v[20:21], v[20:21], v[148:149]\n"
"v_pk_add_f32 v[52:53], v[52:53], v[148:149]\n"
"v_pk_add_f32 v[22:23], v[22:23], v[150:151]\n"
"v_pk_add_f32 v[54:55], v[54:55], v[150:151]\n"
"v_cvt_pk_bf16_f32 v16, v16, v17\n"
"v_cvt_pk_bf16_f32 v48, v48, v49\n"
"v_cvt_pk_bf16_f32 v17, v18, v19\n"
"v_cvt_pk_bf16_f32 v49, v50, v51\n"
"v_cvt_pk_bf16_f32 v18, v20, v21\n"
"v_cvt_pk_bf16_f32 v50, v52, v53\n"
"v_cvt_pk_bf16_f32 v19, v22, v23\n"
"v_cvt_pk_bf16_f32 v51, v54, v55\n"
"v_pk_mul_f32 v[240:241], v[80:81], v[128:129]\n"
"v_pk_mul_f32 v[176:177], v[112:113], v[128:129]\n"
"v_pk_mul_f32 v[242:243], v[82:83], v[130:131]\n"
"v_pk_mul_f32 v[178:179], v[114:115], v[130:131]\n"
"ds_write_b128 v230, v[16:19] offset:16384\n"
"v_pk_mul_f32 v[244:245], v[84:85], v[132:133]\n"
"v_pk_mul_f32 v[180:181], v[116:117], v[132:133]\n"
"v_pk_mul_f32 v[246:247], v[86:87], v[134:135]\n"
"v_pk_mul_f32 v[182:183], v[118:119], v[134:135]\n"
"v_pk_fma_f32 v[80:81], v[80:81], v[136:137], v[240:241]\n"
"v_pk_fma_f32 v[112:113], v[112:113], v[136:137], v[176:177]\n"
"v_pk_fma_f32 v[82:83], v[82:83], v[138:139], v[242:243]\n"
"v_pk_fma_f32 v[114:115], v[114:115], v[138:139], v[178:179]\n"
"v_pk_fma_f32 v[84:85], v[84:85], v[140:141], v[244:245]\n"
"v_pk_fma_f32 v[116:117], v[116:117], v[140:141], v[180:181]\n"
"v_pk_fma_f32 v[86:87], v[86:87], v[142:143], v[246:247]\n"
"v_pk_fma_f32 v[118:119], v[118:119], v[142:143], v[182:183]\n"
"v_pk_add_f32 v[80:81], v[80:81], v[144:145]\n"
"v_pk_add_f32 v[112:113], v[112:113], v[144:145]\n"
"v_pk_add_f32 v[82:83], v[82:83], v[146:147]\n"
"v_pk_add_f32 v[114:115], v[114:115], v[146:147]\n"
"ds_write_b128 v231, v[48:51] offset:16384\n"
"v_pk_add_f32 v[84:85], v[84:85], v[148:149]\n"
"v_pk_add_f32 v[116:117], v[116:117], v[148:149]\n"
"v_pk_add_f32 v[86:87], v[86:87], v[150:151]\n"
"v_pk_add_f32 v[118:119], v[118:119], v[150:151]\n"
"v_cvt_pk_bf16_f32 v80, v80, v81\n"
"v_cvt_pk_bf16_f32 v112, v112, v113\n"
"v_cvt_pk_bf16_f32 v81, v82, v83\n"
"v_cvt_pk_bf16_f32 v113, v114, v115\n"
"v_cvt_pk_bf16_f32 v82, v84, v85\n"
"v_cvt_pk_bf16_f32 v114, v116, v117\n"
"v_cvt_pk_bf16_f32 v83, v86, v87\n"
"v_cvt_pk_bf16_f32 v115, v118, v119\n"
"s_waitcnt lgkmcnt(9)\n"
"v_pk_mul_f32 v[240:241], v[24:25], v[152:153]\n"
"v_pk_mul_f32 v[176:177], v[56:57], v[152:153]\n"
"v_pk_mul_f32 v[242:243], v[26:27], v[154:155]\n"
"v_pk_mul_f32 v[178:179], v[58:59], v[154:155]\n"
"ds_write_b128 v232, v[80:83] offset:16384\n"
"s_waitcnt lgkmcnt(9)\n"
"v_pk_mul_f32 v[244:245], v[28:29], v[156:157]\n"
"v_pk_mul_f32 v[180:181], v[60:61], v[156:157]\n"
"v_pk_mul_f32 v[246:247], v[30:31], v[158:159]\n"
"v_pk_mul_f32 v[182:183], v[62:63], v[158:159]\n"
"s_waitcnt lgkmcnt(8)\n"
"v_pk_fma_f32 v[24:25], v[24:25], v[160:161], v[240:241]\n"
"v_pk_fma_f32 v[56:57], v[56:57], v[160:161], v[176:177]\n"
"v_pk_fma_f32 v[26:27], v[26:27], v[162:163], v[242:243]\n"
"v_pk_fma_f32 v[58:59], v[58:59], v[162:163], v[178:179]\n"
"s_waitcnt lgkmcnt(7)\n"
"v_pk_fma_f32 v[28:29], v[28:29], v[164:165], v[244:245]\n"
"v_pk_fma_f32 v[60:61], v[60:61], v[164:165], v[180:181]\n"
"v_pk_fma_f32 v[30:31], v[30:31], v[166:167], v[246:247]\n"
"v_pk_fma_f32 v[62:63], v[62:63], v[166:167], v[182:183]\n"
"s_waitcnt lgkmcnt(6)\n"
"v_pk_add_f32 v[24:25], v[24:25], v[168:169]\n"
"v_pk_add_f32 v[56:57], v[56:57], v[168:169]\n"
"v_pk_add_f32 v[26:27], v[26:27], v[170:171]\n"
"v_pk_add_f32 v[58:59], v[58:59], v[170:171]\n"
"ds_write_b128 v233, v[112:115] offset:16384\n"
"s_waitcnt lgkmcnt(6)\n"
"v_pk_add_f32 v[28:29], v[28:29], v[172:173]\n"
"v_pk_add_f32 v[60:61], v[60:61], v[172:173]\n"
"v_pk_add_f32 v[30:31], v[30:31], v[174:175]\n"
"v_pk_add_f32 v[62:63], v[62:63], v[174:175]\n"
"v_cvt_pk_bf16_f32 v24, v24, v25\n"
"v_cvt_pk_bf16_f32 v56, v56, v57\n"
"v_cvt_pk_bf16_f32 v25, v26, v27\n"
"v_cvt_pk_bf16_f32 v57, v58, v59\n"
"v_cvt_pk_bf16_f32 v26, v28, v29\n"
"v_cvt_pk_bf16_f32 v58, v60, v61\n"
"v_cvt_pk_bf16_f32 v27, v30, v31\n"
"v_cvt_pk_bf16_f32 v59, v62, v63\n"
"v_pk_mul_f32 v[240:241], v[88:89], v[152:153]\n"
"v_pk_mul_f32 v[176:177], v[120:121], v[152:153]\n"
"v_pk_mul_f32 v[242:243], v[90:91], v[154:155]\n"
"v_pk_mul_f32 v[178:179], v[122:123], v[154:155]\n"
"ds_write_b128 v230, v[24:27] offset:24576\n"
"v_pk_mul_f32 v[244:245], v[92:93], v[156:157]\n"
"v_pk_mul_f32 v[180:181], v[124:125], v[156:157]\n"
"v_pk_mul_f32 v[246:247], v[94:95], v[158:159]\n"
"v_pk_mul_f32 v[182:183], v[126:127], v[158:159]\n"
"v_pk_fma_f32 v[88:89], v[88:89], v[160:161], v[240:241]\n"
"v_pk_fma_f32 v[120:121], v[120:121], v[160:161], v[176:177]\n"
"v_pk_fma_f32 v[90:91], v[90:91], v[162:163], v[242:243]\n"
"v_pk_fma_f32 v[122:123], v[122:123], v[162:163], v[178:179]\n"
"v_pk_fma_f32 v[92:93], v[92:93], v[164:165], v[244:245]\n"
"v_pk_fma_f32 v[124:125], v[124:125], v[164:165], v[180:181]\n"
"v_pk_fma_f32 v[94:95], v[94:95], v[166:167], v[246:247]\n"
"v_pk_fma_f32 v[126:127], v[126:127], v[166:167], v[182:183]\n"
"v_pk_add_f32 v[88:89], v[88:89], v[168:169]\n"
"v_pk_add_f32 v[120:121], v[120:121], v[168:169]\n"
"v_pk_add_f32 v[90:91], v[90:91], v[170:171]\n"
"v_pk_add_f32 v[122:123], v[122:123], v[170:171]\n"
"ds_write_b128 v231, v[56:59] offset:24576\n"
"v_pk_add_f32 v[92:93], v[92:93], v[172:173]\n"
"v_pk_add_f32 v[124:125], v[124:125], v[172:173]\n"
"v_pk_add_f32 v[94:95], v[94:95], v[174:175]\n"
"v_pk_add_f32 v[126:127], v[126:127], v[174:175]\n"
"v_cvt_pk_bf16_f32 v88, v88, v89\n"
"v_cvt_pk_bf16_f32 v120, v120, v121\n"
"v_cvt_pk_bf16_f32 v89, v90, v91\n"
"v_cvt_pk_bf16_f32 v121, v122, v123\n"
"v_cvt_pk_bf16_f32 v90, v92, v93\n"
"v_cvt_pk_bf16_f32 v122, v124, v125\n"
"v_cvt_pk_bf16_f32 v91, v94, v95\n"
"v_cvt_pk_bf16_f32 v123, v126, v127\n"
"ds_write_b128 v232, v[88:91] offset:24576\n"
"ds_write_b128 v233, v[120:123] offset:24576\n"
"s_waitcnt lgkmcnt(0)\n"
"s_barrier\n"
"s_add_u32 m0, s67, 65536\n"
"s_nop 0\n"
"buffer_load_dwordx4 v205, s[44:47], s68 offen offset:0 lds\n"
"buffer_load_dwordx4 v206, s[44:47], s68 offen offset:1024 lds\n"
"buffer_load_dwordx4 v207, s[44:47], s68 offen offset:2048 lds\n"
"buffer_load_dwordx4 v208, s[44:47], s68 offen offset:3072 lds\n"
"s_add_u32 s68, s68, 0x20000\n"
"s_and_b32 s68, s68, 0x3fffff\n"
"s_lshl_b32 s7, s31, 2\n"
"s_add_u32 s7, s7, s64\n"
"s_mul_i32 s7, s7, 160\n"
"ds_read_b128 a[160:163], v193 offset:0\n"
"ds_read_b64_tr_b16 a[128:129], v197 offset:0\n"
"ds_read_b64_tr_b16 a[130:131], v197 offset:1024\n"
"ds_read_b64_tr_b16 a[132:133], v197 offset:8\n"
"ds_read_b64_tr_b16 a[134:135], v197 offset:1032\n"
"ds_read_b128 a[164:167], v193 offset:256\n"
"ds_read_b128 a[168:171], v193 offset:32768\n"
"ds_read_b128 a[172:175], v193 offset:33024\n"
"ds_read_b64_tr_b16 a[136:137], v198 offset:0\n"
"ds_read_b64_tr_b16 a[138:139], v198 offset:1024\n"
"ds_read_b64_tr_b16 a[140:141], v198 offset:8\n"
"ds_read_b64_tr_b16 a[142:143], v198 offset:1032\n"
"s_add_u32 m0, s67, 81920\n"
"s_nop 0\n"
"buffer_load_dwordx4 v205, s[44:47], s68 offen offset:0 lds\n"
"buffer_load_dwordx4 v206, s[44:47], s68 offen offset:1024 lds\n"
"buffer_load_dwordx4 v207, s[44:47], s68 offen offset:2048 lds\n"
"buffer_load_dwordx4 v208, s[44:47], s68 offen offset:3072 lds\n"
"s_add_u32 s68, s68, 0x20000\n"
"s_and_b32 s68, s68, 0x3fffff\n"
"s_waitcnt lgkmcnt(9)\n"
"v_mfma_f32_16x16x32_bf16 v[0:3], a[128:131], a[160:163], 0\n"
"ds_read_b64_tr_b16 a[144:145], v199 offset:0\n"
"ds_read_b64_tr_b16 a[146:147], v199 offset:1024\n"
"s_waitcnt lgkmcnt(9)\n"
"v_mfma_f32_16x16x32_bf16 v[4:7], a[132:135], a[160:163], 0\n"
"ds_read_b64_tr_b16 a[148:149], v199 offset:8\n"
"s_waitcnt lgkmcnt(9)\n"
"v_mfma_f32_16x16x32_bf16 v[8:11], a[128:131], a[164:167], 0\n"
"ds_read_b64_tr_b16 a[150:151], v199 offset:1032\n"
"ds_read_b64_tr_b16 a[152:153], v200 offset:0\n"
"v_mfma_f32_16x16x32_bf16 v[12:15], a[132:135], a[164:167], 0\n"
"ds_read_b64_tr_b16 a[154:155], v200 offset:1024\n"
"s_waitcnt lgkmcnt(11)\n"
"v_mfma_f32_16x16x32_bf16 v[16:19], a[128:131], a[168:171], 0\n"
"ds_read_b64_tr_b16 a[156:157], v200 offset:8\n"
"v_mfma_f32_16x16x32_bf16 v[20:23], a[132:135], a[168:171], 0\n"
"ds_read_b64_tr_b16 a[158:159], v200 offset:1032\n"
"s_waitcnt lgkmcnt(12)\n"
"v_mfma_f32_16x16x32_bf16 v[24:27], a[128:131], a[172:175], 0\n"
"v_mfma_f32_16x16x32_bf16 v[28:31], a[132:135], a[172:175], 0\n"
"s_waitcnt lgkmcnt(10)\n"
"v_mfma_f32_16x16x32_bf16 v[32:35], a[136:139], a[160:163], 0\n"
"s_add_u32 m0, s67, 0\n"
"s_waitcnt lgkmcnt(0)\n"
"v_mfma_f32_16x16x32_bf16 v[36:39], a[140:143], a[160:163], 0\n"
"v_mfma_f32_16x16x32_bf16 v[40:43], a[136:139], a[164:167], 0\n"
"buffer_load_dwordx4 v205, s[44:47], s68 offen offset:0 lds\n"
"ds_read_b128 a[208:211], v194 offset:2048\n"
"v_mfma_f32_16x16x32_bf16 v[44:47], a[140:143], a[164:167], 0\n"
"ds_read_b64_tr_b16 a[176:177], v197 offset:32768\n"
"ds_read_b64_tr_b16 a[178:179], v197 offset:33792\n"
"v_mfma_f32_16x16x32_bf16 v[48:51], a[136:139], a[168:171], 0\n"
"buffer_load_dwordx4 v206, s[44:47], s68 offen offset:1024 lds\n"
"ds_read_b64_tr_b16 a[180:181], v197 offset:32776\n"
"v_mfma_f32_16x16x32_bf16 v[52:55], a[140:143], a[168:171], 0\n"
"ds_read_b64_tr_b16 a[182:183], v197 offset:33800\n"
"v_mfma_f32_16x16x32_bf16 v[56:59], a[136:139], a[172:175], 0\n"
"buffer_load_dwordx4 v207, s[44:47], s68 offen offset:2048 lds\n"
"ds_read_b128 a[212:215], v194 offset:2304\n"
"v_mfma_f32_16x16x32_bf16 v[60:63], a[140:143], a[172:175], 0\n"
"ds_read_b64_tr_b16 a[184:185], v198 offset:32768\n"
"v_mfma_f32_16x16x32_bf16 v[64:67], a[144:147], a[160:163], 0\n"
"buffer_load_dwordx4 v208, s[44:47], s68 offen offset:3072 lds\n"
"ds_read_b64_tr_b16 a[186:187], v198 offset:33792\n"
"ds_read_b64_tr_b16 a[188:189], v198 offset:32776\n"
"v_mfma_f32_16x16x32_bf16 v[68:71], a[148:151], a[160:163], 0\n"
"s_add_u32 s68, s68, 0x20000\n"
"s_and_b32 s68, s68, 0x3fffff\n"
"v_mfma_f32_16x16x32_bf16 v[72:75], a[144:147], a[164:167], 0\n"
"ds_read_b64_tr_b16 a[190:191], v198 offset:33800\n"
"ds_read_b128 a[216:219], v194 offset:34816\n"
"v_mfma_f32_16x16x32_bf16 v[76:79], a[148:151], a[164:167], 0\n"
"ds_read_b64_tr_b16 a[192:193], v199 offset:32768\n"
"s_add_u32 m0, s67, 16384\n"
"v_mfma_f32_16x16x32_bf16 v[80:83], a[144:147], a[168:171], 0\n"
"buffer_load_dwordx4 v205, s[44:47], s68 offen offset:0 lds\n"
"ds_read_b64_tr_b16 a[194:195], v199 offset:33792\n"
"v_mfma_f32_16x16x32_bf16 v[84:87], a[148:151], a[168:171], 0\n"
"ds_read_b64_tr_b16 a[196:197], v199 offset:32776\n"
"v_mfma_f32_16x16x32_bf16 v[88:91], a[144:147], a[172:175], 0\n"
"buffer_load_dwordx4 v206, s[44:47], s68 offen offset:1024 lds\n"
"ds_read_b64_tr_b16 a[198:199], v199 offset:33800\n"
"v_mfma_f32_16x16x32_bf16 v[92:95], a[148:151], a[172:175], 0\n"
"ds_read_b128 a[220:223], v194 offset:35072\n"
"v_mfma_f32_16x16x32_bf16 v[96:99], a[152:155], a[160:163], 0\n"
"ds_read_b64_tr_b16 a[200:201], v200 offset:32768\n"
"v_mfma_f32_16x16x32_bf16 v[100:103], a[156:159], a[160:163], 0\n"
"ds_read_b64_tr_b16 a[202:203], v200 offset:33792\n"
"v_mfma_f32_16x16x32_bf16 v[104:107], a[152:155], a[164:167], 0\n"
"buffer_load_dwordx4 v207, s[44:47], s68 offen offset:2048 lds\n"
"ds_read_b64_tr_b16 a[204:205], v200 offset:32776\n"
"v_mfma_f32_16x16x32_bf16 v[108:111], a[156:159], a[164:167], 0\n"
"ds_read_b64_tr_b16 a[206:207], v200 offset:33800\n"
"v_mfma_f32_16x16x32_bf16 v[112:115], a[152:155], a[168:171], 0\n"
"buffer_load_dwordx4 v208, s[44:47], s68 offen offset:3072 lds\n"
"s_add_u32 s68, s68, 0x20000\n"
"v_mfma_f32_16x16x32_bf16 v[116:119], a[156:159], a[168:171], 0\n"
"s_and_b32 s68, s68, 0x3fffff\n"
"v_mfma_f32_16x16x32_bf16 v[120:123], a[152:155], a[172:175], 0\n"
"s_waitcnt lgkmcnt(0)\n"
"v_mfma_f32_16x16x32_bf16 v[124:127], a[156:159], a[172:175], 0\n"
"v_mfma_f32_16x16x32_bf16 v[0:3], a[176:179], a[208:211], v[0:3]\n"
"v_mfma_f32_16x16x32_bf16 v[4:7], a[180:183], a[208:211], v[4:7]\n"
"v_mfma_f32_16x16x32_bf16 v[8:11], a[176:179], a[212:215], v[8:11]\n"
"s_add_u32 m0, s67, 32768\n"
"v_mfma_f32_16x16x32_bf16 v[12:15], a[180:183], a[212:215], v[12:15]\n"
"v_mfma_f32_16x16x32_bf16 v[16:19], a[176:179], a[216:219], v[16:19]\n"
"buffer_load_dwordx4 v205, s[44:47], s68 offen offset:0 lds\n"
"v_mfma_f32_16x16x32_bf16 v[20:23], a[180:183], a[216:219], v[20:23]\n"
"v_mfma_f32_16x16x32_bf16 v[24:27], a[176:179], a[220:223], v[24:27]\n"
"buffer_load_dwordx4 v206, s[44:47], s68 offen offset:1024 lds\n"
"v_mfma_f32_16x16x32_bf16 v[28:31], a[180:183], a[220:223], v[28:31]\n"
"v_mfma_f32_16x16x32_bf16 v[32:35], a[184:187], a[208:211], v[32:35]\n"
"s_waitcnt vmcnt(10)\n"
"v_mfma_f32_16x16x32_bf16 v[36:39], a[188:191], a[208:211], v[36:39]\n"
"v_mfma_f32_16x16x32_bf16 v[40:43], a[184:187], a[212:215], v[40:43]\n"
"buffer_load_dwordx4 v207, s[44:47], s68 offen offset:2048 lds\n"
"ds_read_b128 a[160:163], v193 offset:4096\n"
"v_mfma_f32_16x16x32_bf16 v[44:47], a[188:191], a[212:215], v[44:47]\n"
"ds_read_b64_tr_b16 a[128:129], v201 offset:0\n"
"ds_read_b64_tr_b16 a[130:131], v201 offset:1024\n"
"v_mfma_f32_16x16x32_bf16 v[48:51], a[184:187], a[216:219], v[48:51]\n"
"buffer_load_dwordx4 v208, s[44:47], s68 offen offset:3072 lds\n"
"ds_read_b64_tr_b16 a[132:133], v201 offset:8\n"
"ds_read_b64_tr_b16 a[134:135], v201 offset:1032\n"
"v_mfma_f32_16x16x32_bf16 v[52:55], a[188:191], a[216:219], v[52:55]\n"
"s_add_u32 s68, s68, 0x20000\n"
"s_and_b32 s68, s68, 0x3fffff\n"
"v_mfma_f32_16x16x32_bf16 v[56:59], a[184:187], a[220:223], v[56:59]\n"
"ds_read_b128 a[164:167], v193 offset:4352\n"
"v_mfma_f32_16x16x32_bf16 v[60:63], a[188:191], a[220:223], v[60:63]\n"
"ds_read_b64_tr_b16 a[136:137], v202 offset:0\n"
"v_mfma_f32_16x16x32_bf16 v[64:67], a[192:195], a[208:211], v[64:67]\n"
"ds_read_b64_tr_b16 a[138:139], v202 offset:1024\n"
"v_mfma_f32_16x16x32_bf16 v[68:71], a[196:199], a[208:211], v[68:71]\n"
"ds_read_b64_tr_b16 a[140:141], v202 offset:8\n"
"v_mfma_f32_16x16x32_bf16 v[72:75], a[192:195], a[212:215], v[72:75]\n"
"ds_read_b64_tr_b16 a[142:143], v202 offset:1032\n"
"ds_read_b128 a[168:171], v193 offset:36864\n"
"v_mfma_f32_16x16x32_bf16 v[76:79], a[196:199], a[212:215], v[76:79]\n"
"Lst2_%=:\n"
"s_cbranch_execz Lst6_%=\n"
"ds_read_b64_tr_b16 a[144:145], v203 offset:0\n"
"s_add_u32 m0, s67, 49152\n"
"v_mfma_f32_16x16x32_bf16 v[80:83], a[192:195], a[216:219], v[80:83]\n"
"buffer_load_dwordx4 v205, s[44:47], s68 offen offset:0 lds\n"
"ds_read_b64_tr_b16 a[146:147], v203 offset:1024\n"
"v_mfma_f32_16x16x32_bf16 v[84:87], a[196:199], a[216:219], v[84:87]\n"
"ds_read_b64_tr_b16 a[148:149], v203 offset:8\n"
"v_mfma_f32_16x16x32_bf16 v[88:91], a[192:195], a[220:223], v[88:91]\n"
"buffer_load_dwordx4 v206, s[44:47], s68 offen offset:1024 lds\n"
"ds_read_b64_tr_b16 a[150:151], v203 offset:1032\n"
"v_mfma_f32_16x16x32_bf16 v[92:95], a[196:199], a[220:223], v[92:95]\n"
"ds_read_b128 a[172:175], v193 offset:37120\n"
"v_mfma_f32_16x16x32_bf16 v[96:99], a[200:203], a[208:211], v[96:99]\n"
"ds_read_b64_tr_b16 a[152:153], v204 offset:0\n"
"v_mfma_f32_16x16x32_bf16 v[100:103], a[204:207], a[208:211], v[100:103]\n"
"ds_read_b64_tr_b16 a[154:155], v204 offset:1024\n"
"v_mfma_f32_16x16x32_bf16 v[104:107], a[200:203], a[212:215], v[104:107]\n"
"buffer_load_dwordx4 v207, s[44:47], s68 offen offset:2048 lds\n"
"ds_read_b64_tr_b16 a[156:157], v204 offset:8\n"
"v_mfma_f32_16x16x32_bf16 v[108:111], a[204:207], a[212:215], v[108:111]\n"
"ds_read_b64_tr_b16 a[158:159], v204 offset:1032\n"
"v_mfma_f32_16x16x32_bf16 v[112:115], a[200:203], a[216:219], v[112:115]\n"
"buffer_load_dwordx4 v208, s[44:47], s68 offen offset:3072 lds\n"
"s_add_u32 s68, s68, 0x20000\n"
"v_mfma_f32_16x16x32_bf16 v[116:119], a[204:207], a[216:219], v[116:119]\n"
"s_and_b32 s68, s68, 0x3fffff\n"
"v_mfma_f32_16x16x32_bf16 v[120:123], a[200:203], a[220:223], v[120:123]\n"
"s_waitcnt lgkmcnt(0)\n"
"v_mfma_f32_16x16x32_bf16 v[124:127], a[204:207], a[220:223], v[124:127]\n"
"v_mfma_f32_16x16x32_bf16 v[0:3], a[128:131], a[160:163], v[0:3]\n"
"v_mfma_f32_16x16x32_bf16 v[4:7], a[132:135], a[160:163], v[4:7]\n"
"v_mfma_f32_16x16x32_bf16 v[8:11], a[128:131], a[164:167], v[8:11]\n"
"s_add_u32 m0, s67, 65536\n"
"v_mfma_f32_16x16x32_bf16 v[12:15], a[132:135], a[164:167], v[12:15]\n"
"v_mfma_f32_16x16x32_bf16 v[16:19], a[128:131], a[168:171], v[16:19]\n"
"buffer_load_dwordx4 v205, s[44:47], s68 offen offset:0 lds\n"
"v_mfma_f32_16x16x32_bf16 v[20:23], a[132:135], a[168:171], v[20:23]\n"
"v_mfma_f32_16x16x32_bf16 v[24:27], a[128:131], a[172:175], v[24:27]\n"
"buffer_load_dwordx4 v206, s[44:47], s68 offen offset:1024 lds\n"
"v_mfma_f32_16x16x32_bf16 v[28:31], a[132:135], a[172:175], v[28:31]\n"
"v_mfma_f32_16x16x32_bf16 v[32:35], a[136:139], a[160:163], v[32:35]\n"
"s_waitcnt vmcnt(10)\n"
"v_mfma_f32_16x16x32_bf16 v[36:39], a[140:143], a[160:163], v[36:39]\n"
"v_mfma_f32_16x16x32_bf16 v[40:43], a[136:139], a[164:167], v[40:43]\n"
"buffer_load_dwordx4 v207, s[44:47], s68 offen offset:2048 lds\n"
"ds_read_b128 a[208:211], v194 offset:6144\n"
"v_mfma_f32_16x16x32_bf16 v[44:47], a[140:143], a[164:167], v[44:47]\n"
"ds_read_b64_tr_b16 a[176:177], v197 offset:0\n"
"ds_read_b64_tr_b16 a[178:179], v197 offset:1024\n"
"v_mfma_f32_16x16x32_bf16 v[48:51], a[136:139], a[168:171], v[48:51]\n"
"buffer_load_dwordx4 v208, s[44:47], s68 offen offset:3072 lds\n"
"ds_read_b64_tr_b16 a[180:181], v197 offset:8\n"
"ds_read_b64_tr_b16 a[182:183], v197 offset:1032\n"
"v_mfma_f32_16x16x32_bf16 v[52:55], a[140:143], a[168:171], v[52:55]\n"
"s_add_u32 s68, s68, 0x20000\n"
"s_and_b32 s68, s68, 0x3fffff\n"
"v_mfma_f32_16x16x32_bf16 v[56:59], a[136:139], a[172:175], v[56:59]\n"
"ds_read_b128 a[212:215], v194 offset:6400\n"
"v_mfma_f32_16x16x32_bf16 v[60:63], a[140:143], a[172:175], v[60:63]\n"
"ds_read_b64_tr_b16 a[184:185], v198 offset:0\n"
"v_mfma_f32_16x16x32_bf16 v[64:67], a[144:147], a[160:163], v[64:67]\n"
"ds_read_b64_tr_b16 a[186:187], v198 offset:1024\n"
"v_mfma_f32_16x16x32_bf16 v[68:71], a[148:151], a[160:163], v[68:71]\n"
"ds_read_b64_tr_b16 a[188:189], v198 offset:8\n"
"v_mfma_f32_16x16x32_bf16 v[72:75], a[144:147], a[164:167], v[72:75]\n"
"ds_read_b64_tr_b16 a[190:191], v198 offset:1032\n"
"ds_read_b128 a[216:219], v194 offset:38912\n"
"v_mfma_f32_16x16x32_bf16 v[76:79], a[148:151], a[164:167], v[76:79]\n"
"ds_read_b64_tr_b16 a[192:193], v199 offset:0\n"
"s_add_u32 m0, s67, 81920\n"
"v_mfma_f32_16x16x32_bf16 v[80:83], a[144:147], a[168:171], v[80:83]\n"
"buffer_load_dwordx4 v205, s[44:47], s68 offen offset:0 lds\n"
"ds_read_b64_tr_b16 a[194:195], v199 offset:1024\n"
"v_mfma_f32_16x16x32_bf16 v[84:87], a[148:151], a[168:171], v[84:87]\n"
"ds_read_b64_tr_b16 a[196:197], v199 offset:8\n"
"v_mfma_f32_16x16x32_bf16 v[88:91], a[144:147], a[172:175], v[88:91]\n"
"buffer_load_dwordx4 v206, s[44:47], s68 offen offset:1024 lds\n"
"ds_read_b64_tr_b16 a[198:199], v199 offset:1032\n"
"v_mfma_f32_16x16x32_bf16 v[92:95], a[148:151], a[172:175], v[92:95]\n"
"ds_read_b128 a[220:223], v194 offset:39168\n"
"v_mfma_f32_16x16x32_bf16 v[96:99], a[152:155], a[160:163], v[96:99]\n"
"ds_read_b64_tr_b16 a[200:201], v200 offset:0\n"
"v_mfma_f32_16x16x32_bf16 v[100:103], a[156:159], a[160:163], v[100:103]\n"
"ds_read_b64_tr_b16 a[202:203], v200 offset:1024\n"
"v_mfma_f32_16x16x32_bf16 v[104:107], a[152:155], a[164:167], v[104:107]\n"
"buffer_load_dwordx4 v207, s[44:47], s68 offen offset:2048 lds\n"
"ds_read_b64_tr_b16 a[204:205], v200 offset:8\n"
"v_mfma_f32_16x16x32_bf16 v[108:111], a[156:159], a[164:167], v[108:111]\n"
"ds_read_b64_tr_b16 a[206:207], v200 offset:1032\n"
"v_mfma_f32_16x16x32_bf16 v[112:115], a[152:155], a[168:171], v[112:115]\n"
"buffer_load_dwordx4 v208, s[44:47], s68 offen offset:3072 lds\n"
"s_add_u32 s68, s68, 0x20000\n"
"v_mfma_f32_16x16x32_bf16 v[116:119], a[156:159], a[168:171], v[116:119]\n"
"s_and_b32 s68, s68, 0x3fffff\n"
"v_mfma_f32_16x16x32_bf16 v[120:123], a[152:155], a[172:175], v[120:123]\n"
"s_waitcnt lgkmcnt(0)\n"
"v_mfma_f32_16x16x32_bf16 v[124:127], a[156:159], a[172:175], v[124:127]\n"
"v_mfma_f32_16x16x32_bf16 v[0:3], a[176:179], a[208:211], v[0:3]\n"
"v_mfma_f32_16x16x32_bf16 v[4:7], a[180:183], a[208:211], v[4:7]\n"
"v_mfma_f32_16x16x32_bf16 v[8:11], a[176:179], a[212:215], v[8:11]\n"
"s_add_u32 m0, s67, 0\n"
"v_mfma_f32_16x16x32_bf16 v[12:15], a[180:183], a[212:215], v[12:15]\n"
"v_mfma_f32_16x16x32_bf16 v[16:19], a[176:179], a[216:219], v[16:19]\n"
"buffer_load_dwordx4 v205, s[44:47], s68 offen offset:0 lds\n"
"v_mfma_f32_16x16x32_bf16 v[20:23], a[180:183], a[216:219], v[20:23]\n"
"v_mfma_f32_16x16x32_bf16 v[24:27], a[176:179], a[220:223], v[24:27]\n"
"buffer_load_dwordx4 v206, s[44:47], s68 offen offset:1024 lds\n"
"v_mfma_f32_16x16x32_bf16 v[28:31], a[180:183], a[220:223], v[28:31]\n"
"v_mfma_f32_16x16x32_bf16 v[32:35], a[184:187], a[208:211], v[32:35]\n"
"s_waitcnt vmcnt(10)\n"
"v_mfma_f32_16x16x32_bf16 v[36:39], a[188:191], a[208:211], v[36:39]\n"
"v_mfma_f32_16x16x32_bf16 v[40:43], a[184:187], a[212:215], v[40:43]\n"
"buffer_load_dwordx4 v207, s[44:47], s68 offen offset:2048 lds\n"
"ds_read_b128 a[160:163], v193 offset:8192\n"
"v_mfma_f32_16x16x32_bf16 v[44:47], a[188:191], a[212:215], v[44:47]\n"
"ds_read_b64_tr_b16 a[128:129], v197 offset:32768\n"
"ds_read_b64_tr_b16 a[130:131], v197 offset:33792\n"
"v_mfma_f32_16x16x32_bf16 v[48:51], a[184:187], a[216:219], v[48:51]\n"
"buffer_load_dwordx4 v208, s[44:47], s68 offen offset:3072 lds\n"
"ds_read_b64_tr_b16 a[132:133], v197 offset:32776\n"
"ds_read_b64_tr_b16 a[134:135], v197 offset:33800\n"
"v_mfma_f32_16x16x32_bf16 v[52:55], a[188:191], a[216:219], v[52:55]\n"
"s_add_u32 s68, s68, 0x20000\n"
"s_and_b32 s68, s68, 0x3fffff\n"
"v_mfma_f32_16x16x32_bf16 v[56:59], a[184:187], a[220:223], v[56:59]\n"
"ds_read_b128 a[164:167], v193 offset:8448\n"
"v_mfma_f32_16x16x32_bf16 v[60:63], a[188:191], a[220:223], v[60:63]\n"
"ds_read_b64_tr_b16 a[136:137], v198 offset:32768\n"
"v_mfma_f32_16x16x32_bf16 v[64:67], a[192:195], a[208:211], v[64:67]\n"
"ds_read_b64_tr_b16 a[138:139], v198 offset:33792\n"
"v_mfma_f32_16x16x32_bf16 v[68:71], a[196:199], a[208:211], v[68:71]\n"
"ds_read_b64_tr_b16 a[140:141], v198 offset:32776\n"
"v_mfma_f32_16x16x32_bf16 v[72:75], a[192:195], a[212:215], v[72:75]\n"
"ds_read_b64_tr_b16 a[142:143], v198 offset:33800\n"
"ds_read_b128 a[168:171], v193 offset:40960\n"
"v_mfma_f32_16x16x32_bf16 v[76:79], a[196:199], a[212:215], v[76:79]\n"
"ds_read_b64_tr_b16 a[144:145], v199 offset:32768\n"
"s_add_u32 m0, s67, 16384\n"
"v_mfma_f32_16x16x32_bf16 v[80:83], a[192:195], a[216:219], v[80:83]\n"
"buffer_load_dwordx4 v205, s[44:47], s68 offen offset:0 lds\n"
"ds_read_b64_tr_b16 a[146:147], v199 offset:33792\n"
"v_mfma_f32_16x16x32_bf16 v[84:87], a[196:199], a[216:219], v[84:87]\n"
"ds_read_b64_tr_b16 a[148:149], v199 offset:32776\n"
"v_mfma_f32_16x16x32_bf16 v[88:91], a[192:195], a[220:223], v[88:91]\n"
"buffer_load_dwordx4 v206, s[44:47], s68 offen offset:1024 lds\n"
"ds_read_b64_tr_b16 a[150:151], v199 offset:33800\n"
"v_mfma_f32_16x16x32_bf16 v[92:95], a[196:199], a[220:223], v[92:95]\n"
"ds_read_b128 a[172:175], v193 offset:41216\n"
"v_mfma_f32_16x16x32_bf16 v[96:99], a[200:203], a[208:211], v[96:99]\n"
"ds_read_b64_tr_b16 a[152:153], v200 offset:32768\n"
"v_mfma_f32_16x16x32_bf16 v[100:103], a[204:207], a[208:211], v[100:103]\n"
"ds_read_b64_tr_b16 a[154:155], v200 offset:33792\n"
"v_mfma_f32_16x16x32_bf16 v[104:107], a[200:203], a[212:215], v[104:107]\n"
"buffer_load_dwordx4 v207, s[44:47], s68 offen offset:2048 lds\n"
"ds_read_b64_tr_b16 a[156:157], v200 offset:32776\n"
"v_mfma_f32_16x16x32_bf16 v[108:111], a[204:207], a[212:215], v[108:111]\n"
"ds_read_b64_tr_b16 a[158:159], v200 offset:33800\n"
"v_mfma_f32_16x16x32_bf16 v[112:115], a[200:203], a[216:219], v[112:115]\n"
"buffer_load_dwordx4 v208, s[44:47], s68 offen offset:3072 lds\n"
"s_add_u32 s68, s68, 0x20000\n"
"v_mfma_f32_16x16x32_bf16 v[116:119], a[204:207], a[216:219], v[116:119]\n"
"s_and_b32 s68, s68, 0x3fffff\n"
"v_mfma_f32_16x16x32_bf16 v[120:123], a[200:203], a[220:223], v[120:123]\n"
"s_waitcnt lgkmcnt(0)\n"
"v_mfma_f32_16x16x32_bf16 v[124:127], a[204:207], a[220:223], v[124:127]\n"
"v_mfma_f32_16x16x32_bf16 v[0:3], a[128:131], a[160:163], v[0:3]\n"
"v_mfma_f32_16x16x32_bf16 v[4:7], a[132:135], a[160:163], v[4:7]\n"
"v_mfma_f32_16x16x32_bf16 v[8:11], a[128:131], a[164:167], v[8:11]\n"
"s_add_u32 m0, s67, 32768\n"
"v_mfma_f32_16x16x32_bf16 v[12:15], a[132:135], a[164:167], v[12:15]\n"
"v_mfma_f32_16x16x32_bf16 v[16:19], a[128:131], a[168:171], v[16:19]\n"
"buffer_load_dwordx4 v205, s[44:47], s68 offen offset:0 lds\n"
"v_mfma_f32_16x16x32_bf16 v[20:23], a[132:135], a[168:171], v[20:23]\n"
"v_mfma_f32_16x16x32_bf16 v[24:27], a[128:131], a[172:175], v[24:27]\n"
"buffer_load_dwordx4 v206, s[44:47], s68 offen offset:1024 lds\n"
"v_mfma_f32_16x16x32_bf16 v[28:31], a[132:135], a[172:175], v[28:31]\n"
"v_mfma_f32_16x16x32_bf16 v[32:35], a[136:139], a[160:163], v[32:35]\n"
"s_waitcnt vmcnt(10)\n"
"v_mfma_f32_16x16x32_bf16 v[36:39], a[140:143], a[160:163], v[36:39]\n"
"v_mfma_f32_16x16x32_bf16 v[40:43], a[136:139], a[164:167], v[40:43]\n"
"buffer_load_dwordx4 v207, s[44:47], s68 offen offset:2048 lds\n"
"ds_read_b128 a[208:211], v194 offset:10240\n"
"v_mfma_f32_16x16x32_bf16 v[44:47], a[140:143], a[164:167], v[44:47]\n"
"ds_read_b64_tr_b16 a[176:177], v201 offset:0\n"
"ds_read_b64_tr_b16 a[178:179], v201 offset:1024\n"
"v_mfma_f32_16x16x32_bf16 v[48:51], a[136:139], a[168:171], v[48:51]\n"
"buffer_load_dwordx4 v208, s[44:47], s68 offen offset:3072 lds\n"
"ds_read_b64_tr_b16 a[180:181], v201 offset:8\n"
"ds_read_b64_tr_b16 a[182:183], v201 offset:1032\n"
"v_mfma_f32_16x16x32_bf16 v[52:55], a[140:143], a[168:171], v[52:55]\n"
"s_add_u32 s68, s68, 0x20000\n"
"s_and_b32 s68, s68, 0x3fffff\n"
"v_mfma_f32_16x16x32_bf16 v[56:59], a[136:139], a[172:175], v[56:59]\n"
"ds_read_b128 a[212:215], v194 offset:10496\n"
"v_mfma_f32_16x16x32_bf16 v[60:63], a[140:143], a[172:175], v[60:63]\n"
"ds_read_b64_tr_b16 a[184:185], v202 offset:0\n"
"v_mfma_f32_16x16x32_bf16 v[64:67], a[144:147], a[160:163], v[64:67]\n"
"ds_read_b64_tr_b16 a[186:187], v202 offset:1024\n"
"v_mfma_f32_16x16x32_bf16 v[68:71], a[148:151], a[160:163], v[68:71]\n"
"ds_read_b64_tr_b16 a[188:189], v202 offset:8\n"
"v_mfma_f32_16x16x32_bf16 v[72:75], a[144:147], a[164:167], v[72:75]\n"
"ds_read_b64_tr_b16 a[190:191], v202 offset:1032\n"
"ds_read_b128 a[216:219], v194 offset:43008\n"
"v_mfma_f32_16x16x32_bf16 v[76:79], a[148:151], a[164:167], v[76:79]\n"
"ds_read_b64_tr_b16 a[192:193], v203 offset:0\n"
"s_add_u32 m0, s67, 49152\n"
"v_mfma_f32_16x16x32_bf16 v[80:83], a[144:147], a[168:171], v[80:83]\n"
"buffer_load_dwordx4 v205, s[44:47], s68 offen offset:0 lds\n"
"ds_read_b64_tr_b16 a[194:195], v203 offset:1024\n"
"v_mfma_f32_16x16x32_bf16 v[84:87], a[148:151], a[168:171], v[84:87]\n"
"ds_read_b64_tr_b16 a[196:197], v203 offset:8\n"
"v_mfma_f32_16x16x32_bf16 v[88:91], a[144:147], a[172:175], v[88:91]\n"
"buffer_load_dwordx4 v206, s[44:47], s68 offen offset:1024 lds\n"
"ds_read_b64_tr_b16 a[198:199], v203 offset:1032\n"
"v_mfma_f32_16x16x32_bf16 v[92:95], a[148:151], a[172:175], v[92:95]\n"
"ds_read_b128 a[220:223], v194 offset:43264\n"
"v_mfma_f32_16x16x32_bf16 v[96:99], a[152:155], a[160:163], v[96:99]\n"
"ds_read_b64_tr_b16 a[200:201], v204 offset:0\n"
"v_mfma_f32_16x16x32_bf16 v[100:103], a[156:159], a[160:163], v[100:103]\n"
"ds_read_b64_tr_b16 a[202:203], v204 offset:1024\n"
"v_mfma_f32_16x16x32_bf16 v[104:107], a[152:155], a[164:167], v[104:107]\n"
"buffer_load_dwordx4 v207, s[44:47], s68 offen offset:2048 lds\n"
"ds_read_b64_tr_b16 a[204:205], v204 offset:8\n"
"v_mfma_f32_16x16x32_bf16 v[108:111], a[156:159], a[164:167], v[108:111]\n"
"ds_read_b64_tr_b16 a[206:207], v204 offset:1032\n"
"v_mfma_f32_16x16x32_bf16 v[112:115], a[152:155], a[168:171], v[112:115]\n"
"buffer_load_dwordx4 v208, s[44:47], s68 offen offset:3072 lds\n"
"s_add_u32 s68, s68, 0x20000\n"
"v_mfma_f32_16x16x32_bf16 v[116:119], a[156:159], a[168:171], v[116:119]\n"
"s_and_b32 s68, s68, 0x3fffff\n"
"v_mfma_f32_16x16x32_bf16 v[120:123], a[152:155], a[172:175], v[120:123]\n"
"s_waitcnt lgkmcnt(0)\n"
"v_mfma_f32_16x16x32_bf16 v[124:127], a[156:159], a[172:175], v[124:127]\n"
"v_mfma_f32_16x16x32_bf16 v[0:3], a[176:179], a[208:211], v[0:3]\n"
"v_mfma_f32_16x16x32_bf16 v[4:7], a[180:183], a[208:211], v[4:7]\n"
"v_mfma_f32_16x16x32_bf16 v[8:11], a[176:179], a[212:215], v[8:11]\n"
"s_add_u32 m0, s67, 65536\n"
"v_mfma_f32_16x16x32_bf16 v[12:15], a[180:183], a[212:215], v[12:15]\n"
"v_mfma_f32_16x16x32_bf16 v[16:19], a[176:179], a[216:219], v[16:19]\n"
"buffer_load_dwordx4 v205, s[44:47], s68 offen offset:0 lds\n"
"v_mfma_f32_16x16x32_bf16 v[20:23], a[180:183], a[216:219], v[20:23]\n"
"v_mfma_f32_16x16x32_bf16 v[24:27], a[176:179], a[220:223], v[24:27]\n"
"buffer_load_dwordx4 v206, s[44:47], s68 offen offset:1024 lds\n"
"v_mfma_f32_16x16x32_bf16 v[28:31], a[180:183], a[220:223], v[28:31]\n"
"v_mfma_f32_16x16x32_bf16 v[32:35], a[184:187], a[208:211], v[32:35]\n"
"s_waitcnt vmcnt(10)\n"
"v_mfma_f32_16x16x32_bf16 v[36:39], a[188:191], a[208:211], v[36:39]\n"
"v_mfma_f32_16x16x32_bf16 v[40:43], a[184:187], a[212:215], v[40:43]\n"
"buffer_load_dwordx4 v207, s[44:47], s68 offen offset:2048 lds\n"
"ds_read_b128 a[160:163], v193 offset:12288\n"
"v_mfma_f32_16x16x32_bf16 v[44:47], a[188:191], a[212:215], v[44:47]\n"
"ds_read_b64_tr_b16 a[128:129], v197 offset:0\n"
"ds_read_b64_tr_b16 a[130:131], v197 offset:1024\n"
"v_mfma_f32_16x16x32_bf16 v[48:51], a[184:187], a[216:219], v[48:51]\n"
"buffer_load_dwordx4 v208, s[44:47], s68 offen offset:3072 lds\n"
"ds_read_b64_tr_b16 a[132:133], v197 offset:8\n"
"ds_read_b64_tr_b16 a[134:135], v197 offset:1032\n"
"v_mfma_f32_16x16x32_bf16 v[52:55], a[188:191], a[216:219], v[52:55]\n"
"s_add_u32 s68, s68, 0x20000\n"
"s_and_b32 s68, s68, 0x3fffff\n"
"v_mfma_f32_16x16x32_bf16 v[56:59], a[184:187], a[220:223], v[56:59]\n"
"ds_read_b128 a[164:167], v193 offset:12544\n"
"v_mfma_f32_16x16x32_bf16 v[60:63], a[188:191], a[220:223], v[60:63]\n"
"ds_read_b64_tr_b16 a[136:137], v198 offset:0\n"
"v_mfma_f32_16x16x32_bf16 v[64:67], a[192:195], a[208:211], v[64:67]\n"
"ds_read_b64_tr_b16 a[138:139], v198 offset:1024\n"
"v_mfma_f32_16x16x32_bf16 v[68:71], a[196:199], a[208:211], v[68:71]\n"
"ds_read_b64_tr_b16 a[140:141], v198 offset:8\n"
"v_mfma_f32_16x16x32_bf16 v[72:75], a[192:195], a[212:215], v[72:75]\n"
"ds_read_b64_tr_b16 a[142:143], v198 offset:1032\n"
"ds_read_b128 a[168:171], v193 offset:45056\n"
"v_mfma_f32_16x16x32_bf16 v[76:79], a[196:199], a[212:215], v[76:79]\n"
"ds_read_b64_tr_b16 a[144:145], v199 offset:0\n"
"s_add_u32 m0, s67, 81920\n"
"v_mfma_f32_16x16x32_bf16 v[80:83], a[192:195], a[216:219], v[80:83]\n"
"buffer_load_dwordx4 v205, s[44:47], s68 offen offset:0 lds\n"
"ds_read_b64_tr_b16 a[146:147], v199 offset:1024\n"
"v_mfma_f32_16x16x32_bf16 v[84:87], a[196:199], a[216:219], v[84:87]\n"
"ds_read_b64_tr_b16 a[148:149], v199 offset:8\n"
"v_mfma_f32_16x16x32_bf16 v[88:91], a[192:195], a[220:223], v[88:91]\n"
"buffer_load_dwordx4 v206, s[44:47], s68 offen offset:1024 lds\n"
"ds_read_b64_tr_b16 a[150:151], v199 offset:1032\n"
"v_mfma_f32_16x16x32_bf16 v[92:95], a[196:199], a[220:223], v[92:95]\n"
"ds_read_b128 a[172:175], v193 offset:45312\n"
"v_mfma_f32_16x16x32_bf16 v[96:99], a[200:203], a[208:211], v[96:99]\n"
"ds_read_b64_tr_b16 a[152:153], v200 offset:0\n"
"v_mfma_f32_16x16x32_bf16 v[100:103], a[204:207], a[208:211], v[100:103]\n"
"ds_read_b64_tr_b16 a[154:155], v200 offset:1024\n"
"v_mfma_f32_16x16x32_bf16 v[104:107], a[200:203], a[212:215], v[104:107]\n"
"buffer_load_dwordx4 v207, s[44:47], s68 offen offset:2048 lds\n"
"ds_read_b64_tr_b16 a[156:157], v200 offset:8\n"
"v_mfma_f32_16x16x32_bf16 v[108:111], a[204:207], a[212:215], v[108:111]\n"
"ds_read_b64_tr_b16 a[158:159], v200 offset:1032\n"
"v_mfma_f32_16x16x32_bf16 v[112:115], a[200:203], a[216:219], v[112:115]\n"
"buffer_load_dwordx4 v208, s[44:47], s68 offen offset:3072 lds\n"
"s_add_u32 s68, s68, 0x20000\n"
"v_mfma_f32_16x16x32_bf16 v[116:119], a[204:207], a[216:219], v[116:119]\n"
"s_and_b32 s68, s68, 0x3fffff\n"
"v_mfma_f32_16x16x32_bf16 v[120:123], a[200:203], a[220:223], v[120:123]\n"
"s_waitcnt lgkmcnt(0)\n"
"v_mfma_f32_16x16x32_bf16 v[124:127], a[204:207], a[220:223], v[124:127]\n"
"v_mfma_f32_16x16x32_bf16 v[0:3], a[128:131], a[160:163], v[0:3]\n"
"v_mfma_f32_16x16x32_bf16 v[4:7], a[132:135], a[160:163], v[4:7]\n"
"v_mfma_f32_16x16x32_bf16 v[8:11], a[128:131], a[164:167], v[8:11]\n"
"s_add_u32 m0, s67, 0\n"
"v_mfma_f32_16x16x32_bf16 v[12:15], a[132:135], a[164:167], v[12:15]\n"
"v_mfma_f32_16x16x32_bf16 v[16:19], a[128:131], a[168:171], v[16:19]\n"
"buffer_load_dwordx4 v205, s[44:47], s68 offen offset:0 lds\n"
"v_mfma_f32_16x16x32_bf16 v[20:23], a[132:135], a[168:171], v[20:23]\n"
"v_mfma_f32_16x16x32_bf16 v[24:27], a[128:131], a[172:175], v[24:27]\n"
"buffer_load_dwordx4 v206, s[44:47], s68 offen offset:1024 lds\n"
"v_mfma_f32_16x16x32_bf16 v[28:31], a[132:135], a[172:175], v[28:31]\n"
"v_mfma_f32_16x16x32_bf16 v[32:35], a[136:139], a[160:163], v[32:35]\n"
"s_waitcnt vmcnt(10)\n"
"v_mfma_f32_16x16x32_bf16 v[36:39], a[140:143], a[160:163], v[36:39]\n"
"v_mfma_f32_16x16x32_bf16 v[40:43], a[136:139], a[164:167], v[40:43]\n"
"buffer_load_dwordx4 v207, s[44:47], s68 offen offset:2048 lds\n"
"ds_read_b128 a[208:211], v194 offset:14336\n"
"v_mfma_f32_16x16x32_bf16 v[44:47], a[140:143], a[164:167], v[44:47]\n"
"ds_read_b64_tr_b16 a[176:177], v197 offset:32768\n"
"ds_read_b64_tr_b16 a[178:179], v197 offset:33792\n"
"v_mfma_f32_16x16x32_bf16 v[48:51], a[136:139], a[168:171], v[48:51]\n"
"buffer_load_dwordx4 v208, s[44:47], s68 offen offset:3072 lds\n"
"ds_read_b64_tr_b16 a[180:181], v197 offset:32776\n"
"ds_read_b64_tr_b16 a[182:183], v197 offset:33800\n"
"v_mfma_f32_16x16x32_bf16 v[52:55], a[140:143], a[168:171], v[52:55]\n"
"s_add_u32 s68, s68, 0x20000\n"
"s_and_b32 s68, s68, 0x3fffff\n"
"v_mfma_f32_16x16x32_bf16 v[56:59], a[136:139], a[172:175], v[56:59]\n"
"ds_read_b128 a[212:215], v194 offset:14592\n"
"v_mfma_f32_16x16x32_bf16 v[60:63], a[140:143], a[172:175], v[60:63]\n"
"ds_read_b64_tr_b16 a[184:185], v198 offset:32768\n"
"v_mfma_f32_16x16x32_bf16 v[64:67], a[144:147], a[160:163], v[64:67]\n"
"ds_read_b64_tr_b16 a[186:187], v198 offset:33792\n"
"v_mfma_f32_16x16x32_bf16 v[68:71], a[148:151], a[160:163], v[68:71]\n"
"ds_read_b64_tr_b16 a[188:189], v198 offset:32776\n"
"v_mfma_f32_16x16x32_bf16 v[72:75], a[144:147], a[164:167], v[72:75]\n"
"ds_read_b64_tr_b16 a[190:191], v198 offset:33800\n"
"ds_read_b128 a[216:219], v194 offset:47104\n"
"v_mfma_f32_16x16x32_bf16 v[76:79], a[148:151], a[164:167], v[76:79]\n"
"ds_read_b64_tr_b16 a[192:193], v199 offset:32768\n"
"s_add_u32 m0, s67, 16384\n"
"v_mfma_f32_16x16x32_bf16 v[80:83], a[144:147], a[168:171], v[80:83]\n"
"buffer_load_dwordx4 v205, s[44:47], s68 offen offset:0 lds\n"
"ds_read_b64_tr_b16 a[194:195], v199 offset:33792\n"
"v_mfma_f32_16x16x32_bf16 v[84:87], a[148:151], a[168:171], v[84:87]\n"
"ds_read_b64_tr_b16 a[196:197], v199 offset:32776\n"
"v_mfma_f32_16x16x32_bf16 v[88:91], a[144:147], a[172:175], v[88:91]\n"
"buffer_load_dwordx4 v206, s[44:47], s68 offen offset:1024 lds\n"
"ds_read_b64_tr_b16 a[198:199], v199 offset:33800\n"
"v_mfma_f32_16x16x32_bf16 v[92:95], a[148:151], a[172:175], v[92:95]\n"
"ds_read_b128 a[220:223], v194 offset:47360\n"
"v_mfma_f32_16x16x32_bf16 v[96:99], a[152:155], a[160:163], v[96:99]\n"
"ds_read_b64_tr_b16 a[200:201], v200 offset:32768\n"
"v_mfma_f32_16x16x32_bf16 v[100:103], a[156:159], a[160:163], v[100:103]\n"
"ds_read_b64_tr_b16 a[202:203], v200 offset:33792\n"
"v_mfma_f32_16x16x32_bf16 v[104:107], a[152:155], a[164:167], v[104:107]\n"
"buffer_load_dwordx4 v207, s[44:47], s68 offen offset:2048 lds\n"
"ds_read_b64_tr_b16 a[204:205], v200 offset:32776\n"
"v_mfma_f32_16x16x32_bf16 v[108:111], a[156:159], a[164:167], v[108:111]\n"
"ds_read_b64_tr_b16 a[206:207], v200 offset:33800\n"
"v_mfma_f32_16x16x32_bf16 v[112:115], a[152:155], a[168:171], v[112:115]\n"
"buffer_load_dwordx4 v208, s[44:47], s68 offen offset:3072 lds\n"
"s_add_u32 s68, s68, 0x20000\n"
"v_mfma_f32_16x16x32_bf16 v[116:119], a[156:159], a[168:171], v[116:119]\n"
"s_and_b32 s68, s68, 0x3fffff\n"
"v_mfma_f32_16x16x32_bf16 v[120:123], a[152:155], a[172:175], v[120:123]\n"
"s_waitcnt lgkmcnt(0)\n"
"v_mfma_f32_16x16x32_bf16 v[124:127], a[156:159], a[172:175], v[124:127]\n"
"v_mfma_f32_16x16x32_bf16 v[0:3], a[176:179], a[208:211], v[0:3]\n"
"v_mfma_f32_16x16x32_bf16 v[4:7], a[180:183], a[208:211], v[4:7]\n"
"v_mfma_f32_16x16x32_bf16 v[8:11], a[176:179], a[212:215], v[8:11]\n"
"s_add_u32 m0, s67, 32768\n"
"v_mfma_f32_16x16x32_bf16 v[12:15], a[180:183], a[212:215], v[12:15]\n"
"v_mfma_f32_16x16x32_bf16 v[16:19], a[176:179], a[216:219], v[16:19]\n"
"buffer_load_dwordx4 v205, s[44:47], s68 offen offset:0 lds\n"
"v_mfma_f32_16x16x32_bf16 v[20:23], a[180:183], a[216:219], v[20:23]\n"
"v_mfma_f32_16x16x32_bf16 v[24:27], a[176:179], a[220:223], v[24:27]\n"
"buffer_load_dwordx4 v206, s[44:47], s68 offen offset:1024 lds\n"
"v_mfma_f32_16x16x32_bf16 v[28:31], a[180:183], a[220:223], v[28:31]\n"
"v_mfma_f32_16x16x32_bf16 v[32:35], a[184:187], a[208:211], v[32:35]\n"
"s_waitcnt vmcnt(10)\n"
"v_mfma_f32_16x16x32_bf16 v[36:39], a[188:191], a[208:211], v[36:39]\n"
"v_mfma_f32_16x16x32_bf16 v[40:43], a[184:187], a[212:215], v[40:43]\n"
"buffer_load_dwordx4 v207, s[44:47], s68 offen offset:2048 lds\n"
"ds_read_b128 a[160:163], v193 offset:16384\n"
"v_mfma_f32_16x16x32_bf16 v[44:47], a[188:191], a[212:215], v[44:47]\n"
"ds_read_b64_tr_b16 a[128:129], v201 offset:0\n"
"ds_read_b64_tr_b16 a[130:131], v201 offset:1024\n"
"v_mfma_f32_16x16x32_bf16 v[48:51], a[184:187], a[216:219], v[48:51]\n"
"buffer_load_dwordx4 v208, s[44:47], s68 offen offset:3072 lds\n"
"ds_read_b64_tr_b16 a[132:133], v201 offset:8\n"
"ds_read_b64_tr_b16 a[134:135], v201 offset:1032\n"
"v_mfma_f32_16x16x32_bf16 v[52:55], a[188:191], a[216:219], v[52:55]\n"
"s_add_u32 s68, s68, 0x20000\n"
"s_and_b32 s68, s68, 0x3fffff\n"
"v_mfma_f32_16x16x32_bf16 v[56:59], a[184:187], a[220:223], v[56:59]\n"
"ds_read_b128 a[164:167], v193 offset:16640\n"
"v_mfma_f32_16x16x32_bf16 v[60:63], a[188:191], a[220:223], v[60:63]\n"
"ds_read_b64_tr_b16 a[136:137], v202 offset:0\n"
"v_mfma_f32_16x16x32_bf16 v[64:67], a[192:195], a[208:211], v[64:67]\n"
"ds_read_b64_tr_b16 a[138:139], v202 offset:1024\n"
"ds_read_b64_tr_b16 a[140:141], v202 offset:8\n"
"v_mfma_f32_16x16x32_bf16 v[68:71], a[196:199], a[208:211], v[68:71]\n"
"v_lshrrev_b32 v242, 5, v192\n"
"v_and_b32 v243, 31, v192\n"
"v_mfma_f32_16x16x32_bf16 v[72:75], a[192:195], a[212:215], v[72:75]\n"
"ds_read_b64_tr_b16 a[142:143], v202 offset:1032\n"
"ds_read_b128 a[168:171], v193 offset:49152\n"
"ds_read_b64_tr_b16 a[144:145], v203 offset:0\n"
"v_mfma_f32_16x16x32_bf16 v[76:79], a[196:199], a[212:215], v[76:79]\n"
"v_and_b32 v240, 15, v192\n"
"s_add_u32 m0, s67, 49152\n"
"v_mfma_f32_16x16x32_bf16 v[80:83], a[192:195], a[216:219], v[80:83]\n"
"buffer_load_dwordx4 v205, s[44:47], s68 offen offset:0 lds\n"
"ds_read_b64_tr_b16 a[146:147], v203 offset:1024\n"
"v_mfma_f32_16x16x32_bf16 v[84:87], a[196:199], a[216:219], v[84:87]\n"
"ds_read_b64_tr_b16 a[148:149], v203 offset:8\n"
"v_xor_b32 v240, v240, v242\n"
"v_mfma_f32_16x16x32_bf16 v[88:91], a[192:195], a[220:223], v[88:91]\n"
"buffer_load_dwordx4 v206, s[44:47], s68 offen offset:1024 lds\n"
"ds_read_b64_tr_b16 a[150:151], v203 offset:1032\n"
"v_mfma_f32_16x16x32_bf16 v[92:95], a[196:199], a[220:223], v[92:95]\n"
"ds_read_b128 a[172:175], v193 offset:49408\n"
"v_xor_b32 v241, 0, v240\n"
"v_mfma_f32_16x16x32_bf16 v[96:99], a[200:203], a[208:211], v[96:99]\n"
"ds_read_b64_tr_b16 a[152:153], v204 offset:0\n"
"ds_read_b64_tr_b16 a[154:155], v204 offset:1024\n"
"v_mfma_f32_16x16x32_bf16 v[100:103], a[204:207], a[208:211], v[100:103]\n"
"v_lshlrev_b32 v241, 4, v241\n"
"v_lshl_add_u32 v216, v243, 8, v241\n"
"v_mfma_f32_16x16x32_bf16 v[104:107], a[200:203], a[212:215], v[104:107]\n"
"buffer_load_dwordx4 v207, s[44:47], s68 offen offset:2048 lds\n"
"ds_read_b64_tr_b16 a[156:157], v204 offset:8\n"
"v_mfma_f32_16x16x32_bf16 v[108:111], a[204:207], a[212:215], v[108:111]\n"
"ds_read_b64_tr_b16 a[158:159], v204 offset:1032\n"
"v_xor_b32 v241, 2, v240\n"
"v_mfma_f32_16x16x32_bf16 v[112:115], a[200:203], a[216:219], v[112:115]\n"
"buffer_load_dwordx4 v208, s[44:47], s68 offen offset:3072 lds\n"
"v_lshlrev_b32 v241, 4, v241\n"
"v_mfma_f32_16x16x32_bf16 v[116:119], a[204:207], a[216:219], v[116:119]\n"
"s_add_u32 s68, s68, 0x20000\n"
"s_and_b32 s68, s68, 0x3fffff\n"
"v_mfma_f32_16x16x32_bf16 v[120:123], a[200:203], a[220:223], v[120:123]\n"
"v_lshl_add_u32 v217, v243, 8, v241\n"
"s_waitcnt lgkmcnt(0)\n"
"v_mfma_f32_16x16x32_bf16 v[124:127], a[204:207], a[220:223], v[124:127]\n"
"v_mfma_f32_16x16x32_bf16 v[0:3], a[128:131], a[160:163], v[0:3]\n"
"v_xor_b32 v241, 4, v240\n"
"v_mfma_f32_16x16x32_bf16 v[4:7], a[132:135], a[160:163], v[4:7]\n"
"v_lshlrev_b32 v241, 4, v241\n"
"v_mfma_f32_16x16x32_bf16 v[8:11], a[128:131], a[164:167], v[8:11]\n"
"v_lshl_add_u32 v218, v243, 8, v241\n"
"v_mfma_f32_16x16x32_bf16 v[12:15], a[132:135], a[164:167], v[12:15]\n"
"s_add_u32 m0, s67, 65536\n"
"v_mfma_f32_16x16x32_bf16 v[16:19], a[128:131], a[168:171], v[16:19]\n"
"buffer_load_dwordx4 v205, s[44:47], s68 offen offset:0 lds\n"
"v_mfma_f32_16x16x32_bf16 v[20:23], a[132:135], a[168:171], v[20:23]\n"
"v_xor_b32 v241, 6, v240\n"
"v_mfma_f32_16x16x32_bf16 v[24:27], a[128:131], a[172:175], v[24:27]\n"
"buffer_load_dwordx4 v206, s[44:47], s68 offen offset:1024 lds\n"
"v_mfma_f32_16x16x32_bf16 v[28:31], a[132:135], a[172:175], v[28:31]\n"
"v_lshlrev_b32 v241, 4, v241\n"
"v_mfma_f32_16x16x32_bf16 v[32:35], a[136:139], a[160:163], v[32:35]\n"
"v_lshl_add_u32 v219, v243, 8, v241\n"
"v_xor_b32 v241, 8, v240\n"
"v_mfma_f32_16x16x32_bf16 v[36:39], a[140:143], a[160:163], v[36:39]\n"
"s_waitcnt vmcnt(10)\n"
"v_mfma_f32_16x16x32_bf16 v[40:43], a[136:139], a[164:167], v[40:43]\n"
"buffer_load_dwordx4 v207, s[44:47], s68 offen offset:2048 lds\n"
"ds_read_b128 a[208:211], v194 offset:18432\n"
"ds_read_b64_tr_b16 a[176:177], v197 offset:0\n"
"v_mfma_f32_16x16x32_bf16 v[44:47], a[140:143], a[164:167], v[44:47]\n"
"ds_read_b64_tr_b16 a[178:179], v197 offset:1024\n"
"v_lshlrev_b32 v241, 4, v241\n"
"v_mfma_f32_16x16x32_bf16 v[48:51], a[136:139], a[168:171], v[48:51]\n"
"buffer_load_dwordx4 v208, s[44:47], s68 offen offset:3072 lds\n"
"ds_read_b64_tr_b16 a[180:181], v197 offset:8\n"
"ds_read_b64_tr_b16 a[182:183], v197 offset:1032\n"
"v_mfma_f32_16x16x32_bf16 v[52:55], a[140:143], a[168:171], v[52:55]\n"
"v_lshl_add_u32 v220, v243, 8, v241\n"
"s_add_u32 s68, s68, 0x20000\n"
"s_and_b32 s68, s68, 0x3fffff\n"
"v_mfma_f32_16x16x32_bf16 v[56:59], a[136:139], a[172:175], v[56:59]\n"
"ds_read_b128 a[212:215], v194 offset:18688\n"
"ds_read_b64_tr_b16 a[184:185], v198 offset:0\n"
"v_mfma_f32_16x16x32_bf16 v[60:63], a[140:143], a[172:175], v[60:63]\n"
"v_xor_b32 v241, 10, v240\n"
"v_mfma_f32_16x16x32_bf16 v[64:67], a[144:147], a[160:163], v[64:67]\n"
"ds_read_b64_tr_b16 a[186:187], v198 offset:1024\n"
"ds_read_b64_tr_b16 a[188:189], v198 offset:8\n"
"v_mfma_f32_16x16x32_bf16 v[68:71], a[148:151], a[160:163], v[68:71]\n"
"v_lshlrev_b32 v241, 4, v241\n"
"v_lshl_add_u32 v221, v243, 8, v241\n"
"v_mfma_f32_16x16x32_bf16 v[72:75], a[144:147], a[164:167], v[72:75]\n"
"ds_read_b64_tr_b16 a[190:191], v198 offset:1032\n"
"ds_read_b128 a[216:219], v194 offset:51200\n"
"ds_read_b64_tr_b16 a[192:193], v199 offset:0\n"
"v_mfma_f32_16x16x32_bf16 v[76:79], a[148:151], a[164:167], v[76:79]\n"
"v_xor_b32 v241, 12, v240\n"
"s_add_u32 m0, s67, 81920\n"
"v_mfma_f32_16x16x32_bf16 v[80:83], a[144:147], a[168:171], v[80:83]\n"
"buffer_load_dwordx4 v205, s[44:47], s68 offen offset:0 lds\n"
"ds_read_b64_tr_b16 a[194:195], v199 offset:1024\n"
"v_mfma_f32_16x16x32_bf16 v[84:87], a[148:151], a[168:171], v[84:87]\n"
"ds_read_b64_tr_b16 a[196:197], v199 offset:8\n"
"v_lshlrev_b32 v241, 4, v241\n"
"v_mfma_f32_16x16x32_bf16 v[88:91], a[144:147], a[172:175], v[88:91]\n"
"buffer_load_dwordx4 v206, s[44:47], s68 offen offset:1024 lds\n"
"ds_read_b64_tr_b16 a[198:199], v199 offset:1032\n"
"v_mfma_f32_16x16x32_bf16 v[92:95], a[148:151], a[172:175], v[92:95]\n"
"ds_read_b128 a[220:223], v194 offset:51456\n"
"v_lshl_add_u32 v222, v243, 8, v241\n"
"v_mfma_f32_16x16x32_bf16 v[96:99], a[152:155], a[160:163], v[96:99]\n"
"ds_read_b64_tr_b16 a[200:201], v200 offset:0\n"
"ds_read_b64_tr_b16 a[202:203], v200 offset:1024\n"
"v_mfma_f32_16x16x32_bf16 v[100:103], a[156:159], a[160:163], v[100:103]\n"
"v_xor_b32 v241, 14, v240\n"
"v_lshlrev_b32 v241, 4, v241\n"
"v_mfma_f32_16x16x32_bf16 v[104:107], a[152:155], a[164:167], v[104:107]\n"
"buffer_load_dwordx4 v207, s[44:47], s68 offen offset:2048 lds\n"
"ds_read_b64_tr_b16 a[204:205], v200 offset:8\n"
"v_mfma_f32_16x16x32_bf16 v[108:111], a[156:159], a[164:167], v[108:111]\n"
"ds_read_b64_tr_b16 a[206:207], v200 offset:1032\n"
"v_lshl_add_u32 v223, v243, 8, v241\n"
"v_mfma_f32_16x16x32_bf16 v[112:115], a[152:155], a[168:171], v[112:115]\n"
"buffer_load_dwordx4 v208, s[44:47], s68 offen offset:3072 lds\n"
"v_bfe_u32 v244, v192, 2, 2\n"
"v_mfma_f32_16x16x32_bf16 v[116:119], a[156:159], a[168:171], v[116:119]\n"
"Lst3_%=:\n"
"s_cbranch_execz Lst7_%=\n"
"s_add_u32 s68, s68, 0x20000\n"
"s_and_b32 s68, s68, 0x3fffff\n"
"v_mfma_f32_16x16x32_bf16 v[120:123], a[152:155], a[172:175], v[120:123]\n"
"v_lshl_add_u32 v240, v244, 8, v215\n"
"s_waitcnt lgkmcnt(0)\n"
"v_mfma_f32_16x16x32_bf16 v[124:127], a[156:159], a[172:175], v[124:127]\n"
"v_mfma_f32_16x16x32_bf16 v[0:3], a[176:179], a[208:211], v[0:3]\n"
"v_lshl_add_u32 v240, v242, 10, v240\n"
"v_mfma_f32_16x16x32_bf16 v[4:7], a[180:183], a[208:211], v[4:7]\n"
"v_xor_b32 v241, 0, v244\n"
"v_mfma_f32_16x16x32_bf16 v[8:11], a[176:179], a[212:215], v[8:11]\n"
"v_lshl_add_u32 v224, v241, 6, v240\n"
"v_mfma_f32_16x16x32_bf16 v[12:15], a[180:183], a[212:215], v[12:15]\n"
"s_add_u32 m0, s67, 0\n"
"v_mfma_f32_16x16x32_bf16 v[16:19], a[176:179], a[216:219], v[16:19]\n"
"buffer_load_dwordx4 v205, s[44:47], s68 offen offset:0 lds\n"
"v_mfma_f32_16x16x32_bf16 v[20:23], a[180:183], a[216:219], v[20:23]\n"
"v_xor_b32 v241, 1, v244\n"
"v_mfma_f32_16x16x32_bf16 v[24:27], a[176:179], a[220:223], v[24:27]\n"
"buffer_load_dwordx4 v206, s[44:47], s68 offen offset:1024 lds\n"
"v_mfma_f32_16x16x32_bf16 v[28:31], a[180:183], a[220:223], v[28:31]\n"
"v_lshl_add_u32 v225, v241, 6, v240\n"
"v_mfma_f32_16x16x32_bf16 v[32:35], a[184:187], a[208:211], v[32:35]\n"
"v_xor_b32 v241, 2, v244\n"
"v_lshl_add_u32 v226, v241, 6, v240\n"
"v_mfma_f32_16x16x32_bf16 v[36:39], a[188:191], a[208:211], v[36:39]\n"
"s_waitcnt vmcnt(10)\n"
"v_mfma_f32_16x16x32_bf16 v[40:43], a[184:187], a[212:215], v[40:43]\n"
"buffer_load_dwordx4 v207, s[44:47], s68 offen offset:2048 lds\n"
"ds_read_b128 a[160:163], v193 offset:20480\n"
"ds_read_b64_tr_b16 a[128:129], v197 offset:32768\n"
"v_mfma_f32_16x16x32_bf16 v[44:47], a[188:191], a[212:215], v[44:47]\n"
"ds_read_b64_tr_b16 a[130:131], v197 offset:33792\n"
"v_xor_b32 v241, 3, v244\n"
"v_mfma_f32_16x16x32_bf16 v[48:51], a[184:187], a[216:219], v[48:51]\n"
"buffer_load_dwordx4 v208, s[44:47], s68 offen offset:3072 lds\n"
"ds_read_b64_tr_b16 a[132:133], v197 offset:32776\n"
"ds_read_b64_tr_b16 a[134:135], v197 offset:33800\n"
"v_mfma_f32_16x16x32_bf16 v[52:55], a[188:191], a[216:219], v[52:55]\n"
"v_lshl_add_u32 v227, v241, 6, v240\n"
"s_add_u32 s68, s68, 0x20000\n"
"s_and_b32 s68, s68, 0x3fffff\n"
"v_mfma_f32_16x16x32_bf16 v[56:59], a[184:187], a[220:223], v[56:59]\n"
"ds_read_b128 a[164:167], v193 offset:20736\n"
"ds_read_b64_tr_b16 a[136:137], v198 offset:32768\n"
"v_mfma_f32_16x16x32_bf16 v[60:63], a[188:191], a[220:223], v[60:63]\n"
"v_bfe_u32 v240, v192, 2, 2\n"
"v_mfma_f32_16x16x32_bf16 v[64:67], a[192:195], a[208:211], v[64:67]\n"
"ds_read_b64_tr_b16 a[138:139], v198 offset:33792\n"
"ds_read_b64_tr_b16 a[140:141], v198 offset:32776\n"
"v_mfma_f32_16x16x32_bf16 v[68:71], a[196:199], a[208:211], v[68:71]\n"
"v_lshlrev_b32 v240, 7, v240\n"
"v_and_b32 v241, 1, v192\n"
"v_mfma_f32_16x16x32_bf16 v[72:75], a[192:195], a[212:215], v[72:75]\n"
"ds_read_b64_tr_b16 a[142:143], v198 offset:33800\n"
"ds_read_b128 a[168:171], v193 offset:53248\n"
"ds_read_b64_tr_b16 a[144:145], v199 offset:32768\n"
"v_mfma_f32_16x16x32_bf16 v[76:79], a[196:199], a[212:215], v[76:79]\n"
"v_lshl_add_u32 v240, v241, 5, v240\n"
"s_add_u32 m0, s67, 16384\n"
"v_mfma_f32_16x16x32_bf16 v[80:83], a[192:195], a[216:219], v[80:83]\n"
"buffer_load_dwordx4 v205, s[44:47], s68 offen offset:0 lds\n"
"ds_read_b64_tr_b16 a[146:147], v199 offset:33792\n"
"v_mfma_f32_16x16x32_bf16 v[84:87], a[196:199], a[216:219], v[84:87]\n"
"ds_read_b64_tr_b16 a[148:149], v199 offset:32776\n"
"v_bfe_u32 v241, v192, 1, 1\n"
"v_mfma_f32_16x16x32_bf16 v[88:91], a[192:195], a[220:223], v[88:91]\n"
"buffer_load_dwordx4 v206, s[44:47], s68 offen offset:1024 lds\n"
"ds_read_b64_tr_b16 a[150:151], v199 offset:33800\n"
"v_mfma_f32_16x16x32_bf16 v[92:95], a[196:199], a[220:223], v[92:95]\n"
"ds_read_b128 a[172:175], v193 offset:53504\n"
"v_lshl_add_u32 v240, v241, 4, v240\n"
"v_mfma_f32_16x16x32_bf16 v[96:99], a[200:203], a[208:211], v[96:99]\n"
"ds_read_b64_tr_b16 a[152:153], v200 offset:32768\n"
"ds_read_b64_tr_b16 a[154:155], v200 offset:33792\n"
"v_mfma_f32_16x16x32_bf16 v[100:103], a[204:207], a[208:211], v[100:103]\n"
"v_bfe_u32 v241, v192, 4, 1\n"
"v_lshl_add_u32 v240, v241, 11, v240\n"
"v_mfma_f32_16x16x32_bf16 v[104:107], a[200:203], a[212:215], v[104:107]\n"
"buffer_load_dwordx4 v207, s[44:47], s68 offen offset:2048 lds\n"
"ds_read_b64_tr_b16 a[156:157], v200 offset:32776\n"
"v_mfma_f32_16x16x32_bf16 v[108:111], a[204:207], a[212:215], v[108:111]\n"
"ds_read_b64_tr_b16 a[158:159], v200 offset:33800\n"
"v_lshrrev_b32 v241, 5, v192\n"
"v_mfma_f32_16x16x32_bf16 v[112:115], a[200:203], a[216:219], v[112:115]\n"
"buffer_load_dwordx4 v208, s[44:47], s68 offen offset:3072 lds\n"
"v_lshl_add_u32 v240, v241, 10, v240\n"
"v_mfma_f32_16x16x32_bf16 v[116:119], a[204:207], a[216:219], v[116:119]\n"
"s_add_u32 s68, s68, 0x20000\n"
"s_and_b32 s68, s68, 0x3fffff\n"
"v_mfma_f32_16x16x32_bf16 v[120:123], a[200:203], a[220:223], v[120:123]\n"
"v_bfe_u32 v241, v192, 3, 1\n"
"s_waitcnt lgkmcnt(0)\n"
"v_mfma_f32_16x16x32_bf16 v[124:127], a[204:207], a[220:223], v[124:127]\n"
"v_mfma_f32_16x16x32_bf16 v[0:3], a[128:131], a[160:163], v[0:3]\n"
"v_lshl_add_u32 v240, v241, 6, v240\n"
"v_mfma_f32_16x16x32_bf16 v[4:7], a[132:135], a[160:163], v[4:7]\n"
"v_add_u32 v240, 0x10000, v240\n"
"v_mfma_f32_16x16x32_bf16 v[8:11], a[128:131], a[164:167], v[8:11]\n"
"v_add_u32 v232, s67, v240\n"
"v_mfma_f32_16x16x32_bf16 v[12:15], a[132:135], a[164:167], v[12:15]\n"
"s_add_u32 m0, s67, 32768\n"
"v_mfma_f32_16x16x32_bf16 v[16:19], a[128:131], a[168:171], v[16:19]\n"
"buffer_load_dwordx4 v205, s[44:47], s68 offen offset:0 lds\n"
"v_mfma_f32_16x16x32_bf16 v[20:23], a[132:135], a[168:171], v[20:23]\n"
"v_xor_b32 v233, 64, v232\n"
"v_mfma_f32_16x16x32_bf16 v[24:27], a[128:131], a[172:175], v[24:27]\n"
"buffer_load_dwordx4 v206, s[44:47], s68 offen offset:1024 lds\n"
"v_mfma_f32_16x16x32_bf16 v[28:31], a[132:135], a[172:175], v[28:31]\n"
"v_add_u32 v234, 0x10000, v232\n"
"v_mfma_f32_16x16x32_bf16 v[32:35], a[136:139], a[160:163], v[32:35]\n"
"v_add_u32 v235, 0x10000, v233\n"
"v_mov_b32 v230, 0\n"
"v_mfma_f32_16x16x32_bf16 v[36:39], a[140:143], a[160:163], v[36:39]\n"
"s_waitcnt vmcnt(10)\n"
"v_mfma_f32_16x16x32_bf16 v[40:43], a[136:139], a[164:167], v[40:43]\n"
"buffer_load_dwordx4 v207, s[44:47], s68 offen offset:2048 lds\n"
"ds_read_b128 a[208:211], v194 offset:22528\n"
"ds_read_b64_tr_b16 a[176:177], v201 offset:0\n"
"v_mfma_f32_16x16x32_bf16 v[44:47], a[140:143], a[164:167], v[44:47]\n"
"ds_read_b64_tr_b16 a[178:179], v201 offset:1024\n"
"v_mov_b32 v231, 0\n"
"v_mfma_f32_16x16x32_bf16 v[48:51], a[136:139], a[168:171], v[48:51]\n"
"buffer_load_dwordx4 v208, s[44:47], s68 offen offset:3072 lds\n"
"ds_read_b64_tr_b16 a[180:181], v201 offset:8\n"
"ds_read_b64_tr_b16 a[182:183], v201 offset:1032\n"
"v_mfma_f32_16x16x32_bf16 v[52:55], a[140:143], a[168:171], v[52:55]\n"
"v_lshlrev_b32 v238, 6, v192\n"
"s_add_u32 s68, s68, 0x20000\n"
"s_and_b32 s68, s68, 0x3fffff\n"
"v_mfma_f32_16x16x32_bf16 v[56:59], a[136:139], a[172:175], v[56:59]\n"
"ds_read_b128 a[212:215], v194 offset:22784\n"
"ds_read_b64_tr_b16 a[184:185], v202 offset:0\n"
"v_mfma_f32_16x16x32_bf16 v[60:63], a[140:143], a[172:175], v[60:63]\n"
"v_min_u32 v239, 3456, v238\n"
"v_mfma_f32_16x16x32_bf16 v[64:67], a[144:147], a[160:163], v[64:67]\n"
"ds_read_b64_tr_b16 a[186:187], v202 offset:1024\n"
"ds_read_b64_tr_b16 a[188:189], v202 offset:8\n"
"v_mfma_f32_16x16x32_bf16 v[68:71], a[148:151], a[160:163], v[68:71]\n"
"v_min_u32 v238, 4032, v238\n"
"v_mfma_f32_16x16x32_bf16 v[72:75], a[144:147], a[164:167], v[72:75]\n"
"ds_read_b64_tr_b16 a[190:191], v202 offset:1032\n"
"ds_read_b128 a[216:219], v194 offset:55296\n"
"v_mfma_f32_16x16x32_bf16 v[76:79], a[148:151], a[164:167], v[76:79]\n"
"ds_read_b64_tr_b16 a[192:193], v203 offset:0\n"
"s_add_u32 m0, s67, 49152\n"
"v_mfma_f32_16x16x32_bf16 v[80:83], a[144:147], a[168:171], v[80:83]\n"
"buffer_load_dwordx4 v205, s[44:47], s68 offen offset:0 lds\n"
"ds_read_b64_tr_b16 a[194:195], v203 offset:1024\n"
"v_mfma_f32_16x16x32_bf16 v[84:87], a[148:151], a[168:171], v[84:87]\n"
"ds_read_b64_tr_b16 a[196:197], v203 offset:8\n"
"v_lshrrev_b32 v248, 2, v192\n"
"v_mfma_f32_16x16x32_bf16 v[88:91], a[144:147], a[172:175], v[88:91]\n"
"buffer_load_dwordx4 v206, s[44:47], s68 offen offset:1024 lds\n"
"ds_read_b64_tr_b16 a[198:199], v203 offset:1032\n"
"v_mfma_f32_16x16x32_bf16 v[92:95], a[148:151], a[172:175], v[92:95]\n"
"ds_read_b128 a[220:223], v194 offset:55552\n"
"v_lshrrev_b32 v249, 4, v192\n"
"v_mfma_f32_16x16x32_bf16 v[96:99], a[152:155], a[160:163], v[96:99]\n"
"ds_read_b64_tr_b16 a[200:201], v204 offset:0\n"
"ds_read_b64_tr_b16 a[202:203], v204 offset:1024\n"
"v_mfma_f32_16x16x32_bf16 v[100:103], a[156:159], a[160:163], v[100:103]\n"
"v_xor_b32 v248, v248, v249\n"
"v_mfma_f32_16x16x32_bf16 v[104:107], a[152:155], a[164:167], v[104:107]\n"
"buffer_load_dwordx4 v207, s[44:47], s68 offen offset:2048 lds\n"
"ds_read_b64_tr_b16 a[204:205], v204 offset:8\n"
"v_mfma_f32_16x16x32_bf16 v[108:111], a[156:159], a[164:167], v[108:111]\n"
"ds_read_b64_tr_b16 a[206:207], v204 offset:1032\n"
"v_mfma_f32_16x16x32_bf16 v[112:115], a[152:155], a[168:171], v[112:115]\n"
"buffer_load_dwordx4 v208, s[44:47], s68 offen offset:3072 lds\n"
"v_and_b32 v248, 1, v248\n"
"v_mfma_f32_16x16x32_bf16 v[116:119], a[156:159], a[168:171], v[116:119]\n"
"s_add_u32 s68, s68, 0x20000\n"
"s_and_b32 s68, s68, 0x3fffff\n"
"v_mfma_f32_16x16x32_bf16 v[120:123], a[152:155], a[172:175], v[120:123]\n"
"v_subrev_u32 v248, 1, v248\n"
"s_waitcnt lgkmcnt(0)\n"
"v_mfma_f32_16x16x32_bf16 v[124:127], a[156:159], a[172:175], v[124:127]\n"
"v_mfma_f32_16x16x32_bf16 v[0:3], a[176:179], a[208:211], v[0:3]\n"
"v_mfma_f32_16x16x32_bf16 v[4:7], a[180:183], a[208:211], v[4:7]\n"
"v_mfma_f32_16x16x32_bf16 v[8:11], a[176:179], a[212:215], v[8:11]\n"
"v_and_b32 v248, s72, v248\n"
"v_mfma_f32_16x16x32_bf16 v[12:15], a[180:183], a[212:215], v[12:15]\n"
"s_add_u32 m0, s67, 65536\n"
"v_mfma_f32_16x16x32_bf16 v[16:19], a[176:179], a[216:219], v[16:19]\n"
"buffer_load_dwordx4 v205, s[44:47], s68 offen offset:0 lds\n"
"v_mfma_f32_16x16x32_bf16 v[20:23], a[180:183], a[216:219], v[20:23]\n"
"v_mov_b32 v249, v248\n"
"v_mfma_f32_16x16x32_bf16 v[24:27], a[176:179], a[220:223], v[24:27]\n"
"buffer_load_dwordx4 v206, s[44:47], s68 offen offset:1024 lds\n"
"v_mfma_f32_16x16x32_bf16 v[28:31], a[180:183], a[220:223], v[28:31]\n"
"v_mfma_f32_16x16x32_bf16 v[32:35], a[184:187], a[208:211], v[32:35]\n"
"v_mov_b32 v250, v248\n"
"v_mfma_f32_16x16x32_bf16 v[36:39], a[188:191], a[208:211], v[36:39]\n"
"s_waitcnt vmcnt(10)\n"
"v_mfma_f32_16x16x32_bf16 v[40:43], a[184:187], a[212:215], v[40:43]\n"
"buffer_load_dwordx4 v207, s[44:47], s68 offen offset:2048 lds\n"
"ds_read_b128 a[160:163], v193 offset:24576\n"
"ds_read_b64_tr_b16 a[128:129], v197 offset:0\n"
"v_mfma_f32_16x16x32_bf16 v[44:47], a[188:191], a[212:215], v[44:47]\n"
"ds_read_b64_tr_b16 a[130:131], v197 offset:1024\n"
"v_mov_b32 v251, v248\n"
"v_mfma_f32_16x16x32_bf16 v[48:51], a[184:187], a[216:219], v[48:51]\n"
"buffer_load_dwordx4 v208, s[44:47], s68 offen offset:3072 lds\n"
"ds_read_b64_tr_b16 a[132:133], v197 offset:8\n"
"ds_read_b64_tr_b16 a[134:135], v197 offset:1032\n"
"v_mfma_f32_16x16x32_bf16 v[52:55], a[188:191], a[216:219], v[52:55]\n"
"s_add_u32 s68, s68, 0x20000\n"
"s_and_b32 s68, s68, 0x3fffff\n"
"v_mfma_f32_16x16x32_bf16 v[56:59], a[184:187], a[220:223], v[56:59]\n"
"ds_read_b128 a[164:167], v193 offset:24832\n"
"v_mfma_f32_16x16x32_bf16 v[60:63], a[188:191], a[220:223], v[60:63]\n"
"ds_read_b64_tr_b16 a[136:137], v198 offset:0\n"
"v_mfma_f32_16x16x32_bf16 v[64:67], a[192:195], a[208:211], v[64:67]\n"
"ds_read_b64_tr_b16 a[138:139], v198 offset:1024\n"
"v_mfma_f32_16x16x32_bf16 v[68:71], a[196:199], a[208:211], v[68:71]\n"
"ds_read_b64_tr_b16 a[140:141], v198 offset:8\n"
"v_mfma_f32_16x16x32_bf16 v[72:75], a[192:195], a[212:215], v[72:75]\n"
"ds_read_b64_tr_b16 a[142:143], v198 offset:1032\n"
"ds_read_b128 a[168:171], v193 offset:57344\n"
"v_mfma_f32_16x16x32_bf16 v[76:79], a[196:199], a[212:215], v[76:79]\n"
"ds_read_b64_tr_b16 a[144:145], v199 offset:0\n"
"s_add_u32 m0, s67, 81920\n"
"v_mfma_f32_16x16x32_bf16 v[80:83], a[192:195], a[216:219], v[80:83]\n"
"buffer_load_dwordx4 v205, s[44:47], s68 offen offset:0 lds\n"
"ds_read_b64_tr_b16 a[146:147], v199 offset:1024\n"
"v_mfma_f32_16x16x32_bf16 v[84:87], a[196:199], a[216:219], v[84:87]\n"
"ds_read_b64_tr_b16 a[148:149], v199 offset:8\n"
"v_mfma_f32_16x16x32_bf16 v[88:91], a[192:195], a[220:223], v[88:91]\n"
"buffer_load_dwordx4 v206, s[44:47], s68 offen offset:1024 lds\n"
"ds_read_b64_tr_b16 a[150:151], v199 offset:1032\n"
"v_mfma_f32_16x16x32_bf16 v[92:95], a[196:199], a[220:223], v[92:95]\n"
"ds_read_b128 a[172:175], v193 offset:57600\n"
"v_mfma_f32_16x16x32_bf16 v[96:99], a[200:203], a[208:211], v[96:99]\n"
"ds_read_b64_tr_b16 a[152:153], v200 offset:0\n"
"v_mfma_f32_16x16x32_bf16 v[100:103], a[204:207], a[208:211], v[100:103]\n"
"ds_read_b64_tr_b16 a[154:155], v200 offset:1024\n"
"v_mfma_f32_16x16x32_bf16 v[104:107], a[200:203], a[212:215], v[104:107]\n"
"buffer_load_dwordx4 v207, s[44:47], s68 offen offset:2048 lds\n"
"ds_read_b64_tr_b16 a[156:157], v200 offset:8\n"
"v_mfma_f32_16x16x32_bf16 v[108:111], a[204:207], a[212:215], v[108:111]\n"
"ds_read_b64_tr_b16 a[158:159], v200 offset:1032\n"
"v_mfma_f32_16x16x32_bf16 v[112:115], a[200:203], a[216:219], v[112:115]\n"
"buffer_load_dwordx4 v208, s[44:47], s68 offen offset:3072 lds\n"
"s_add_u32 s68, s68, 0x20000\n"
"v_mfma_f32_16x16x32_bf16 v[116:119], a[204:207], a[216:219], v[116:119]\n"
"s_and_b32 s68, s68, 0x3fffff\n"
"v_mfma_f32_16x16x32_bf16 v[120:123], a[200:203], a[220:223], v[120:123]\n"
"s_waitcnt lgkmcnt(0)\n"
"v_mfma_f32_16x16x32_bf16 v[124:127], a[204:207], a[220:223], v[124:127]\n"
"v_mfma_f32_16x16x32_bf16 v[0:3], a[128:131], a[160:163], v[0:3]\n"
"v_mfma_f32_16x16x32_bf16 v[4:7], a[132:135], a[160:163], v[4:7]\n"
"v_mfma_f32_16x16x32_bf16 v[8:11], a[128:131], a[164:167], v[8:11]\n"
"s_add_u32 m0, s67, 0\n"
"v_mfma_f32_16x16x32_bf16 v[12:15], a[132:135], a[164:167], v[12:15]\n"
"v_mfma_f32_16x16x32_bf16 v[16:19], a[128:131], a[168:171], v[16:19]\n"
"buffer_load_dwordx4 v205, s[44:47], s68 offen offset:0 lds\n"
"v_mfma_f32_16x16x32_bf16 v[20:23], a[132:135], a[168:171], v[20:23]\n"
"v_mfma_f32_16x16x32_bf16 v[24:27], a[128:131], a[172:175], v[24:27]\n"
"buffer_load_dwordx4 v206, s[44:47], s68 offen offset:1024 lds\n"
"v_mfma_f32_16x16x32_bf16 v[28:31], a[132:135], a[172:175], v[28:31]\n"
"v_mfma_f32_16x16x32_bf16 v[32:35], a[136:139], a[160:163], v[32:35]\n"
"s_waitcnt vmcnt(10)\n"
"v_mfma_f32_16x16x32_bf16 v[36:39], a[140:143], a[160:163], v[36:39]\n"
"v_mfma_f32_16x16x32_bf16 v[40:43], a[136:139], a[164:167], v[40:43]\n"
"buffer_load_dwordx4 v207, s[44:47], s68 offen offset:2048 lds\n"
"ds_read_b128 a[208:211], v194 offset:26624\n"
"v_mfma_f32_16x16x32_bf16 v[44:47], a[140:143], a[164:167], v[44:47]\n"
"ds_read_b64_tr_b16 a[176:177], v197 offset:32768\n"
"ds_read_b64_tr_b16 a[178:179], v197 offset:33792\n"
"v_mfma_f32_16x16x32_bf16 v[48:51], a[136:139], a[168:171], v[48:51]\n"
"buffer_load_dwordx4 v208, s[44:47], s68 offen offset:3072 lds\n"
"ds_read_b64_tr_b16 a[180:181], v197 offset:32776\n"
"ds_read_b64_tr_b16 a[182:183], v197 offset:33800\n"
"v_mfma_f32_16x16x32_bf16 v[52:55], a[140:143], a[168:171], v[52:55]\n"
"s_add_u32 s68, s68, 0x20000\n"
"s_and_b32 s68, s68, 0x3fffff\n"
"v_mfma_f32_16x16x32_bf16 v[56:59], a[136:139], a[172:175], v[56:59]\n"
"ds_read_b128 a[212:215], v194 offset:26880\n"
"v_mfma_f32_16x16x32_bf16 v[60:63], a[140:143], a[172:175], v[60:63]\n"
"ds_read_b64_tr_b16 a[184:185], v198 offset:32768\n"
"v_mfma_f32_16x16x32_bf16 v[64:67], a[144:147], a[160:163], v[64:67]\n"
"ds_read_b64_tr_b16 a[186:187], v198 offset:33792\n"
"v_mfma_f32_16x16x32_bf16 v[68:71], a[148:151], a[160:163], v[68:71]\n"
"ds_read_b64_tr_b16 a[188:189], v198 offset:32776\n"
"v_mfma_f32_16x16x32_bf16 v[72:75], a[144:147], a[164:167], v[72:75]\n"
"ds_read_b64_tr_b16 a[190:191], v198 offset:33800\n"
"ds_read_b128 a[216:219], v194 offset:59392\n"
"v_mfma_f32_16x16x32_bf16 v[76:79], a[148:151], a[164:167], v[76:79]\n"
"ds_read_b64_tr_b16 a[192:193], v199 offset:32768\n"
"s_add_u32 m0, s67, 16384\n"
"v_mfma_f32_16x16x32_bf16 v[80:83], a[144:147], a[168:171], v[80:83]\n"
"buffer_load_dwordx4 v205, s[44:47], s68 offen offset:0 lds\n"
"ds_read_b64_tr_b16 a[194:195], v199 offset:33792\n"
"v_mfma_f32_16x16x32_bf16 v[84:87], a[148:151], a[168:171], v[84:87]\n"
"ds_read_b64_tr_b16 a[196:197], v199 offset:32776\n"
"v_mfma_f32_16x16x32_bf16 v[88:91], a[144:147], a[172:175], v[88:91]\n"
"buffer_load_dwordx4 v206, s[44:47], s68 offen offset:1024 lds\n"
"ds_read_b64_tr_b16 a[198:199], v199 offset:33800\n"
"v_mfma_f32_16x16x32_bf16 v[92:95], a[148:151], a[172:175], v[92:95]\n"
"ds_read_b128 a[220:223], v194 offset:59648\n"
"v_mfma_f32_16x16x32_bf16 v[96:99], a[152:155], a[160:163], v[96:99]\n"
"ds_read_b64_tr_b16 a[200:201], v200 offset:32768\n"
"v_mfma_f32_16x16x32_bf16 v[100:103], a[156:159], a[160:163], v[100:103]\n"
"ds_read_b64_tr_b16 a[202:203], v200 offset:33792\n"
"v_mfma_f32_16x16x32_bf16 v[104:107], a[152:155], a[164:167], v[104:107]\n"
"buffer_load_dwordx4 v207, s[44:47], s68 offen offset:2048 lds\n"
"ds_read_b64_tr_b16 a[204:205], v200 offset:32776\n"
"v_mfma_f32_16x16x32_bf16 v[108:111], a[156:159], a[164:167], v[108:111]\n"
"ds_read_b64_tr_b16 a[206:207], v200 offset:33800\n"
"v_mfma_f32_16x16x32_bf16 v[112:115], a[152:155], a[168:171], v[112:115]\n"
"buffer_load_dwordx4 v208, s[44:47], s68 offen offset:3072 lds\n"
"s_add_u32 s68, s68, 0x20000\n"
"v_mfma_f32_16x16x32_bf16 v[116:119], a[156:159], a[168:171], v[116:119]\n"
"s_and_b32 s68, s68, 0x3fffff\n"
"v_mfma_f32_16x16x32_bf16 v[120:123], a[152:155], a[172:175], v[120:123]\n"
"s_waitcnt lgkmcnt(0)\n"
"v_mfma_f32_16x16x32_bf16 v[124:127], a[156:159], a[172:175], v[124:127]\n"
"v_mfma_f32_16x16x32_bf16 v[0:3], a[176:179], a[208:211], v[0:3]\n"
"v_mfma_f32_16x16x32_bf16 v[4:7], a[180:183], a[208:211], v[4:7]\n"
"v_mfma_f32_16x16x32_bf16 v[8:11], a[176:179], a[212:215], v[8:11]\n"
"s_lshl_b32 s30, s64, 8\n"
"v_lshrrev_b32 v240, 2, v192\n"
"v_mfma_f32_16x16x32_bf16 v[12:15], a[180:183], a[212:215], v[12:15]\n"
"s_add_u32 m0, s67, 32768\n"
"v_mfma_f32_16x16x32_bf16 v[16:19], a[176:179], a[216:219], v[16:19]\n"
"buffer_load_dwordx4 v209, s[48:51], s75 offen offset:0 lds\n"
"v_mfma_f32_16x16x32_bf16 v[20:23], a[180:183], a[216:219], v[20:23]\n"
"v_lshrrev_b32 v241, 4, v192\n"
"v_mfma_f32_16x16x32_bf16 v[24:27], a[176:179], a[220:223], v[24:27]\n"
"buffer_load_dwordx4 v210, s[48:51], s75 offen offset:1024 lds\n"
"v_mfma_f32_16x16x32_bf16 v[28:31], a[180:183], a[220:223], v[28:31]\n"
"v_xor_b32 v240, v240, v241\n"
"v_mfma_f32_16x16x32_bf16 v[32:35], a[184:187], a[208:211], v[32:35]\n"
"v_and_b32 v240, 1, v240\n"
"v_lshlrev_b32 v240, 6, v240\n"
"v_mfma_f32_16x16x32_bf16 v[36:39], a[188:191], a[208:211], v[36:39]\n"
"s_waitcnt vmcnt(10)\n"
"v_mfma_f32_16x16x32_bf16 v[40:43], a[184:187], a[212:215], v[40:43]\n"
"buffer_load_dwordx4 v211, s[48:51], s75 offen offset:2048 lds\n"
"ds_read_b128 a[160:163], v193 offset:28672\n"
"ds_read_b64_tr_b16 a[128:129], v201 offset:0\n"
"v_mfma_f32_16x16x32_bf16 v[44:47], a[188:191], a[212:215], v[44:47]\n"
"ds_read_b64_tr_b16 a[130:131], v201 offset:1024\n"
"v_and_b32 v241, 3, v192\n"
"v_mfma_f32_16x16x32_bf16 v[48:51], a[184:187], a[216:219], v[48:51]\n"
"buffer_load_dwordx4 v212, s[48:51], s75 offen offset:3072 lds\n"
"ds_read_b64_tr_b16 a[132:133], v201 offset:8\n"
"ds_read_b64_tr_b16 a[134:135], v201 offset:1032\n"
"v_mfma_f32_16x16x32_bf16 v[52:55], a[188:191], a[216:219], v[52:55]\n"
"v_lshl_add_u32 v240, v241, 4, v240\n"
"s_add_u32 s75, s75, 0x4000\n"
"s_and_b32 s75, s75, 0x1ffff\n"
"v_mfma_f32_16x16x32_bf16 v[56:59], a[184:187], a[220:223], v[56:59]\n"
"ds_read_b128 a[164:167], v193 offset:28928\n"
"ds_read_b64_tr_b16 a[136:137], v202 offset:0\n"
"v_mfma_f32_16x16x32_bf16 v[60:63], a[188:191], a[220:223], v[60:63]\n"
"v_lshrrev_b32 v241, 3, v192\n"
"v_mul_u32_u24 v241, 1024, v241\n"
"v_mfma_f32_16x16x32_bf16 v[64:67], a[192:195], a[208:211], v[64:67]\n"
"ds_read_b64_tr_b16 a[138:139], v202 offset:1024\n"
"ds_read_b64_tr_b16 a[140:141], v202 offset:8\n"
"v_mfma_f32_16x16x32_bf16 v[68:71], a[196:199], a[208:211], v[68:71]\n"
"v_add3_u32 v205, v240, v241, s30\n"
"v_mfma_f32_16x16x32_bf16 v[72:75], a[192:195], a[212:215], v[72:75]\n"
"ds_read_b64_tr_b16 a[142:143], v202 offset:1032\n"
"ds_read_b128 a[168:171], v193 offset:61440\n"
"ds_read_b64_tr_b16 a[144:145], v203 offset:0\n"
"v_mfma_f32_16x16x32_bf16 v[76:79], a[196:199], a[212:215], v[76:79]\n"
"v_add_u32 v206, 7168, v205\n"
"s_add_u32 m0, s67, 49152\n"
"v_mfma_f32_16x16x32_bf16 v[80:83], a[192:195], a[216:219], v[80:83]\n"
"buffer_load_dwordx4 v213, s[52:55], s76 offen offset:0 lds\n"
"ds_read_b64_tr_b16 a[146:147], v203 offset:1024\n"
"ds_read_b64_tr_b16 a[148:149], v203 offset:8\n"
"v_mfma_f32_16x16x32_bf16 v[84:87], a[196:199], a[216:219], v[84:87]\n"
"v_add_u32 v207, 14336, v205\n"
"v_add_u32 v208, 21504, v205\n"
"v_mfma_f32_16x16x32_bf16 v[88:91], a[192:195], a[220:223], v[88:91]\n"
"buffer_load_dwordx4 v213, s[52:55], s76 offen offset:1024 lds\n"
"ds_read_b64_tr_b16 a[150:151], v203 offset:1032\n"
"v_mfma_f32_16x16x32_bf16 v[92:95], a[196:199], a[220:223], v[92:95]\n"
"ds_read_b128 a[172:175], v193 offset:61696\n"
"s_lshl_b32 s68, s64, 17\n"
"v_mfma_f32_16x16x32_bf16 v[96:99], a[200:203], a[208:211], v[96:99]\n"
"ds_read_b64_tr_b16 a[152:153], v204 offset:0\n"
"ds_read_b64_tr_b16 a[154:155], v204 offset:1024\n"
"v_mfma_f32_16x16x32_bf16 v[100:103], a[204:207], a[208:211], v[100:103]\n"
"s_lshl_b32 s69, s65, 19\n"
"v_mfma_f32_16x16x32_bf16 v[104:107], a[200:203], a[212:215], v[104:107]\n"
"buffer_load_dwordx4 v213, s[52:55], s76 offen offset:2048 lds\n"
"ds_read_b64_tr_b16 a[156:157], v204 offset:8\n"
"v_mfma_f32_16x16x32_bf16 v[108:111], a[204:207], a[212:215], v[108:111]\n"
"ds_read_b64_tr_b16 a[158:159], v204 offset:1032\n"
"s_add_u32 s35, s69, 128\n"
"v_mfma_f32_16x16x32_bf16 v[112:115], a[200:203], a[216:219], v[112:115]\n"
"buffer_load_dwordx4 v213, s[52:55], s76 offen offset:3072 lds\n"
"s_add_u32 s76, s76, 0x4000\n"
"v_mfma_f32_16x16x32_bf16 v[116:119], a[204:207], a[216:219], v[116:119]\n"
"s_and_b32 s76, s76, 0x1ffff\n"
"v_mfma_f32_16x16x32_bf16 v[120:123], a[200:203], a[220:223], v[120:123]\n"
"s_waitcnt lgkmcnt(0)\n"
"v_mfma_f32_16x16x32_bf16 v[124:127], a[204:207], a[220:223], v[124:127]\n"
"v_mfma_f32_16x16x32_bf16 v[0:3], a[128:131], a[160:163], v[0:3]\n"
"s_add_u32 s74, s68, 0\n"
"v_mfma_f32_16x16x32_bf16 v[4:7], a[132:135], a[160:163], v[4:7]\n"
"v_mfma_f32_16x16x32_bf16 v[8:11], a[128:131], a[164:167], v[8:11]\n"
"s_and_b32 s74, s74, 0x7ffff\n"
"s_add_u32 s74, s74, s69\n"
"v_mfma_f32_16x16x32_bf16 v[12:15], a[132:135], a[164:167], v[12:15]\n"
"s_add_u32 m0, s67, 65536\n"
"v_mfma_f32_16x16x32_bf16 v[16:19], a[128:131], a[168:171], v[16:19]\n"
"buffer_load_dwordx4 v205, s[56:59], s74 offen offset:0 lds\n"
"v_mfma_f32_16x16x32_bf16 v[20:23], a[132:135], a[168:171], v[20:23]\n"
"v_mfma_f32_16x16x32_bf16 v[24:27], a[128:131], a[172:175], v[24:27]\n"
"buffer_load_dwordx4 v206, s[56:59], s74 offen offset:1024 lds\n"
"v_mfma_f32_16x16x32_bf16 v[28:31], a[132:135], a[172:175], v[28:31]\n"
"v_mfma_f32_16x16x32_bf16 v[32:35], a[136:139], a[160:163], v[32:35]\n"
"s_waitcnt vmcnt(10)\n"
"v_mfma_f32_16x16x32_bf16 v[36:39], a[140:143], a[160:163], v[36:39]\n"
"v_mfma_f32_16x16x32_bf16 v[40:43], a[136:139], a[164:167], v[40:43]\n"
"buffer_load_dwordx4 v207, s[56:59], s74 offen offset:2048 lds\n"
"ds_read_b128 a[208:211], v194 offset:30720\n"
"v_mfma_f32_16x16x32_bf16 v[44:47], a[140:143], a[164:167], v[44:47]\n"
"ds_read_b64_tr_b16 a[176:177], v197 offset:0\n"
"ds_read_b64_tr_b16 a[178:179], v197 offset:1024\n"
"v_mfma_f32_16x16x32_bf16 v[48:51], a[136:139], a[168:171], v[48:51]\n"
"buffer_load_dwordx4 v208, s[56:59], s74 offen offset:3072 lds\n"
"ds_read_b64_tr_b16 a[180:181], v197 offset:8\n"
"v_mfma_f32_16x16x32_bf16 v[52:55], a[140:143], a[168:171], v[52:55]\n"
"ds_read_b64_tr_b16 a[182:183], v197 offset:1032\n"
"v_mfma_f32_16x16x32_bf16 v[56:59], a[136:139], a[172:175], v[56:59]\n"
"ds_read_b128 a[212:215], v194 offset:30976\n"
"v_mfma_f32_16x16x32_bf16 v[60:63], a[140:143], a[172:175], v[60:63]\n"
"ds_read_b64_tr_b16 a[184:185], v198 offset:0\n"
"v_mfma_f32_16x16x32_bf16 v[64:67], a[144:147], a[160:163], v[64:67]\n"
"ds_read_b64_tr_b16 a[186:187], v198 offset:1024\n"
"ds_read_b64_tr_b16 a[188:189], v198 offset:8\n"
"v_mfma_f32_16x16x32_bf16 v[68:71], a[148:151], a[160:163], v[68:71]\n"
"v_lshlrev_b32 v254, 6, v192\n"
"s_add_u32 s74, s68, 32768\n"
"v_mfma_f32_16x16x32_bf16 v[72:75], a[144:147], a[164:167], v[72:75]\n"
"ds_read_b64_tr_b16 a[190:191], v198 offset:1032\n"
"ds_read_b128 a[216:219], v194 offset:63488\n"
"ds_read_b64_tr_b16 a[192:193], v199 offset:0\n"
"s_lshl_b32 s30, s77, 2\n"
"v_mfma_f32_16x16x32_bf16 v[76:79], a[148:151], a[164:167], v[76:79]\n"
"s_and_b32 s74, s74, 0x7ffff\n"
"s_add_u32 s74, s74, s69\n"
"s_add_u32 m0, s67, 81920\n"
"v_mfma_f32_16x16x32_bf16 v[80:83], a[144:147], a[168:171], v[80:83]\n"
"buffer_load_dwordx4 v205, s[56:59], s74 offen offset:0 lds\n"
"ds_read_b64_tr_b16 a[194:195], v199 offset:1024\n"
"v_mfma_f32_16x16x32_bf16 v[84:87], a[148:151], a[168:171], v[84:87]\n"
"ds_read_b64_tr_b16 a[196:197], v199 offset:8\n"
"s_add_u32 s30, s30, s64\n"
"v_mfma_f32_16x16x32_bf16 v[88:91], a[144:147], a[172:175], v[88:91]\n"
"buffer_load_dwordx4 v206, s[56:59], s74 offen offset:1024 lds\n"
"ds_read_b64_tr_b16 a[198:199], v199 offset:1032\n"
"v_mfma_f32_16x16x32_bf16 v[92:95], a[148:151], a[172:175], v[92:95]\n"
"ds_read_b128 a[220:223], v194 offset:63744\n"
"s_mul_i32 s30, s30, 2048\n"
"v_mfma_f32_16x16x32_bf16 v[96:99], a[152:155], a[160:163], v[96:99]\n"
"ds_read_b64_tr_b16 a[200:201], v200 offset:0\n"
"ds_read_b64_tr_b16 a[202:203], v200 offset:1024\n"
"v_mfma_f32_16x16x32_bf16 v[100:103], a[156:159], a[160:163], v[100:103]\n"
"v_and_b32 v254, 2047, v254\n"
"v_mfma_f32_16x16x32_bf16 v[104:107], a[152:155], a[164:167], v[104:107]\n"
"buffer_load_dwordx4 v207, s[56:59], s74 offen offset:2048 lds\n"
"ds_read_b64_tr_b16 a[204:205], v200 offset:8\n"
"v_mfma_f32_16x16x32_bf16 v[108:111], a[156:159], a[164:167], v[108:111]\n"
"ds_read_b64_tr_b16 a[206:207], v200 offset:1032\n"
"v_mfma_f32_16x16x32_bf16 v[112:115], a[152:155], a[168:171], v[112:115]\n"
"buffer_load_dwordx4 v208, s[56:59], s74 offen offset:3072 lds\n"
"v_mfma_f32_16x16x32_bf16 v[116:119], a[156:159], a[168:171], v[116:119]\n"
"buffer_load_dword v255, v254, s[48:51], s30 offen\n"
"v_mfma_f32_16x16x32_bf16 v[120:123], a[152:155], a[172:175], v[120:123]\n"
"buffer_load_dword v255, v254, s[52:55], s30 offen\n"
"s_waitcnt vmcnt(10) lgkmcnt(0)\n"
"s_barrier\n"
"v_mfma_f32_16x16x32_bf16 v[124:127], a[156:159], a[172:175], v[124:127]\n"
"v_mfma_f32_16x16x32_bf16 v[0:3], a[176:179], a[208:211], v[0:3]\n"
"ds_read_b128 a[64:67], v216 offset:32768\n"
"ds_read_b128 a[68:71], v217 offset:32768\n"
"v_mfma_f32_16x16x32_bf16 v[4:7], a[180:183], a[208:211], v[4:7]\n"
"s_add_u32 m0, s67, 0\n"
"v_mfma_f32_16x16x32_bf16 v[8:11], a[176:179], a[212:215], v[8:11]\n"
"buffer_load_dwordx4 v209, s[48:51], s75 offen offset:0 lds\n"
"ds_read_b128 a[72:75], v218 offset:32768\n"
"v_mfma_f32_16x16x32_bf16 v[12:15], a[180:183], a[212:215], v[12:15]\n"
"ds_read_b128 a[76:79], v219 offset:32768\n"
"v_mfma_f32_16x16x32_bf16 v[32:35], a[184:187], a[208:211], v[32:35]\n"
"buffer_load_dwordx4 v210, s[48:51], s75 offen offset:1024 lds\n"
"ds_read_b128 a[80:83], v220 offset:32768\n"
"v_mfma_f32_16x16x32_bf16 v[36:39], a[188:191], a[208:211], v[36:39]\n"
"ds_read_b128 a[84:87], v221 offset:32768\n"
"v_mfma_f32_16x16x32_bf16 v[40:43], a[184:187], a[212:215], v[40:43]\n"
"buffer_load_dwordx4 v211, s[48:51], s75 offen offset:2048 lds\n"
"ds_read_b128 a[88:91], v222 offset:32768\n"
"ds_read_b128 a[92:95], v223 offset:32768\n"
"v_cvt_pk_bf16_f32 v0, v0, v1\n"
"v_mfma_f32_16x16x32_bf16 v[44:47], a[188:191], a[212:215], v[44:47]\n"
"v_cvt_pk_bf16_f32 v1, v2, v3\n"
"v_cvt_pk_bf16_f32 v2, v4, v5\n"
"v_cvt_pk_bf16_f32 v3, v6, v7\n"
"v_cvt_pk_bf16_f32 v8, v8, v9\n"
"v_mfma_f32_16x16x32_bf16 v[64:67], a[192:195], a[208:211], v[64:67]\n"
"buffer_load_dwordx4 v212, s[48:51], s75 offen offset:3072 lds\n"
"v_cvt_pk_bf16_f32 v9, v10, v11\n"
"v_cvt_pk_bf16_f32 v10, v12, v13\n"
"v_cvt_pk_bf16_f32 v11, v14, v15\n"
"v_mfma_f32_16x16x32_bf16 v[68:71], a[196:199], a[208:211], v[68:71]\n"
"v_permlane16_swap_b32 v0, v8\n"
"v_permlane16_swap_b32 v1, v9\n"
"s_add_u32 s75, s75, 0x4000\n"
"s_and_b32 s75, s75, 0x1ffff\n"
"v_mfma_f32_16x16x32_bf16 v[72:75], a[192:195], a[212:215], v[72:75]\n"
"v_permlane16_swap_b32 v2, v10\n"
"v_permlane16_swap_b32 v3, v11\n"
"v_accvgpr_write_b32 a0, v0\n"
"v_mfma_f32_16x16x32_bf16 v[76:79], a[196:199], a[212:215], v[76:79]\n"
"v_accvgpr_write_b32 a1, v1\n"
"v_accvgpr_write_b32 a2, v2\n"
"v_mfma_f32_16x16x32_bf16 v[96:99], a[200:203], a[208:211], v[96:99]\n"
"v_accvgpr_write_b32 a3, v3\n"
"v_accvgpr_write_b32 a4, v8\n"
"v_accvgpr_write_b32 a5, v9\n"
"v_mfma_f32_16x16x32_bf16 v[100:103], a[204:207], a[208:211], v[100:103]\n"
"v_accvgpr_write_b32 a6, v10\n"
"v_accvgpr_write_b32 a7, v11\n"
"v_mfma_f32_16x16x32_bf16 v[104:107], a[200:203], a[212:215], v[104:107]\n"
"v_cvt_pk_bf16_f32 v32, v32, v33\n"
"v_cvt_pk_bf16_f32 v33, v34, v35\n"
"v_cvt_pk_bf16_f32 v34, v36, v37\n"
"v_mfma_f32_16x16x32_bf16 v[108:111], a[204:207], a[212:215], v[108:111]\n"
"v_cvt_pk_bf16_f32 v35, v38, v39\n"
"v_cvt_pk_bf16_f32 v40, v40, v41\n"
"v_mfma_f32_16x16x32_bf16 v[16:19], a[176:179], a[216:219], v[16:19]\n"
"v_cvt_pk_bf16_f32 v41, v42, v43\n"
"v_cvt_pk_bf16_f32 v42, v44, v45\n"
"v_cvt_pk_bf16_f32 v43, v46, v47\n"
"v_mfma_f32_16x16x32_bf16 v[20:23], a[180:183], a[216:219], v[20:23]\n"
"Lst4_%=:\n"
"s_cbranch_execz Lst8_%=\n"
"v_permlane16_swap_b32 v32, v40\n"
"v_permlane16_swap_b32 v33, v41\n"
"v_mfma_f32_16x16x32_bf16 v[24:27], a[176:179], a[220:223], v[24:27]\n"
"v_permlane16_swap_b32 v34, v42\n"
"v_permlane16_swap_b32 v35, v43\n"
"v_accvgpr_write_b32 a8, v32\n"
"v_mfma_f32_16x16x32_bf16 v[28:31], a[180:183], a[220:223], v[28:31]\n"
"v_accvgpr_write_b32 a9, v33\n"
"v_accvgpr_write_b32 a10, v34\n"
"v_mfma_f32_16x16x32_bf16 v[48:51], a[184:187], a[216:219], v[48:51]\n"
"v_accvgpr_write_b32 a11, v35\n"
"v_accvgpr_write_b32 a12, v40\n"
"v_accvgpr_write_b32 a13, v41\n"
"v_mfma_f32_16x16x32_bf16 v[52:55], a[188:191], a[216:219], v[52:55]\n"
"v_accvgpr_write_b32 a14, v42\n"
"v_accvgpr_write_b32 a15, v43\n"
"v_mfma_f32_16x16x32_bf16 v[56:59], a[184:187], a[220:223], v[56:59]\n"
"v_cvt_pk_bf16_f32 v64, v64, v65\n"
"v_cvt_pk_bf16_f32 v65, v66, v67\n"
"v_cvt_pk_bf16_f32 v66, v68, v69\n"
"v_mfma_f32_16x16x32_bf16 v[60:63], a[188:191], a[220:223], v[60:63]\n"
"v_cvt_pk_bf16_f32 v67, v70, v71\n"
"v_cvt_pk_bf16_f32 v72, v72, v73\n"
"v_mfma_f32_16x16x32_bf16 v[80:83], a[192:195], a[216:219], v[80:83]\n"
"v_cvt_pk_bf16_f32 v73, v74, v75\n"
"v_cvt_pk_bf16_f32 v74, v76, v77\n"
"v_cvt_pk_bf16_f32 v75, v78, v79\n"
"v_mfma_f32_16x16x32_bf16 v[84:87], a[196:199], a[216:219], v[84:87]\n"
"v_permlane16_swap_b32 v64, v72\n"
"v_permlane16_swap_b32 v65, v73\n"
"v_mfma_f32_16x16x32_bf16 v[88:91], a[192:195], a[220:223], v[88:91]\n"
"v_permlane16_swap_b32 v66, v74\n"
"v_permlane16_swap_b32 v67, v75\n"
"v_accvgpr_write_b32 a16, v64\n"
"v_mfma_f32_16x16x32_bf16 v[92:95], a[196:199], a[220:223], v[92:95]\n"
"v_accvgpr_write_b32 a17, v65\n"
"v_accvgpr_write_b32 a18, v66\n"
"v_mfma_f32_16x16x32_bf16 v[112:115], a[200:203], a[216:219], v[112:115]\n"
"v_accvgpr_write_b32 a19, v67\n"
"v_accvgpr_write_b32 a20, v72\n"
"v_accvgpr_write_b32 a21, v73\n"
"v_mfma_f32_16x16x32_bf16 v[116:119], a[204:207], a[216:219], v[116:119]\n"
"v_accvgpr_write_b32 a22, v74\n"
"v_accvgpr_write_b32 a23, v75\n"
"v_mfma_f32_16x16x32_bf16 v[120:123], a[200:203], a[220:223], v[120:123]\n"
"v_cvt_pk_bf16_f32 v96, v96, v97\n"
"v_cvt_pk_bf16_f32 v97, v98, v99\n"
"v_cvt_pk_bf16_f32 v98, v100, v101\n"
"v_mfma_f32_16x16x32_bf16 v[124:127], a[204:207], a[220:223], v[124:127]\n"
"v_cvt_pk_bf16_f32 v99, v102, v103\n"
"v_cvt_pk_bf16_f32 v104, v104, v105\n"
"s_waitcnt lgkmcnt(0)\n"
"v_mfma_f32_32x32x16_bf16 v[128:143], a[64:67], a[0:3], 0\n"
"v_cvt_pk_bf16_f32 v105, v106, v107\n"
"v_cvt_pk_bf16_f32 v106, v108, v109\n"
"v_cvt_pk_bf16_f32 v107, v110, v111\n"
"v_permlane16_swap_b32 v96, v104\n"
"v_permlane16_swap_b32 v97, v105\n"
"s_add_u32 m0, s67, 16384\n"
"v_mfma_f32_32x32x16_bf16 v[128:143], a[68:71], a[4:7], v[128:143]\n"
"buffer_load_dwordx4 v213, s[52:55], s76 offen offset:0 lds\n"
"v_permlane16_swap_b32 v98, v106\n"
"v_permlane16_swap_b32 v99, v107\n"
"v_accvgpr_write_b32 a24, v96\n"
"v_accvgpr_write_b32 a25, v97\n"
"v_accvgpr_write_b32 a26, v98\n"
"v_mfma_f32_32x32x16_bf16 v[128:143], a[72:75], a[8:11], v[128:143]\n"
"buffer_load_dwordx4 v213, s[52:55], s76 offen offset:1024 lds\n"
"v_accvgpr_write_b32 a27, v99\n"
"v_accvgpr_write_b32 a28, v104\n"
"v_accvgpr_write_b32 a29, v105\n"
"v_accvgpr_write_b32 a30, v106\n"
"v_accvgpr_write_b32 a31, v107\n"
"v_mfma_f32_32x32x16_bf16 v[128:143], a[76:79], a[12:15], v[128:143]\n"
"v_cvt_pk_bf16_f32 v16, v16, v17\n"
"v_cvt_pk_bf16_f32 v17, v18, v19\n"
"v_cvt_pk_bf16_f32 v18, v20, v21\n"
"v_cvt_pk_bf16_f32 v19, v22, v23\n"
"v_cvt_pk_bf16_f32 v24, v24, v25\n"
"v_cvt_pk_bf16_f32 v25, v26, v27\n"
"v_cvt_pk_bf16_f32 v26, v28, v29\n"
"v_cvt_pk_bf16_f32 v27, v30, v31\n"
"v_mfma_f32_32x32x16_bf16 v[128:143], a[80:83], a[16:19], v[128:143]\n"
"buffer_load_dwordx4 v213, s[52:55], s76 offen offset:2048 lds\n"
"v_permlane16_swap_b32 v16, v24\n"
"v_permlane16_swap_b32 v17, v25\n"
"v_permlane16_swap_b32 v18, v26\n"
"v_permlane16_swap_b32 v19, v27\n"
"v_accvgpr_write_b32 a32, v16\n"
"v_accvgpr_write_b32 a33, v17\n"
"v_accvgpr_write_b32 a34, v18\n"
"v_accvgpr_write_b32 a35, v19\n"
"v_mfma_f32_32x32x16_bf16 v[128:143], a[84:87], a[20:23], v[128:143]\n"
"v_accvgpr_write_b32 a36, v24\n"
"v_accvgpr_write_b32 a37, v25\n"
"v_accvgpr_write_b32 a38, v26\n"
"v_accvgpr_write_b32 a39, v27\n"
"v_cvt_pk_bf16_f32 v48, v48, v49\n"
"v_cvt_pk_bf16_f32 v49, v50, v51\n"
"v_cvt_pk_bf16_f32 v50, v52, v53\n"
"v_cvt_pk_bf16_f32 v51, v54, v55\n"
"v_mfma_f32_32x32x16_bf16 v[128:143], a[88:91], a[24:27], v[128:143]\n"
"buffer_load_dwordx4 v213, s[52:55], s76 offen offset:3072 lds\n"
"v_cvt_pk_bf16_f32 v56, v56, v57\n"
"v_cvt_pk_bf16_f32 v57, v58, v59\n"
"v_cvt_pk_bf16_f32 v58, v60, v61\n"
"v_cvt_pk_bf16_f32 v59, v62, v63\n"
"v_permlane16_swap_b32 v48, v56\n"
"v_permlane16_swap_b32 v49, v57\n"
"v_permlane16_swap_b32 v50, v58\n"
"v_permlane16_swap_b32 v51, v59\n"
"s_add_u32 s76, s76, 0x4000\n"
"s_and_b32 s76, s76, 0x1ffff\n"
"v_mfma_f32_32x32x16_bf16 v[128:143], a[92:95], a[28:31], v[128:143]\n"
"v_accvgpr_write_b32 a40, v48\n"
"v_accvgpr_write_b32 a41, v49\n"
"v_accvgpr_write_b32 a42, v50\n"
"v_accvgpr_write_b32 a43, v51\n"
"v_accvgpr_write_b32 a44, v56\n"
"v_accvgpr_write_b32 a45, v57\n"
"v_accvgpr_write_b32 a46, v58\n"
"v_accvgpr_write_b32 a47, v59\n"
"v_mfma_f32_32x32x16_bf16 v[144:159], a[64:67], a[32:35], 0\n"
"ds_read_b128 a[64:67], v216 offset:40960\n"
"v_cvt_pk_bf16_f32 v80, v80, v81\n"
"v_cvt_pk_bf16_f32 v81, v82, v83\n"
"v_cvt_pk_bf16_f32 v82, v84, v85\n"
"v_cvt_pk_bf16_f32 v83, v86, v87\n"
"v_cvt_pk_bf16_f32 v88, v88, v89\n"
"v_cvt_pk_bf16_f32 v89, v90, v91\n"
"v_cvt_pk_bf16_f32 v90, v92, v93\n"
"v_cvt_pk_bf16_f32 v91, v94, v95\n"
"v_mfma_f32_32x32x16_bf16 v[144:159], a[68:71], a[36:39], v[144:159]\n"
"ds_read_b128 a[68:71], v217 offset:40960\n"
"v_permlane16_swap_b32 v80, v88\n"
"v_permlane16_swap_b32 v81, v89\n"
"v_permlane16_swap_b32 v82, v90\n"
"v_permlane16_swap_b32 v83, v91\n"
"v_accvgpr_write_b32 a48, v80\n"
"v_accvgpr_write_b32 a49, v81\n"
"v_accvgpr_write_b32 a50, v82\n"
"v_accvgpr_write_b32 a51, v83\n"
"v_mfma_f32_32x32x16_bf16 v[144:159], a[72:75], a[40:43], v[144:159]\n"
"ds_read_b128 a[72:75], v218 offset:40960\n"
"v_accvgpr_write_b32 a52, v88\n"
"v_accvgpr_write_b32 a53, v89\n"
"v_accvgpr_write_b32 a54, v90\n"
"v_accvgpr_write_b32 a55, v91\n"
"v_cvt_pk_bf16_f32 v112, v112, v113\n"
"v_cvt_pk_bf16_f32 v113, v114, v115\n"
"v_cvt_pk_bf16_f32 v114, v116, v117\n"
"v_cvt_pk_bf16_f32 v115, v118, v119\n"
"v_mfma_f32_32x32x16_bf16 v[144:159], a[76:79], a[44:47], v[144:159]\n"
"ds_read_b128 a[76:79], v219 offset:40960\n"
"v_cvt_pk_bf16_f32 v120, v120, v121\n"
"v_cvt_pk_bf16_f32 v121, v122, v123\n"
"v_cvt_pk_bf16_f32 v122, v124, v125\n"
"v_cvt_pk_bf16_f32 v123, v126, v127\n"
"v_permlane16_swap_b32 v112, v120\n"
"v_permlane16_swap_b32 v113, v121\n"
"v_permlane16_swap_b32 v114, v122\n"
"v_permlane16_swap_b32 v115, v123\n"
"v_mfma_f32_32x32x16_bf16 v[144:159], a[80:83], a[48:51], v[144:159]\n"
"ds_read_b128 a[80:83], v220 offset:40960\n"
"v_max3_f32 v228, v128, v129, v130\n"
"v_max3_f32 v240, v131, v132, v133\n"
"v_max3_f32 v228, v228, v134, v135\n"
"v_max3_f32 v240, v240, v136, v137\n"
"v_max3_f32 v228, v228, v138, v139\n"
"v_accvgpr_write_b32 a56, v112\n"
"v_accvgpr_write_b32 a57, v113\n"
"v_accvgpr_write_b32 a58, v114\n"
"v_accvgpr_write_b32 a59, v115\n"
"v_accvgpr_write_b32 a60, v120\n"
"v_accvgpr_write_b32 a61, v121\n"
"v_accvgpr_write_b32 a62, v122\n"
"v_accvgpr_write_b32 a63, v123\n"
"v_mfma_f32_32x32x16_bf16 v[144:159], a[84:87], a[52:55], v[144:159]\n"
"ds_read_b128 a[84:87], v221 offset:40960\n"
"v_max3_f32 v240, v240, v140, v141\n"
"v_max3_f32 v228, v228, v142, v143\n"
"v_max_f32 v228, v228, v240\n"
"v_mov_b32 v240, v228\n"
"v_mov_b32 v242, 0xc2ba0000\n"
"v_mfma_f32_32x32x16_bf16 v[144:159], a[88:91], a[56:59], v[144:159]\n"
"ds_read_b128 a[88:91], v222 offset:40960\n"
"s_nop 0\n"
"v_permlane32_swap_b32 v228, v240\n"
"v_max_f32 v228, v228, v240\n"
"v_fma_f32 v228, -v228, s73, v242\n"
"v_mfma_f32_32x32x16_bf16 v[144:159], a[92:95], a[60:63], v[144:159]\n"
"ds_read_b128 a[92:95], v223 offset:40960\n"
"v_cvt_pk_bf16_f32 v128, 0, v128\n"
"v_fma_f32 v128, v128, s73, v228\n"
"v_cvt_pk_bf16_f32 v129, 0, v129\n"
"v_exp_f32 v128, v128\n"
"v_fma_f32 v129, v129, s73, v228\n"
"s_waitcnt lgkmcnt(7)\n"
"v_mfma_f32_32x32x16_bf16 v[160:175], a[64:67], a[0:3], 0\n"
"ds_read_b64_tr_b16 a[96:97], v224 offset:57344\n"
"ds_read_b64_tr_b16 a[98:99], v224 offset:59392\n"
"v_cvt_pk_bf16_f32 v130, 0, v130\n"
"v_exp_f32 v129, v129\n"
"v_fma_f32 v130, v130, s73, v228\n"
"v_cvt_pk_bf16_f32 v131, 0, v131\n"
"s_waitcnt lgkmcnt(8)\n"
"v_mfma_f32_32x32x16_bf16 v[160:175], a[68:71], a[4:7], v[160:175]\n"
"ds_read_b64_tr_b16 a[100:101], v225 offset:57344\n"
"v_cvt_pk_bf16_f32 v128, v128, v129\n"
"v_exp_f32 v130, v130\n"
"v_fma_f32 v131, v131, s73, v228\n"
"v_cvt_pk_bf16_f32 v132, 0, v132\n"
"s_waitcnt lgkmcnt(8)\n"
"v_mfma_f32_32x32x16_bf16 v[160:175], a[72:75], a[8:11], v[160:175]\n"
"ds_read_b64_tr_b16 a[102:103], v225 offset:59392\n"
"v_max3_f32 v229, v144, v145, v146\n"
"v_max3_f32 v241, v147, v148, v149\n"
"v_exp_f32 v131, v131\n"
"v_fma_f32 v132, v132, s73, v228\n"
"v_cvt_pk_bf16_f32 v133, 0, v133\n"
"s_waitcnt lgkmcnt(8)\n"
"v_mfma_f32_32x32x16_bf16 v[160:175], a[76:79], a[12:15], v[160:175]\n"
"ds_read_b64_tr_b16 a[104:105], v226 offset:57344\n"
"ds_read_b64_tr_b16 a[106:107], v226 offset:59392\n"
"v_max3_f32 v229, v229, v150, v151\n"
"v_max3_f32 v241, v241, v152, v153\n"
"v_cvt_pk_bf16_f32 v129, v130, v131\n"
"v_exp_f32 v132, v132\n"
"s_waitcnt lgkmcnt(9)\n"
"v_mfma_f32_32x32x16_bf16 v[160:175], a[80:83], a[16:19], v[160:175]\n"
"ds_read_b64_tr_b16 a[108:109], v227 offset:57344\n"
"v_max3_f32 v229, v229, v154, v155\n"
"v_max3_f32 v241, v241, v156, v157\n"
"v_fma_f32 v133, v133, s73, v228\n"
"v_cvt_pk_bf16_f32 v134, 0, v134\n"
"v_exp_f32 v133, v133\n"
"s_waitcnt lgkmcnt(9)\n"
"v_mfma_f32_32x32x16_bf16 v[160:175], a[84:87], a[20:23], v[160:175]\n"
"ds_read_b64_tr_b16 a[110:111], v227 offset:59392\n"
"v_max3_f32 v229, v229, v158, v159\n"
"v_max_f32 v229, v229, v241\n"
"v_fma_f32 v134, v134, s73, v228\n"
"v_cvt_pk_bf16_f32 v135, 0, v135\n"
"v_cvt_pk_bf16_f32 v130, v132, v133\n"
"v_exp_f32 v134, v134\n"
"s_waitcnt lgkmcnt(9)\n"
"v_mfma_f32_32x32x16_bf16 v[160:175], a[88:91], a[24:27], v[160:175]\n"
"ds_read_b64_tr_b16 a[112:113], v224 offset:61440\n"
"ds_read_b64_tr_b16 a[114:115], v224 offset:63488\n"
"v_mov_b32 v241, v229\n"
"v_mov_b32 v243, 0xc2ba0000\n"
"v_fma_f32 v135, v135, s73, v228\n"
"v_cvt_pk_bf16_f32 v136, 0, v136\n"
"v_exp_f32 v135, v135\n"
"s_waitcnt lgkmcnt(10)\n"
"v_mfma_f32_32x32x16_bf16 v[160:175], a[92:95], a[28:31], v[160:175]\n"
"ds_read_b64_tr_b16 a[116:117], v225 offset:61440\n"
"s_nop 0\n"
"v_permlane32_swap_b32 v229, v241\n"
"v_fma_f32 v136, v136, s73, v228\n"
"v_cvt_pk_bf16_f32 v137, 0, v137\n"
"v_cvt_pk_bf16_f32 v131, v134, v135\n"
"v_exp_f32 v136, v136\n"
"v_mfma_f32_32x32x16_bf16 v[176:191], a[64:67], a[32:35], 0\n"
"ds_read_b64_tr_b16 a[118:119], v225 offset:63488\n"
"v_max_f32 v229, v229, v241\n"
"v_fma_f32 v229, -v229, s73, v243\n"
"v_fma_f32 v137, v137, s73, v228\n"
"v_cvt_pk_bf16_f32 v138, 0, v138\n"
"v_exp_f32 v137, v137\n"
"v_fma_f32 v138, v138, s73, v228\n"
"v_mfma_f32_32x32x16_bf16 v[176:191], a[68:71], a[36:39], v[176:191]\n"
"ds_read_b64_tr_b16 a[120:121], v226 offset:61440\n"
"ds_read_b64_tr_b16 a[122:123], v226 offset:63488\n"
"v_cvt_pk_bf16_f32 v139, 0, v139\n"
"v_cvt_pk_bf16_f32 v136, v136, v137\n"
"v_exp_f32 v138, v138\n"
"v_fma_f32 v139, v139, s73, v228\n"
"v_cvt_pk_bf16_f32 v140, 0, v140\n"
"v_mfma_f32_32x32x16_bf16 v[176:191], a[72:75], a[40:43], v[176:191]\n"
"ds_read_b64_tr_b16 a[124:125], v227 offset:61440\n"
"v_exp_f32 v139, v139\n"
"v_fma_f32 v140, v140, s73, v228\n"
"v_cvt_pk_bf16_f32 v141, 0, v141\n"
"v_cvt_pk_bf16_f32 v137, v138, v139\n"
"v_exp_f32 v140, v140\n"
"v_mfma_f32_32x32x16_bf16 v[176:191], a[76:79], a[44:47], v[176:191]\n"
"Lcp_4_%=:\n"
"ds_read_b64_tr_b16 a[126:127], v227 offset:63488\n"
"v_fma_f32 v141, v141, s73, v228\n"
"v_cvt_pk_bf16_f32 v142, 0, v142\n"
"v_exp_f32 v141, v141\n"
"v_fma_f32 v142, v142, s73, v228\n"
"v_cvt_pk_bf16_f32 v143, 0, v143\n"
"v_cvt_pk_bf16_f32 v138, v140, v141\n"
"v_mfma_f32_32x32x16_bf16 v[176:191], a[80:83], a[48:51], v[176:191]\n"
"v_exp_f32 v142, v142\n"
"v_fma_f32 v143, v143, s73, v228\n"
"v_cvt_pk_bf16_f32 v144, 0, v144\n"
"v_exp_f32 v143, v143\n"
"v_fma_f32 v144, v144, s73, v229\n"
"v_lshl_add_u32 v254, v192, 2, s7\n"
"v_mfma_f32_32x32x16_bf16 v[176:191], a[84:87], a[52:55], v[176:191]\n"
"v_cvt_pk_bf16_f32 v145, 0, v145\n"
"v_cvt_pk_bf16_f32 v139, v142, v143\n"
"v_exp_f32 v144, v144\n"
"v_fma_f32 v145, v145, s73, v229\n"
"v_cvt_pk_bf16_f32 v146, 0, v146\n"
"v_add_u32 v254, Lcp_4_%=-Lpc0_%=+1024, v254\n"
"v_mfma_f32_32x32x16_bf16 v[176:191], a[88:91], a[56:59], v[176:191]\n"
"v_exp_f32 v145, v145\n"
"v_fma_f32 v146, v146, s73, v229\n"
"v_cvt_pk_bf16_f32 v147, 0, v147\n"
"v_cvt_pk_bf16_f32 v144, v144, v145\n"
"v_exp_f32 v146, v146\n"
"v_min_u32 v254, Lfin_%=-Lpc0_%=, v254\n"
"v_mfma_f32_32x32x16_bf16 v[176:191], a[92:95], a[60:63], v[176:191]\n"
"s_waitcnt vmcnt(0) lgkmcnt(0)\n"
"global_load_dword v255, v254, s[18:19]\n"
"v_fma_f32 v147, v147, s73, v229\n"
"v_cvt_pk_bf16_f32 v148, 0, v148\n"
"v_exp_f32 v147, v147\n"
"v_fma_f32 v148, v148, s73, v229\n"
"s_barrier\n"
"v_mfma_f32_32x32x16_bf16 v[0:15], a[96:99], v[128:131], 0\n"
"ds_read_b128 a[64:67], v216 offset:0\n"
"v_cvt_pk_bf16_f32 v149, 0, v149\n"
"v_cvt_pk_bf16_f32 v145, v146, v147\n"
"v_exp_f32 v148, v148\n"
"v_fma_f32 v149, v149, s73, v229\n"
"v_cvt_pk_bf16_f32 v150, 0, v150\n"
"v_mfma_f32_32x32x16_bf16 v[32:47], a[100:103], v[128:131], 0\n"
"s_add_u32 m0, s67, 32768\n"
"ds_read_b128 a[68:71], v217 offset:0\n"
"v_exp_f32 v149, v149\n"
"v_fma_f32 v150, v150, s73, v229\n"
"v_cvt_pk_bf16_f32 v151, 0, v151\n"
"v_cvt_pk_bf16_f32 v146, v148, v149\n"
"v_exp_f32 v150, v150\n"
"v_mfma_f32_16x16x32_bf16 v[200:203], v[248:251], v[128:131], 0\n"
"v_fma_f32 v151, v151, s73, v229\n"
"v_cvt_pk_bf16_f32 v152, 0, v152\n"
"v_mfma_f32_32x32x16_bf16 v[64:79], a[104:107], v[128:131], 0\n"
"buffer_load_dwordx4 v209, s[48:51], s75 offen offset:0 lds\n"
"ds_read_b128 a[72:75], v218 offset:0\n"
"v_exp_f32 v151, v151\n"
"v_fma_f32 v152, v152, s73, v229\n"
"v_cvt_pk_bf16_f32 v153, 0, v153\n"
"v_cvt_pk_bf16_f32 v147, v150, v151\n"
"v_mfma_f32_32x32x16_bf16 v[96:111], a[108:111], v[128:131], 0\n"
"ds_read_b128 a[76:79], v219 offset:0\n"
"v_exp_f32 v152, v152\n"
"v_fma_f32 v153, v153, s73, v229\n"
"v_cvt_pk_bf16_f32 v154, 0, v154\n"
"v_mfma_f32_32x32x16_bf16 v[16:31], a[96:99], v[144:147], 0\n"
"buffer_load_dwordx4 v210, s[48:51], s75 offen offset:1024 lds\n"
"ds_read_b128 a[80:83], v220 offset:0\n"
"v_exp_f32 v153, v153\n"
"v_fma_f32 v154, v154, s73, v229\n"
"v_mfma_f32_32x32x16_bf16 v[48:63], a[100:103], v[144:147], 0\n"
"ds_read_b128 a[84:87], v221 offset:0\n"
"v_cvt_pk_bf16_f32 v155, 0, v155\n"
"v_cvt_pk_bf16_f32 v152, v152, v153\n"
"v_exp_f32 v154, v154\n"
"v_fma_f32 v155, v155, s73, v229\n"
"v_mfma_f32_16x16x32_bf16 v[244:247], v[248:251], v[144:147], 0\n"
"v_cvt_pk_bf16_f32 v156, 0, v156\n"
"v_mfma_f32_32x32x16_bf16 v[80:95], a[104:107], v[144:147], 0\n"
"ds_read_b128 a[88:91], v222 offset:0\n"
"v_exp_f32 v155, v155\n"
"v_fma_f32 v156, v156, s73, v229\n"
"v_cvt_pk_bf16_f32 v157, 0, v157\n"
"v_cvt_pk_bf16_f32 v153, v154, v155\n"
"v_mfma_f32_32x32x16_bf16 v[112:127], a[108:111], v[144:147], 0\n"
"buffer_load_dwordx4 v211, s[48:51], s75 offen offset:2048 lds\n"
"ds_read_b128 a[92:95], v223 offset:0\n"
"v_exp_f32 v156, v156\n"
"v_fma_f32 v157, v157, s73, v229\n"
"v_mfma_f32_32x32x16_bf16 v[0:15], a[112:115], v[136:139], v[0:15]\n"
"ds_read_b64_tr_b16 a[96:97], v224 offset:16384\n"
"v_cvt_pk_bf16_f32 v158, 0, v158\n"
"v_exp_f32 v157, v157\n"
"v_fma_f32 v158, v158, s73, v229\n"
"v_cvt_pk_bf16_f32 v159, 0, v159\n"
"v_mfma_f32_32x32x16_bf16 v[32:47], a[116:119], v[136:139], v[32:47]\n"
"buffer_load_dwordx4 v212, s[48:51], s75 offen offset:3072 lds\n"
"ds_read_b64_tr_b16 a[98:99], v224 offset:18432\n"
"v_cvt_pk_bf16_f32 v154, v156, v157\n"
"v_exp_f32 v158, v158\n"
"v_mfma_f32_16x16x32_bf16 v[200:203], v[248:251], v[136:139], v[200:203]\n"
"v_fma_f32 v159, v159, s73, v229\n"
"v_cvt_pk_bf16_f32 v160, 0, v160\n"
"v_mfma_f32_32x32x16_bf16 v[64:79], a[120:123], v[136:139], v[64:79]\n"
"s_add_u32 s75, s75, 0x4000\n"
"ds_read_b64_tr_b16 a[100:101], v225 offset:16384\n"
"v_exp_f32 v159, v159\n"
"v_fma_f32 v160, v160, s73, v228\n"
"v_cvt_pk_bf16_f32 v161, 0, v161\n"
"v_cvt_pk_bf16_f32 v155, v158, v159\n"
"v_mfma_f32_32x32x16_bf16 v[96:111], a[124:127], v[136:139], v[96:111]\n"
"s_and_b32 s75, s75, 0x1ffff\n"
"ds_read_b64_tr_b16 a[102:103], v225 offset:18432\n"
"v_exp_f32 v160, v160\n"
"v_fma_f32 v161, v161, s73, v228\n"
"v_cvt_pk_bf16_f32 v162, 0, v162\n"
"v_mfma_f32_32x32x16_bf16 v[16:31], a[112:115], v[152:155], v[16:31]\n"
"ds_read_b64_tr_b16 a[104:105], v226 offset:16384\n"
"v_exp_f32 v161, v161\n"
"v_fma_f32 v162, v162, s73, v228\n"
"v_cvt_pk_bf16_f32 v163, 0, v163\n"
"v_cvt_pk_bf16_f32 v160, v160, v161\n"
"v_mfma_f32_32x32x16_bf16 v[48:63], a[116:119], v[152:155], v[48:63]\n"
"ds_read_b64_tr_b16 a[106:107], v226 offset:18432\n"
"v_exp_f32 v162, v162\n"
"v_fma_f32 v163, v163, s73, v228\n"
"v_cvt_pk_bf16_f32 v164, 0, v164\n"
"v_mfma_f32_16x16x32_bf16 v[244:247], v[248:251], v[152:155], v[244:247]\n"
"v_exp_f32 v163, v163\n"
"v_mfma_f32_32x32x16_bf16 v[80:95], a[120:123], v[152:155], v[80:95]\n"
"ds_read_b64_tr_b16 a[108:109], v227 offset:16384\n"
"v_fma_f32 v164, v164, s73, v228\n"
"v_cvt_pk_bf16_f32 v165, 0, v165\n"
"v_cvt_pk_bf16_f32 v161, v162, v163\n"
"v_exp_f32 v164, v164\n"
"v_mfma_f32_32x32x16_bf16 v[112:127], a[124:127], v[152:155], v[112:127]\n"
"s_waitcnt lgkmcnt(7)\n"
"ds_read_b64_tr_b16 a[110:111], v227 offset:18432\n"
"v_fma_f32 v165, v165, s73, v228\n"
"v_cvt_pk_bf16_f32 v166, 0, v166\n"
"v_exp_f32 v165, v165\n"
"v_fma_f32 v166, v166, s73, v228\n"
"v_mfma_f32_32x32x16_bf16 v[128:143], a[64:67], a[0:3], 0\n"
"ds_read_b64_tr_b16 a[112:113], v224 offset:20480\n"
"v_cvt_pk_bf16_f32 v167, 0, v167\n"
"v_cvt_pk_bf16_f32 v162, v164, v165\n"
"v_exp_f32 v166, v166\n"
"v_fma_f32 v167, v167, s73, v228\n"
"v_mfma_f32_32x32x16_bf16 v[128:143], a[68:71], a[4:7], v[128:143]\n"
"ds_read_b64_tr_b16 a[114:115], v224 offset:22528\n"
"v_cvt_pk_bf16_f32 v168, 0, v168\n"
"v_exp_f32 v167, v167\n"
"v_fma_f32 v168, v168, s73, v228\n"
"v_cvt_pk_bf16_f32 v169, 0, v169\n"
"v_mfma_f32_32x32x16_bf16 v[128:143], a[72:75], a[8:11], v[128:143]\n"
"ds_read_b64_tr_b16 a[116:117], v225 offset:20480\n"
"v_cvt_pk_bf16_f32 v163, v166, v167\n"
"v_exp_f32 v168, v168\n"
"v_fma_f32 v169, v169, s73, v228\n"
"v_cvt_pk_bf16_f32 v170, 0, v170\n"
"v_mfma_f32_32x32x16_bf16 v[128:143], a[76:79], a[12:15], v[128:143]\n"
"ds_read_b64_tr_b16 a[118:119], v225 offset:22528\n"
"v_exp_f32 v169, v169\n"
"v_fma_f32 v170, v170, s73, v228\n"
"v_cvt_pk_bf16_f32 v171, 0, v171\n"
"v_cvt_pk_bf16_f32 v168, v168, v169\n"
"v_mfma_f32_32x32x16_bf16 v[128:143], a[80:83], a[16:19], v[128:143]\n"
"s_add_u32 s74, s68, 65536\n"
"ds_read_b64_tr_b16 a[120:121], v226 offset:20480\n"
"v_exp_f32 v170, v170\n"
"v_fma_f32 v171, v171, s73, v228\n"
"v_cvt_pk_bf16_f32 v172, 0, v172\n"
"v_mfma_f32_32x32x16_bf16 v[128:143], a[84:87], a[20:23], v[128:143]\n"
"s_and_b32 s74, s74, 0x7ffff\n"
"ds_read_b64_tr_b16 a[122:123], v226 offset:22528\n"
"v_exp_f32 v171, v171\n"
"v_fma_f32 v172, v172, s73, v228\n"
"v_cvt_pk_bf16_f32 v173, 0, v173\n"
"v_cvt_pk_bf16_f32 v169, v170, v171\n"
"v_mfma_f32_32x32x16_bf16 v[128:143], a[88:91], a[24:27], v[128:143]\n"
"s_add_u32 s74, s74, s69\n"
"ds_read_b64_tr_b16 a[124:125], v227 offset:20480\n"
"v_exp_f32 v172, v172\n"
"v_fma_f32 v173, v173, s73, v228\n"
"v_cvt_pk_bf16_f32 v174, 0, v174\n"
"v_mfma_f32_32x32x16_bf16 v[128:143], a[92:95], a[28:31], v[128:143]\n"
"s_add_u32 m0, s67, 98304\n"
"ds_read_b64_tr_b16 a[126:127], v227 offset:22528\n"
"v_exp_f32 v173, v173\n"
"v_fma_f32 v174, v174, s73, v228\n"
"v_cvt_pk_bf16_f32 v175, 0, v175\n"
"v_cvt_pk_bf16_f32 v170, v172, v173\n"
"v_mfma_f32_32x32x16_bf16 v[144:159], a[64:67], a[32:35], 0\n"
"buffer_load_dwordx4 v205, s[56:59], s74 offen offset:0 lds\n"
"v_exp_f32 v174, v174\n"
"v_fma_f32 v175, v175, s73, v228\n"
"v_cvt_pk_bf16_f32 v176, 0, v176\n"
"v_mfma_f32_32x32x16_bf16 v[144:159], a[68:71], a[36:39], v[144:159]\n"
"buffer_load_dwordx4 v206, s[56:59], s74 offen offset:1024 lds\n"
"v_exp_f32 v175, v175\n"
"v_fma_f32 v176, v176, s73, v229\n"
"v_cvt_pk_bf16_f32 v177, 0, v177\n"
"v_mfma_f32_32x32x16_bf16 v[144:159], a[72:75], a[40:43], v[144:159]\n"
"v_cvt_pk_bf16_f32 v171, v174, v175\n"
"v_exp_f32 v176, v176\n"
"v_fma_f32 v177, v177, s73, v229\n"
"v_cvt_pk_bf16_f32 v178, 0, v178\n"
"v_mfma_f32_32x32x16_bf16 v[144:159], a[76:79], a[44:47], v[144:159]\n"
"buffer_load_dwordx4 v207, s[56:59], s74 offen offset:2048 lds\n"
"v_exp_f32 v177, v177\n"
"v_fma_f32 v178, v178, s73, v229\n"
"v_cvt_pk_bf16_f32 v179, 0, v179\n"
"v_mfma_f32_32x32x16_bf16 v[144:159], a[80:83], a[48:51], v[144:159]\n"
"v_cvt_pk_bf16_f32 v176, v176, v177\n"
"v_exp_f32 v178, v178\n"
"v_fma_f32 v179, v179, s73, v229\n"
"v_cvt_pk_bf16_f32 v180, 0, v180\n"
"v_mfma_f32_32x32x16_bf16 v[144:159], a[84:87], a[52:55], v[144:159]\n"
"buffer_load_dwordx4 v208, s[56:59], s74 offen offset:3072 lds\n"
"v_exp_f32 v179, v179\n"
"v_fma_f32 v180, v180, s73, v229\n"
"v_cvt_pk_bf16_f32 v181, 0, v181\n"
"v_mfma_f32_32x32x16_bf16 v[144:159], a[88:91], a[56:59], v[144:159]\n"
"v_cvt_pk_bf16_f32 v177, v178, v179\n"
"v_exp_f32 v180, v180\n"
"v_fma_f32 v181, v181, s73, v229\n"
"v_cvt_pk_bf16_f32 v182, 0, v182\n"
"v_mfma_f32_32x32x16_bf16 v[144:159], a[92:95], a[60:63], v[144:159]\n"
"s_waitcnt lgkmcnt(0)\n"
"v_exp_f32 v181, v181\n"
"v_fma_f32 v182, v182, s73, v229\n"
"v_cvt_pk_bf16_f32 v183, 0, v183\n"
"v_cvt_pk_bf16_f32 v178, v180, v181\n"
"v_mfma_f32_32x32x16_bf16 v[0:15], a[96:99], v[160:163], v[0:15]\n"
"ds_read_b128 a[64:67], v216 offset:8192\n"
"v_exp_f32 v182, v182\n"
"v_fma_f32 v183, v183, s73, v229\n"
"v_cvt_pk_bf16_f32 v184, 0, v184\n"
"v_mfma_f32_32x32x16_bf16 v[32:47], a[100:103], v[160:163], v[32:47]\n"
"s_add_u32 m0, s67, 49152\n"
"ds_read_b128 a[68:71], v217 offset:8192\n"
"v_exp_f32 v183, v183\n"
"v_fma_f32 v184, v184, s73, v229\n"
"v_cvt_pk_bf16_f32 v185, 0, v185\n"
"v_cvt_pk_bf16_f32 v179, v182, v183\n"
"v_mfma_f32_16x16x32_bf16 v[200:203], v[248:251], v[160:163], v[200:203]\n"
"v_exp_f32 v184, v184\n"
"v_mfma_f32_32x32x16_bf16 v[64:79], a[104:107], v[160:163], v[64:79]\n"
"buffer_load_dwordx4 v213, s[52:55], s76 offen offset:0 lds\n"
"ds_read_b128 a[72:75], v218 offset:8192\n"
"v_fma_f32 v185, v185, s73, v229\n"
"v_cvt_pk_bf16_f32 v186, 0, v186\n"
"v_mfma_f32_32x32x16_bf16 v[96:111], a[108:111], v[160:163], v[96:111]\n"
"ds_read_b128 a[76:79], v219 offset:8192\n"
"v_exp_f32 v185, v185\n"
"v_fma_f32 v186, v186, s73, v229\n"
"v_cvt_pk_bf16_f32 v187, 0, v187\n"
"v_cvt_pk_bf16_f32 v184, v184, v185\n"
"v_mfma_f32_32x32x16_bf16 v[16:31], a[96:99], v[176:179], v[16:31]\n"
"buffer_load_dwordx4 v213, s[52:55], s76 offen offset:1024 lds\n"
"ds_read_b128 a[80:83], v220 offset:8192\n"
"v_exp_f32 v186, v186\n"
"v_fma_f32 v187, v187, s73, v229\n"
"v_mfma_f32_32x32x16_bf16 v[48:63], a[100:103], v[176:179], v[48:63]\n"
"ds_read_b128 a[84:87], v221 offset:8192\n"
"v_cvt_pk_bf16_f32 v188, 0, v188\n"
"v_exp_f32 v187, v187\n"
"v_fma_f32 v188, v188, s73, v229\n"
"v_cvt_pk_bf16_f32 v189, 0, v189\n"
"v_mfma_f32_16x16x32_bf16 v[244:247], v[248:251], v[176:179], v[244:247]\n"
"v_cvt_pk_bf16_f32 v185, v186, v187\n"
"v_mfma_f32_32x32x16_bf16 v[80:95], a[104:107], v[176:179], v[80:95]\n"
"ds_read_b128 a[88:91], v222 offset:8192\n"
"v_exp_f32 v188, v188\n"
"v_fma_f32 v189, v189, s73, v229\n"
"v_cvt_pk_bf16_f32 v190, 0, v190\n"
"v_mfma_f32_32x32x16_bf16 v[112:127], a[108:111], v[176:179], v[112:127]\n"
"buffer_load_dwordx4 v213, s[52:55], s76 offen offset:2048 lds\n"
"ds_read_b128 a[92:95], v223 offset:8192\n"
"v_exp_f32 v189, v189\n"
"v_fma_f32 v190, v190, s73, v229\n"
"v_mfma_f32_32x32x16_bf16 v[0:15], a[112:115], v[168:171], v[0:15]\n"
"Lst5_%=:\n"
"s_cbranch_execz Lst9_%=\n"
"ds_read_b64_tr_b16 a[96:97], v224 offset:24576\n"
"v_cvt_pk_bf16_f32 v191, 0, v191\n"
"v_cvt_pk_bf16_f32 v186, v188, v189\n"
"v_exp_f32 v190, v190\n"
"v_fma_f32 v191, v191, s73, v229\n"
"v_mfma_f32_32x32x16_bf16 v[32:47], a[116:119], v[168:171], v[32:47]\n"
"buffer_load_dwordx4 v213, s[52:55], s76 offen offset:3072 lds\n"
"ds_read_b64_tr_b16 a[98:99], v224 offset:26624\n"
"v_cvt_pk_bf16_f32 v128, 0, v128\n"
"v_exp_f32 v191, v191\n"
"v_mfma_f32_16x16x32_bf16 v[200:203], v[248:251], v[168:171], v[200:203]\n"
"v_fma_f32 v128, v128, s73, v228\n"
"v_cvt_pk_bf16_f32 v129, 0, v129\n"
"v_mfma_f32_32x32x16_bf16 v[64:79], a[120:123], v[168:171], v[64:79]\n"
"s_add_u32 s76, s76, 0x4000\n"
"ds_read_b64_tr_b16 a[100:101], v225 offset:24576\n"
"v_cvt_pk_bf16_f32 v187, v190, v191\n"
"v_exp_f32 v128, v128\n"
"v_fma_f32 v129, v129, s73, v228\n"
"v_cvt_pk_bf16_f32 v130, 0, v130\n"
"v_mfma_f32_32x32x16_bf16 v[96:111], a[124:127], v[168:171], v[96:111]\n"
"s_and_b32 s76, s76, 0x1ffff\n"
"ds_read_b64_tr_b16 a[102:103], v225 offset:26624\n"
"v_exp_f32 v129, v129\n"
"v_fma_f32 v130, v130, s73, v228\n"
"v_cvt_pk_bf16_f32 v131, 0, v131\n"
"v_cvt_pk_bf16_f32 v128, v128, v129\n"
"v_mfma_f32_32x32x16_bf16 v[16:31], a[112:115], v[184:187], v[16:31]\n"
"ds_read_b64_tr_b16 a[104:105], v226 offset:24576\n"
"v_exp_f32 v130, v130\n"
"v_fma_f32 v131, v131, s73, v228\n"
"v_cvt_pk_bf16_f32 v132, 0, v132\n"
"v_mfma_f32_32x32x16_bf16 v[48:63], a[116:119], v[184:187], v[48:63]\n"
"ds_read_b64_tr_b16 a[128:129], v232 offset:0\n"
"ds_read_b64_tr_b16 a[106:107], v226 offset:26624\n"
"v_exp_f32 v131, v131\n"
"v_fma_f32 v132, v132, s73, v228\n"
"v_cvt_pk_bf16_f32 v133, 0, v133\n"
"v_mfma_f32_16x16x32_bf16 v[244:247], v[248:251], v[184:187], v[244:247]\n"
"v_cvt_pk_bf16_f32 v129, v130, v131\n"
"v_mfma_f32_32x32x16_bf16 v[80:95], a[120:123], v[184:187], v[80:95]\n"
"ds_read_b64_tr_b16 a[108:109], v227 offset:24576\n"
"v_exp_f32 v132, v132\n"
"v_fma_f32 v133, v133, s73, v228\n"
"v_cvt_pk_bf16_f32 v134, 0, v134\n"
"v_mfma_f32_32x32x16_bf16 v[112:127], a[124:127], v[184:187], v[112:127]\n"
"s_waitcnt lgkmcnt(8)\n"
"ds_read_b64_tr_b16 a[130:131], v232 offset:512\n"
"ds_read_b64_tr_b16 a[110:111], v227 offset:26624\n"
"v_exp_f32 v133, v133\n"
"v_fma_f32 v134, v134, s73, v228\n"
"v_cvt_pk_bf16_f32 v135, 0, v135\n"
"v_mfma_f32_32x32x16_bf16 v[160:175], a[64:67], a[0:3], 0\n"
"ds_read_b64_tr_b16 a[112:113], v224 offset:28672\n"
"v_cvt_pk_bf16_f32 v130, v132, v133\n"
"v_exp_f32 v134, v134\n"
"v_fma_f32 v135, v135, s73, v228\n"
"v_cvt_pk_bf16_f32 v136, 0, v136\n"
"v_mfma_f32_32x32x16_bf16 v[160:175], a[68:71], a[4:7], v[160:175]\n"
"ds_read_b64_tr_b16 a[132:133], v233 offset:0\n"
"ds_read_b64_tr_b16 a[114:115], v224 offset:30720\n"
"v_exp_f32 v135, v135\n"
"v_fma_f32 v136, v136, s73, v228\n"
"v_cvt_pk_bf16_f32 v137, 0, v137\n"
"v_mfma_f32_32x32x16_bf16 v[160:175], a[72:75], a[8:11], v[160:175]\n"
"ds_read_b64_tr_b16 a[116:117], v225 offset:28672\n"
"v_cvt_pk_bf16_f32 v131, v134, v135\n"
"v_exp_f32 v136, v136\n"
"v_fma_f32 v137, v137, s73, v228\n"
"v_cvt_pk_bf16_f32 v138, 0, v138\n"
"v_mfma_f32_32x32x16_bf16 v[160:175], a[76:79], a[12:15], v[160:175]\n"
"ds_read_b64_tr_b16 a[134:135], v233 offset:512\n"
"ds_read_b64_tr_b16 a[118:119], v225 offset:30720\n"
"v_exp_f32 v137, v137\n"
"v_fma_f32 v138, v138, s73, v228\n"
"v_cvt_pk_bf16_f32 v139, 0, v139\n"
"v_mfma_f32_32x32x16_bf16 v[160:175], a[80:83], a[16:19], v[160:175]\n"
"s_add_u32 s74, s68, 98304\n"
"ds_read_b64_tr_b16 a[120:121], v226 offset:28672\n"
"v_cvt_pk_bf16_f32 v136, v136, v137\n"
"v_exp_f32 v138, v138\n"
"v_fma_f32 v139, v139, s73, v228\n"
"v_cvt_pk_bf16_f32 v140, 0, v140\n"
"v_mfma_f32_32x32x16_bf16 v[160:175], a[84:87], a[20:23], v[160:175]\n"
"s_and_b32 s74, s74, 0x7ffff\n"
"ds_read_b64_tr_b16 a[136:137], v232 offset:8\n"
"ds_read_b64_tr_b16 a[122:123], v226 offset:30720\n"
"v_exp_f32 v139, v139\n"
"v_fma_f32 v140, v140, s73, v228\n"
"v_cvt_pk_bf16_f32 v141, 0, v141\n"
"v_mfma_f32_32x32x16_bf16 v[160:175], a[88:91], a[24:27], v[160:175]\n"
"s_add_u32 s74, s74, s69\n"
"ds_read_b64_tr_b16 a[124:125], v227 offset:28672\n"
"v_cvt_pk_bf16_f32 v137, v138, v139\n"
"v_exp_f32 v140, v140\n"
"v_fma_f32 v141, v141, s73, v228\n"
"v_cvt_pk_bf16_f32 v142, 0, v142\n"
"v_mfma_f32_32x32x16_bf16 v[160:175], a[92:95], a[28:31], v[160:175]\n"
"s_add_u32 m0, s67, 114688\n"
"ds_read_b64_tr_b16 a[138:139], v232 offset:520\n"
"ds_read_b64_tr_b16 a[126:127], v227 offset:30720\n"
"v_exp_f32 v141, v141\n"
"v_fma_f32 v142, v142, s73, v228\n"
"v_cvt_pk_bf16_f32 v143, 0, v143\n"
"v_mfma_f32_32x32x16_bf16 v[176:191], a[64:67], a[32:35], 0\n"
"buffer_load_dwordx4 v205, s[56:59], s74 offen offset:0 lds\n"
"v_cvt_pk_bf16_f32 v138, v140, v141\n"
"v_exp_f32 v142, v142\n"
"v_fma_f32 v143, v143, s73, v228\n"
"v_mfma_f32_32x32x16_bf16 v[176:191], a[68:71], a[36:39], v[176:191]\n"
"buffer_load_dwordx4 v206, s[56:59], s74 offen offset:1024 lds\n"
"v_cvt_pk_bf16_f32 v144, 0, v144\n"
"v_exp_f32 v143, v143\n"
"v_fma_f32 v144, v144, s73, v229\n"
"v_mfma_f32_32x32x16_bf16 v[176:191], a[72:75], a[40:43], v[176:191]\n"
"ds_read_b64_tr_b16 a[140:141], v233 offset:8\n"
"v_cvt_pk_bf16_f32 v145, 0, v145\n"
"v_cvt_pk_bf16_f32 v139, v142, v143\n"
"v_exp_f32 v144, v144\n"
"v_fma_f32 v145, v145, s73, v229\n"
"v_mfma_f32_32x32x16_bf16 v[176:191], a[76:79], a[44:47], v[176:191]\n"
"buffer_load_dwordx4 v207, s[56:59], s74 offen offset:2048 lds\n"
"v_cvt_pk_bf16_f32 v146, 0, v146\n"
"v_exp_f32 v145, v145\n"
"v_fma_f32 v146, v146, s73, v229\n"
"v_mfma_f32_32x32x16_bf16 v[176:191], a[80:83], a[48:51], v[176:191]\n"
"ds_read_b64_tr_b16 a[142:143], v233 offset:520\n"
"v_cvt_pk_bf16_f32 v147, 0, v147\n"
"v_cvt_pk_bf16_f32 v144, v144, v145\n"
"v_exp_f32 v146, v146\n"
"v_fma_f32 v147, v147, s73, v229\n"
"v_mfma_f32_32x32x16_bf16 v[176:191], a[84:87], a[52:55], v[176:191]\n"
"buffer_load_dwordx4 v208, s[56:59], s74 offen offset:3072 lds\n"
"v_cvt_pk_bf16_f32 v148, 0, v148\n"
"v_exp_f32 v147, v147\n"
"v_fma_f32 v148, v148, s73, v229\n"
"v_mfma_f32_32x32x16_bf16 v[176:191], a[88:91], a[56:59], v[176:191]\n"
"v_cvt_pk_bf16_f32 v149, 0, v149\n"
"v_cvt_pk_bf16_f32 v145, v146, v147\n"
"v_exp_f32 v148, v148\n"
"v_fma_f32 v149, v149, s73, v229\n"
"v_cvt_pk_bf16_f32 v150, 0, v150\n"
"v_mfma_f32_32x32x16_bf16 v[176:191], a[92:95], a[60:63], v[176:191]\n"
"s_waitcnt vmcnt(4) lgkmcnt(0)\n"
"v_exp_f32 v149, v149\n"
"v_fma_f32 v150, v150, s73, v229\n"
"v_cvt_pk_bf16_f32 v151, 0, v151\n"
"v_cvt_pk_bf16_f32 v146, v148, v149\n"
"s_barrier\n"
"v_mfma_f32_32x32x16_bf16 v[0:15], a[96:99], v[128:131], v[0:15]\n"
"ds_read_b128 a[64:67], v216 offset:32768\n"
"v_exp_f32 v150, v150\n"
"v_fma_f32 v151, v151, s73, v229\n"
"v_cvt_pk_bf16_f32 v152, 0, v152\n"
"v_mfma_f32_32x32x16_bf16 v[32:47], a[100:103], v[128:131], v[32:47]\n"
"s_add_u32 m0, s67, 0\n"
"ds_read_b128 a[68:71], v217 offset:32768\n"
"v_exp_f32 v151, v151\n"
"v_fma_f32 v152, v152, s73, v229\n"
"v_cvt_pk_bf16_f32 v153, 0, v153\n"
"v_cvt_pk_bf16_f32 v147, v150, v151\n"
"v_mfma_f32_16x16x32_bf16 v[200:203], v[248:251], v[128:131], v[200:203]\n"
"v_exp_f32 v152, v152\n"
"v_mfma_f32_32x32x16_bf16 v[64:79], a[104:107], v[128:131], v[64:79]\n"
"buffer_load_dwordx4 v209, s[48:51], s75 offen offset:0 lds\n"
"ds_read_b128 a[72:75], v218 offset:32768\n"
"v_fma_f32 v153, v153, s73, v229\n"
"v_cvt_pk_bf16_f32 v154, 0, v154\n"
"v_mfma_f32_32x32x16_bf16 v[96:111], a[108:111], v[128:131], v[96:111]\n"
"ds_read_b128 a[76:79], v219 offset:32768\n"
"v_exp_f32 v153, v153\n"
"v_fma_f32 v154, v154, s73, v229\n"
"v_cvt_pk_bf16_f32 v155, 0, v155\n"
"v_cvt_pk_bf16_f32 v152, v152, v153\n"
"v_mfma_f32_32x32x16_bf16 v[16:31], a[96:99], v[144:147], v[16:31]\n"
"buffer_load_dwordx4 v210, s[48:51], s75 offen offset:1024 lds\n"
"ds_read_b128 a[80:83], v220 offset:32768\n"
"v_exp_f32 v154, v154\n"
"v_fma_f32 v155, v155, s73, v229\n"
"v_mfma_f32_32x32x16_bf16 v[48:63], a[100:103], v[144:147], v[48:63]\n"
"ds_read_b128 a[84:87], v221 offset:32768\n"
"v_cvt_pk_bf16_f32 v156, 0, v156\n"
"v_exp_f32 v155, v155\n"
"v_fma_f32 v156, v156, s73, v229\n"
"v_cvt_pk_bf16_f32 v157, 0, v157\n"
"v_mfma_f32_16x16x32_bf16 v[244:247], v[248:251], v[144:147], v[244:247]\n"
"v_cvt_pk_bf16_f32 v153, v154, v155\n"
"v_mfma_f32_32x32x16_bf16 v[80:95], a[104:107], v[144:147], v[80:95]\n"
"ds_read_b128 a[88:91], v222 offset:32768\n"
"v_exp_f32 v156, v156\n"
"v_fma_f32 v157, v157, s73, v229\n"
"v_cvt_pk_bf16_f32 v158, 0, v158\n"
"v_mfma_f32_32x32x16_bf16 v[112:127], a[108:111], v[144:147], v[112:127]\n"
"buffer_load_dwordx4 v211, s[48:51], s75 offen offset:2048 lds\n"
"ds_read_b128 a[92:95], v223 offset:32768\n"
"v_exp_f32 v157, v157\n"
"v_fma_f32 v158, v158, s73, v229\n"
"v_mfma_f32_32x32x16_bf16 v[0:15], a[112:115], v[136:139], v[0:15]\n"
"ds_read_b64_tr_b16 a[96:97], v224 offset:49152\n"
"v_cvt_pk_bf16_f32 v159, 0, v159\n"
"v_cvt_pk_bf16_f32 v154, v156, v157\n"
"v_exp_f32 v158, v158\n"
"v_fma_f32 v159, v159, s73, v229\n"
"v_mfma_f32_32x32x16_bf16 v[32:47], a[116:119], v[136:139], v[32:47]\n"
"buffer_load_dwordx4 v212, s[48:51], s75 offen offset:3072 lds\n"
"ds_read_b64_tr_b16 a[98:99], v224 offset:51200\n"
"v_cvt_pk_bf16_f32 v160, 0, v160\n"
"v_exp_f32 v159, v159\n"
"v_mfma_f32_16x16x32_bf16 v[200:203], v[248:251], v[136:139], v[200:203]\n"
"v_fma_f32 v160, v160, s73, v228\n"
"v_cvt_pk_bf16_f32 v161, 0, v161\n"
"v_mfma_f32_32x32x16_bf16 v[64:79], a[120:123], v[136:139], v[64:79]\n"
"s_add_u32 s75, s75, 0x4000\n"
"ds_read_b64_tr_b16 a[100:101], v225 offset:49152\n"
"v_cvt_pk_bf16_f32 v155, v158, v159\n"
"v_exp_f32 v160, v160\n"
"v_fma_f32 v161, v161, s73, v228\n"
"v_cvt_pk_bf16_f32 v162, 0, v162\n"
"v_mfma_f32_32x32x16_bf16 v[96:111], a[124:127], v[136:139], v[96:111]\n"
"s_and_b32 s75, s75, 0x1ffff\n"
"ds_read_b64_tr_b16 a[102:103], v225 offset:51200\n"
"v_exp_f32 v161, v161\n"
"v_fma_f32 v162, v162, s73, v228\n"
"v_cvt_pk_bf16_f32 v163, 0, v163\n"
"v_cvt_pk_bf16_f32 v160, v160, v161\n"
"v_mfma_f32_32x32x16_bf16 v[16:31], a[112:115], v[152:155], v[16:31]\n"
"ds_read_b64_tr_b16 a[104:105], v226 offset:49152\n"
"v_exp_f32 v162, v162\n"
"v_fma_f32 v163, v163, s73, v228\n"
"v_cvt_pk_bf16_f32 v164, 0, v164\n"
"v_mfma_f32_32x32x16_bf16 v[48:63], a[116:119], v[152:155], v[48:63]\n"
"ds_read_b64_tr_b16 a[144:145], v232 offset:16384\n"
"ds_read_b64_tr_b16 a[106:107], v226 offset:51200\n"
"v_exp_f32 v163, v163\n"
"v_fma_f32 v164, v164, s73, v228\n"
"v_cvt_pk_bf16_f32 v165, 0, v165\n"
"v_mfma_f32_16x16x32_bf16 v[244:247], v[248:251], v[152:155], v[244:247]\n"
"v_cvt_pk_bf16_f32 v161, v162, v163\n"
"v_mfma_f32_32x32x16_bf16 v[80:95], a[120:123], v[152:155], v[80:95]\n"
"ds_read_b64_tr_b16 a[108:109], v227 offset:49152\n"
"v_exp_f32 v164, v164\n"
"v_fma_f32 v165, v165, s73, v228\n"
"v_cvt_pk_bf16_f32 v166, 0, v166\n"
"v_mfma_f32_32x32x16_bf16 v[112:127], a[124:127], v[152:155], v[112:127]\n"
"s_waitcnt lgkmcnt(8)\n"
"ds_read_b64_tr_b16 a[146:147], v232 offset:16896\n"
"ds_read_b64_tr_b16 a[110:111], v227 offset:51200\n"
"v_exp_f32 v165, v165\n"
"v_fma_f32 v166, v166, s73, v228\n"
"v_cvt_pk_bf16_f32 v167, 0, v167\n"
"v_mfma_f32_32x32x16_bf16 v[128:143], a[64:67], a[0:3], 0\n"
"ds_read_b64_tr_b16 a[112:113], v224 offset:53248\n"
"v_cvt_pk_bf16_f32 v162, v164, v165\n"
"v_exp_f32 v166, v166\n"
"v_fma_f32 v167, v167, s73, v228\n"
"v_cvt_pk_bf16_f32 v168, 0, v168\n"
"v_mfma_f32_32x32x16_bf16 v[128:143], a[68:71], a[4:7], v[128:143]\n"
"ds_read_b64_tr_b16 a[148:149], v233 offset:16384\n"
"ds_read_b64_tr_b16 a[114:115], v224 offset:55296\n"
"v_exp_f32 v167, v167\n"
"v_fma_f32 v168, v168, s73, v228\n"
"v_cvt_pk_bf16_f32 v169, 0, v169\n"
"v_mfma_f32_32x32x16_bf16 v[128:143], a[72:75], a[8:11], v[128:143]\n"
"ds_read_b64_tr_b16 a[116:117], v225 offset:53248\n"
"v_cvt_pk_bf16_f32 v163, v166, v167\n"
"v_exp_f32 v168, v168\n"
"v_fma_f32 v169, v169, s73, v228\n"
"v_cvt_pk_bf16_f32 v170, 0, v170\n"
"v_mfma_f32_32x32x16_bf16 v[128:143], a[76:79], a[12:15], v[128:143]\n"
"ds_read_b64_tr_b16 a[150:151], v233 offset:16896\n"
"ds_read_b64_tr_b16 a[118:119], v225 offset:55296\n"
"v_exp_f32 v169, v169\n"
"v_fma_f32 v170, v170, s73, v228\n"
"v_cvt_pk_bf16_f32 v171, 0, v171\n"
"v_mfma_f32_32x32x16_bf16 v[128:143], a[80:83], a[16:19], v[128:143]\n"
"s_add_u32 s74, s68, 196608\n"
"ds_read_b64_tr_b16 a[120:121], v226 offset:53248\n"
"v_cvt_pk_bf16_f32 v168, v168, v169\n"
"v_exp_f32 v170, v170\n"
"v_fma_f32 v171, v171, s73, v228\n"
"v_cvt_pk_bf16_f32 v172, 0, v172\n"
"v_mfma_f32_32x32x16_bf16 v[128:143], a[84:87], a[20:23], v[128:143]\n"
"s_and_b32 s74, s74, 0x7ffff\n"
"ds_read_b64_tr_b16 a[152:153], v232 offset:16392\n"
"ds_read_b64_tr_b16 a[122:123], v226 offset:55296\n"
"v_exp_f32 v171, v171\n"
"v_fma_f32 v172, v172, s73, v228\n"
"v_cvt_pk_bf16_f32 v173, 0, v173\n"
"v_mfma_f32_32x32x16_bf16 v[128:143], a[88:91], a[24:27], v[128:143]\n"
"s_add_u32 s74, s74, s69\n"
"ds_read_b64_tr_b16 a[124:125], v227 offset:53248\n"
"v_cvt_pk_bf16_f32 v169, v170, v171\n"
"v_exp_f32 v172, v172\n"
"v_fma_f32 v173, v173, s73, v228\n"
"v_cvt_pk_bf16_f32 v174, 0, v174\n"
"v_mfma_f32_32x32x16_bf16 v[128:143], a[92:95], a[28:31], v[128:143]\n"
"s_add_u32 m0, s67, 131072\n"
"ds_read_b64_tr_b16 a[154:155], v232 offset:16904\n"
"ds_read_b64_tr_b16 a[126:127], v227 offset:55296\n"
"v_exp_f32 v173, v173\n"
"v_fma_f32 v174, v174, s73, v228\n"
"v_cvt_pk_bf16_f32 v175, 0, v175\n"
"v_mfma_f32_32x32x16_bf16 v[144:159], a[64:67], a[32:35], 0\n"
"buffer_load_dwordx4 v205, s[56:59], s74 offen offset:0 lds\n"
"v_cvt_pk_bf16_f32 v170, v172, v173\n"
"v_exp_f32 v174, v174\n"
"v_fma_f32 v175, v175, s73, v228\n"
"v_mfma_f32_32x32x16_bf16 v[144:159], a[68:71], a[36:39], v[144:159]\n"
"buffer_load_dwordx4 v206, s[56:59], s74 offen offset:1024 lds\n"
"v_cvt_pk_bf16_f32 v176, 0, v176\n"
"v_exp_f32 v175, v175\n"
"v_fma_f32 v176, v176, s73, v229\n"
"v_mfma_f32_32x32x16_bf16 v[144:159], a[72:75], a[40:43], v[144:159]\n"
"ds_read_b64_tr_b16 a[156:157], v233 offset:16392\n"
"v_cvt_pk_bf16_f32 v177, 0, v177\n"
"v_cvt_pk_bf16_f32 v171, v174, v175\n"
"v_exp_f32 v176, v176\n"
"v_fma_f32 v177, v177, s73, v229\n"
"v_mfma_f32_32x32x16_bf16 v[144:159], a[76:79], a[44:47], v[144:159]\n"
"buffer_load_dwordx4 v207, s[56:59], s74 offen offset:2048 lds\n"
"v_cvt_pk_bf16_f32 v178, 0, v178\n"
"v_exp_f32 v177, v177\n"
"v_fma_f32 v178, v178, s73, v229\n"
"v_mfma_f32_32x32x16_bf16 v[144:159], a[80:83], a[48:51], v[144:159]\n"
"ds_read_b64_tr_b16 a[158:159], v233 offset:16904\n"
"v_cvt_pk_bf16_f32 v179, 0, v179\n"
"v_cvt_pk_bf16_f32 v176, v176, v177\n"
"v_exp_f32 v178, v178\n"
"v_fma_f32 v179, v179, s73, v229\n"
"v_mfma_f32_32x32x16_bf16 v[144:159], a[84:87], a[52:55], v[144:159]\n"
"buffer_load_dwordx4 v208, s[56:59], s74 offen offset:3072 lds\n"
"v_cvt_pk_bf16_f32 v180, 0, v180\n"
"v_exp_f32 v179, v179\n"
"v_fma_f32 v180, v180, s73, v229\n"
"v_mfma_f32_32x32x16_bf16 v[144:159], a[88:91], a[56:59], v[144:159]\n"
"v_cvt_pk_bf16_f32 v181, 0, v181\n"
"v_cvt_pk_bf16_f32 v177, v178, v179\n"
"v_exp_f32 v180, v180\n"
"v_fma_f32 v181, v181, s73, v229\n"
"v_cvt_pk_bf16_f32 v182, 0, v182\n"
"v_mfma_f32_32x32x16_bf16 v[144:159], a[92:95], a[60:63], v[144:159]\n"
"s_waitcnt lgkmcnt(2)\n"
"v_exp_f32 v181, v181\n"
"v_fma_f32 v182, v182, s73, v229\n"
"v_cvt_pk_bf16_f32 v183, 0, v183\n"
"v_cvt_pk_bf16_f32 v178, v180, v181\n"
"v_mfma_f32_32x32x16_bf16 v[0:15], a[96:99], v[160:163], v[0:15]\n"
"ds_read_b128 a[64:67], v216 offset:40960\n"
"v_exp_f32 v182, v182\n"
"v_fma_f32 v183, v183, s73, v229\n"
"v_cvt_pk_bf16_f32 v184, 0, v184\n"
"v_mfma_f32_32x32x16_bf16 v[32:47], a[100:103], v[160:163], v[32:47]\n"
"s_add_u32 m0, s67, 16384\n"
"ds_read_b128 a[68:71], v217 offset:40960\n"
"v_exp_f32 v183, v183\n"
"v_fma_f32 v184, v184, s73, v229\n"
"v_cvt_pk_bf16_f32 v185, 0, v185\n"
"v_cvt_pk_bf16_f32 v179, v182, v183\n"
"v_mfma_f32_16x16x32_bf16 v[200:203], v[248:251], v[160:163], v[200:203]\n"
"v_exp_f32 v184, v184\n"
"v_mfma_f32_32x32x16_bf16 v[64:79], a[104:107], v[160:163], v[64:79]\n"
"buffer_load_dwordx4 v213, s[52:55], s76 offen offset:0 lds\n"
"ds_read_b128 a[72:75], v218 offset:40960\n"
"v_fma_f32 v185, v185, s73, v229\n"
"v_cvt_pk_bf16_f32 v186, 0, v186\n"
"v_mfma_f32_32x32x16_bf16 v[96:111], a[108:111], v[160:163], v[96:111]\n"
"s_mov_b32 s40, s24\n"
"ds_read_b128 a[76:79], v219 offset:40960\n"
"v_exp_f32 v185, v185\n"
"v_fma_f32 v186, v186, s73, v229\n"
"v_cvt_pk_bf16_f32 v187, 0, v187\n"
"v_cvt_pk_bf16_f32 v184, v184, v185\n"
"v_mfma_f32_32x32x16_bf16 v[16:31], a[96:99], v[176:179], v[16:31]\n"
"s_and_b32 s41, s25, 0xffff\n"
"buffer_load_dwordx4 v213, s[52:55], s76 offen offset:1024 lds\n"
"ds_read_b128 a[80:83], v220 offset:40960\n"
"v_exp_f32 v186, v186\n"
"v_fma_f32 v187, v187, s73, v229\n"
"v_mfma_f32_32x32x16_bf16 v[48:63], a[100:103], v[176:179], v[48:63]\n"
"s_mov_b32 s42, 0x1c0000\n"
"ds_read_b128 a[84:87], v221 offset:40960\n"
"v_cvt_pk_bf16_f32 v188, 0, v188\n"
"v_exp_f32 v187, v187\n"
"v_fma_f32 v188, v188, s73, v229\n"
"v_cvt_pk_bf16_f32 v189, 0, v189\n"
"v_mfma_f32_16x16x32_bf16 v[244:247], v[248:251], v[176:179], v[244:247]\n"
"v_cvt_pk_bf16_f32 v185, v186, v187\n"
"v_mfma_f32_32x32x16_bf16 v[80:95], a[104:107], v[176:179], v[80:95]\n"
"s_mov_b32 s43, 0x20000\n"
"ds_read_b128 a[88:91], v222 offset:40960\n"
"v_exp_f32 v188, v188\n"
"v_fma_f32 v189, v189, s73, v229\n"
"v_cvt_pk_bf16_f32 v190, 0, v190\n"
"v_mfma_f32_32x32x16_bf16 v[112:127], a[108:111], v[176:179], v[112:127]\n"
"s_lshl_b32 s30, s6, 2\n"
"buffer_load_dwordx4 v213, s[52:55], s76 offen offset:2048 lds\n"
"ds_read_b128 a[92:95], v223 offset:40960\n"
"v_exp_f32 v189, v189\n"
"v_fma_f32 v190, v190, s73, v229\n"
"v_mfma_f32_32x32x16_bf16 v[0:15], a[112:115], v[168:171], v[0:15]\n"
"s_add_u32 s30, s30, s64\n"
"ds_read_b64_tr_b16 a[96:97], v224 offset:57344\n"
"v_cvt_pk_bf16_f32 v191, 0, v191\n"
"v_cvt_pk_bf16_f32 v186, v188, v189\n"
"v_exp_f32 v190, v190\n"
"v_fma_f32 v191, v191, s73, v229\n"
"v_mfma_f32_32x32x16_bf16 v[32:47], a[116:119], v[168:171], v[32:47]\n"
"s_mul_i32 s30, s30, 1792\n"
"buffer_load_dwordx4 v213, s[52:55], s76 offen offset:3072 lds\n"
"ds_read_b64_tr_b16 a[98:99], v224 offset:59392\n"
"v_cvt_pk_bf16_f32 v128, 0, v128\n"
"v_exp_f32 v191, v191\n"
"v_mfma_f32_16x16x32_bf16 v[200:203], v[248:251], v[168:171], v[200:203]\n"
"v_fma_f32 v128, v128, s73, v228\n"
"v_cvt_pk_bf16_f32 v129, 0, v129\n"
"v_mfma_f32_32x32x16_bf16 v[64:79], a[120:123], v[168:171], v[64:79]\n"
"s_add_u32 s76, s76, 0x4000\n"
"ds_read_b64_tr_b16 a[100:101], v225 offset:57344\n"
"v_cvt_pk_bf16_f32 v187, v190, v191\n"
"v_exp_f32 v128, v128\n"
"v_fma_f32 v129, v129, s73, v228\n"
"v_cvt_pk_bf16_f32 v130, 0, v130\n"
"v_mfma_f32_32x32x16_bf16 v[96:111], a[124:127], v[168:171], v[96:111]\n"
"s_and_b32 s76, s76, 0x1ffff\n"
"ds_read_b64_tr_b16 a[102:103], v225 offset:59392\n"
"v_exp_f32 v129, v129\n"
"v_fma_f32 v130, v130, s73, v228\n"
"v_cvt_pk_bf16_f32 v131, 0, v131\n"
"v_min_u32 v239, 27, v192\n"
"v_mfma_f32_32x32x16_bf16 v[16:31], a[112:115], v[184:187], v[16:31]\n"
"ds_read_b64_tr_b16 a[104:105], v226 offset:57344\n"
"v_cvt_pk_bf16_f32 v128, v128, v129\n"
"v_exp_f32 v130, v130\n"
"v_fma_f32 v131, v131, s73, v228\n"
"v_lshlrev_b32 v239, 6, v239\n"
"v_mfma_f32_32x32x16_bf16 v[48:63], a[116:119], v[184:187], v[48:63]\n"
"ds_read_b64_tr_b16 a[160:161], v232 offset:32768\n"
"buffer_load_dword v255, v239, s[40:43], s30 offen\n"
"ds_read_b64_tr_b16 a[106:107], v226 offset:59392\n"
"v_cvt_pk_bf16_f32 v132, 0, v132\n"
"v_mfma_f32_16x16x32_bf16 v[244:247], v[248:251], v[184:187], v[244:247]\n"
"v_exp_f32 v131, v131\n"
"v_mfma_f32_32x32x16_bf16 v[80:95], a[120:123], v[184:187], v[80:95]\n"
"ds_read_b64_tr_b16 a[108:109], v227 offset:57344\n"
"v_fma_f32 v132, v132, s73, v228\n"
"v_cvt_pk_bf16_f32 v133, 0, v133\n"
"v_cvt_pk_bf16_f32 v129, v130, v131\n"
"v_exp_f32 v132, v132\n"
"v_mfma_f32_32x32x16_bf16 v[112:127], a[124:127], v[184:187], v[112:127]\n"
"s_waitcnt lgkmcnt(8)\n"
"ds_read_b64_tr_b16 a[162:163], v232 offset:33280\n"
"ds_read_b64_tr_b16 a[110:111], v227 offset:59392\n"
"v_fma_f32 v133, v133, s73, v228\n"
"v_cvt_pk_bf16_f32 v134, 0, v134\n"
"v_exp_f32 v133, v133\n"
"v_mfma_f32_32x32x16_bf16 v[160:175], a[64:67], a[0:3], 0\n"
"ds_read_b64_tr_b16 a[112:113], v224 offset:61440\n"
"v_fma_f32 v134, v134, s73, v228\n"
"v_cvt_pk_bf16_f32 v135, 0, v135\n"
"v_cvt_pk_bf16_f32 v130, v132, v133\n"
"v_exp_f32 v134, v134\n"
"v_mfma_f32_32x32x16_bf16 v[160:175], a[68:71], a[4:7], v[160:175]\n"
"ds_read_b64_tr_b16 a[164:165], v233 offset:32768\n"
"ds_read_b64_tr_b16 a[114:115], v224 offset:63488\n"
"v_fma_f32 v135, v135, s73, v228\n"
"v_cvt_pk_bf16_f32 v136, 0, v136\n"
"v_exp_f32 v135, v135\n"
"v_mfma_f32_32x32x16_bf16 v[160:175], a[72:75], a[8:11], v[160:175]\n"
"ds_read_b64_tr_b16 a[116:117], v225 offset:61440\n"
"v_fma_f32 v136, v136, s73, v228\n"
"v_cvt_pk_bf16_f32 v137, 0, v137\n"
"v_cvt_pk_bf16_f32 v131, v134, v135\n"
"v_exp_f32 v136, v136\n"
"v_mfma_f32_32x32x16_bf16 v[160:175], a[76:79], a[12:15], v[160:175]\n"
"ds_read_b64_tr_b16 a[166:167], v233 offset:33280\n"
"ds_read_b64_tr_b16 a[118:119], v225 offset:63488\n"
"v_fma_f32 v137, v137, s73, v228\n"
"v_cvt_pk_bf16_f32 v138, 0, v138\n"
"v_exp_f32 v137, v137\n"
"v_mfma_f32_32x32x16_bf16 v[160:175], a[80:83], a[16:19], v[160:175]\n"
"s_add_u32 s74, s68, 294912\n"
"ds_read_b64_tr_b16 a[120:121], v226 offset:61440\n"
"v_fma_f32 v138, v138, s73, v228\n"
"v_cvt_pk_bf16_f32 v139, 0, v139\n"
"v_cvt_pk_bf16_f32 v136, v136, v137\n"
"v_exp_f32 v138, v138\n"
"v_mfma_f32_32x32x16_bf16 v[160:175], a[84:87], a[20:23], v[160:175]\n"
"s_and_b32 s74, s74, 0x7ffff\n"
"ds_read_b64_tr_b16 a[168:169], v232 offset:32776\n"
"ds_read_b64_tr_b16 a[122:123], v226 offset:63488\n"
"v_fma_f32 v139, v139, s73, v228\n"
"v_cvt_pk_bf16_f32 v140, 0, v140\n"
"v_exp_f32 v139, v139\n"
"v_mfma_f32_32x32x16_bf16 v[160:175], a[88:91], a[24:27], v[160:175]\n"
"s_add_u32 s74, s74, s69\n"
"ds_read_b64_tr_b16 a[124:125], v227 offset:61440\n"
"v_fma_f32 v140, v140, s73, v228\n"
"v_cvt_pk_bf16_f32 v141, 0, v141\n"
"v_cvt_pk_bf16_f32 v137, v138, v139\n"
"v_exp_f32 v140, v140\n"
"v_mfma_f32_32x32x16_bf16 v[160:175], a[92:95], a[28:31], v[160:175]\n"
"s_add_u32 m0, s67, 147456\n"
"ds_read_b64_tr_b16 a[170:171], v232 offset:33288\n"
"ds_read_b64_tr_b16 a[126:127], v227 offset:63488\n"
"v_fma_f32 v141, v141, s73, v228\n"
"v_cvt_pk_bf16_f32 v142, 0, v142\n"
"v_exp_f32 v141, v141\n"
"v_mfma_f32_32x32x16_bf16 v[176:191], a[64:67], a[32:35], 0\n"
"buffer_load_dwordx4 v205, s[56:59], s74 offen offset:0 lds\n"
"v_fma_f32 v142, v142, s73, v228\n"
"v_cvt_pk_bf16_f32 v143, 0, v143\n"
"v_cvt_pk_bf16_f32 v138, v140, v141\n"
"v_mfma_f32_32x32x16_bf16 v[176:191], a[68:71], a[36:39], v[176:191]\n"
"buffer_load_dwordx4 v206, s[56:59], s74 offen offset:1024 lds\n"
"v_exp_f32 v142, v142\n"
"v_fma_f32 v143, v143, s73, v228\n"
"v_cvt_pk_bf16_f32 v144, 0, v144\n"
"v_mfma_f32_32x32x16_bf16 v[176:191], a[72:75], a[40:43], v[176:191]\n"
"ds_read_b64_tr_b16 a[172:173], v233 offset:32776\n"
"v_exp_f32 v143, v143\n"
"v_fma_f32 v144, v144, s73, v229\n"
"v_cvt_pk_bf16_f32 v145, 0, v145\n"
"v_cvt_pk_bf16_f32 v139, v142, v143\n"
"v_mfma_f32_32x32x16_bf16 v[176:191], a[76:79], a[44:47], v[176:191]\n"
"buffer_load_dwordx4 v207, s[56:59], s74 offen offset:2048 lds\n"
"v_exp_f32 v144, v144\n"
"v_fma_f32 v145, v145, s73, v229\n"
"v_cvt_pk_bf16_f32 v146, 0, v146\n"
"v_mfma_f32_32x32x16_bf16 v[176:191], a[80:83], a[48:51], v[176:191]\n"
"ds_read_b64_tr_b16 a[174:175], v233 offset:33288\n"
"v_exp_f32 v145, v145\n"
"v_fma_f32 v146, v146, s73, v229\n"
"v_cvt_pk_bf16_f32 v147, 0, v147\n"
"v_cvt_pk_bf16_f32 v144, v144, v145\n"
"v_mfma_f32_32x32x16_bf16 v[176:191], a[84:87], a[52:55], v[176:191]\n"
"buffer_load_dwordx4 v208, s[56:59], s74 offen offset:3072 lds\n"
"v_exp_f32 v146, v146\n"
"v_fma_f32 v147, v147, s73, v229\n"
"v_cvt_pk_bf16_f32 v148, 0, v148\n"
"v_mfma_f32_32x32x16_bf16 v[176:191], a[88:91], a[56:59], v[176:191]\n"
"v_exp_f32 v147, v147\n"
"v_fma_f32 v148, v148, s73, v229\n"
"v_cvt_pk_bf16_f32 v149, 0, v149\n"
"v_cvt_pk_bf16_f32 v145, v146, v147\n"
"v_mfma_f32_32x32x16_bf16 v[176:191], a[92:95], a[60:63], v[176:191]\n"
"s_waitcnt vmcnt(5) lgkmcnt(0)\n"
"v_exp_f32 v148, v148\n"
"v_fma_f32 v149, v149, s73, v229\n"
"v_cvt_pk_bf16_f32 v150, 0, v150\n"
"s_barrier\n"
"v_mfma_f32_32x32x16_bf16 v[0:15], a[96:99], v[128:131], v[0:15]\n"
"ds_read_b128 a[64:67], v216 offset:0\n"
"v_exp_f32 v149, v149\n"
"v_fma_f32 v150, v150, s73, v229\n"
"v_cvt_pk_bf16_f32 v151, 0, v151\n"
"v_cvt_pk_bf16_f32 v146, v148, v149\n"
"v_mfma_f32_32x32x16_bf16 v[32:47], a[100:103], v[128:131], v[32:47]\n"
"s_add_u32 m0, s67, 32768\n"
"ds_read_b128 a[68:71], v217 offset:0\n"
"v_exp_f32 v150, v150\n"
"v_fma_f32 v151, v151, s73, v229\n"
"v_cvt_pk_bf16_f32 v152, 0, v152\n"
"v_mfma_f32_16x16x32_bf16 v[200:203], v[248:251], v[128:131], v[200:203]\n"
"Lst6_%=:\n"
"s_cbranch_execz Lst10_%=\n"
"v_exp_f32 v151, v151\n"
"v_mfma_f32_32x32x16_bf16 v[64:79], a[104:107], v[128:131], v[64:79]\n"
"buffer_load_dwordx4 v209, s[48:51], s75 offen offset:0 lds\n"
"ds_read_b128 a[72:75], v218 offset:0\n"
"v_fma_f32 v152, v152, s73, v229\n"
"v_cvt_pk_bf16_f32 v153, 0, v153\n"
"v_cvt_pk_bf16_f32 v147, v150, v151\n"
"v_mfma_f32_32x32x16_bf16 v[96:111], a[108:111], v[128:131], v[96:111]\n"
"ds_read_b128 a[76:79], v219 offset:0\n"
"v_exp_f32 v152, v152\n"
"v_fma_f32 v153, v153, s73, v229\n"
"v_cvt_pk_bf16_f32 v154, 0, v154\n"
"v_mfma_f32_32x32x16_bf16 v[16:31], a[96:99], v[144:147], v[16:31]\n"
"buffer_load_dwordx4 v210, s[48:51], s75 offen offset:1024 lds\n"
"ds_read_b128 a[80:83], v220 offset:0\n"
"v_exp_f32 v153, v153\n"
"v_fma_f32 v154, v154, s73, v229\n"
"v_mfma_f32_32x32x16_bf16 v[48:63], a[100:103], v[144:147], v[48:63]\n"
"Lcp_6_%=:\n"
"ds_read_b128 a[84:87], v221 offset:0\n"
"v_cvt_pk_bf16_f32 v155, 0, v155\n"
"v_cvt_pk_bf16_f32 v152, v152, v153\n"
"v_exp_f32 v154, v154\n"
"v_fma_f32 v155, v155, s73, v229\n"
"v_mfma_f32_16x16x32_bf16 v[244:247], v[248:251], v[144:147], v[244:247]\n"
"v_cvt_pk_bf16_f32 v156, 0, v156\n"
"v_mfma_f32_32x32x16_bf16 v[80:95], a[104:107], v[144:147], v[80:95]\n"
"s_lshl_b32 s30, s77, 2\n"
"ds_read_b128 a[88:91], v222 offset:0\n"
"v_exp_f32 v155, v155\n"
"v_fma_f32 v156, v156, s73, v229\n"
"v_cvt_pk_bf16_f32 v157, 0, v157\n"
"v_cvt_pk_bf16_f32 v153, v154, v155\n"
"v_mfma_f32_32x32x16_bf16 v[112:127], a[108:111], v[144:147], v[112:127]\n"
"s_add_u32 s30, s30, s64\n"
"buffer_load_dwordx4 v211, s[48:51], s75 offen offset:2048 lds\n"
"ds_read_b128 a[92:95], v223 offset:0\n"
"v_exp_f32 v156, v156\n"
"v_fma_f32 v157, v157, s73, v229\n"
"v_mfma_f32_32x32x16_bf16 v[0:15], a[112:115], v[136:139], v[0:15]\n"
"s_mul_i32 s30, s30, 8192\n"
"ds_read_b64_tr_b16 a[96:97], v224 offset:16384\n"
"v_cvt_pk_bf16_f32 v158, 0, v158\n"
"v_exp_f32 v157, v157\n"
"v_fma_f32 v158, v158, s73, v229\n"
"v_cvt_pk_bf16_f32 v159, 0, v159\n"
"v_mfma_f32_32x32x16_bf16 v[32:47], a[116:119], v[136:139], v[32:47]\n"
"s_add_u32 s30, s30, s69\n"
"buffer_load_dwordx4 v212, s[48:51], s75 offen offset:3072 lds\n"
"ds_read_b64_tr_b16 a[98:99], v224 offset:18432\n"
"v_cvt_pk_bf16_f32 v154, v156, v157\n"
"v_exp_f32 v158, v158\n"
"v_mfma_f32_16x16x32_bf16 v[200:203], v[248:251], v[136:139], v[200:203]\n"
"v_fma_f32 v159, v159, s73, v229\n"
"v_cvt_pk_bf16_f32 v160, 0, v160\n"
"v_mfma_f32_32x32x16_bf16 v[64:79], a[120:123], v[136:139], v[64:79]\n"
"s_add_u32 s75, s75, 0x4000\n"
"ds_read_b64_tr_b16 a[100:101], v225 offset:16384\n"
"v_exp_f32 v159, v159\n"
"v_fma_f32 v160, v160, s73, v228\n"
"v_cvt_pk_bf16_f32 v161, 0, v161\n"
"v_cvt_pk_bf16_f32 v155, v158, v159\n"
"v_mfma_f32_32x32x16_bf16 v[96:111], a[124:127], v[136:139], v[96:111]\n"
"s_and_b32 s75, s75, 0x1ffff\n"
"ds_read_b64_tr_b16 a[102:103], v225 offset:18432\n"
"v_exp_f32 v160, v160\n"
"v_fma_f32 v161, v161, s73, v228\n"
"v_cvt_pk_bf16_f32 v162, 0, v162\n"
"v_mfma_f32_32x32x16_bf16 v[16:31], a[112:115], v[152:155], v[16:31]\n"
"ds_read_b64_tr_b16 a[104:105], v226 offset:16384\n"
"v_exp_f32 v161, v161\n"
"v_fma_f32 v162, v162, s73, v228\n"
"v_min_u32 v238, 1984, v238\n"
"v_lshl_add_u32 v254, v192, 2, s7\n"
"v_mfma_f32_32x32x16_bf16 v[48:63], a[116:119], v[152:155], v[48:63]\n"
"ds_read_b64_tr_b16 a[176:177], v232 offset:49152\n"
"buffer_load_dword v255, v238, s[56:59], s30 offen\n"
"ds_read_b64_tr_b16 a[106:107], v226 offset:18432\n"
"v_cvt_pk_bf16_f32 v163, 0, v163\n"
"v_add_u32 v254, Lcp_6_%=-Lpc0_%=+1024, v254\n"
"v_mfma_f32_16x16x32_bf16 v[244:247], v[248:251], v[152:155], v[244:247]\n"
"v_cvt_pk_bf16_f32 v160, v160, v161\n"
"v_mfma_f32_32x32x16_bf16 v[80:95], a[120:123], v[152:155], v[80:95]\n"
"ds_read_b64_tr_b16 a[108:109], v227 offset:16384\n"
"v_exp_f32 v162, v162\n"
"v_fma_f32 v163, v163, s73, v228\n"
"v_cvt_pk_bf16_f32 v164, 0, v164\n"
"v_min_u32 v254, Lfin_%=-Lpc0_%=, v254\n"
"v_mfma_f32_32x32x16_bf16 v[112:127], a[124:127], v[152:155], v[112:127]\n"
"s_waitcnt lgkmcnt(8)\n"
"ds_read_b64_tr_b16 a[178:179], v232 offset:49664\n"
"global_load_dword v255, v254, s[18:19]\n"
"ds_read_b64_tr_b16 a[110:111], v227 offset:18432\n"
"v_exp_f32 v163, v163\n"
"v_mfma_f32_32x32x16_bf16 v[128:143], a[64:67], a[0:3], 0\n"
"ds_read_b64_tr_b16 a[112:113], v224 offset:20480\n"
"v_fma_f32 v164, v164, s73, v228\n"
"v_cvt_pk_bf16_f32 v165, 0, v165\n"
"v_cvt_pk_bf16_f32 v161, v162, v163\n"
"v_exp_f32 v164, v164\n"
"v_mfma_f32_32x32x16_bf16 v[128:143], a[68:71], a[4:7], v[128:143]\n"
"ds_read_b64_tr_b16 a[180:181], v233 offset:49152\n"
"ds_read_b64_tr_b16 a[114:115], v224 offset:22528\n"
"v_fma_f32 v165, v165, s73, v228\n"
"v_cvt_pk_bf16_f32 v166, 0, v166\n"
"v_exp_f32 v165, v165\n"
"v_mfma_f32_32x32x16_bf16 v[128:143], a[72:75], a[8:11], v[128:143]\n"
"ds_read_b64_tr_b16 a[116:117], v225 offset:20480\n"
"v_fma_f32 v166, v166, s73, v228\n"
"v_cvt_pk_bf16_f32 v167, 0, v167\n"
"v_cvt_pk_bf16_f32 v162, v164, v165\n"
"v_exp_f32 v166, v166\n"
"v_mfma_f32_32x32x16_bf16 v[128:143], a[76:79], a[12:15], v[128:143]\n"
"ds_read_b64_tr_b16 a[182:183], v233 offset:49664\n"
"ds_read_b64_tr_b16 a[118:119], v225 offset:22528\n"
"v_fma_f32 v167, v167, s73, v228\n"
"v_cvt_pk_bf16_f32 v168, 0, v168\n"
"v_exp_f32 v167, v167\n"
"v_mfma_f32_32x32x16_bf16 v[128:143], a[80:83], a[16:19], v[128:143]\n"
"s_add_u32 s74, s68, 458752\n"
"ds_read_b64_tr_b16 a[120:121], v226 offset:20480\n"
"v_fma_f32 v168, v168, s73, v228\n"
"v_cvt_pk_bf16_f32 v169, 0, v169\n"
"v_cvt_pk_bf16_f32 v163, v166, v167\n"
"v_exp_f32 v168, v168\n"
"v_mfma_f32_32x32x16_bf16 v[128:143], a[84:87], a[20:23], v[128:143]\n"
"s_and_b32 s74, s74, 0x7ffff\n"
"ds_read_b64_tr_b16 a[184:185], v232 offset:49160\n"
"ds_read_b64_tr_b16 a[122:123], v226 offset:22528\n"
"v_fma_f32 v169, v169, s73, v228\n"
"v_cvt_pk_bf16_f32 v170, 0, v170\n"
"v_exp_f32 v169, v169\n"
"v_mfma_f32_32x32x16_bf16 v[128:143], a[88:91], a[24:27], v[128:143]\n"
"s_add_u32 s74, s74, s69\n"
"ds_read_b64_tr_b16 a[124:125], v227 offset:20480\n"
"v_fma_f32 v170, v170, s73, v228\n"
"v_cvt_pk_bf16_f32 v171, 0, v171\n"
"v_cvt_pk_bf16_f32 v168, v168, v169\n"
"v_exp_f32 v170, v170\n"
"v_mfma_f32_32x32x16_bf16 v[128:143], a[92:95], a[28:31], v[128:143]\n"
"s_add_u32 m0, s67, 65536\n"
"ds_read_b64_tr_b16 a[186:187], v232 offset:49672\n"
"ds_read_b64_tr_b16 a[126:127], v227 offset:22528\n"
"v_fma_f32 v171, v171, s73, v228\n"
"v_cvt_pk_bf16_f32 v172, 0, v172\n"
"v_exp_f32 v171, v171\n"
"v_mfma_f32_32x32x16_bf16 v[144:159], a[64:67], a[32:35], 0\n"
"buffer_load_dwordx4 v205, s[56:59], s74 offen offset:0 lds\n"
"v_fma_f32 v172, v172, s73, v228\n"
"v_cvt_pk_bf16_f32 v173, 0, v173\n"
"v_cvt_pk_bf16_f32 v169, v170, v171\n"
"v_exp_f32 v172, v172\n"
"v_mfma_f32_32x32x16_bf16 v[144:159], a[68:71], a[36:39], v[144:159]\n"
"buffer_load_dwordx4 v206, s[56:59], s74 offen offset:1024 lds\n"
"v_fma_f32 v173, v173, s73, v228\n"
"v_cvt_pk_bf16_f32 v174, 0, v174\n"
"v_exp_f32 v173, v173\n"
"v_fma_f32 v174, v174, s73, v228\n"
"v_mfma_f32_32x32x16_bf16 v[144:159], a[72:75], a[40:43], v[144:159]\n"
"ds_read_b64_tr_b16 a[188:189], v233 offset:49160\n"
"v_cvt_pk_bf16_f32 v175, 0, v175\n"
"v_cvt_pk_bf16_f32 v170, v172, v173\n"
"v_exp_f32 v174, v174\n"
"v_fma_f32 v175, v175, s73, v228\n"
"v_cvt_pk_bf16_f32 v176, 0, v176\n"
"v_mfma_f32_32x32x16_bf16 v[144:159], a[76:79], a[44:47], v[144:159]\n"
"buffer_load_dwordx4 v207, s[56:59], s74 offen offset:2048 lds\n"
"v_exp_f32 v175, v175\n"
"v_fma_f32 v176, v176, s73, v229\n"
"v_cvt_pk_bf16_f32 v177, 0, v177\n"
"v_cvt_pk_bf16_f32 v171, v174, v175\n"
"v_mfma_f32_32x32x16_bf16 v[144:159], a[80:83], a[48:51], v[144:159]\n"
"ds_read_b64_tr_b16 a[190:191], v233 offset:49672\n"
"v_exp_f32 v176, v176\n"
"v_fma_f32 v177, v177, s73, v229\n"
"v_cvt_pk_bf16_f32 v178, 0, v178\n"
"v_exp_f32 v177, v177\n"
"v_mfma_f32_32x32x16_bf16 v[144:159], a[84:87], a[52:55], v[144:159]\n"
"buffer_load_dwordx4 v208, s[56:59], s74 offen offset:3072 lds\n"
"v_fma_f32 v178, v178, s73, v229\n"
"v_cvt_pk_bf16_f32 v179, 0, v179\n"
"v_cvt_pk_bf16_f32 v176, v176, v177\n"
"v_exp_f32 v178, v178\n"
"v_mfma_f32_32x32x16_bf16 v[144:159], a[88:91], a[56:59], v[144:159]\n"
"v_fma_f32 v179, v179, s73, v229\n"
"v_cvt_pk_bf16_f32 v180, 0, v180\n"
"v_exp_f32 v179, v179\n"
"v_fma_f32 v180, v180, s73, v229\n"
"v_cvt_pk_bf16_f32 v181, 0, v181\n"
"v_cvt_pk_bf16_f32 v177, v178, v179\n"
"v_mfma_f32_32x32x16_bf16 v[144:159], a[92:95], a[60:63], v[144:159]\n"
"s_waitcnt lgkmcnt(2)\n"
"v_exp_f32 v180, v180\n"
"v_fma_f32 v181, v181, s73, v229\n"
"v_cvt_pk_bf16_f32 v182, 0, v182\n"
"v_exp_f32 v181, v181\n"
"v_fma_f32 v182, v182, s73, v229\n"
"v_mfma_f32_32x32x16_bf16 v[0:15], a[96:99], v[160:163], v[0:15]\n"
"ds_read_b128 a[64:67], v216 offset:8192\n"
"v_cvt_pk_bf16_f32 v183, 0, v183\n"
"v_cvt_pk_bf16_f32 v178, v180, v181\n"
"v_exp_f32 v182, v182\n"
"v_fma_f32 v183, v183, s73, v229\n"
"v_cvt_pk_bf16_f32 v184, 0, v184\n"
"v_mfma_f32_32x32x16_bf16 v[32:47], a[100:103], v[160:163], v[32:47]\n"
"s_add_u32 m0, s67, 49152\n"
"ds_read_b128 a[68:71], v217 offset:8192\n"
"v_exp_f32 v183, v183\n"
"v_fma_f32 v184, v184, s73, v229\n"
"v_cvt_pk_bf16_f32 v185, 0, v185\n"
"v_cvt_pk_bf16_f32 v179, v182, v183\n"
"v_mfma_f32_16x16x32_bf16 v[200:203], v[248:251], v[160:163], v[200:203]\n"
"v_exp_f32 v184, v184\n"
"v_mfma_f32_32x32x16_bf16 v[64:79], a[104:107], v[160:163], v[64:79]\n"
"buffer_load_dwordx4 v213, s[52:55], s76 offen offset:0 lds\n"
"ds_read_b128 a[72:75], v218 offset:8192\n"
"v_fma_f32 v185, v185, s73, v229\n"
"v_cvt_pk_bf16_f32 v186, 0, v186\n"
"v_exp_f32 v185, v185\n"
"v_mfma_f32_32x32x16_bf16 v[96:111], a[108:111], v[160:163], v[96:111]\n"
"ds_read_b128 a[76:79], v219 offset:8192\n"
"v_fma_f32 v186, v186, s73, v229\n"
"v_cvt_pk_bf16_f32 v187, 0, v187\n"
"v_cvt_pk_bf16_f32 v184, v184, v185\n"
"v_exp_f32 v186, v186\n"
"v_mfma_f32_32x32x16_bf16 v[16:31], a[96:99], v[176:179], v[16:31]\n"
"buffer_load_dwordx4 v213, s[52:55], s76 offen offset:1024 lds\n"
"ds_read_b128 a[80:83], v220 offset:8192\n"
"v_fma_f32 v187, v187, s73, v229\n"
"v_cvt_pk_bf16_f32 v188, 0, v188\n"
"v_mfma_f32_32x32x16_bf16 v[48:63], a[100:103], v[176:179], v[48:63]\n"
"ds_read_b128 a[84:87], v221 offset:8192\n"
"v_exp_f32 v187, v187\n"
"v_fma_f32 v188, v188, s73, v229\n"
"v_cvt_pk_bf16_f32 v189, 0, v189\n"
"v_cvt_pk_bf16_f32 v185, v186, v187\n"
"v_mfma_f32_16x16x32_bf16 v[244:247], v[248:251], v[176:179], v[244:247]\n"
"v_exp_f32 v188, v188\n"
"v_mfma_f32_32x32x16_bf16 v[80:95], a[104:107], v[176:179], v[80:95]\n"
"ds_read_b128 a[88:91], v222 offset:8192\n"
"v_fma_f32 v189, v189, s73, v229\n"
"v_cvt_pk_bf16_f32 v190, 0, v190\n"
"v_exp_f32 v189, v189\n"
"v_fma_f32 v190, v190, s73, v229\n"
"v_mfma_f32_32x32x16_bf16 v[112:127], a[108:111], v[176:179], v[112:127]\n"
"buffer_load_dwordx4 v213, s[52:55], s76 offen offset:2048 lds\n"
"ds_read_b128 a[92:95], v223 offset:8192\n"
"v_cvt_pk_bf16_f32 v191, 0, v191\n"
"v_cvt_pk_bf16_f32 v186, v188, v189\n"
"v_mfma_f32_32x32x16_bf16 v[0:15], a[112:115], v[168:171], v[0:15]\n"
"ds_read_b64_tr_b16 a[96:97], v224 offset:24576\n"
"v_exp_f32 v190, v190\n"
"v_fma_f32 v191, v191, s73, v229\n"
"v_cvt_pk_bf16_f32 v128, 0, v128\n"
"v_mfma_f32_32x32x16_bf16 v[32:47], a[116:119], v[168:171], v[32:47]\n"
"s_add_u32 s30, s30, 2048\n"
"buffer_load_dwordx4 v213, s[52:55], s76 offen offset:3072 lds\n"
"ds_read_b64_tr_b16 a[98:99], v224 offset:26624\n"
"v_exp_f32 v191, v191\n"
"v_fma_f32 v128, v128, s73, v228\n"
"v_mfma_f32_16x16x32_bf16 v[200:203], v[248:251], v[168:171], v[200:203]\n"
"v_cvt_pk_bf16_f32 v129, 0, v129\n"
"v_cvt_pk_bf16_f32 v187, v190, v191\n"
"v_mfma_f32_32x32x16_bf16 v[64:79], a[120:123], v[168:171], v[64:79]\n"
"s_add_u32 s76, s76, 0x4000\n"
"ds_read_b64_tr_b16 a[100:101], v225 offset:24576\n"
"v_exp_f32 v128, v128\n"
"v_fma_f32 v129, v129, s73, v228\n"
"v_cvt_pk_bf16_f32 v130, 0, v130\n"
"v_mfma_f32_32x32x16_bf16 v[96:111], a[124:127], v[168:171], v[96:111]\n"
"s_and_b32 s76, s76, 0x1ffff\n"
"ds_read_b64_tr_b16 a[102:103], v225 offset:26624\n"
"v_exp_f32 v129, v129\n"
"v_fma_f32 v130, v130, s73, v228\n"
"v_cvt_pk_bf16_f32 v131, 0, v131\n"
"v_cvt_pk_bf16_f32 v128, v128, v129\n"
"v_mfma_f32_32x32x16_bf16 v[16:31], a[112:115], v[184:187], v[16:31]\n"
"ds_read_b64_tr_b16 a[104:105], v226 offset:24576\n"
"v_exp_f32 v130, v130\n"
"v_fma_f32 v131, v131, s73, v228\n"
"v_cvt_pk_bf16_f32 v132, 0, v132\n"
"v_mfma_f32_32x32x16_bf16 v[48:63], a[116:119], v[184:187], v[48:63]\n"
"ds_read_b64_tr_b16 a[192:193], v234 offset:0\n"
"buffer_load_dword v255, v238, s[56:59], s30 offen\n"
"ds_read_b64_tr_b16 a[106:107], v226 offset:26624\n"
"v_exp_f32 v131, v131\n"
"v_mfma_f32_16x16x32_bf16 v[244:247], v[248:251], v[184:187], v[244:247]\n"
"v_fma_f32 v132, v132, s73, v228\n"
"v_cvt_pk_bf16_f32 v133, 0, v133\n"
"v_mfma_f32_32x32x16_bf16 v[80:95], a[120:123], v[184:187], v[80:95]\n"
"ds_read_b64_tr_b16 a[108:109], v227 offset:24576\n"
"v_cvt_pk_bf16_f32 v129, v130, v131\n"
"v_exp_f32 v132, v132\n"
"v_fma_f32 v133, v133, s73, v228\n"
"v_cvt_pk_bf16_f32 v134, 0, v134\n"
"v_mfma_f32_32x32x16_bf16 v[112:127], a[124:127], v[184:187], v[112:127]\n"
"s_waitcnt lgkmcnt(8)\n"
"ds_read_b64_tr_b16 a[194:195], v234 offset:512\n"
"ds_read_b64_tr_b16 a[110:111], v227 offset:26624\n"
"v_exp_f32 v133, v133\n"
"v_fma_f32 v134, v134, s73, v228\n"
"v_cvt_pk_bf16_f32 v135, 0, v135\n"
"v_mfma_f32_32x32x16_bf16 v[160:175], a[64:67], a[0:3], 0\n"
"ds_read_b64_tr_b16 a[112:113], v224 offset:28672\n"
"v_cvt_pk_bf16_f32 v130, v132, v133\n"
"v_exp_f32 v134, v134\n"
"v_fma_f32 v135, v135, s73, v228\n"
"v_cvt_pk_bf16_f32 v136, 0, v136\n"
"v_mfma_f32_32x32x16_bf16 v[160:175], a[68:71], a[4:7], v[160:175]\n"
"ds_read_b64_tr_b16 a[196:197], v235 offset:0\n"
"ds_read_b64_tr_b16 a[114:115], v224 offset:30720\n"
"v_exp_f32 v135, v135\n"
"v_fma_f32 v136, v136, s73, v228\n"
"v_cvt_pk_bf16_f32 v137, 0, v137\n"
"v_mfma_f32_32x32x16_bf16 v[160:175], a[72:75], a[8:11], v[160:175]\n"
"ds_read_b64_tr_b16 a[116:117], v225 offset:28672\n"
"v_cvt_pk_bf16_f32 v131, v134, v135\n"
"v_exp_f32 v136, v136\n"
"v_fma_f32 v137, v137, s73, v228\n"
"v_cvt_pk_bf16_f32 v138, 0, v138\n"
"v_mfma_f32_32x32x16_bf16 v[160:175], a[76:79], a[12:15], v[160:175]\n"
"ds_read_b64_tr_b16 a[198:199], v235 offset:512\n"
"ds_read_b64_tr_b16 a[118:119], v225 offset:30720\n"
"v_exp_f32 v137, v137\n"
"v_fma_f32 v138, v138, s73, v228\n"
"v_cvt_pk_bf16_f32 v139, 0, v139\n"
"v_mfma_f32_32x32x16_bf16 v[160:175], a[80:83], a[16:19], v[160:175]\n"
"s_add_u32 s74, s68, 131072\n"
"ds_read_b64_tr_b16 a[120:121], v226 offset:28672\n"
"v_cvt_pk_bf16_f32 v136, v136, v137\n"
"v_exp_f32 v138, v138\n"
"v_fma_f32 v139, v139, s73, v228\n"
"v_cvt_pk_bf16_f32 v140, 0, v140\n"
"v_mfma_f32_32x32x16_bf16 v[160:175], a[84:87], a[20:23], v[160:175]\n"
"s_and_b32 s74, s74, 0x7ffff\n"
"ds_read_b64_tr_b16 a[200:201], v234 offset:8\n"
"ds_read_b64_tr_b16 a[122:123], v226 offset:30720\n"
"v_exp_f32 v139, v139\n"
"v_fma_f32 v140, v140, s73, v228\n"
"v_cvt_pk_bf16_f32 v141, 0, v141\n"
"v_mfma_f32_32x32x16_bf16 v[160:175], a[88:91], a[24:27], v[160:175]\n"
"s_add_u32 s74, s74, s35\n"
"ds_read_b64_tr_b16 a[124:125], v227 offset:28672\n"
"v_cvt_pk_bf16_f32 v137, v138, v139\n"
"v_exp_f32 v140, v140\n"
"v_fma_f32 v141, v141, s73, v228\n"
"v_cvt_pk_bf16_f32 v142, 0, v142\n"
"v_mfma_f32_32x32x16_bf16 v[160:175], a[92:95], a[28:31], v[160:175]\n"
"s_add_u32 m0, s67, 81920\n"
"ds_read_b64_tr_b16 a[202:203], v234 offset:520\n"
"ds_read_b64_tr_b16 a[126:127], v227 offset:30720\n"
"v_exp_f32 v141, v141\n"
"v_fma_f32 v142, v142, s73, v228\n"
"v_cvt_pk_bf16_f32 v143, 0, v143\n"
"v_mfma_f32_32x32x16_bf16 v[176:191], a[64:67], a[32:35], 0\n"
"buffer_load_dwordx4 v205, s[56:59], s74 offen offset:0 lds\n"
"v_cvt_pk_bf16_f32 v138, v140, v141\n"
"v_exp_f32 v142, v142\n"
"v_fma_f32 v143, v143, s73, v228\n"
"v_mfma_f32_32x32x16_bf16 v[176:191], a[68:71], a[36:39], v[176:191]\n"
"buffer_load_dwordx4 v206, s[56:59], s74 offen offset:1024 lds\n"
"v_cvt_pk_bf16_f32 v144, 0, v144\n"
"v_exp_f32 v143, v143\n"
"v_fma_f32 v144, v144, s73, v229\n"
"v_mfma_f32_32x32x16_bf16 v[176:191], a[72:75], a[40:43], v[176:191]\n"
"ds_read_b64_tr_b16 a[204:205], v235 offset:8\n"
"v_cvt_pk_bf16_f32 v145, 0, v145\n"
"v_cvt_pk_bf16_f32 v139, v142, v143\n"
"v_exp_f32 v144, v144\n"
"v_fma_f32 v145, v145, s73, v229\n"
"v_mfma_f32_32x32x16_bf16 v[176:191], a[76:79], a[44:47], v[176:191]\n"
"buffer_load_dwordx4 v207, s[56:59], s74 offen offset:2048 lds\n"
"v_cvt_pk_bf16_f32 v146, 0, v146\n"
"v_exp_f32 v145, v145\n"
"v_fma_f32 v146, v146, s73, v229\n"
"v_mfma_f32_32x32x16_bf16 v[176:191], a[80:83], a[48:51], v[176:191]\n"
"ds_read_b64_tr_b16 a[206:207], v235 offset:520\n"
"v_cvt_pk_bf16_f32 v147, 0, v147\n"
"v_cvt_pk_bf16_f32 v144, v144, v145\n"
"v_exp_f32 v146, v146\n"
"v_fma_f32 v147, v147, s73, v229\n"
"v_mfma_f32_32x32x16_bf16 v[176:191], a[84:87], a[52:55], v[176:191]\n"
"buffer_load_dwordx4 v208, s[56:59], s74 offen offset:3072 lds\n"
"v_cvt_pk_bf16_f32 v148, 0, v148\n"
"v_exp_f32 v147, v147\n"
"v_fma_f32 v148, v148, s73, v229\n"
"v_mfma_f32_32x32x16_bf16 v[176:191], a[88:91], a[56:59], v[176:191]\n"
"v_cvt_pk_bf16_f32 v149, 0, v149\n"
"v_cvt_pk_bf16_f32 v145, v146, v147\n"
"v_exp_f32 v148, v148\n"
"v_fma_f32 v149, v149, s73, v229\n"
"v_cvt_pk_bf16_f32 v150, 0, v150\n"
"v_mfma_f32_32x32x16_bf16 v[176:191], a[92:95], a[60:63], v[176:191]\n"
"s_waitcnt vmcnt(5) lgkmcnt(0)\n"
"v_exp_f32 v149, v149\n"
"v_fma_f32 v150, v150, s73, v229\n"
"v_cvt_pk_bf16_f32 v151, 0, v151\n"
"v_cvt_pk_bf16_f32 v146, v148, v149\n"
"s_barrier\n"
"v_mfma_f32_32x32x16_bf16 v[0:15], a[96:99], v[128:131], v[0:15]\n"
"ds_read_b128 a[64:67], v216 offset:32768\n"
"v_exp_f32 v150, v150\n"
"v_fma_f32 v151, v151, s73, v229\n"
"v_cvt_pk_bf16_f32 v152, 0, v152\n"
"v_mfma_f32_32x32x16_bf16 v[32:47], a[100:103], v[128:131], v[32:47]\n"
"s_add_u32 m0, s67, 0\n"
"ds_read_b128 a[68:71], v217 offset:32768\n"
"v_exp_f32 v151, v151\n"
"v_fma_f32 v152, v152, s73, v229\n"
"v_cvt_pk_bf16_f32 v153, 0, v153\n"
"v_cvt_pk_bf16_f32 v147, v150, v151\n"
"v_mfma_f32_16x16x32_bf16 v[200:203], v[248:251], v[128:131], v[200:203]\n"
"v_exp_f32 v152, v152\n"
"v_mfma_f32_32x32x16_bf16 v[64:79], a[104:107], v[128:131], v[64:79]\n"
"buffer_load_dwordx4 v209, s[48:51], s75 offen offset:0 lds\n"
"ds_read_b128 a[72:75], v218 offset:32768\n"
"v_fma_f32 v153, v153, s73, v229\n"
"v_cvt_pk_bf16_f32 v154, 0, v154\n"
"v_mfma_f32_32x32x16_bf16 v[96:111], a[108:111], v[128:131], v[96:111]\n"
"ds_read_b128 a[76:79], v219 offset:32768\n"
"v_exp_f32 v153, v153\n"
"v_fma_f32 v154, v154, s73, v229\n"
"v_cvt_pk_bf16_f32 v155, 0, v155\n"
"v_cvt_pk_bf16_f32 v152, v152, v153\n"
"v_mfma_f32_32x32x16_bf16 v[16:31], a[96:99], v[144:147], v[16:31]\n"
"buffer_load_dwordx4 v210, s[48:51], s75 offen offset:1024 lds\n"
"ds_read_b128 a[80:83], v220 offset:32768\n"
"v_exp_f32 v154, v154\n"
"v_fma_f32 v155, v155, s73, v229\n"
"v_mfma_f32_32x32x16_bf16 v[48:63], a[100:103], v[144:147], v[48:63]\n"
"ds_read_b128 a[84:87], v221 offset:32768\n"
"v_cvt_pk_bf16_f32 v156, 0, v156\n"
"v_exp_f32 v155, v155\n"
"v_fma_f32 v156, v156, s73, v229\n"
"v_cvt_pk_bf16_f32 v157, 0, v157\n"
"v_mfma_f32_16x16x32_bf16 v[244:247], v[248:251], v[144:147], v[244:247]\n"
"v_cvt_pk_bf16_f32 v153, v154, v155\n"
"v_mfma_f32_32x32x16_bf16 v[80:95], a[104:107], v[144:147], v[80:95]\n"
"ds_read_b128 a[88:91], v222 offset:32768\n"
"v_exp_f32 v156, v156\n"
"v_fma_f32 v157, v157, s73, v229\n"
"v_cvt_pk_bf16_f32 v158, 0, v158\n"
"v_mfma_f32_32x32x16_bf16 v[112:127], a[108:111], v[144:147], v[112:127]\n"
"buffer_load_dwordx4 v211, s[48:51], s75 offen offset:2048 lds\n"
"ds_read_b128 a[92:95], v223 offset:32768\n"
"v_exp_f32 v157, v157\n"
"v_fma_f32 v158, v158, s73, v229\n"
"v_mfma_f32_32x32x16_bf16 v[0:15], a[112:115], v[136:139], v[0:15]\n"
"ds_read_b64_tr_b16 a[96:97], v224 offset:49152\n"
"v_cvt_pk_bf16_f32 v159, 0, v159\n"
"v_cvt_pk_bf16_f32 v154, v156, v157\n"
"v_exp_f32 v158, v158\n"
"v_fma_f32 v159, v159, s73, v229\n"
"v_mfma_f32_32x32x16_bf16 v[32:47], a[116:119], v[136:139], v[32:47]\n"
"s_add_u32 s30, s30, 2048\n"
"buffer_load_dwordx4 v212, s[48:51], s75 offen offset:3072 lds\n"
"ds_read_b64_tr_b16 a[98:99], v224 offset:51200\n"
"v_cvt_pk_bf16_f32 v160, 0, v160\n"
"v_exp_f32 v159, v159\n"
"v_mfma_f32_16x16x32_bf16 v[200:203], v[248:251], v[136:139], v[200:203]\n"
"v_fma_f32 v160, v160, s73, v228\n"
"v_cvt_pk_bf16_f32 v161, 0, v161\n"
"v_mfma_f32_32x32x16_bf16 v[64:79], a[120:123], v[136:139], v[64:79]\n"
"s_add_u32 s75, s75, 0x4000\n"
"ds_read_b64_tr_b16 a[100:101], v225 offset:49152\n"
"v_cvt_pk_bf16_f32 v155, v158, v159\n"
"v_exp_f32 v160, v160\n"
"v_fma_f32 v161, v161, s73, v228\n"
"v_cvt_pk_bf16_f32 v162, 0, v162\n"
"v_mfma_f32_32x32x16_bf16 v[96:111], a[124:127], v[136:139], v[96:111]\n"
"s_and_b32 s75, s75, 0x1ffff\n"
"ds_read_b64_tr_b16 a[102:103], v225 offset:51200\n"
"v_exp_f32 v161, v161\n"
"v_fma_f32 v162, v162, s73, v228\n"
"v_cvt_pk_bf16_f32 v163, 0, v163\n"
"v_cvt_pk_bf16_f32 v160, v160, v161\n"
"v_mfma_f32_32x32x16_bf16 v[16:31], a[112:115], v[152:155], v[16:31]\n"
"ds_read_b64_tr_b16 a[104:105], v226 offset:49152\n"
"v_exp_f32 v162, v162\n"
"v_fma_f32 v163, v163, s73, v228\n"
"v_cvt_pk_bf16_f32 v164, 0, v164\n"
"v_mfma_f32_32x32x16_bf16 v[48:63], a[116:119], v[152:155], v[48:63]\n"
"ds_read_b64_tr_b16 a[208:209], v234 offset:16384\n"
"buffer_load_dword v255, v238, s[56:59], s30 offen\n"
"ds_read_b64_tr_b16 a[106:107], v226 offset:51200\n"
"v_exp_f32 v163, v163\n"
"v_mfma_f32_16x16x32_bf16 v[244:247], v[248:251], v[152:155], v[244:247]\n"
"v_fma_f32 v164, v164, s73, v228\n"
"v_cvt_pk_bf16_f32 v165, 0, v165\n"
"v_mfma_f32_32x32x16_bf16 v[80:95], a[120:123], v[152:155], v[80:95]\n"
"ds_read_b64_tr_b16 a[108:109], v227 offset:49152\n"
"v_cvt_pk_bf16_f32 v161, v162, v163\n"
"v_exp_f32 v164, v164\n"
"v_fma_f32 v165, v165, s73, v228\n"
"v_cvt_pk_bf16_f32 v166, 0, v166\n"
"v_mfma_f32_32x32x16_bf16 v[112:127], a[124:127], v[152:155], v[112:127]\n"
"s_waitcnt lgkmcnt(8)\n"
"ds_read_b64_tr_b16 a[210:211], v234 offset:16896\n"
"ds_read_b64_tr_b16 a[110:111], v227 offset:51200\n"
"v_exp_f32 v165, v165\n"
"v_fma_f32 v166, v166, s73, v228\n"
"v_cvt_pk_bf16_f32 v167, 0, v167\n"
"v_mfma_f32_32x32x16_bf16 v[128:143], a[64:67], a[0:3], 0\n"
"ds_read_b64_tr_b16 a[112:113], v224 offset:53248\n"
"v_cvt_pk_bf16_f32 v162, v164, v165\n"
"v_exp_f32 v166, v166\n"
"v_fma_f32 v167, v167, s73, v228\n"
"v_cvt_pk_bf16_f32 v168, 0, v168\n"
"v_mfma_f32_32x32x16_bf16 v[128:143], a[68:71], a[4:7], v[128:143]\n"
"ds_read_b64_tr_b16 a[212:213], v235 offset:16384\n"
"ds_read_b64_tr_b16 a[114:115], v224 offset:55296\n"
"v_exp_f32 v167, v167\n"
"v_fma_f32 v168, v168, s73, v228\n"
"v_cvt_pk_bf16_f32 v169, 0, v169\n"
"v_mfma_f32_32x32x16_bf16 v[128:143], a[72:75], a[8:11], v[128:143]\n"
"ds_read_b64_tr_b16 a[116:117], v225 offset:53248\n"
"v_cvt_pk_bf16_f32 v163, v166, v167\n"
"v_exp_f32 v168, v168\n"
"v_fma_f32 v169, v169, s73, v228\n"
"v_cvt_pk_bf16_f32 v170, 0, v170\n"
"v_mfma_f32_32x32x16_bf16 v[128:143], a[76:79], a[12:15], v[128:143]\n"
"ds_read_b64_tr_b16 a[214:215], v235 offset:16896\n"
"ds_read_b64_tr_b16 a[118:119], v225 offset:55296\n"
"v_exp_f32 v169, v169\n"
"v_fma_f32 v170, v170, s73, v228\n"
"v_cvt_pk_bf16_f32 v171, 0, v171\n"
"v_mfma_f32_32x32x16_bf16 v[128:143], a[80:83], a[16:19], v[128:143]\n"
"s_add_u32 s74, s68, 229376\n"
"ds_read_b64_tr_b16 a[120:121], v226 offset:53248\n"
"v_cvt_pk_bf16_f32 v168, v168, v169\n"
"v_exp_f32 v170, v170\n"
"v_fma_f32 v171, v171, s73, v228\n"
"v_cvt_pk_bf16_f32 v172, 0, v172\n"
"v_mfma_f32_32x32x16_bf16 v[128:143], a[84:87], a[20:23], v[128:143]\n"
"s_and_b32 s74, s74, 0x7ffff\n"
"ds_read_b64_tr_b16 a[216:217], v234 offset:16392\n"
"ds_read_b64_tr_b16 a[122:123], v226 offset:55296\n"
"v_exp_f32 v171, v171\n"
"v_fma_f32 v172, v172, s73, v228\n"
"v_cvt_pk_bf16_f32 v173, 0, v173\n"
"v_mfma_f32_32x32x16_bf16 v[128:143], a[88:91], a[24:27], v[128:143]\n"
"s_add_u32 s74, s74, s69\n"
"ds_read_b64_tr_b16 a[124:125], v227 offset:53248\n"
"v_cvt_pk_bf16_f32 v169, v170, v171\n"
"v_exp_f32 v172, v172\n"
"v_fma_f32 v173, v173, s73, v228\n"
"v_cvt_pk_bf16_f32 v174, 0, v174\n"
"v_mfma_f32_32x32x16_bf16 v[128:143], a[92:95], a[28:31], v[128:143]\n"
"s_add_u32 m0, s67, 98304\n"
"ds_read_b64_tr_b16 a[218:219], v234 offset:16904\n"
"ds_read_b64_tr_b16 a[126:127], v227 offset:55296\n"
"v_exp_f32 v173, v173\n"
"v_fma_f32 v174, v174, s73, v228\n"
"v_cvt_pk_bf16_f32 v175, 0, v175\n"
"v_mfma_f32_32x32x16_bf16 v[144:159], a[64:67], a[32:35], 0\n"
"buffer_load_dwordx4 v205, s[56:59], s74 offen offset:0 lds\n"
"v_cvt_pk_bf16_f32 v170, v172, v173\n"
"v_exp_f32 v174, v174\n"
"v_fma_f32 v175, v175, s73, v228\n"
"v_mfma_f32_32x32x16_bf16 v[144:159], a[68:71], a[36:39], v[144:159]\n"
"buffer_load_dwordx4 v206, s[56:59], s74 offen offset:1024 lds\n"
"v_cvt_pk_bf16_f32 v176, 0, v176\n"
"v_exp_f32 v175, v175\n"
"v_fma_f32 v176, v176, s73, v229\n"
"v_mfma_f32_32x32x16_bf16 v[144:159], a[72:75], a[40:43], v[144:159]\n"
"Lst7_%=:\n"
"s_cbranch_execz Lst11_%=\n"
"ds_read_b64_tr_b16 a[220:221], v235 offset:16392\n"
"v_cvt_pk_bf16_f32 v177, 0, v177\n"
"v_cvt_pk_bf16_f32 v171, v174, v175\n"
"v_exp_f32 v176, v176\n"
"v_fma_f32 v177, v177, s73, v229\n"
"v_mfma_f32_32x32x16_bf16 v[144:159], a[76:79], a[44:47], v[144:159]\n"
"buffer_load_dwordx4 v207, s[56:59], s74 offen offset:2048 lds\n"
"v_cvt_pk_bf16_f32 v178, 0, v178\n"
"v_exp_f32 v177, v177\n"
"v_fma_f32 v178, v178, s73, v229\n"
"v_mfma_f32_32x32x16_bf16 v[144:159], a[80:83], a[48:51], v[144:159]\n"
"ds_read_b64_tr_b16 a[222:223], v235 offset:16904\n"
"v_cvt_pk_bf16_f32 v179, 0, v179\n"
"v_cvt_pk_bf16_f32 v176, v176, v177\n"
"v_exp_f32 v178, v178\n"
"v_fma_f32 v179, v179, s73, v229\n"
"v_mfma_f32_32x32x16_bf16 v[144:159], a[84:87], a[52:55], v[144:159]\n"
"buffer_load_dwordx4 v208, s[56:59], s74 offen offset:3072 lds\n"
"v_cvt_pk_bf16_f32 v180, 0, v180\n"
"v_exp_f32 v179, v179\n"
"v_fma_f32 v180, v180, s73, v229\n"
"v_mfma_f32_32x32x16_bf16 v[144:159], a[88:91], a[56:59], v[144:159]\n"
"v_cvt_pk_bf16_f32 v181, 0, v181\n"
"v_cvt_pk_bf16_f32 v177, v178, v179\n"
"v_exp_f32 v180, v180\n"
"v_fma_f32 v181, v181, s73, v229\n"
"v_cvt_pk_bf16_f32 v182, 0, v182\n"
"v_mfma_f32_32x32x16_bf16 v[144:159], a[92:95], a[60:63], v[144:159]\n"
"s_waitcnt lgkmcnt(2)\n"
"v_exp_f32 v181, v181\n"
"v_fma_f32 v182, v182, s73, v229\n"
"v_cvt_pk_bf16_f32 v183, 0, v183\n"
"v_cvt_pk_bf16_f32 v178, v180, v181\n"
"v_mfma_f32_32x32x16_bf16 v[0:15], a[96:99], v[160:163], v[0:15]\n"
"ds_read_b128 a[64:67], v216 offset:40960\n"
"v_exp_f32 v182, v182\n"
"v_fma_f32 v183, v183, s73, v229\n"
"v_cvt_pk_bf16_f32 v184, 0, v184\n"
"v_mfma_f32_32x32x16_bf16 v[32:47], a[100:103], v[160:163], v[32:47]\n"
"s_add_u32 m0, s67, 16384\n"
"ds_read_b128 a[68:71], v217 offset:40960\n"
"v_exp_f32 v183, v183\n"
"v_fma_f32 v184, v184, s73, v229\n"
"v_cvt_pk_bf16_f32 v185, 0, v185\n"
"v_cvt_pk_bf16_f32 v179, v182, v183\n"
"v_mfma_f32_16x16x32_bf16 v[200:203], v[248:251], v[160:163], v[200:203]\n"
"v_exp_f32 v184, v184\n"
"v_mfma_f32_32x32x16_bf16 v[64:79], a[104:107], v[160:163], v[64:79]\n"
"buffer_load_dwordx4 v213, s[52:55], s76 offen offset:0 lds\n"
"ds_read_b128 a[72:75], v218 offset:40960\n"
"v_fma_f32 v185, v185, s73, v229\n"
"v_cvt_pk_bf16_f32 v186, 0, v186\n"
"v_mfma_f32_32x32x16_bf16 v[96:111], a[108:111], v[160:163], v[96:111]\n"
"ds_read_b128 a[76:79], v219 offset:40960\n"
"v_exp_f32 v185, v185\n"
"v_fma_f32 v186, v186, s73, v229\n"
"v_cvt_pk_bf16_f32 v187, 0, v187\n"
"v_cvt_pk_bf16_f32 v184, v184, v185\n"
"v_mfma_f32_32x32x16_bf16 v[16:31], a[96:99], v[176:179], v[16:31]\n"
"buffer_load_dwordx4 v213, s[52:55], s76 offen offset:1024 lds\n"
"ds_read_b128 a[80:83], v220 offset:40960\n"
"v_exp_f32 v186, v186\n"
"v_fma_f32 v187, v187, s73, v229\n"
"v_mfma_f32_32x32x16_bf16 v[48:63], a[100:103], v[176:179], v[48:63]\n"
"ds_read_b128 a[84:87], v221 offset:40960\n"
"v_cvt_pk_bf16_f32 v188, 0, v188\n"
"v_exp_f32 v187, v187\n"
"v_fma_f32 v188, v188, s73, v229\n"
"v_cvt_pk_bf16_f32 v189, 0, v189\n"
"v_mfma_f32_16x16x32_bf16 v[244:247], v[248:251], v[176:179], v[244:247]\n"
"v_cvt_pk_bf16_f32 v185, v186, v187\n"
"v_mfma_f32_32x32x16_bf16 v[80:95], a[104:107], v[176:179], v[80:95]\n"
"ds_read_b128 a[88:91], v222 offset:40960\n"
"v_exp_f32 v188, v188\n"
"v_fma_f32 v189, v189, s73, v229\n"
"v_cvt_pk_bf16_f32 v190, 0, v190\n"
"v_mfma_f32_32x32x16_bf16 v[112:127], a[108:111], v[176:179], v[112:127]\n"
"buffer_load_dwordx4 v213, s[52:55], s76 offen offset:2048 lds\n"
"ds_read_b128 a[92:95], v223 offset:40960\n"
"v_exp_f32 v189, v189\n"
"v_fma_f32 v190, v190, s73, v229\n"
"v_mfma_f32_32x32x16_bf16 v[0:15], a[112:115], v[168:171], v[0:15]\n"
"ds_read_b64_tr_b16 a[96:97], v224 offset:57344\n"
"v_cvt_pk_bf16_f32 v191, 0, v191\n"
"v_cvt_pk_bf16_f32 v186, v188, v189\n"
"v_exp_f32 v190, v190\n"
"v_fma_f32 v191, v191, s73, v229\n"
"v_mfma_f32_32x32x16_bf16 v[32:47], a[116:119], v[168:171], v[32:47]\n"
"s_add_u32 s30, s30, 2048\n"
"buffer_load_dwordx4 v213, s[52:55], s76 offen offset:3072 lds\n"
"ds_read_b64_tr_b16 a[98:99], v224 offset:59392\n"
"v_cvt_pk_bf16_f32 v128, 0, v128\n"
"v_exp_f32 v191, v191\n"
"v_mfma_f32_16x16x32_bf16 v[200:203], v[248:251], v[168:171], v[200:203]\n"
"v_fma_f32 v128, v128, s73, v228\n"
"v_cvt_pk_bf16_f32 v129, 0, v129\n"
"v_mfma_f32_32x32x16_bf16 v[64:79], a[120:123], v[168:171], v[64:79]\n"
"s_add_u32 s76, s76, 0x4000\n"
"ds_read_b64_tr_b16 a[100:101], v225 offset:57344\n"
"v_cvt_pk_bf16_f32 v187, v190, v191\n"
"v_exp_f32 v128, v128\n"
"v_fma_f32 v129, v129, s73, v228\n"
"v_cvt_pk_bf16_f32 v130, 0, v130\n"
"v_mfma_f32_32x32x16_bf16 v[96:111], a[124:127], v[168:171], v[96:111]\n"
"s_and_b32 s76, s76, 0x1ffff\n"
"ds_read_b64_tr_b16 a[102:103], v225 offset:59392\n"
"v_exp_f32 v129, v129\n"
"v_fma_f32 v130, v130, s73, v228\n"
"v_cvt_pk_bf16_f32 v131, 0, v131\n"
"v_cvt_pk_bf16_f32 v128, v128, v129\n"
"v_mfma_f32_32x32x16_bf16 v[16:31], a[112:115], v[184:187], v[16:31]\n"
"ds_read_b64_tr_b16 a[104:105], v226 offset:57344\n"
"v_exp_f32 v130, v130\n"
"v_fma_f32 v131, v131, s73, v228\n"
"v_cvt_pk_bf16_f32 v132, 0, v132\n"
"v_mfma_f32_32x32x16_bf16 v[48:63], a[116:119], v[184:187], v[48:63]\n"
"ds_read_b64_tr_b16 a[224:225], v232 offset:0\n"
"buffer_load_dword v255, v238, s[56:59], s30 offen\n"
"ds_read_b64_tr_b16 a[106:107], v226 offset:59392\n"
"v_exp_f32 v131, v131\n"
"v_mfma_f32_16x16x32_bf16 v[244:247], v[248:251], v[184:187], v[244:247]\n"
"v_fma_f32 v132, v132, s73, v228\n"
"v_cvt_pk_bf16_f32 v133, 0, v133\n"
"v_mfma_f32_32x32x16_bf16 v[80:95], a[120:123], v[184:187], v[80:95]\n"
"ds_read_b64_tr_b16 a[108:109], v227 offset:57344\n"
"v_cvt_pk_bf16_f32 v129, v130, v131\n"
"v_exp_f32 v132, v132\n"
"v_fma_f32 v133, v133, s73, v228\n"
"v_cvt_pk_bf16_f32 v134, 0, v134\n"
"v_mfma_f32_32x32x16_bf16 v[112:127], a[124:127], v[184:187], v[112:127]\n"
"s_waitcnt lgkmcnt(8)\n"
"ds_read_b64_tr_b16 a[226:227], v232 offset:512\n"
"ds_read_b64_tr_b16 a[110:111], v227 offset:59392\n"
"v_exp_f32 v133, v133\n"
"v_fma_f32 v134, v134, s73, v228\n"
"v_cvt_pk_bf16_f32 v135, 0, v135\n"
"v_mfma_f32_32x32x16_bf16 v[160:175], a[64:67], a[0:3], 0\n"
"ds_read_b64_tr_b16 a[112:113], v224 offset:61440\n"
"v_cvt_pk_bf16_f32 v130, v132, v133\n"
"v_exp_f32 v134, v134\n"
"v_fma_f32 v135, v135, s73, v228\n"
"v_cvt_pk_bf16_f32 v136, 0, v136\n"
"v_mfma_f32_32x32x16_bf16 v[160:175], a[68:71], a[4:7], v[160:175]\n"
"ds_read_b64_tr_b16 a[228:229], v233 offset:0\n"
"ds_read_b64_tr_b16 a[114:115], v224 offset:63488\n"
"v_exp_f32 v135, v135\n"
"v_fma_f32 v136, v136, s73, v228\n"
"v_cvt_pk_bf16_f32 v137, 0, v137\n"
"v_mfma_f32_32x32x16_bf16 v[160:175], a[72:75], a[8:11], v[160:175]\n"
"ds_read_b64_tr_b16 a[116:117], v225 offset:61440\n"
"v_cvt_pk_bf16_f32 v131, v134, v135\n"
"v_exp_f32 v136, v136\n"
"v_fma_f32 v137, v137, s73, v228\n"
"v_cvt_pk_bf16_f32 v138, 0, v138\n"
"v_mfma_f32_32x32x16_bf16 v[160:175], a[76:79], a[12:15], v[160:175]\n"
"ds_read_b64_tr_b16 a[230:231], v233 offset:512\n"
"ds_read_b64_tr_b16 a[118:119], v225 offset:63488\n"
"v_exp_f32 v137, v137\n"
"v_fma_f32 v138, v138, s73, v228\n"
"v_cvt_pk_bf16_f32 v139, 0, v139\n"
"v_mfma_f32_32x32x16_bf16 v[160:175], a[80:83], a[16:19], v[160:175]\n"
"s_add_u32 s74, s68, 262144\n"
"ds_read_b64_tr_b16 a[120:121], v226 offset:61440\n"
"v_cvt_pk_bf16_f32 v136, v136, v137\n"
"v_exp_f32 v138, v138\n"
"v_fma_f32 v139, v139, s73, v228\n"
"v_cvt_pk_bf16_f32 v140, 0, v140\n"
"v_mfma_f32_32x32x16_bf16 v[160:175], a[84:87], a[20:23], v[160:175]\n"
"s_and_b32 s74, s74, 0x7ffff\n"
"ds_read_b64_tr_b16 a[232:233], v232 offset:8\n"
"ds_read_b64_tr_b16 a[122:123], v226 offset:63488\n"
"v_exp_f32 v139, v139\n"
"v_fma_f32 v140, v140, s73, v228\n"
"v_cvt_pk_bf16_f32 v141, 0, v141\n"
"v_mfma_f32_32x32x16_bf16 v[160:175], a[88:91], a[24:27], v[160:175]\n"
"s_add_u32 s74, s74, s69\n"
"ds_read_b64_tr_b16 a[124:125], v227 offset:61440\n"
"v_cvt_pk_bf16_f32 v137, v138, v139\n"
"v_exp_f32 v140, v140\n"
"v_fma_f32 v141, v141, s73, v228\n"
"v_cvt_pk_bf16_f32 v142, 0, v142\n"
"v_mfma_f32_32x32x16_bf16 v[160:175], a[92:95], a[28:31], v[160:175]\n"
"s_add_u32 m0, s67, 114688\n"
"ds_read_b64_tr_b16 a[234:235], v232 offset:520\n"
"ds_read_b64_tr_b16 a[126:127], v227 offset:63488\n"
"v_exp_f32 v141, v141\n"
"v_fma_f32 v142, v142, s73, v228\n"
"v_cvt_pk_bf16_f32 v143, 0, v143\n"
"v_mfma_f32_32x32x16_bf16 v[176:191], a[64:67], a[32:35], 0\n"
"buffer_load_dwordx4 v205, s[56:59], s74 offen offset:0 lds\n"
"v_cvt_pk_bf16_f32 v138, v140, v141\n"
"v_exp_f32 v142, v142\n"
"v_fma_f32 v143, v143, s73, v228\n"
"v_mfma_f32_32x32x16_bf16 v[176:191], a[68:71], a[36:39], v[176:191]\n"
"buffer_load_dwordx4 v206, s[56:59], s74 offen offset:1024 lds\n"
"v_cvt_pk_bf16_f32 v144, 0, v144\n"
"v_exp_f32 v143, v143\n"
"v_fma_f32 v144, v144, s73, v229\n"
"v_mfma_f32_32x32x16_bf16 v[176:191], a[72:75], a[40:43], v[176:191]\n"
"ds_read_b64_tr_b16 a[236:237], v233 offset:8\n"
"v_cvt_pk_bf16_f32 v145, 0, v145\n"
"v_cvt_pk_bf16_f32 v139, v142, v143\n"
"v_exp_f32 v144, v144\n"
"v_fma_f32 v145, v145, s73, v229\n"
"v_mfma_f32_32x32x16_bf16 v[176:191], a[76:79], a[44:47], v[176:191]\n"
"buffer_load_dwordx4 v207, s[56:59], s74 offen offset:2048 lds\n"
"v_cvt_pk_bf16_f32 v146, 0, v146\n"
"v_exp_f32 v145, v145\n"
"v_fma_f32 v146, v146, s73, v229\n"
"v_mfma_f32_32x32x16_bf16 v[176:191], a[80:83], a[48:51], v[176:191]\n"
"ds_read_b64_tr_b16 a[238:239], v233 offset:520\n"
"v_cvt_pk_bf16_f32 v147, 0, v147\n"
"v_cvt_pk_bf16_f32 v144, v144, v145\n"
"v_exp_f32 v146, v146\n"
"v_fma_f32 v147, v147, s73, v229\n"
"v_mfma_f32_32x32x16_bf16 v[176:191], a[84:87], a[52:55], v[176:191]\n"
"buffer_load_dwordx4 v208, s[56:59], s74 offen offset:3072 lds\n"
"v_cvt_pk_bf16_f32 v148, 0, v148\n"
"v_exp_f32 v147, v147\n"
"v_fma_f32 v148, v148, s73, v229\n"
"v_mfma_f32_32x32x16_bf16 v[176:191], a[88:91], a[56:59], v[176:191]\n"
"s_mov_b32 s40, s20\n"
"v_cvt_pk_bf16_f32 v149, 0, v149\n"
"v_cvt_pk_bf16_f32 v145, v146, v147\n"
"v_exp_f32 v148, v148\n"
"v_fma_f32 v149, v149, s73, v229\n"
"v_cvt_pk_bf16_f32 v150, 0, v150\n"
"v_mfma_f32_32x32x16_bf16 v[176:191], a[92:95], a[60:63], v[176:191]\n"
"s_waitcnt vmcnt(5) lgkmcnt(0)\n"
"v_exp_f32 v149, v149\n"
"v_fma_f32 v150, v150, s73, v229\n"
"v_cvt_pk_bf16_f32 v151, 0, v151\n"
"v_cvt_pk_bf16_f32 v146, v148, v149\n"
"s_barrier\n"
"v_mfma_f32_32x32x16_bf16 v[0:15], a[96:99], v[128:131], v[0:15]\n"
"s_and_b32 s41, s21, 0xffff\n"
"ds_read_b128 a[64:67], v216 offset:0\n"
"v_exp_f32 v150, v150\n"
"v_fma_f32 v151, v151, s73, v229\n"
"v_cvt_pk_bf16_f32 v152, 0, v152\n"
"v_mfma_f32_32x32x16_bf16 v[32:47], a[100:103], v[128:131], v[32:47]\n"
"s_add_u32 m0, s67, 32768\n"
"ds_read_b128 a[68:71], v217 offset:0\n"
"v_exp_f32 v151, v151\n"
"v_fma_f32 v152, v152, s73, v229\n"
"v_cvt_pk_bf16_f32 v153, 0, v153\n"
"v_cvt_pk_bf16_f32 v147, v150, v151\n"
"v_mfma_f32_16x16x32_bf16 v[200:203], v[248:251], v[128:131], v[200:203]\n"
"v_exp_f32 v152, v152\n"
"v_mfma_f32_32x32x16_bf16 v[64:79], a[104:107], v[128:131], v[64:79]\n"
"s_mov_b32 s42, 0x1c0000\n"
"buffer_load_dwordx4 v209, s[48:51], s75 offen offset:0 lds\n"
"ds_read_b128 a[72:75], v218 offset:0\n"
"v_fma_f32 v153, v153, s73, v229\n"
"v_cvt_pk_bf16_f32 v154, 0, v154\n"
"v_mfma_f32_32x32x16_bf16 v[96:111], a[108:111], v[128:131], v[96:111]\n"
"s_mov_b32 s43, 0x20000\n"
"ds_read_b128 a[76:79], v219 offset:0\n"
"v_exp_f32 v153, v153\n"
"v_fma_f32 v154, v154, s73, v229\n"
"v_cvt_pk_bf16_f32 v155, 0, v155\n"
"v_cvt_pk_bf16_f32 v152, v152, v153\n"
"v_mfma_f32_32x32x16_bf16 v[16:31], a[96:99], v[144:147], v[16:31]\n"
"s_lshl_b32 s30, s31, 2\n"
"buffer_load_dwordx4 v210, s[48:51], s75 offen offset:1024 lds\n"
"ds_read_b128 a[80:83], v220 offset:0\n"
"v_exp_f32 v154, v154\n"
"v_fma_f32 v155, v155, s73, v229\n"
"v_mfma_f32_32x32x16_bf16 v[48:63], a[100:103], v[144:147], v[48:63]\n"
"s_add_u32 s30, s30, s64\n"
"ds_read_b128 a[84:87], v221 offset:0\n"
"v_cvt_pk_bf16_f32 v156, 0, v156\n"
"v_exp_f32 v155, v155\n"
"v_fma_f32 v156, v156, s73, v229\n"
"v_cvt_pk_bf16_f32 v157, 0, v157\n"
"v_mfma_f32_16x16x32_bf16 v[244:247], v[248:251], v[144:147], v[244:247]\n"
"v_cvt_pk_bf16_f32 v153, v154, v155\n"
"v_mfma_f32_32x32x16_bf16 v[80:95], a[104:107], v[144:147], v[80:95]\n"
"s_and_b32 s34, s6, 1\n"
"ds_read_b128 a[88:91], v222 offset:0\n"
"v_exp_f32 v156, v156\n"
"v_fma_f32 v157, v157, s73, v229\n"
"v_cvt_pk_bf16_f32 v158, 0, v158\n"
"v_mfma_f32_32x32x16_bf16 v[112:127], a[108:111], v[144:147], v[112:127]\n"
"s_mul_i32 s30, s30, 14336\n"
"buffer_load_dwordx4 v211, s[48:51], s75 offen offset:2048 lds\n"
"ds_read_b128 a[92:95], v223 offset:0\n"
"v_exp_f32 v157, v157\n"
"v_fma_f32 v158, v158, s73, v229\n"
"v_mfma_f32_32x32x16_bf16 v[0:15], a[112:115], v[136:139], v[0:15]\n"
"s_mul_i32 s34, s34, 1792\n"
"ds_read_b64_tr_b16 a[96:97], v224 offset:16384\n"
"v_cvt_pk_bf16_f32 v159, 0, v159\n"
"v_cvt_pk_bf16_f32 v154, v156, v157\n"
"v_exp_f32 v158, v158\n"
"v_fma_f32 v159, v159, s73, v229\n"
"v_mfma_f32_32x32x16_bf16 v[32:47], a[116:119], v[136:139], v[32:47]\n"
"s_add_u32 s30, s30, s34\n"
"buffer_load_dwordx4 v212, s[48:51], s75 offen offset:3072 lds\n"
"ds_read_b64_tr_b16 a[98:99], v224 offset:18432\n"
"v_cvt_pk_bf16_f32 v160, 0, v160\n"
"v_exp_f32 v159, v159\n"
"v_mfma_f32_16x16x32_bf16 v[200:203], v[248:251], v[136:139], v[200:203]\n"
"v_fma_f32 v160, v160, s73, v228\n"
"v_cvt_pk_bf16_f32 v161, 0, v161\n"
"v_mfma_f32_32x32x16_bf16 v[64:79], a[120:123], v[136:139], v[64:79]\n"
"s_add_u32 s75, s75, 0x4000\n"
"ds_read_b64_tr_b16 a[100:101], v225 offset:16384\n"
"v_cvt_pk_bf16_f32 v155, v158, v159\n"
"v_exp_f32 v160, v160\n"
"v_fma_f32 v161, v161, s73, v228\n"
"v_cvt_pk_bf16_f32 v162, 0, v162\n"
"v_mfma_f32_32x32x16_bf16 v[96:111], a[124:127], v[136:139], v[96:111]\n"
"s_and_b32 s75, s75, 0x1ffff\n"
"ds_read_b64_tr_b16 a[102:103], v225 offset:18432\n"
"v_exp_f32 v161, v161\n"
"v_fma_f32 v162, v162, s73, v228\n"
"v_cvt_pk_bf16_f32 v163, 0, v163\n"
"v_cvt_pk_bf16_f32 v160, v160, v161\n"
"v_mfma_f32_32x32x16_bf16 v[16:31], a[112:115], v[152:155], v[16:31]\n"
"ds_read_b64_tr_b16 a[104:105], v226 offset:16384\n"
"v_exp_f32 v162, v162\n"
"v_fma_f32 v163, v163, s73, v228\n"
"v_cvt_pk_bf16_f32 v164, 0, v164\n"
"v_mfma_f32_32x32x16_bf16 v[48:63], a[116:119], v[152:155], v[48:63]\n"
"ds_read_b64_tr_b16 a[240:241], v232 offset:16384\n"
"buffer_load_dword v255, v239, s[40:43], s30 offen\n"
"ds_read_b64_tr_b16 a[106:107], v226 offset:18432\n"
"v_exp_f32 v163, v163\n"
"v_mfma_f32_16x16x32_bf16 v[244:247], v[248:251], v[152:155], v[244:247]\n"
"v_fma_f32 v164, v164, s73, v228\n"
"v_cvt_pk_bf16_f32 v165, 0, v165\n"
"v_mfma_f32_32x32x16_bf16 v[80:95], a[120:123], v[152:155], v[80:95]\n"
"ds_read_b64_tr_b16 a[108:109], v227 offset:16384\n"
"v_cvt_pk_bf16_f32 v161, v162, v163\n"
"v_exp_f32 v164, v164\n"
"v_fma_f32 v165, v165, s73, v228\n"
"v_cvt_pk_bf16_f32 v166, 0, v166\n"
"v_mfma_f32_32x32x16_bf16 v[112:127], a[124:127], v[152:155], v[112:127]\n"
"s_waitcnt lgkmcnt(8)\n"
"ds_read_b64_tr_b16 a[242:243], v232 offset:16896\n"
"ds_read_b64_tr_b16 a[110:111], v227 offset:18432\n"
"v_exp_f32 v165, v165\n"
"v_fma_f32 v166, v166, s73, v228\n"
"v_cvt_pk_bf16_f32 v167, 0, v167\n"
"v_mfma_f32_32x32x16_bf16 v[128:143], a[64:67], a[0:3], 0\n"
"ds_read_b64_tr_b16 a[112:113], v224 offset:20480\n"
"v_cvt_pk_bf16_f32 v162, v164, v165\n"
"v_exp_f32 v166, v166\n"
"v_fma_f32 v167, v167, s73, v228\n"
"v_cvt_pk_bf16_f32 v168, 0, v168\n"
"v_mfma_f32_32x32x16_bf16 v[128:143], a[68:71], a[4:7], v[128:143]\n"
"ds_read_b64_tr_b16 a[244:245], v233 offset:16384\n"
"ds_read_b64_tr_b16 a[114:115], v224 offset:22528\n"
"v_exp_f32 v167, v167\n"
"v_fma_f32 v168, v168, s73, v228\n"
"v_cvt_pk_bf16_f32 v169, 0, v169\n"
"v_mfma_f32_32x32x16_bf16 v[128:143], a[72:75], a[8:11], v[128:143]\n"
"ds_read_b64_tr_b16 a[116:117], v225 offset:20480\n"
"v_cvt_pk_bf16_f32 v163, v166, v167\n"
"v_exp_f32 v168, v168\n"
"v_fma_f32 v169, v169, s73, v228\n"
"v_cvt_pk_bf16_f32 v170, 0, v170\n"
"v_mfma_f32_32x32x16_bf16 v[128:143], a[76:79], a[12:15], v[128:143]\n"
"ds_read_b64_tr_b16 a[246:247], v233 offset:16896\n"
"ds_read_b64_tr_b16 a[118:119], v225 offset:22528\n"
"v_exp_f32 v169, v169\n"
"v_fma_f32 v170, v170, s73, v228\n"
"v_cvt_pk_bf16_f32 v171, 0, v171\n"
"v_mfma_f32_32x32x16_bf16 v[128:143], a[80:83], a[16:19], v[128:143]\n"
"s_add_u32 s74, s68, 327680\n"
"ds_read_b64_tr_b16 a[120:121], v226 offset:20480\n"
"v_cvt_pk_bf16_f32 v168, v168, v169\n"
"v_exp_f32 v170, v170\n"
"v_fma_f32 v171, v171, s73, v228\n"
"v_cvt_pk_bf16_f32 v172, 0, v172\n"
"v_mfma_f32_32x32x16_bf16 v[128:143], a[84:87], a[20:23], v[128:143]\n"
"s_and_b32 s74, s74, 0x7ffff\n"
"ds_read_b64_tr_b16 a[248:249], v232 offset:16392\n"
"ds_read_b64_tr_b16 a[122:123], v226 offset:22528\n"
"v_exp_f32 v171, v171\n"
"v_fma_f32 v172, v172, s73, v228\n"
"v_cvt_pk_bf16_f32 v173, 0, v173\n"
"v_mfma_f32_32x32x16_bf16 v[128:143], a[88:91], a[24:27], v[128:143]\n"
"s_add_u32 s74, s74, s69\n"
"ds_read_b64_tr_b16 a[124:125], v227 offset:20480\n"
"v_cvt_pk_bf16_f32 v169, v170, v171\n"
"v_exp_f32 v172, v172\n"
"v_fma_f32 v173, v173, s73, v228\n"
"v_cvt_pk_bf16_f32 v174, 0, v174\n"
"v_mfma_f32_32x32x16_bf16 v[128:143], a[92:95], a[28:31], v[128:143]\n"
"s_add_u32 m0, s67, 131072\n"
"ds_read_b64_tr_b16 a[250:251], v232 offset:16904\n"
"ds_read_b64_tr_b16 a[126:127], v227 offset:22528\n"
"v_exp_f32 v173, v173\n"
"v_fma_f32 v174, v174, s73, v228\n"
"v_cvt_pk_bf16_f32 v175, 0, v175\n"
"v_mfma_f32_32x32x16_bf16 v[144:159], a[64:67], a[32:35], 0\n"
"buffer_load_dwordx4 v205, s[56:59], s74 offen offset:0 lds\n"
"v_cvt_pk_bf16_f32 v170, v172, v173\n"
"v_exp_f32 v174, v174\n"
"v_fma_f32 v175, v175, s73, v228\n"
"v_mfma_f32_32x32x16_bf16 v[144:159], a[68:71], a[36:39], v[144:159]\n"
"buffer_load_dwordx4 v206, s[56:59], s74 offen offset:1024 lds\n"
"v_cvt_pk_bf16_f32 v176, 0, v176\n"
"v_exp_f32 v175, v175\n"
"v_fma_f32 v176, v176, s73, v229\n"
"v_mfma_f32_32x32x16_bf16 v[144:159], a[72:75], a[40:43], v[144:159]\n"
"ds_read_b64_tr_b16 a[252:253], v233 offset:16392\n"
"v_cvt_pk_bf16_f32 v177, 0, v177\n"
"v_cvt_pk_bf16_f32 v171, v174, v175\n"
"v_exp_f32 v176, v176\n"
"v_fma_f32 v177, v177, s73, v229\n"
"v_mfma_f32_32x32x16_bf16 v[144:159], a[76:79], a[44:47], v[144:159]\n"
"buffer_load_dwordx4 v207, s[56:59], s74 offen offset:2048 lds\n"
"v_cvt_pk_bf16_f32 v178, 0, v178\n"
"v_exp_f32 v177, v177\n"
"v_fma_f32 v178, v178, s73, v229\n"
"v_mfma_f32_32x32x16_bf16 v[144:159], a[80:83], a[48:51], v[144:159]\n"
"ds_read_b64_tr_b16 a[254:255], v233 offset:16904\n"
"v_cvt_pk_bf16_f32 v179, 0, v179\n"
"v_cvt_pk_bf16_f32 v176, v176, v177\n"
"v_exp_f32 v178, v178\n"
"v_fma_f32 v179, v179, s73, v229\n"
"v_mfma_f32_32x32x16_bf16 v[144:159], a[84:87], a[52:55], v[144:159]\n"
"buffer_load_dwordx4 v208, s[56:59], s74 offen offset:3072 lds\n"
"v_cvt_pk_bf16_f32 v180, 0, v180\n"
"v_exp_f32 v179, v179\n"
"v_fma_f32 v180, v180, s73, v229\n"
"v_mfma_f32_32x32x16_bf16 v[144:159], a[88:91], a[56:59], v[144:159]\n"
"v_cvt_pk_bf16_f32 v181, 0, v181\n"
"v_cvt_pk_bf16_f32 v177, v178, v179\n"
"v_exp_f32 v180, v180\n"
"v_fma_f32 v181, v181, s73, v229\n"
"v_cvt_pk_bf16_f32 v182, 0, v182\n"
"v_mfma_f32_32x32x16_bf16 v[144:159], a[92:95], a[60:63], v[144:159]\n"
"s_waitcnt lgkmcnt(2)\n"
"v_exp_f32 v181, v181\n"
"v_fma_f32 v182, v182, s73, v229\n"
"v_cvt_pk_bf16_f32 v183, 0, v183\n"
"v_cvt_pk_bf16_f32 v178, v180, v181\n"
"v_mfma_f32_32x32x16_bf16 v[0:15], a[96:99], v[160:163], v[0:15]\n"
"ds_read_b128 a[64:67], v216 offset:8192\n"
"v_exp_f32 v182, v182\n"
"v_fma_f32 v183, v183, s73, v229\n"
"v_cvt_pk_bf16_f32 v184, 0, v184\n"
"v_mfma_f32_32x32x16_bf16 v[32:47], a[100:103], v[160:163], v[32:47]\n"
"s_add_u32 m0, s67, 49152\n"
"ds_read_b128 a[68:71], v217 offset:8192\n"
"v_exp_f32 v183, v183\n"
"v_fma_f32 v184, v184, s73, v229\n"
"v_cvt_pk_bf16_f32 v185, 0, v185\n"
"v_cvt_pk_bf16_f32 v179, v182, v183\n"
"v_mfma_f32_16x16x32_bf16 v[200:203], v[248:251], v[160:163], v[200:203]\n"
"v_exp_f32 v184, v184\n"
"v_mfma_f32_32x32x16_bf16 v[64:79], a[104:107], v[160:163], v[64:79]\n"
"buffer_load_dwordx4 v213, s[52:55], s76 offen offset:0 lds\n"
"ds_read_b128 a[72:75], v218 offset:8192\n"
"v_fma_f32 v185, v185, s73, v229\n"
"v_cvt_pk_bf16_f32 v186, 0, v186\n"
"v_mfma_f32_32x32x16_bf16 v[96:111], a[108:111], v[160:163], v[96:111]\n"
"ds_read_b128 a[76:79], v219 offset:8192\n"
"v_exp_f32 v185, v185\n"
"v_fma_f32 v186, v186, s73, v229\n"
"v_cvt_pk_bf16_f32 v187, 0, v187\n"
"v_cvt_pk_bf16_f32 v184, v184, v185\n"
"v_mfma_f32_32x32x16_bf16 v[16:31], a[96:99], v[176:179], v[16:31]\n"
"buffer_load_dwordx4 v213, s[52:55], s76 offen offset:1024 lds\n"
"ds_read_b128 a[80:83], v220 offset:8192\n"
"v_exp_f32 v186, v186\n"
"v_fma_f32 v187, v187, s73, v229\n"
"v_mfma_f32_32x32x16_bf16 v[48:63], a[100:103], v[176:179], v[48:63]\n"
"ds_read_b128 a[84:87], v221 offset:8192\n"
"v_cvt_pk_bf16_f32 v188, 0, v188\n"
"v_exp_f32 v187, v187\n"
"v_fma_f32 v188, v188, s73, v229\n"
"v_cvt_pk_bf16_f32 v189, 0, v189\n"
"v_mfma_f32_16x16x32_bf16 v[244:247], v[248:251], v[176:179], v[244:247]\n"
"v_cvt_pk_bf16_f32 v185, v186, v187\n"
"v_mfma_f32_32x32x16_bf16 v[80:95], a[104:107], v[176:179], v[80:95]\n"
"ds_read_b128 a[88:91], v222 offset:8192\n"
"v_exp_f32 v188, v188\n"
"v_fma_f32 v189, v189, s73, v229\n"
"v_cvt_pk_bf16_f32 v190, 0, v190\n"
"v_mfma_f32_32x32x16_bf16 v[112:127], a[108:111], v[176:179], v[112:127]\n"
"buffer_load_dwordx4 v213, s[52:55], s76 offen offset:2048 lds\n"
"ds_read_b128 a[92:95], v223 offset:8192\n"
"v_exp_f32 v189, v189\n"
"v_fma_f32 v190, v190, s73, v229\n"
"v_mfma_f32_32x32x16_bf16 v[0:15], a[112:115], v[168:171], v[0:15]\n"
"ds_read_b64_tr_b16 a[96:97], v224 offset:24576\n"
"v_cvt_pk_bf16_f32 v191, 0, v191\n"
"v_cvt_pk_bf16_f32 v186, v188, v189\n"
"v_exp_f32 v190, v190\n"
"v_fma_f32 v191, v191, s73, v229\n"
"v_mfma_f32_32x32x16_bf16 v[32:47], a[116:119], v[168:171], v[32:47]\n"
"buffer_load_dwordx4 v213, s[52:55], s76 offen offset:3072 lds\n"
"ds_read_b64_tr_b16 a[98:99], v224 offset:26624\n"
"v_cvt_pk_bf16_f32 v128, 0, v128\n"
"v_exp_f32 v191, v191\n"
"v_mfma_f32_16x16x32_bf16 v[200:203], v[248:251], v[168:171], v[200:203]\n"
"v_fma_f32 v128, v128, s73, v228\n"
"v_cvt_pk_bf16_f32 v129, 0, v129\n"
"v_mfma_f32_32x32x16_bf16 v[64:79], a[120:123], v[168:171], v[64:79]\n"
"s_add_u32 s76, s76, 0x4000\n"
"ds_read_b64_tr_b16 a[100:101], v225 offset:24576\n"
"v_cvt_pk_bf16_f32 v187, v190, v191\n"
"v_exp_f32 v128, v128\n"
"v_fma_f32 v129, v129, s73, v228\n"
"v_cvt_pk_bf16_f32 v130, 0, v130\n"
"v_mfma_f32_32x32x16_bf16 v[96:111], a[124:127], v[168:171], v[96:111]\n"
"s_and_b32 s76, s76, 0x1ffff\n"
"ds_read_b64_tr_b16 a[102:103], v225 offset:26624\n"
"v_exp_f32 v129, v129\n"
"v_fma_f32 v130, v130, s73, v228\n"
"v_cvt_pk_bf16_f32 v131, 0, v131\n"
"v_cvt_pk_bf16_f32 v128, v128, v129\n"
"v_mfma_f32_32x32x16_bf16 v[16:31], a[112:115], v[184:187], v[16:31]\n"
"s_add_u32 s30, s30, 3584\n"
"ds_read_b64_tr_b16 a[104:105], v226 offset:24576\n"
"v_exp_f32 v130, v130\n"
"v_fma_f32 v131, v131, s73, v228\n"
"v_cvt_pk_bf16_f32 v132, 0, v132\n"
"v_mfma_f32_32x32x16_bf16 v[48:63], a[116:119], v[184:187], v[48:63]\n"
"buffer_load_dword v255, v239, s[40:43], s30 offen\n"
"ds_read_b64_tr_b16 a[106:107], v226 offset:26624\n"
"v_exp_f32 v131, v131\n"
"v_fma_f32 v132, v132, s73, v228\n"
"v_mfma_f32_16x16x32_bf16 v[244:247], v[248:251], v[184:187], v[244:247]\n"
"v_cvt_pk_bf16_f32 v133, 0, v133\n"
"v_cvt_pk_bf16_f32 v129, v130, v131\n"
"v_mfma_f32_32x32x16_bf16 v[80:95], a[120:123], v[184:187], v[80:95]\n"
"ds_read_b64_tr_b16 a[108:109], v227 offset:24576\n"
"v_exp_f32 v132, v132\n"
"v_fma_f32 v133, v133, s73, v228\n"
"v_cvt_pk_bf16_f32 v134, 0, v134\n"
"v_mfma_f32_32x32x16_bf16 v[112:127], a[124:127], v[184:187], v[112:127]\n"
"s_waitcnt lgkmcnt(7)\n"
"ds_read_b64_tr_b16 a[110:111], v227 offset:26624\n"
"v_exp_f32 v133, v133\n"
"v_fma_f32 v134, v134, s73, v228\n"
"v_cvt_pk_bf16_f32 v135, 0, v135\n"
"v_cvt_pk_bf16_f32 v130, v132, v133\n"
"v_mfma_f32_32x32x16_bf16 v[160:175], a[64:67], a[0:3], 0\n"
"ds_read_b64_tr_b16 a[112:113], v224 offset:28672\n"
"v_exp_f32 v134, v134\n"
"v_fma_f32 v135, v135, s73, v228\n"
"v_cvt_pk_bf16_f32 v136, 0, v136\n"
"v_mfma_f32_32x32x16_bf16 v[160:175], a[68:71], a[4:7], v[160:175]\n"
"ds_read_b64_tr_b16 a[114:115], v224 offset:30720\n"
"v_exp_f32 v135, v135\n"
"v_fma_f32 v136, v136, s73, v228\n"
"v_cvt_pk_bf16_f32 v137, 0, v137\n"
"v_cvt_pk_bf16_f32 v131, v134, v135\n"
"v_mfma_f32_32x32x16_bf16 v[160:175], a[72:75], a[8:11], v[160:175]\n"
"ds_read_b64_tr_b16 a[116:117], v225 offset:28672\n"
"v_exp_f32 v136, v136\n"
"v_fma_f32 v137, v137, s73, v228\n"
"v_cvt_pk_bf16_f32 v138, 0, v138\n"
"v_mfma_f32_32x32x16_bf16 v[160:175], a[76:79], a[12:15], v[160:175]\n"
"Lst8_%=:\n"
"s_cbranch_execz Lst12_%=\n"
"ds_read_b64_tr_b16 a[118:119], v225 offset:30720\n"
"v_exp_f32 v137, v137\n"
"v_fma_f32 v138, v138, s73, v228\n"
"v_cvt_pk_bf16_f32 v139, 0, v139\n"
"v_cvt_pk_bf16_f32 v136, v136, v137\n"
"v_mfma_f32_32x32x16_bf16 v[160:175], a[80:83], a[16:19], v[160:175]\n"
"s_add_u32 s74, s68, 360448\n"
"ds_read_b64_tr_b16 a[120:121], v226 offset:28672\n"
"v_exp_f32 v138, v138\n"
"v_fma_f32 v139, v139, s73, v228\n"
"v_cvt_pk_bf16_f32 v140, 0, v140\n"
"v_mfma_f32_32x32x16_bf16 v[160:175], a[84:87], a[20:23], v[160:175]\n"
"s_and_b32 s74, s74, 0x7ffff\n"
"ds_read_b64_tr_b16 a[122:123], v226 offset:30720\n"
"v_exp_f32 v139, v139\n"
"v_fma_f32 v140, v140, s73, v228\n"
"v_cvt_pk_bf16_f32 v141, 0, v141\n"
"v_cvt_pk_bf16_f32 v137, v138, v139\n"
"v_mfma_f32_32x32x16_bf16 v[160:175], a[88:91], a[24:27], v[160:175]\n"
"s_add_u32 s74, s74, s69\n"
"ds_read_b64_tr_b16 a[124:125], v227 offset:28672\n"
"v_exp_f32 v140, v140\n"
"v_fma_f32 v141, v141, s73, v228\n"
"v_cvt_pk_bf16_f32 v142, 0, v142\n"
"v_mfma_f32_32x32x16_bf16 v[160:175], a[92:95], a[28:31], v[160:175]\n"
"s_add_u32 m0, s67, 147456\n"
"ds_read_b64_tr_b16 a[126:127], v227 offset:30720\n"
"v_exp_f32 v141, v141\n"
"v_fma_f32 v142, v142, s73, v228\n"
"v_cvt_pk_bf16_f32 v143, 0, v143\n"
"v_cvt_pk_bf16_f32 v138, v140, v141\n"
"v_mfma_f32_32x32x16_bf16 v[176:191], a[64:67], a[32:35], 0\n"
"buffer_load_dwordx4 v205, s[56:59], s74 offen offset:0 lds\n"
"v_exp_f32 v142, v142\n"
"v_fma_f32 v143, v143, s73, v228\n"
"v_cvt_pk_bf16_f32 v144, 0, v144\n"
"v_mfma_f32_32x32x16_bf16 v[176:191], a[68:71], a[36:39], v[176:191]\n"
"buffer_load_dwordx4 v206, s[56:59], s74 offen offset:1024 lds\n"
"v_exp_f32 v143, v143\n"
"v_fma_f32 v144, v144, s73, v229\n"
"v_cvt_pk_bf16_f32 v145, 0, v145\n"
"v_mfma_f32_32x32x16_bf16 v[176:191], a[72:75], a[40:43], v[176:191]\n"
"v_cvt_pk_bf16_f32 v139, v142, v143\n"
"v_exp_f32 v144, v144\n"
"v_fma_f32 v145, v145, s73, v229\n"
"v_cvt_pk_bf16_f32 v146, 0, v146\n"
"v_mfma_f32_32x32x16_bf16 v[176:191], a[76:79], a[44:47], v[176:191]\n"
"buffer_load_dwordx4 v207, s[56:59], s74 offen offset:2048 lds\n"
"v_exp_f32 v145, v145\n"
"v_fma_f32 v146, v146, s73, v229\n"
"v_cvt_pk_bf16_f32 v147, 0, v147\n"
"v_mfma_f32_32x32x16_bf16 v[176:191], a[80:83], a[48:51], v[176:191]\n"
"v_cvt_pk_bf16_f32 v144, v144, v145\n"
"v_exp_f32 v146, v146\n"
"v_fma_f32 v147, v147, s73, v229\n"
"v_cvt_pk_bf16_f32 v148, 0, v148\n"
"v_mfma_f32_32x32x16_bf16 v[176:191], a[84:87], a[52:55], v[176:191]\n"
"buffer_load_dwordx4 v208, s[56:59], s74 offen offset:3072 lds\n"
"v_exp_f32 v147, v147\n"
"v_fma_f32 v148, v148, s73, v229\n"
"v_cvt_pk_bf16_f32 v149, 0, v149\n"
"v_mfma_f32_32x32x16_bf16 v[176:191], a[88:91], a[56:59], v[176:191]\n"
"v_cvt_pk_bf16_f32 v145, v146, v147\n"
"v_exp_f32 v148, v148\n"
"v_fma_f32 v149, v149, s73, v229\n"
"v_cvt_pk_bf16_f32 v150, 0, v150\n"
"v_mfma_f32_32x32x16_bf16 v[176:191], a[92:95], a[60:63], v[176:191]\n"
"s_waitcnt vmcnt(5) lgkmcnt(0)\n"
"v_exp_f32 v149, v149\n"
"v_fma_f32 v150, v150, s73, v229\n"
"v_cvt_pk_bf16_f32 v151, 0, v151\n"
"v_cvt_pk_bf16_f32 v146, v148, v149\n"
"s_barrier\n"
"v_mfma_f32_32x32x16_bf16 v[0:15], a[96:99], v[128:131], v[0:15]\n"
"ds_read_b128 a[64:67], v216 offset:32768\n"
"v_exp_f32 v150, v150\n"
"v_fma_f32 v151, v151, s73, v229\n"
"v_cvt_pk_bf16_f32 v152, 0, v152\n"
"v_mfma_f32_32x32x16_bf16 v[32:47], a[100:103], v[128:131], v[32:47]\n"
"s_add_u32 m0, s67, 0\n"
"ds_read_b128 a[68:71], v217 offset:32768\n"
"v_exp_f32 v151, v151\n"
"v_fma_f32 v152, v152, s73, v229\n"
"v_cvt_pk_bf16_f32 v153, 0, v153\n"
"v_cvt_pk_bf16_f32 v147, v150, v151\n"
"v_mfma_f32_16x16x32_bf16 v[200:203], v[248:251], v[128:131], v[200:203]\n"
"v_exp_f32 v152, v152\n"
"v_mfma_f32_32x32x16_bf16 v[64:79], a[104:107], v[128:131], v[64:79]\n"
"buffer_load_dwordx4 v209, s[48:51], s75 offen offset:0 lds\n"
"ds_read_b128 a[72:75], v218 offset:32768\n"
"v_fma_f32 v153, v153, s73, v229\n"
"v_cvt_pk_bf16_f32 v154, 0, v154\n"
"v_mfma_f32_32x32x16_bf16 v[96:111], a[108:111], v[128:131], v[96:111]\n"
"ds_read_b128 a[76:79], v219 offset:32768\n"
"v_exp_f32 v153, v153\n"
"v_fma_f32 v154, v154, s73, v229\n"
"v_cvt_pk_bf16_f32 v155, 0, v155\n"
"v_cvt_pk_bf16_f32 v152, v152, v153\n"
"v_mfma_f32_32x32x16_bf16 v[16:31], a[96:99], v[144:147], v[16:31]\n"
"buffer_load_dwordx4 v210, s[48:51], s75 offen offset:1024 lds\n"
"ds_read_b128 a[80:83], v220 offset:32768\n"
"v_exp_f32 v154, v154\n"
"v_fma_f32 v155, v155, s73, v229\n"
"v_mfma_f32_32x32x16_bf16 v[48:63], a[100:103], v[144:147], v[48:63]\n"
"ds_read_b128 a[84:87], v221 offset:32768\n"
"v_cvt_pk_bf16_f32 v156, 0, v156\n"
"v_exp_f32 v155, v155\n"
"v_fma_f32 v156, v156, s73, v229\n"
"v_cvt_pk_bf16_f32 v157, 0, v157\n"
"v_mfma_f32_16x16x32_bf16 v[244:247], v[248:251], v[144:147], v[244:247]\n"
"v_cvt_pk_bf16_f32 v153, v154, v155\n"
"v_mfma_f32_32x32x16_bf16 v[80:95], a[104:107], v[144:147], v[80:95]\n"
"ds_read_b128 a[88:91], v222 offset:32768\n"
"v_exp_f32 v156, v156\n"
"v_fma_f32 v157, v157, s73, v229\n"
"v_cvt_pk_bf16_f32 v158, 0, v158\n"
"v_mfma_f32_32x32x16_bf16 v[112:127], a[108:111], v[144:147], v[112:127]\n"
"buffer_load_dwordx4 v211, s[48:51], s75 offen offset:2048 lds\n"
"ds_read_b128 a[92:95], v223 offset:32768\n"
"v_exp_f32 v157, v157\n"
"v_fma_f32 v158, v158, s73, v229\n"
"v_mfma_f32_32x32x16_bf16 v[0:15], a[112:115], v[136:139], v[0:15]\n"
"ds_read_b64_tr_b16 a[96:97], v224 offset:49152\n"
"v_cvt_pk_bf16_f32 v159, 0, v159\n"
"v_cvt_pk_bf16_f32 v154, v156, v157\n"
"v_exp_f32 v158, v158\n"
"v_fma_f32 v159, v159, s73, v229\n"
"v_mfma_f32_32x32x16_bf16 v[32:47], a[116:119], v[136:139], v[32:47]\n"
"buffer_load_dwordx4 v212, s[48:51], s75 offen offset:3072 lds\n"
"ds_read_b64_tr_b16 a[98:99], v224 offset:51200\n"
"v_cvt_pk_bf16_f32 v160, 0, v160\n"
"v_exp_f32 v159, v159\n"
"v_mfma_f32_16x16x32_bf16 v[200:203], v[248:251], v[136:139], v[200:203]\n"
"v_fma_f32 v160, v160, s73, v228\n"
"v_cvt_pk_bf16_f32 v161, 0, v161\n"
"v_mfma_f32_32x32x16_bf16 v[64:79], a[120:123], v[136:139], v[64:79]\n"
"s_add_u32 s75, s75, 0x4000\n"
"ds_read_b64_tr_b16 a[100:101], v225 offset:49152\n"
"v_cvt_pk_bf16_f32 v155, v158, v159\n"
"v_exp_f32 v160, v160\n"
"v_fma_f32 v161, v161, s73, v228\n"
"v_cvt_pk_bf16_f32 v162, 0, v162\n"
"v_mfma_f32_32x32x16_bf16 v[96:111], a[124:127], v[136:139], v[96:111]\n"
"s_and_b32 s75, s75, 0x1ffff\n"
"ds_read_b64_tr_b16 a[102:103], v225 offset:51200\n"
"v_exp_f32 v161, v161\n"
"v_fma_f32 v162, v162, s73, v228\n"
"v_cvt_pk_bf16_f32 v163, 0, v163\n"
"v_cvt_pk_bf16_f32 v160, v160, v161\n"
"v_mfma_f32_32x32x16_bf16 v[16:31], a[112:115], v[152:155], v[16:31]\n"
"s_add_u32 s30, s30, 3584\n"
"ds_read_b64_tr_b16 a[104:105], v226 offset:49152\n"
"v_exp_f32 v162, v162\n"
"v_fma_f32 v163, v163, s73, v228\n"
"v_cvt_pk_bf16_f32 v164, 0, v164\n"
"v_mfma_f32_32x32x16_bf16 v[48:63], a[116:119], v[152:155], v[48:63]\n"
"buffer_load_dword v255, v239, s[40:43], s30 offen\n"
"ds_read_b64_tr_b16 a[106:107], v226 offset:51200\n"
"v_exp_f32 v163, v163\n"
"v_fma_f32 v164, v164, s73, v228\n"
"v_mfma_f32_16x16x32_bf16 v[244:247], v[248:251], v[152:155], v[244:247]\n"
"v_cvt_pk_bf16_f32 v165, 0, v165\n"
"v_cvt_pk_bf16_f32 v161, v162, v163\n"
"v_mfma_f32_32x32x16_bf16 v[80:95], a[120:123], v[152:155], v[80:95]\n"
"ds_read_b64_tr_b16 a[108:109], v227 offset:49152\n"
"v_exp_f32 v164, v164\n"
"v_fma_f32 v165, v165, s73, v228\n"
"v_cvt_pk_bf16_f32 v166, 0, v166\n"
"v_mfma_f32_32x32x16_bf16 v[112:127], a[124:127], v[152:155], v[112:127]\n"
"s_waitcnt lgkmcnt(7)\n"
"ds_read_b64_tr_b16 a[110:111], v227 offset:51200\n"
"v_exp_f32 v165, v165\n"
"v_fma_f32 v166, v166, s73, v228\n"
"v_cvt_pk_bf16_f32 v167, 0, v167\n"
"v_cvt_pk_bf16_f32 v162, v164, v165\n"
"v_mfma_f32_32x32x16_bf16 v[128:143], a[64:67], a[0:3], 0\n"
"ds_read_b64_tr_b16 a[112:113], v224 offset:53248\n"
"v_exp_f32 v166, v166\n"
"v_fma_f32 v167, v167, s73, v228\n"
"v_cvt_pk_bf16_f32 v168, 0, v168\n"
"v_mfma_f32_32x32x16_bf16 v[128:143], a[68:71], a[4:7], v[128:143]\n"
"ds_read_b64_tr_b16 a[114:115], v224 offset:55296\n"
"v_exp_f32 v167, v167\n"
"v_fma_f32 v168, v168, s73, v228\n"
"v_cvt_pk_bf16_f32 v169, 0, v169\n"
"v_cvt_pk_bf16_f32 v163, v166, v167\n"
"v_mfma_f32_32x32x16_bf16 v[128:143], a[72:75], a[8:11], v[128:143]\n"
"ds_read_b64_tr_b16 a[116:117], v225 offset:53248\n"
"v_exp_f32 v168, v168\n"
"v_fma_f32 v169, v169, s73, v228\n"
"v_cvt_pk_bf16_f32 v170, 0, v170\n"
"v_mfma_f32_32x32x16_bf16 v[128:143], a[76:79], a[12:15], v[128:143]\n"
"ds_read_b64_tr_b16 a[118:119], v225 offset:55296\n"
"v_exp_f32 v169, v169\n"
"v_fma_f32 v170, v170, s73, v228\n"
"v_cvt_pk_bf16_f32 v171, 0, v171\n"
"v_cvt_pk_bf16_f32 v168, v168, v169\n"
"v_mfma_f32_32x32x16_bf16 v[128:143], a[80:83], a[16:19], v[128:143]\n"
"s_add_u32 s74, s68, 131072\n"
"ds_read_b64_tr_b16 a[120:121], v226 offset:53248\n"
"v_exp_f32 v170, v170\n"
"v_fma_f32 v171, v171, s73, v228\n"
"v_cvt_pk_bf16_f32 v172, 0, v172\n"
"v_mfma_f32_32x32x16_bf16 v[128:143], a[84:87], a[20:23], v[128:143]\n"
"s_and_b32 s74, s74, 0x7ffff\n"
"ds_read_b64_tr_b16 a[122:123], v226 offset:55296\n"
"v_exp_f32 v171, v171\n"
"v_fma_f32 v172, v172, s73, v228\n"
"v_cvt_pk_bf16_f32 v173, 0, v173\n"
"v_cvt_pk_bf16_f32 v169, v170, v171\n"
"v_mfma_f32_32x32x16_bf16 v[128:143], a[88:91], a[24:27], v[128:143]\n"
"s_add_u32 s74, s74, s69\n"
"ds_read_b64_tr_b16 a[124:125], v227 offset:53248\n"
"v_exp_f32 v172, v172\n"
"v_fma_f32 v173, v173, s73, v228\n"
"v_cvt_pk_bf16_f32 v174, 0, v174\n"
"v_mfma_f32_32x32x16_bf16 v[128:143], a[92:95], a[28:31], v[128:143]\n"
"s_add_u32 m0, s67, 65536\n"
"ds_read_b64_tr_b16 a[126:127], v227 offset:55296\n"
"v_exp_f32 v173, v173\n"
"v_fma_f32 v174, v174, s73, v228\n"
"v_cvt_pk_bf16_f32 v175, 0, v175\n"
"v_cvt_pk_bf16_f32 v170, v172, v173\n"
"v_mfma_f32_32x32x16_bf16 v[144:159], a[64:67], a[32:35], 0\n"
"buffer_load_dwordx4 v205, s[56:59], s74 offen offset:0 lds\n"
"v_exp_f32 v174, v174\n"
"v_fma_f32 v175, v175, s73, v228\n"
"v_cvt_pk_bf16_f32 v176, 0, v176\n"
"v_mfma_f32_32x32x16_bf16 v[144:159], a[68:71], a[36:39], v[144:159]\n"
"buffer_load_dwordx4 v206, s[56:59], s74 offen offset:1024 lds\n"
"v_exp_f32 v175, v175\n"
"v_fma_f32 v176, v176, s73, v229\n"
"v_cvt_pk_bf16_f32 v177, 0, v177\n"
"v_mfma_f32_32x32x16_bf16 v[144:159], a[72:75], a[40:43], v[144:159]\n"
"v_cvt_pk_bf16_f32 v171, v174, v175\n"
"v_exp_f32 v176, v176\n"
"v_fma_f32 v177, v177, s73, v229\n"
"v_cvt_pk_bf16_f32 v178, 0, v178\n"
"v_mfma_f32_32x32x16_bf16 v[144:159], a[76:79], a[44:47], v[144:159]\n"
"buffer_load_dwordx4 v207, s[56:59], s74 offen offset:2048 lds\n"
"v_exp_f32 v177, v177\n"
"v_fma_f32 v178, v178, s73, v229\n"
"v_cvt_pk_bf16_f32 v179, 0, v179\n"
"v_mfma_f32_32x32x16_bf16 v[144:159], a[80:83], a[48:51], v[144:159]\n"
"v_cvt_pk_bf16_f32 v176, v176, v177\n"
"v_exp_f32 v178, v178\n"
"v_fma_f32 v179, v179, s73, v229\n"
"v_cvt_pk_bf16_f32 v180, 0, v180\n"
"v_mfma_f32_32x32x16_bf16 v[144:159], a[84:87], a[52:55], v[144:159]\n"
"buffer_load_dwordx4 v208, s[56:59], s74 offen offset:3072 lds\n"
"v_exp_f32 v179, v179\n"
"v_fma_f32 v180, v180, s73, v229\n"
"v_cvt_pk_bf16_f32 v181, 0, v181\n"
"v_mfma_f32_32x32x16_bf16 v[144:159], a[88:91], a[56:59], v[144:159]\n"
"v_cvt_pk_bf16_f32 v177, v178, v179\n"
"v_exp_f32 v180, v180\n"
"v_fma_f32 v181, v181, s73, v229\n"
"v_cvt_pk_bf16_f32 v182, 0, v182\n"
"v_mfma_f32_32x32x16_bf16 v[144:159], a[92:95], a[60:63], v[144:159]\n"
"s_waitcnt lgkmcnt(0)\n"
"v_exp_f32 v181, v181\n"
"v_fma_f32 v182, v182, s73, v229\n"
"v_cvt_pk_bf16_f32 v183, 0, v183\n"
"v_cvt_pk_bf16_f32 v178, v180, v181\n"
"v_mfma_f32_32x32x16_bf16 v[0:15], a[96:99], v[160:163], v[0:15]\n"
"ds_read_b128 a[64:67], v216 offset:40960\n"
"v_exp_f32 v182, v182\n"
"v_fma_f32 v183, v183, s73, v229\n"
"v_cvt_pk_bf16_f32 v184, 0, v184\n"
"v_mfma_f32_32x32x16_bf16 v[32:47], a[100:103], v[160:163], v[32:47]\n"
"s_add_u32 m0, s67, 16384\n"
"ds_read_b128 a[68:71], v217 offset:40960\n"
"v_exp_f32 v183, v183\n"
"v_fma_f32 v184, v184, s73, v229\n"
"v_cvt_pk_bf16_f32 v185, 0, v185\n"
"v_cvt_pk_bf16_f32 v179, v182, v183\n"
"v_mfma_f32_16x16x32_bf16 v[200:203], v[248:251], v[160:163], v[200:203]\n"
"v_exp_f32 v184, v184\n"
"v_mfma_f32_32x32x16_bf16 v[64:79], a[104:107], v[160:163], v[64:79]\n"
"buffer_load_dwordx4 v213, s[52:55], s76 offen offset:0 lds\n"
"ds_read_b128 a[72:75], v218 offset:40960\n"
"v_fma_f32 v185, v185, s73, v229\n"
"v_cvt_pk_bf16_f32 v186, 0, v186\n"
"v_mfma_f32_32x32x16_bf16 v[96:111], a[108:111], v[160:163], v[96:111]\n"
"ds_read_b128 a[76:79], v219 offset:40960\n"
"v_exp_f32 v185, v185\n"
"v_fma_f32 v186, v186, s73, v229\n"
"v_cvt_pk_bf16_f32 v187, 0, v187\n"
"v_cvt_pk_bf16_f32 v184, v184, v185\n"
"v_mfma_f32_32x32x16_bf16 v[16:31], a[96:99], v[176:179], v[16:31]\n"
"buffer_load_dwordx4 v213, s[52:55], s76 offen offset:1024 lds\n"
"ds_read_b128 a[80:83], v220 offset:40960\n"
"v_exp_f32 v186, v186\n"
"v_fma_f32 v187, v187, s73, v229\n"
"v_mfma_f32_32x32x16_bf16 v[48:63], a[100:103], v[176:179], v[48:63]\n"
"ds_read_b128 a[84:87], v221 offset:40960\n"
"v_cvt_pk_bf16_f32 v188, 0, v188\n"
"v_exp_f32 v187, v187\n"
"v_fma_f32 v188, v188, s73, v229\n"
"v_cvt_pk_bf16_f32 v189, 0, v189\n"
"v_mfma_f32_16x16x32_bf16 v[244:247], v[248:251], v[176:179], v[244:247]\n"
"v_cvt_pk_bf16_f32 v185, v186, v187\n"
"v_mfma_f32_32x32x16_bf16 v[80:95], a[104:107], v[176:179], v[80:95]\n"
"ds_read_b128 a[88:91], v222 offset:40960\n"
"v_exp_f32 v188, v188\n"
"v_fma_f32 v189, v189, s73, v229\n"
"v_cvt_pk_bf16_f32 v190, 0, v190\n"
"v_mfma_f32_32x32x16_bf16 v[112:127], a[108:111], v[176:179], v[112:127]\n"
"buffer_load_dwordx4 v213, s[52:55], s76 offen offset:2048 lds\n"
"ds_read_b128 a[92:95], v223 offset:40960\n"
"v_exp_f32 v189, v189\n"
"v_fma_f32 v190, v190, s73, v229\n"
"v_mfma_f32_32x32x16_bf16 v[0:15], a[112:115], v[168:171], v[0:15]\n"
"ds_read_b64_tr_b16 a[96:97], v224 offset:57344\n"
"v_cvt_pk_bf16_f32 v191, 0, v191\n"
"v_cvt_pk_bf16_f32 v186, v188, v189\n"
"v_exp_f32 v190, v190\n"
"v_fma_f32 v191, v191, s73, v229\n"
"v_mfma_f32_32x32x16_bf16 v[32:47], a[116:119], v[168:171], v[32:47]\n"
"Lcp_8_%=:\n"
"buffer_load_dwordx4 v213, s[52:55], s76 offen offset:3072 lds\n"
"ds_read_b64_tr_b16 a[98:99], v224 offset:59392\n"
"v_cvt_pk_bf16_f32 v128, 0, v128\n"
"v_exp_f32 v191, v191\n"
"v_mfma_f32_16x16x32_bf16 v[200:203], v[248:251], v[168:171], v[200:203]\n"
"v_fma_f32 v128, v128, s73, v228\n"
"v_cvt_pk_bf16_f32 v129, 0, v129\n"
"v_mfma_f32_32x32x16_bf16 v[64:79], a[120:123], v[168:171], v[64:79]\n"
"s_add_u32 s76, s76, 0x4000\n"
"ds_read_b64_tr_b16 a[100:101], v225 offset:57344\n"
"v_cvt_pk_bf16_f32 v187, v190, v191\n"
"v_exp_f32 v128, v128\n"
"v_fma_f32 v129, v129, s73, v228\n"
"v_cvt_pk_bf16_f32 v130, 0, v130\n"
"v_mfma_f32_32x32x16_bf16 v[96:111], a[124:127], v[168:171], v[96:111]\n"
"s_and_b32 s76, s76, 0x1ffff\n"
"ds_read_b64_tr_b16 a[102:103], v225 offset:59392\n"
"v_exp_f32 v129, v129\n"
"v_fma_f32 v130, v130, s73, v228\n"
"v_cvt_pk_bf16_f32 v131, 0, v131\n"
"v_cvt_pk_bf16_f32 v128, v128, v129\n"
"v_mfma_f32_32x32x16_bf16 v[16:31], a[112:115], v[184:187], v[16:31]\n"
"s_add_u32 s30, s30, 3584\n"
"ds_read_b64_tr_b16 a[104:105], v226 offset:57344\n"
"v_exp_f32 v130, v130\n"
"v_fma_f32 v131, v131, s73, v228\n"
"v_cvt_pk_bf16_f32 v132, 0, v132\n"
"v_lshl_add_u32 v254, v192, 2, s7\n"
"v_mfma_f32_32x32x16_bf16 v[48:63], a[116:119], v[184:187], v[48:63]\n"
"buffer_load_dword v255, v239, s[40:43], s30 offen\n"
"ds_read_b64_tr_b16 a[106:107], v226 offset:59392\n"
"v_exp_f32 v131, v131\n"
"v_add_u32 v254, Lcp_8_%=-Lpc0_%=+1024, v254\n"
"v_mfma_f32_16x16x32_bf16 v[244:247], v[248:251], v[184:187], v[244:247]\n"
"v_fma_f32 v132, v132, s73, v228\n"
"v_cvt_pk_bf16_f32 v133, 0, v133\n"
"v_mfma_f32_32x32x16_bf16 v[80:95], a[120:123], v[184:187], v[80:95]\n"
"ds_read_b64_tr_b16 a[108:109], v227 offset:57344\n"
"v_cvt_pk_bf16_f32 v129, v130, v131\n"
"v_exp_f32 v132, v132\n"
"v_fma_f32 v133, v133, s73, v228\n"
"v_min_u32 v254, Lfin_%=-Lpc0_%=, v254\n"
"v_mfma_f32_32x32x16_bf16 v[112:127], a[124:127], v[184:187], v[112:127]\n"
"s_waitcnt lgkmcnt(7)\n"
"global_load_dword v255, v254, s[18:19]\n"
"ds_read_b64_tr_b16 a[110:111], v227 offset:59392\n"
"v_cvt_pk_bf16_f32 v134, 0, v134\n"
"v_exp_f32 v133, v133\n"
"v_mfma_f32_32x32x16_bf16 v[160:175], a[64:67], a[0:3], 0\n"
"ds_read_b64_tr_b16 a[112:113], v224 offset:61440\n"
"v_fma_f32 v134, v134, s73, v228\n"
"v_cvt_pk_bf16_f32 v135, 0, v135\n"
"v_cvt_pk_bf16_f32 v130, v132, v133\n"
"v_exp_f32 v134, v134\n"
"v_mfma_f32_32x32x16_bf16 v[160:175], a[68:71], a[4:7], v[160:175]\n"
"ds_read_b64_tr_b16 a[114:115], v224 offset:63488\n"
"v_fma_f32 v135, v135, s73, v228\n"
"v_cvt_pk_bf16_f32 v136, 0, v136\n"
"v_exp_f32 v135, v135\n"
"v_fma_f32 v136, v136, s73, v228\n"
"v_mfma_f32_32x32x16_bf16 v[160:175], a[72:75], a[8:11], v[160:175]\n"
"ds_read_b64_tr_b16 a[116:117], v225 offset:61440\n"
"v_cvt_pk_bf16_f32 v137, 0, v137\n"
"v_cvt_pk_bf16_f32 v131, v134, v135\n"
"v_exp_f32 v136, v136\n"
"v_fma_f32 v137, v137, s73, v228\n"
"v_mfma_f32_32x32x16_bf16 v[160:175], a[76:79], a[12:15], v[160:175]\n"
"ds_read_b64_tr_b16 a[118:119], v225 offset:63488\n"
"v_cvt_pk_bf16_f32 v138, 0, v138\n"
"v_exp_f32 v137, v137\n"
"v_fma_f32 v138, v138, s73, v228\n"
"v_cvt_pk_bf16_f32 v139, 0, v139\n"
"v_mfma_f32_32x32x16_bf16 v[160:175], a[80:83], a[16:19], v[160:175]\n"
"s_add_u32 s74, s68, 163840\n"
"ds_read_b64_tr_b16 a[120:121], v226 offset:61440\n"
"v_cvt_pk_bf16_f32 v136, v136, v137\n"
"v_exp_f32 v138, v138\n"
"v_fma_f32 v139, v139, s73, v228\n"
"v_cvt_pk_bf16_f32 v140, 0, v140\n"
"v_mfma_f32_32x32x16_bf16 v[160:175], a[84:87], a[20:23], v[160:175]\n"
"s_and_b32 s74, s74, 0x7ffff\n"
"ds_read_b64_tr_b16 a[122:123], v226 offset:63488\n"
"v_exp_f32 v139, v139\n"
"v_fma_f32 v140, v140, s73, v228\n"
"v_cvt_pk_bf16_f32 v141, 0, v141\n"
"v_cvt_pk_bf16_f32 v137, v138, v139\n"
"v_mfma_f32_32x32x16_bf16 v[160:175], a[88:91], a[24:27], v[160:175]\n"
"s_add_u32 s74, s74, s69\n"
"ds_read_b64_tr_b16 a[124:125], v227 offset:61440\n"
"v_exp_f32 v140, v140\n"
"v_fma_f32 v141, v141, s73, v228\n"
"v_cvt_pk_bf16_f32 v142, 0, v142\n"
"v_mfma_f32_32x32x16_bf16 v[160:175], a[92:95], a[28:31], v[160:175]\n"
"s_add_u32 m0, s67, 81920\n"
"ds_read_b64_tr_b16 a[126:127], v227 offset:63488\n"
"v_exp_f32 v141, v141\n"
"v_fma_f32 v142, v142, s73, v228\n"
"v_cvt_pk_bf16_f32 v143, 0, v143\n"
"v_cvt_pk_bf16_f32 v138, v140, v141\n"
"v_mfma_f32_32x32x16_bf16 v[176:191], a[64:67], a[32:35], 0\n"
"buffer_load_dwordx4 v205, s[56:59], s74 offen offset:0 lds\n"
"v_exp_f32 v142, v142\n"
"v_fma_f32 v143, v143, s73, v228\n"
"v_cvt_pk_bf16_f32 v144, 0, v144\n"
"v_mfma_f32_32x32x16_bf16 v[176:191], a[68:71], a[36:39], v[176:191]\n"
"buffer_load_dwordx4 v206, s[56:59], s74 offen offset:1024 lds\n"
"v_exp_f32 v143, v143\n"
"v_fma_f32 v144, v144, s73, v229\n"
"v_cvt_pk_bf16_f32 v145, 0, v145\n"
"v_mfma_f32_32x32x16_bf16 v[176:191], a[72:75], a[40:43], v[176:191]\n"
"v_cvt_pk_bf16_f32 v139, v142, v143\n"
"v_exp_f32 v144, v144\n"
"v_fma_f32 v145, v145, s73, v229\n"
"v_cvt_pk_bf16_f32 v146, 0, v146\n"
"v_mfma_f32_32x32x16_bf16 v[176:191], a[76:79], a[44:47], v[176:191]\n"
"buffer_load_dwordx4 v207, s[56:59], s74 offen offset:2048 lds\n"
"v_exp_f32 v145, v145\n"
"v_fma_f32 v146, v146, s73, v229\n"
"v_cvt_pk_bf16_f32 v147, 0, v147\n"
"v_mfma_f32_32x32x16_bf16 v[176:191], a[80:83], a[48:51], v[176:191]\n"
"v_cvt_pk_bf16_f32 v144, v144, v145\n"
"v_exp_f32 v146, v146\n"
"v_fma_f32 v147, v147, s73, v229\n"
"v_cvt_pk_bf16_f32 v148, 0, v148\n"
"v_mfma_f32_32x32x16_bf16 v[176:191], a[84:87], a[52:55], v[176:191]\n"
"buffer_load_dwordx4 v208, s[56:59], s74 offen offset:3072 lds\n"
"v_exp_f32 v147, v147\n"
"v_fma_f32 v148, v148, s73, v229\n"
"v_cvt_pk_bf16_f32 v149, 0, v149\n"
"v_mfma_f32_32x32x16_bf16 v[176:191], a[88:91], a[56:59], v[176:191]\n"
"v_cvt_pk_bf16_f32 v145, v146, v147\n"
"v_exp_f32 v148, v148\n"
"v_fma_f32 v149, v149, s73, v229\n"
"v_cvt_pk_bf16_f32 v150, 0, v150\n"
"v_mfma_f32_32x32x16_bf16 v[176:191], a[92:95], a[60:63], v[176:191]\n"
"s_waitcnt vmcnt(6) lgkmcnt(0)\n"
"v_exp_f32 v149, v149\n"
"v_fma_f32 v150, v150, s73, v229\n"
"v_cvt_pk_bf16_f32 v151, 0, v151\n"
"v_cvt_pk_bf16_f32 v146, v148, v149\n"
"s_barrier\n"
"v_mfma_f32_32x32x16_bf16 v[0:15], a[96:99], v[128:131], v[0:15]\n"
"ds_read_b128 a[64:67], v216 offset:0\n"
"v_exp_f32 v150, v150\n"
"v_fma_f32 v151, v151, s73, v229\n"
"v_cvt_pk_bf16_f32 v152, 0, v152\n"
"v_mfma_f32_32x32x16_bf16 v[32:47], a[100:103], v[128:131], v[32:47]\n"
"ds_read_b128 a[68:71], v217 offset:0\n"
"v_exp_f32 v151, v151\n"
"v_fma_f32 v152, v152, s73, v229\n"
"v_cvt_pk_bf16_f32 v153, 0, v153\n"
"v_cvt_pk_bf16_f32 v147, v150, v151\n"
"v_mfma_f32_16x16x32_bf16 v[200:203], v[248:251], v[128:131], v[200:203]\n"
"v_exp_f32 v152, v152\n"
"v_mfma_f32_32x32x16_bf16 v[64:79], a[104:107], v[128:131], v[64:79]\n"
"ds_read_b128 a[72:75], v218 offset:0\n"
"v_fma_f32 v153, v153, s73, v229\n"
"v_cvt_pk_bf16_f32 v154, 0, v154\n"
"v_exp_f32 v153, v153\n"
"v_fma_f32 v154, v154, s73, v229\n"
"v_mfma_f32_32x32x16_bf16 v[96:111], a[108:111], v[128:131], v[96:111]\n"
"ds_read_b128 a[76:79], v219 offset:0\n"
"v_cvt_pk_bf16_f32 v155, 0, v155\n"
"v_cvt_pk_bf16_f32 v152, v152, v153\n"
"v_exp_f32 v154, v154\n"
"v_fma_f32 v155, v155, s73, v229\n"
"v_mfma_f32_32x32x16_bf16 v[16:31], a[96:99], v[144:147], v[16:31]\n"
"ds_read_b128 a[80:83], v220 offset:0\n"
"v_cvt_pk_bf16_f32 v156, 0, v156\n"
"v_exp_f32 v155, v155\n"
"v_fma_f32 v156, v156, s73, v229\n"
"v_cvt_pk_bf16_f32 v157, 0, v157\n"
"v_mfma_f32_32x32x16_bf16 v[48:63], a[100:103], v[144:147], v[48:63]\n"
"ds_read_b128 a[84:87], v221 offset:0\n"
"v_cvt_pk_bf16_f32 v153, v154, v155\n"
"v_exp_f32 v156, v156\n"
"v_fma_f32 v157, v157, s73, v229\n"
"v_cvt_pk_bf16_f32 v158, 0, v158\n"
"v_mfma_f32_16x16x32_bf16 v[244:247], v[248:251], v[144:147], v[244:247]\n"
"v_exp_f32 v157, v157\n"
"v_mfma_f32_32x32x16_bf16 v[80:95], a[104:107], v[144:147], v[80:95]\n"
"ds_read_b128 a[88:91], v222 offset:0\n"
"v_fma_f32 v158, v158, s73, v229\n"
"v_cvt_pk_bf16_f32 v159, 0, v159\n"
"v_cvt_pk_bf16_f32 v154, v156, v157\n"
"v_exp_f32 v158, v158\n"
"v_mfma_f32_32x32x16_bf16 v[112:127], a[108:111], v[144:147], v[112:127]\n"
"ds_read_b128 a[92:95], v223 offset:0\n"
"v_fma_f32 v159, v159, s73, v229\n"
"v_cvt_pk_bf16_f32 v160, 0, v160\n"
"v_exp_f32 v159, v159\n"
"v_fma_f32 v160, v160, s73, v228\n"
"v_mfma_f32_32x32x16_bf16 v[0:15], a[112:115], v[136:139], v[0:15]\n"
"ds_read_b64_tr_b16 a[96:97], v224 offset:16384\n"
"v_cvt_pk_bf16_f32 v161, 0, v161\n"
"v_cvt_pk_bf16_f32 v155, v158, v159\n"
"v_exp_f32 v160, v160\n"
"v_fma_f32 v161, v161, s73, v228\n"
"v_mfma_f32_32x32x16_bf16 v[32:47], a[116:119], v[136:139], v[32:47]\n"
"ds_read_b64_tr_b16 a[98:99], v224 offset:18432\n"
"v_cvt_pk_bf16_f32 v162, 0, v162\n"
"v_exp_f32 v161, v161\n"
"v_fma_f32 v162, v162, s73, v228\n"
"v_cvt_pk_bf16_f32 v163, 0, v163\n"
"v_mfma_f32_16x16x32_bf16 v[200:203], v[248:251], v[136:139], v[200:203]\n"
"v_cvt_pk_bf16_f32 v160, v160, v161\n"
"v_mfma_f32_32x32x16_bf16 v[64:79], a[120:123], v[136:139], v[64:79]\n"
"ds_read_b64_tr_b16 a[100:101], v225 offset:16384\n"
"v_exp_f32 v162, v162\n"
"v_fma_f32 v163, v163, s73, v228\n"
"v_cvt_pk_bf16_f32 v164, 0, v164\n"
"v_mfma_f32_32x32x16_bf16 v[96:111], a[124:127], v[136:139], v[96:111]\n"
"ds_read_b64_tr_b16 a[102:103], v225 offset:18432\n"
"v_exp_f32 v163, v163\n"
"v_fma_f32 v164, v164, s73, v228\n"
"v_cvt_pk_bf16_f32 v165, 0, v165\n"
"v_cvt_pk_bf16_f32 v161, v162, v163\n"
"v_mfma_f32_32x32x16_bf16 v[16:31], a[112:115], v[152:155], v[16:31]\n"
"ds_read_b64_tr_b16 a[104:105], v226 offset:16384\n"
"v_exp_f32 v164, v164\n"
"v_fma_f32 v165, v165, s73, v228\n"
"v_cvt_pk_bf16_f32 v166, 0, v166\n"
"v_mfma_f32_32x32x16_bf16 v[48:63], a[116:119], v[152:155], v[48:63]\n"
"ds_read_b64_tr_b16 a[106:107], v226 offset:18432\n"
"v_exp_f32 v165, v165\n"
"v_fma_f32 v166, v166, s73, v228\n"
"v_cvt_pk_bf16_f32 v167, 0, v167\n"
"v_cvt_pk_bf16_f32 v162, v164, v165\n"
"v_mfma_f32_16x16x32_bf16 v[244:247], v[248:251], v[152:155], v[244:247]\n"
"v_exp_f32 v166, v166\n"
"v_mfma_f32_32x32x16_bf16 v[80:95], a[120:123], v[152:155], v[80:95]\n"
"ds_read_b64_tr_b16 a[108:109], v227 offset:16384\n"
"v_fma_f32 v167, v167, s73, v228\n"
"v_cvt_pk_bf16_f32 v168, 0, v168\n"
"v_exp_f32 v167, v167\n"
"v_fma_f32 v168, v168, s73, v228\n"
"v_mfma_f32_32x32x16_bf16 v[112:127], a[124:127], v[152:155], v[112:127]\n"
"s_waitcnt lgkmcnt(7)\n"
"ds_read_b64_tr_b16 a[110:111], v227 offset:18432\n"
"v_cvt_pk_bf16_f32 v169, 0, v169\n"
"v_cvt_pk_bf16_f32 v163, v166, v167\n"
"v_exp_f32 v168, v168\n"
"v_fma_f32 v169, v169, s73, v228\n"
"v_mfma_f32_32x32x16_bf16 v[128:143], a[64:67], a[0:3], 0\n"
"ds_read_b64_tr_b16 a[112:113], v224 offset:20480\n"
"v_cvt_pk_bf16_f32 v170, 0, v170\n"
"v_exp_f32 v169, v169\n"
"v_fma_f32 v170, v170, s73, v228\n"
"v_cvt_pk_bf16_f32 v171, 0, v171\n"
"v_mfma_f32_32x32x16_bf16 v[128:143], a[68:71], a[4:7], v[128:143]\n"
"Lst9_%=:\n"
"s_cbranch_execz Lback0_%=\n"
"ds_read_b64_tr_b16 a[114:115], v224 offset:22528\n"
"v_cvt_pk_bf16_f32 v168, v168, v169\n"
"v_exp_f32 v170, v170\n"
"v_fma_f32 v171, v171, s73, v228\n"
"v_cvt_pk_bf16_f32 v172, 0, v172\n"
"v_mfma_f32_32x32x16_bf16 v[128:143], a[72:75], a[8:11], v[128:143]\n"
"ds_read_b64_tr_b16 a[116:117], v225 offset:20480\n"
"v_exp_f32 v171, v171\n"
"v_fma_f32 v172, v172, s73, v228\n"
"v_cvt_pk_bf16_f32 v173, 0, v173\n"
"v_cvt_pk_bf16_f32 v169, v170, v171\n"
"v_mfma_f32_32x32x16_bf16 v[128:143], a[76:79], a[12:15], v[128:143]\n"
"ds_read_b64_tr_b16 a[118:119], v225 offset:22528\n"
"v_exp_f32 v172, v172\n"
"v_fma_f32 v173, v173, s73, v228\n"
"v_cvt_pk_bf16_f32 v174, 0, v174\n"
"v_mfma_f32_32x32x16_bf16 v[128:143], a[80:83], a[16:19], v[128:143]\n"
"ds_read_b64_tr_b16 a[120:121], v226 offset:20480\n"
"v_exp_f32 v173, v173\n"
"v_fma_f32 v174, v174, s73, v228\n"
"v_cvt_pk_bf16_f32 v175, 0, v175\n"
"v_cvt_pk_bf16_f32 v170, v172, v173\n"
"v_mfma_f32_32x32x16_bf16 v[128:143], a[84:87], a[20:23], v[128:143]\n"
"ds_read_b64_tr_b16 a[122:123], v226 offset:22528\n"
"v_exp_f32 v174, v174\n"
"v_fma_f32 v175, v175, s73, v228\n"
"v_cvt_pk_bf16_f32 v176, 0, v176\n"
"v_mfma_f32_32x32x16_bf16 v[128:143], a[88:91], a[24:27], v[128:143]\n"
"ds_read_b64_tr_b16 a[124:125], v227 offset:20480\n"
"v_exp_f32 v175, v175\n"
"v_fma_f32 v176, v176, s73, v229\n"
"v_cvt_pk_bf16_f32 v177, 0, v177\n"
"v_cvt_pk_bf16_f32 v171, v174, v175\n"
"v_mfma_f32_32x32x16_bf16 v[128:143], a[92:95], a[28:31], v[128:143]\n"
"ds_read_b64_tr_b16 a[126:127], v227 offset:22528\n"
"v_exp_f32 v176, v176\n"
"v_fma_f32 v177, v177, s73, v229\n"
"v_cvt_pk_bf16_f32 v178, 0, v178\n"
"v_mfma_f32_32x32x16_bf16 v[144:159], a[64:67], a[32:35], 0\n"
"v_exp_f32 v177, v177\n"
"v_fma_f32 v178, v178, s73, v229\n"
"v_cvt_pk_bf16_f32 v179, 0, v179\n"
"v_cvt_pk_bf16_f32 v176, v176, v177\n"
"v_mfma_f32_32x32x16_bf16 v[144:159], a[68:71], a[36:39], v[144:159]\n"
"v_exp_f32 v178, v178\n"
"v_fma_f32 v179, v179, s73, v229\n"
"v_cvt_pk_bf16_f32 v180, 0, v180\n"
"v_exp_f32 v179, v179\n"
"v_mfma_f32_32x32x16_bf16 v[144:159], a[72:75], a[40:43], v[144:159]\n"
"v_fma_f32 v180, v180, s73, v229\n"
"v_cvt_pk_bf16_f32 v181, 0, v181\n"
"v_cvt_pk_bf16_f32 v177, v178, v179\n"
"v_exp_f32 v180, v180\n"
"v_fma_f32 v181, v181, s73, v229\n"
"v_mfma_f32_32x32x16_bf16 v[144:159], a[76:79], a[44:47], v[144:159]\n"
"v_cvt_pk_bf16_f32 v182, 0, v182\n"
"v_exp_f32 v181, v181\n"
"v_fma_f32 v182, v182, s73, v229\n"
"v_cvt_pk_bf16_f32 v183, 0, v183\n"
"v_cvt_pk_bf16_f32 v178, v180, v181\n"
"v_mfma_f32_32x32x16_bf16 v[144:159], a[80:83], a[48:51], v[144:159]\n"
"v_exp_f32 v182, v182\n"
"v_fma_f32 v183, v183, s73, v229\n"
"v_cvt_pk_bf16_f32 v184, 0, v184\n"
"v_exp_f32 v183, v183\n"
"v_mfma_f32_32x32x16_bf16 v[144:159], a[84:87], a[52:55], v[144:159]\n"
"v_fma_f32 v184, v184, s73, v229\n"
"v_cvt_pk_bf16_f32 v185, 0, v185\n"
"v_cvt_pk_bf16_f32 v179, v182, v183\n"
"v_exp_f32 v184, v184\n"
"v_fma_f32 v185, v185, s73, v229\n"
"v_mfma_f32_32x32x16_bf16 v[144:159], a[88:91], a[56:59], v[144:159]\n"
"v_cvt_pk_bf16_f32 v186, 0, v186\n"
"v_exp_f32 v185, v185\n"
"v_fma_f32 v186, v186, s73, v229\n"
"v_cvt_pk_bf16_f32 v187, 0, v187\n"
"v_cvt_pk_bf16_f32 v184, v184, v185\n"
"v_mfma_f32_32x32x16_bf16 v[144:159], a[92:95], a[60:63], v[144:159]\n"
"s_waitcnt lgkmcnt(0)\n"
"v_exp_f32 v186, v186\n"
"v_fma_f32 v187, v187, s73, v229\n"
"v_cvt_pk_bf16_f32 v188, 0, v188\n"
"v_exp_f32 v187, v187\n"
"v_mfma_f32_32x32x16_bf16 v[0:15], a[96:99], v[160:163], v[0:15]\n"
"ds_read_b128 a[64:67], v216 offset:8192\n"
"v_fma_f32 v188, v188, s73, v229\n"
"v_cvt_pk_bf16_f32 v189, 0, v189\n"
"v_cvt_pk_bf16_f32 v185, v186, v187\n"
"v_exp_f32 v188, v188\n"
"v_mfma_f32_32x32x16_bf16 v[32:47], a[100:103], v[160:163], v[32:47]\n"
"s_add_u32 m0, s67, 49152\n"
"ds_read_b128 a[68:71], v217 offset:8192\n"
"v_fma_f32 v189, v189, s73, v229\n"
"v_cvt_pk_bf16_f32 v190, 0, v190\n"
"v_exp_f32 v189, v189\n"
"v_fma_f32 v190, v190, s73, v229\n"
"v_mfma_f32_16x16x32_bf16 v[200:203], v[248:251], v[160:163], v[200:203]\n"
"v_cvt_pk_bf16_f32 v191, 0, v191\n"
"v_cvt_pk_bf16_f32 v186, v188, v189\n"
"v_mfma_f32_32x32x16_bf16 v[64:79], a[104:107], v[160:163], v[64:79]\n"
"buffer_load_dwordx4 v213, s[52:55], s76 offen offset:0 lds\n"
"ds_read_b128 a[72:75], v218 offset:8192\n"
"v_exp_f32 v190, v190\n"
"v_fma_f32 v191, v191, s73, v229\n"
"v_mfma_f32_32x32x16_bf16 v[96:111], a[108:111], v[160:163], v[96:111]\n"
"ds_read_b128 a[76:79], v219 offset:8192\n"
"v_cvt_pk_bf16_f32 v128, 0, v128\n"
"v_exp_f32 v191, v191\n"
"v_fma_f32 v128, v128, s73, v228\n"
"v_cvt_pk_bf16_f32 v129, 0, v129\n"
"v_mfma_f32_32x32x16_bf16 v[16:31], a[96:99], v[176:179], v[16:31]\n"
"buffer_load_dwordx4 v213, s[52:55], s76 offen offset:1024 lds\n"
"ds_read_b128 a[80:83], v220 offset:8192\n"
"v_cvt_pk_bf16_f32 v187, v190, v191\n"
"v_exp_f32 v128, v128\n"
"v_mfma_f32_32x32x16_bf16 v[48:63], a[100:103], v[176:179], v[48:63]\n"
"ds_read_b128 a[84:87], v221 offset:8192\n"
"v_fma_f32 v129, v129, s73, v228\n"
"v_cvt_pk_bf16_f32 v130, 0, v130\n"
"v_exp_f32 v129, v129\n"
"v_fma_f32 v130, v130, s73, v228\n"
"v_mfma_f32_16x16x32_bf16 v[244:247], v[248:251], v[176:179], v[244:247]\n"
"v_cvt_pk_bf16_f32 v131, 0, v131\n"
"v_cvt_pk_bf16_f32 v128, v128, v129\n"
"v_mfma_f32_32x32x16_bf16 v[80:95], a[104:107], v[176:179], v[80:95]\n"
"ds_read_b128 a[88:91], v222 offset:8192\n"
"v_exp_f32 v130, v130\n"
"v_fma_f32 v131, v131, s73, v228\n"
"v_cvt_pk_bf16_f32 v132, 0, v132\n"
"v_mfma_f32_32x32x16_bf16 v[112:127], a[108:111], v[176:179], v[112:127]\n"
"buffer_load_dwordx4 v213, s[52:55], s76 offen offset:2048 lds\n"
"ds_read_b128 a[92:95], v223 offset:8192\n"
"v_exp_f32 v131, v131\n"
"v_fma_f32 v132, v132, s73, v228\n"
"v_mfma_f32_32x32x16_bf16 v[0:15], a[112:115], v[168:171], v[0:15]\n"
"ds_read_b64_tr_b16 a[96:97], v224 offset:24576\n"
"v_cvt_pk_bf16_f32 v133, 0, v133\n"
"v_cvt_pk_bf16_f32 v129, v130, v131\n"
"v_exp_f32 v132, v132\n"
"v_fma_f32 v133, v133, s73, v228\n"
"v_mfma_f32_32x32x16_bf16 v[32:47], a[116:119], v[168:171], v[32:47]\n"
"buffer_load_dwordx4 v213, s[52:55], s76 offen offset:3072 lds\n"
"ds_read_b64_tr_b16 a[98:99], v224 offset:26624\n"
"v_cvt_pk_bf16_f32 v134, 0, v134\n"
"v_exp_f32 v133, v133\n"
"v_mfma_f32_16x16x32_bf16 v[200:203], v[248:251], v[168:171], v[200:203]\n"
"v_fma_f32 v134, v134, s73, v228\n"
"v_cvt_pk_bf16_f32 v135, 0, v135\n"
"v_mfma_f32_32x32x16_bf16 v[64:79], a[120:123], v[168:171], v[64:79]\n"
"s_add_u32 s76, s76, 0x4000\n"
"ds_read_b64_tr_b16 a[100:101], v225 offset:24576\n"
"v_cvt_pk_bf16_f32 v130, v132, v133\n"
"v_exp_f32 v134, v134\n"
"v_fma_f32 v135, v135, s73, v228\n"
"v_cvt_pk_bf16_f32 v136, 0, v136\n"
"v_mfma_f32_32x32x16_bf16 v[96:111], a[124:127], v[168:171], v[96:111]\n"
"s_and_b32 s76, s76, 0x1ffff\n"
"ds_read_b64_tr_b16 a[102:103], v225 offset:26624\n"
"v_exp_f32 v135, v135\n"
"v_fma_f32 v136, v136, s73, v228\n"
"v_cvt_pk_bf16_f32 v137, 0, v137\n"
"v_cvt_pk_bf16_f32 v131, v134, v135\n"
"v_mfma_f32_32x32x16_bf16 v[16:31], a[112:115], v[184:187], v[16:31]\n"
"ds_read_b64_tr_b16 a[104:105], v226 offset:24576\n"
"v_exp_f32 v136, v136\n"
"v_fma_f32 v137, v137, s73, v228\n"
"v_cvt_pk_bf16_f32 v138, 0, v138\n"
"v_mfma_f32_32x32x16_bf16 v[48:63], a[116:119], v[184:187], v[48:63]\n"
"ds_read_b64_tr_b16 a[106:107], v226 offset:26624\n"
"v_exp_f32 v137, v137\n"
"v_fma_f32 v138, v138, s73, v228\n"
"v_cvt_pk_bf16_f32 v139, 0, v139\n"
"v_cvt_pk_bf16_f32 v136, v136, v137\n"
"v_mfma_f32_16x16x32_bf16 v[244:247], v[248:251], v[184:187], v[244:247]\n"
"v_exp_f32 v138, v138\n"
"v_mfma_f32_32x32x16_bf16 v[80:95], a[120:123], v[184:187], v[80:95]\n"
"ds_read_b64_tr_b16 a[108:109], v227 offset:24576\n"
"v_fma_f32 v139, v139, s73, v228\n"
"v_cvt_pk_bf16_f32 v140, 0, v140\n"
"v_exp_f32 v139, v139\n"
"v_fma_f32 v140, v140, s73, v228\n"
"v_mfma_f32_32x32x16_bf16 v[112:127], a[124:127], v[184:187], v[112:127]\n"
"s_waitcnt lgkmcnt(7)\n"
"ds_read_b64_tr_b16 a[110:111], v227 offset:26624\n"
"v_cvt_pk_bf16_f32 v141, 0, v141\n"
"v_cvt_pk_bf16_f32 v137, v138, v139\n"
"v_exp_f32 v140, v140\n"
"v_fma_f32 v141, v141, s73, v228\n"
"v_mfma_f32_32x32x16_bf16 v[160:175], a[64:67], a[0:3], 0\n"
"ds_read_b64_tr_b16 a[112:113], v224 offset:28672\n"
"v_cvt_pk_bf16_f32 v142, 0, v142\n"
"v_exp_f32 v141, v141\n"
"v_fma_f32 v142, v142, s73, v228\n"
"v_cvt_pk_bf16_f32 v143, 0, v143\n"
"v_mfma_f32_32x32x16_bf16 v[160:175], a[68:71], a[4:7], v[160:175]\n"
"ds_read_b64_tr_b16 a[114:115], v224 offset:30720\n"
"v_cvt_pk_bf16_f32 v138, v140, v141\n"
"v_exp_f32 v142, v142\n"
"v_fma_f32 v143, v143, s73, v228\n"
"v_cvt_pk_bf16_f32 v144, 0, v144\n"
"v_mfma_f32_32x32x16_bf16 v[160:175], a[72:75], a[8:11], v[160:175]\n"
"ds_read_b64_tr_b16 a[116:117], v225 offset:28672\n"
"v_exp_f32 v143, v143\n"
"v_fma_f32 v144, v144, s73, v229\n"
"v_cvt_pk_bf16_f32 v145, 0, v145\n"
"v_cvt_pk_bf16_f32 v139, v142, v143\n"
"v_mfma_f32_32x32x16_bf16 v[160:175], a[76:79], a[12:15], v[160:175]\n"
"ds_read_b64_tr_b16 a[118:119], v225 offset:30720\n"
"v_exp_f32 v144, v144\n"
"v_fma_f32 v145, v145, s73, v229\n"
"v_cvt_pk_bf16_f32 v146, 0, v146\n"
"v_mfma_f32_32x32x16_bf16 v[160:175], a[80:83], a[16:19], v[160:175]\n"
"ds_read_b64_tr_b16 a[120:121], v226 offset:28672\n"
"v_exp_f32 v145, v145\n"
"v_fma_f32 v146, v146, s73, v229\n"
"v_cvt_pk_bf16_f32 v147, 0, v147\n"
"v_cvt_pk_bf16_f32 v144, v144, v145\n"
"v_mfma_f32_32x32x16_bf16 v[160:175], a[84:87], a[20:23], v[160:175]\n"
"ds_read_b64_tr_b16 a[122:123], v226 offset:30720\n"
"v_exp_f32 v146, v146\n"
"v_fma_f32 v147, v147, s73, v229\n"
"v_cvt_pk_bf16_f32 v148, 0, v148\n"
"v_mfma_f32_32x32x16_bf16 v[160:175], a[88:91], a[24:27], v[160:175]\n"
"ds_read_b64_tr_b16 a[124:125], v227 offset:28672\n"
"v_exp_f32 v147, v147\n"
"v_fma_f32 v148, v148, s73, v229\n"
"v_cvt_pk_bf16_f32 v149, 0, v149\n"
"v_cvt_pk_bf16_f32 v145, v146, v147\n"
"v_mfma_f32_32x32x16_bf16 v[160:175], a[92:95], a[28:31], v[160:175]\n"
"ds_read_b64_tr_b16 a[126:127], v227 offset:30720\n"
"v_exp_f32 v148, v148\n"
"v_fma_f32 v149, v149, s73, v229\n"
"v_cvt_pk_bf16_f32 v150, 0, v150\n"
"v_mfma_f32_32x32x16_bf16 v[176:191], a[64:67], a[32:35], 0\n"
"v_exp_f32 v149, v149\n"
"v_fma_f32 v150, v150, s73, v229\n"
"v_cvt_pk_bf16_f32 v151, 0, v151\n"
"v_cvt_pk_bf16_f32 v146, v148, v149\n"
"v_mfma_f32_32x32x16_bf16 v[176:191], a[68:71], a[36:39], v[176:191]\n"
"v_exp_f32 v150, v150\n"
"v_fma_f32 v151, v151, s73, v229\n"
"v_cvt_pk_bf16_f32 v152, 0, v152\n"
"v_exp_f32 v151, v151\n"
"v_mfma_f32_32x32x16_bf16 v[176:191], a[72:75], a[40:43], v[176:191]\n"
"v_fma_f32 v152, v152, s73, v229\n"
"v_cvt_pk_bf16_f32 v153, 0, v153\n"
"v_cvt_pk_bf16_f32 v147, v150, v151\n"
"v_exp_f32 v152, v152\n"
"v_fma_f32 v153, v153, s73, v229\n"
"v_mfma_f32_32x32x16_bf16 v[176:191], a[76:79], a[44:47], v[176:191]\n"
"v_cvt_pk_bf16_f32 v154, 0, v154\n"
"v_exp_f32 v153, v153\n"
"v_fma_f32 v154, v154, s73, v229\n"
"v_cvt_pk_bf16_f32 v155, 0, v155\n"
"v_cvt_pk_bf16_f32 v152, v152, v153\n"
"v_mfma_f32_32x32x16_bf16 v[176:191], a[80:83], a[48:51], v[176:191]\n"
"v_exp_f32 v154, v154\n"
"v_fma_f32 v155, v155, s73, v229\n"
"v_cvt_pk_bf16_f32 v156, 0, v156\n"
"v_exp_f32 v155, v155\n"
"v_mfma_f32_32x32x16_bf16 v[176:191], a[84:87], a[52:55], v[176:191]\n"
"v_fma_f32 v156, v156, s73, v229\n"
"v_cvt_pk_bf16_f32 v157, 0, v157\n"
"v_cvt_pk_bf16_f32 v153, v154, v155\n"
"v_exp_f32 v156, v156\n"
"v_fma_f32 v157, v157, s73, v229\n"
"v_mfma_f32_32x32x16_bf16 v[176:191], a[88:91], a[56:59], v[176:191]\n"
"v_cvt_pk_bf16_f32 v158, 0, v158\n"
"v_exp_f32 v157, v157\n"
"v_fma_f32 v158, v158, s73, v229\n"
"v_cvt_pk_bf16_f32 v159, 0, v159\n"
"v_cvt_pk_bf16_f32 v154, v156, v157\n"
"v_mfma_f32_32x32x16_bf16 v[176:191], a[92:95], a[60:63], v[176:191]\n"
"s_waitcnt vmcnt(0) lgkmcnt(0)\n"
"v_exp_f32 v158, v158\n"
"v_fma_f32 v159, v159, s73, v229\n"
"v_cvt_pk_bf16_f32 v160, 0, v160\n"
"s_barrier\n"
"v_mfma_f32_32x32x16_bf16 v[0:15], a[96:99], v[128:131], v[0:15]\n"
"v_exp_f32 v159, v159\n"
"v_fma_f32 v160, v160, s73, v228\n"
"v_cvt_pk_bf16_f32 v161, 0, v161\n"
"v_cvt_pk_bf16_f32 v155, v158, v159\n"
"v_mfma_f32_32x32x16_bf16 v[32:47], a[100:103], v[128:131], v[32:47]\n"
"v_exp_f32 v160, v160\n"
"v_fma_f32 v161, v161, s73, v228\n"
"v_cvt_pk_bf16_f32 v162, 0, v162\n"
"v_exp_f32 v161, v161\n"
"v_mfma_f32_16x16x32_bf16 v[200:203], v[248:251], v[128:131], v[200:203]\n"
"v_fma_f32 v162, v162, s73, v228\n"
"v_cvt_pk_bf16_f32 v163, 0, v163\n"
"v_mfma_f32_32x32x16_bf16 v[64:79], a[104:107], v[128:131], v[64:79]\n"
"v_cvt_pk_bf16_f32 v160, v160, v161\n"
"v_exp_f32 v162, v162\n"
"v_fma_f32 v163, v163, s73, v228\n"
"v_cvt_pk_bf16_f32 v164, 0, v164\n"
"v_exp_f32 v163, v163\n"
"v_mfma_f32_32x32x16_bf16 v[96:111], a[108:111], v[128:131], v[96:111]\n"
"v_fma_f32 v164, v164, s73, v228\n"
"v_cvt_pk_bf16_f32 v165, 0, v165\n"
"v_cvt_pk_bf16_f32 v161, v162, v163\n"
"v_exp_f32 v164, v164\n"
"v_fma_f32 v165, v165, s73, v228\n"
"v_cvt_pk_bf16_f32 v166, 0, v166\n"
"v_mfma_f32_32x32x16_bf16 v[16:31], a[96:99], v[144:147], v[16:31]\n"
"ds_read_b64_tr_b16 a[96:97], v224 offset:49152\n"
"ds_read_b64_tr_b16 a[98:99], v224 offset:51200\n"
"v_exp_f32 v165, v165\n"
"v_fma_f32 v166, v166, s73, v228\n"
"v_cvt_pk_bf16_f32 v167, 0, v167\n"
"v_cvt_pk_bf16_f32 v162, v164, v165\n"
"v_mfma_f32_32x32x16_bf16 v[48:63], a[100:103], v[144:147], v[48:63]\n"
"ds_read_b64_tr_b16 a[100:101], v225 offset:49152\n"
"ds_read_b64_tr_b16 a[102:103], v225 offset:51200\n"
"v_exp_f32 v166, v166\n"
"v_fma_f32 v167, v167, s73, v228\n"
"v_cvt_pk_bf16_f32 v168, 0, v168\n"
"v_mfma_f32_16x16x32_bf16 v[244:247], v[248:251], v[144:147], v[244:247]\n"
"v_exp_f32 v167, v167\n"
"v_mfma_f32_32x32x16_bf16 v[80:95], a[104:107], v[144:147], v[80:95]\n"
"ds_read_b64_tr_b16 a[104:105], v226 offset:49152\n"
"ds_read_b64_tr_b16 a[106:107], v226 offset:51200\n"
"v_fma_f32 v168, v168, s73, v228\n"
"v_cvt_pk_bf16_f32 v169, 0, v169\n"
"v_cvt_pk_bf16_f32 v163, v166, v167\n"
"v_exp_f32 v168, v168\n"
"v_mfma_f32_32x32x16_bf16 v[112:127], a[108:111], v[144:147], v[112:127]\n"
"ds_read_b64_tr_b16 a[108:109], v227 offset:49152\n"
"ds_read_b64_tr_b16 a[110:111], v227 offset:51200\n"
"v_fma_f32 v169, v169, s73, v228\n"
"v_cvt_pk_bf16_f32 v170, 0, v170\n"
"v_exp_f32 v169, v169\n"
"v_fma_f32 v170, v170, s73, v228\n"
"v_mfma_f32_32x32x16_bf16 v[0:15], a[112:115], v[136:139], v[0:15]\n"
"v_cvt_pk_bf16_f32 v171, 0, v171\n"
"v_cvt_pk_bf16_f32 v168, v168, v169\n"
"v_exp_f32 v170, v170\n"
"v_fma_f32 v171, v171, s73, v228\n"
"v_cvt_pk_bf16_f32 v172, 0, v172\n"
"v_mfma_f32_32x32x16_bf16 v[32:47], a[116:119], v[136:139], v[32:47]\n"
"v_exp_f32 v171, v171\n"
"v_fma_f32 v172, v172, s73, v228\n"
"v_cvt_pk_bf16_f32 v173, 0, v173\n"
"v_cvt_pk_bf16_f32 v169, v170, v171\n"
"v_exp_f32 v172, v172\n"
"v_mfma_f32_16x16x32_bf16 v[200:203], v[248:251], v[136:139], v[200:203]\n"
"v_fma_f32 v173, v173, s73, v228\n"
"v_cvt_pk_bf16_f32 v174, 0, v174\n"
"v_mfma_f32_32x32x16_bf16 v[64:79], a[120:123], v[136:139], v[64:79]\n"
"v_exp_f32 v173, v173\n"
"v_fma_f32 v174, v174, s73, v228\n"
"v_cvt_pk_bf16_f32 v175, 0, v175\n"
"v_cvt_pk_bf16_f32 v170, v172, v173\n"
"v_exp_f32 v174, v174\n"
"v_mfma_f32_32x32x16_bf16 v[96:111], a[124:127], v[136:139], v[96:111]\n"
"v_fma_f32 v175, v175, s73, v228\n"
"v_cvt_pk_bf16_f32 v176, 0, v176\n"
"v_exp_f32 v175, v175\n"
"v_fma_f32 v176, v176, s73, v229\n"
"v_cvt_pk_bf16_f32 v177, 0, v177\n"
"v_cvt_pk_bf16_f32 v171, v174, v175\n"
"v_mfma_f32_32x32x16_bf16 v[16:31], a[112:115], v[152:155], v[16:31]\n"
"ds_read_b64_tr_b16 a[112:113], v224 offset:53248\n"
"ds_read_b64_tr_b16 a[114:115], v224 offset:55296\n"
"v_exp_f32 v176, v176\n"
"v_fma_f32 v177, v177, s73, v229\n"
"v_cvt_pk_bf16_f32 v178, 0, v178\n"
"v_mfma_f32_32x32x16_bf16 v[48:63], a[116:119], v[152:155], v[48:63]\n"
"ds_read_b64_tr_b16 a[116:117], v225 offset:53248\n"
"ds_read_b64_tr_b16 a[118:119], v225 offset:55296\n"
"v_exp_f32 v177, v177\n"
"v_fma_f32 v178, v178, s73, v229\n"
"v_cvt_pk_bf16_f32 v179, 0, v179\n"
"v_cvt_pk_bf16_f32 v176, v176, v177\n"
"v_mfma_f32_16x16x32_bf16 v[244:247], v[248:251], v[152:155], v[244:247]\n"
"v_exp_f32 v178, v178\n"
"v_mfma_f32_32x32x16_bf16 v[80:95], a[120:123], v[152:155], v[80:95]\n"
"ds_read_b64_tr_b16 a[120:121], v226 offset:53248\n"
"ds_read_b64_tr_b16 a[122:123], v226 offset:55296\n"
"v_fma_f32 v179, v179, s73, v229\n"
"v_cvt_pk_bf16_f32 v180, 0, v180\n"
"v_exp_f32 v179, v179\n"
"v_fma_f32 v180, v180, s73, v229\n"
"v_mfma_f32_32x32x16_bf16 v[112:127], a[124:127], v[152:155], v[112:127]\n"
"ds_read_b64_tr_b16 a[124:125], v227 offset:53248\n"
"ds_read_b64_tr_b16 a[126:127], v227 offset:55296\n"
"v_cvt_pk_bf16_f32 v181, 0, v181\n"
"v_cvt_pk_bf16_f32 v177, v178, v179\n"
"v_exp_f32 v180, v180\n"
"s_waitcnt lgkmcnt(0)\n"
"s_barrier\n"
"v_mfma_f32_32x32x16_bf16 v[0:15], a[96:99], v[160:163], v[0:15]\n"
"v_fma_f32 v181, v181, s73, v229\n"
"v_cvt_pk_bf16_f32 v182, 0, v182\n"
"v_exp_f32 v181, v181\n"
"v_fma_f32 v182, v182, s73, v229\n"
"s_add_u32 s74, s64, 0\n"
"s_and_b32 s74, s74, 3\n"
"v_mfma_f32_16x16x32_bf16 v[200:203], v[248:251], v[160:163], v[200:203]\n"
"v_cvt_pk_bf16_f32 v183, 0, v183\n"
"v_cvt_pk_bf16_f32 v178, v180, v181\n"
"v_mfma_f32_32x32x16_bf16 v[32:47], a[100:103], v[160:163], v[32:47]\n"
"v_exp_f32 v182, v182\n"
"v_fma_f32 v183, v183, s73, v229\n"
"v_cvt_pk_bf16_f32 v184, 0, v184\n"
"s_lshl_b32 s74, s74, 14\n"
"v_lshl_add_u32 v216, v192, 4, s74\n"
"v_mfma_f32_32x32x16_bf16 v[64:79], a[104:107], v[160:163], v[64:79]\n"
"v_exp_f32 v183, v183\n"
"v_fma_f32 v184, v184, s73, v229\n"
"v_cvt_pk_bf16_f32 v185, 0, v185\n"
"v_cvt_pk_bf16_f32 v179, v182, v183\n"
"s_add_u32 s74, s64, 1\n"
"s_and_b32 s74, s74, 3\n"
"v_mfma_f32_32x32x16_bf16 v[96:111], a[108:111], v[160:163], v[96:111]\n"
"v_exp_f32 v184, v184\n"
"v_fma_f32 v185, v185, s73, v229\n"
"v_cvt_pk_bf16_f32 v186, 0, v186\n"
"s_lshl_b32 s74, s74, 14\n"
"v_lshl_add_u32 v217, v192, 4, s74\n"
"v_mfma_f32_32x32x16_bf16 v[16:31], a[96:99], v[176:179], v[16:31]\n"
"v_exp_f32 v185, v185\n"
"v_fma_f32 v186, v186, s73, v229\n"
"v_cvt_pk_bf16_f32 v187, 0, v187\n"
"s_add_u32 s74, s64, 2\n"
"s_and_b32 s74, s74, 3\n"
"v_mfma_f32_16x16x32_bf16 v[244:247], v[248:251], v[176:179], v[244:247]\n"
"v_cvt_pk_bf16_f32 v184, v184, v185\n"
"v_mfma_f32_32x32x16_bf16 v[48:63], a[100:103], v[176:179], v[48:63]\n"
"v_exp_f32 v186, v186\n"
"v_fma_f32 v187, v187, s73, v229\n"
"v_cvt_pk_bf16_f32 v188, 0, v188\n"
"s_lshl_b32 s74, s74, 14\n"
"v_lshl_add_u32 v218, v192, 4, s74\n"
"v_mfma_f32_32x32x16_bf16 v[80:95], a[104:107], v[176:179], v[80:95]\n"
"v_exp_f32 v187, v187\n"
"v_fma_f32 v188, v188, s73, v229\n"
"v_cvt_pk_bf16_f32 v189, 0, v189\n"
"s_add_u32 s74, s64, 3\n"
"s_and_b32 s74, s74, 3\n"
"v_mfma_f32_32x32x16_bf16 v[112:127], a[108:111], v[176:179], v[112:127]\n"
"v_cvt_pk_bf16_f32 v185, v186, v187\n"
"v_exp_f32 v188, v188\n"
"v_fma_f32 v189, v189, s73, v229\n"
"s_lshl_b32 s74, s74, 14\n"
"v_lshl_add_u32 v219, v192, 4, s74\n"
"v_mfma_f32_32x32x16_bf16 v[0:15], a[112:115], v[168:171], v[0:15]\n"
"v_cvt_pk_bf16_f32 v190, 0, v190\n"
"v_exp_f32 v189, v189\n"
"s_mov_b32 s40, s8\n"
"s_and_b32 s41, s9, 0xffff\n"
"s_mov_b32 s42, 0x200000\n"
"v_mfma_f32_16x16x32_bf16 v[200:203], v[248:251], v[168:171], v[200:203]\n"
"v_fma_f32 v190, v190, s73, v229\n"
"v_cvt_pk_bf16_f32 v191, 0, v191\n"
"v_mfma_f32_32x32x16_bf16 v[32:47], a[116:119], v[168:171], v[32:47]\n"
"v_cvt_pk_bf16_f32 v186, v188, v189\n"
"v_exp_f32 v190, v190\n"
"s_mov_b32 s43, 0x20000\n"
"v_and_b32 v195, 7, v192\n"
"v_lshlrev_b32 v195, 10, v195\n"
"v_mfma_f32_32x32x16_bf16 v[64:79], a[120:123], v[168:171], v[64:79]\n"
"v_fma_f32 v191, v191, s73, v229\n"
"v_exp_f32 v191, v191\n"
"s_nop 0\n"
"v_cvt_pk_bf16_f32 v187, v190, v191\n"
"v_lshrrev_b32 v196, 3, v192\n"
"v_lshl_add_u32 v195, v196, 4, v195\n"
"v_mfma_f32_32x32x16_bf16 v[96:111], a[124:127], v[168:171], v[96:111]\n"
"v_lshlrev_b32 v196, 5, v196\n"
"v_bfe_u32 v197, v192, 2, 1\n"
"v_lshl_add_u32 v196, v197, 8, v196\n"
"v_mfma_f32_32x32x16_bf16 v[16:31], a[112:115], v[184:187], v[16:31]\n"
"v_and_b32 v197, 3, v192\n"
"v_lshl_add_u32 v196, v197, 3, v196\n"
"s_lshl_b32 s8, s78, 3\n"
"v_mfma_f32_16x16x32_bf16 v[244:247], v[248:251], v[184:187], v[244:247]\n"
"v_mfma_f32_32x32x16_bf16 v[48:63], a[116:119], v[184:187], v[48:63]\n"
"s_add_u32 s8, s8, s79\n"
"s_lshl_b32 s8, s8, 13\n"
"v_mfma_f32_32x32x16_bf16 v[80:95], a[120:123], v[184:187], v[80:95]\n"
"s_lshl_b32 s9, s64, 8\n"
"s_add_u32 s8, s8, s9\n"
"s_lshl_b32 s9, s64, 9\n"
"s_nop 0\n"
"v_add_f32 v240, v200, v244\n"
"v_rcp_f32 v230, v200\n"
"v_mfma_f32_32x32x16_bf16 v[112:127], a[124:127], v[184:187], v[112:127]\n"
"s_add_u32 s9, s9, 40960\n"
"s_add_u32 s10, s9, 4096\n"
"v_cmp_nge_f32 vcc, 0x4d000000, v240\n"
"v_rcp_f32 v231, v244\n"
"s_cbranch_vccnz Lslow_9_%=\n"
"Ljoin_10_%=:\n"
"v_pk_mul_f32 v[0:1], v[0:1], v[230:231] op_sel_hi:[1,0]\n"
"v_pk_mul_f32 v[2:3], v[2:3], v[230:231] op_sel_hi:[1,0]\n"
"v_pk_mul_f32 v[4:5], v[4:5], v[230:231] op_sel_hi:[1,0]\n"
"v_pk_mul_f32 v[6:7], v[6:7], v[230:231] op_sel_hi:[1,0]\n"
"v_cvt_pk_bf16_f32 v0, v0, v1\n"
"v_cvt_pk_bf16_f32 v1, v2, v3\n"
"v_cvt_pk_bf16_f32 v2, v4, v5\n"
"v_cvt_pk_bf16_f32 v3, v6, v7\n"
"v_pk_mul_f32 v[8:9], v[8:9], v[230:231] op_sel_hi:[1,0]\n"
"v_pk_mul_f32 v[10:11], v[10:11], v[230:231] op_sel_hi:[1,0]\n"
"v_pk_mul_f32 v[12:13], v[12:13], v[230:231] op_sel_hi:[1,0]\n"
"v_pk_mul_f32 v[14:15], v[14:15], v[230:231] op_sel_hi:[1,0]\n"
"v_cvt_pk_bf16_f32 v4, v8, v9\n"
"v_cvt_pk_bf16_f32 v5, v10, v11\n"
"v_cvt_pk_bf16_f32 v6, v12, v13\n"
"v_cvt_pk_bf16_f32 v7, v14, v15\n"
"v_permlane16_swap_b32 v0, v4\n"
"v_permlane16_swap_b32 v1, v5\n"
"v_permlane16_swap_b32 v2, v6\n"
"v_permlane16_swap_b32 v3, v7\n"
"v_pk_mul_f32 v[16:17], v[16:17], v[230:231] op_sel:[0,1] op_sel_hi:[1,1]\n"
"v_pk_mul_f32 v[18:19], v[18:19], v[230:231] op_sel:[0,1] op_sel_hi:[1,1]\n"
"v_pk_mul_f32 v[20:21], v[20:21], v[230:231] op_sel:[0,1] op_sel_hi:[1,1]\n"
"v_pk_mul_f32 v[22:23], v[22:23], v[230:231] op_sel:[0,1] op_sel_hi:[1,1]\n"
"v_cvt_pk_bf16_f32 v8, v16, v17\n"
"v_cvt_pk_bf16_f32 v9, v18, v19\n"
"v_cvt_pk_bf16_f32 v10, v20, v21\n"
"v_cvt_pk_bf16_f32 v11, v22, v23\n"
"v_pk_mul_f32 v[24:25], v[24:25], v[230:231] op_sel:[0,1] op_sel_hi:[1,1]\n"
"v_pk_mul_f32 v[26:27], v[26:27], v[230:231] op_sel:[0,1] op_sel_hi:[1,1]\n"
"v_pk_mul_f32 v[28:29], v[28:29], v[230:231] op_sel:[0,1] op_sel_hi:[1,1]\n"
"v_pk_mul_f32 v[30:31], v[30:31], v[230:231] op_sel:[0,1] op_sel_hi:[1,1]\n"
"v_cvt_pk_bf16_f32 v12, v24, v25\n"
"v_cvt_pk_bf16_f32 v13, v26, v27\n"
"v_cvt_pk_bf16_f32 v14, v28, v29\n"
"v_cvt_pk_bf16_f32 v15, v30, v31\n"
"v_permlane16_swap_b32 v8, v12\n"
"v_permlane16_swap_b32 v9, v13\n"
"v_permlane16_swap_b32 v10, v14\n"
"v_permlane16_swap_b32 v11, v15\n"
"v_pk_mul_f32 v[32:33], v[32:33], v[230:231] op_sel_hi:[1,0]\n"
"v_pk_mul_f32 v[34:35], v[34:35], v[230:231] op_sel_hi:[1,0]\n"
"v_pk_mul_f32 v[36:37], v[36:37], v[230:231] op_sel_hi:[1,0]\n"
"v_pk_mul_f32 v[38:39], v[38:39], v[230:231] op_sel_hi:[1,0]\n"
"v_cvt_pk_bf16_f32 v16, v32, v33\n"
"v_cvt_pk_bf16_f32 v17, v34, v35\n"
"v_cvt_pk_bf16_f32 v18, v36, v37\n"
"v_cvt_pk_bf16_f32 v19, v38, v39\n"
"v_pk_mul_f32 v[40:41], v[40:41], v[230:231] op_sel_hi:[1,0]\n"
"v_pk_mul_f32 v[42:43], v[42:43], v[230:231] op_sel_hi:[1,0]\n"
"v_pk_mul_f32 v[44:45], v[44:45], v[230:231] op_sel_hi:[1,0]\n"
"v_pk_mul_f32 v[46:47], v[46:47], v[230:231] op_sel_hi:[1,0]\n"
"v_cvt_pk_bf16_f32 v20, v40, v41\n"
"v_cvt_pk_bf16_f32 v21, v42, v43\n"
"v_cvt_pk_bf16_f32 v22, v44, v45\n"
"v_cvt_pk_bf16_f32 v23, v46, v47\n"
"v_permlane16_swap_b32 v16, v20\n"
"v_permlane16_swap_b32 v17, v21\n"
"v_permlane16_swap_b32 v18, v22\n"
"v_permlane16_swap_b32 v19, v23\n"
"v_pk_mul_f32 v[48:49], v[48:49], v[230:231] op_sel:[0,1] op_sel_hi:[1,1]\n"
"v_pk_mul_f32 v[50:51], v[50:51], v[230:231] op_sel:[0,1] op_sel_hi:[1,1]\n"
"v_pk_mul_f32 v[52:53], v[52:53], v[230:231] op_sel:[0,1] op_sel_hi:[1,1]\n"
"v_pk_mul_f32 v[54:55], v[54:55], v[230:231] op_sel:[0,1] op_sel_hi:[1,1]\n"
"v_cvt_pk_bf16_f32 v24, v48, v49\n"
"v_cvt_pk_bf16_f32 v25, v50, v51\n"
"v_cvt_pk_bf16_f32 v26, v52, v53\n"
"v_cvt_pk_bf16_f32 v27, v54, v55\n"
"v_pk_mul_f32 v[56:57], v[56:57], v[230:231] op_sel:[0,1] op_sel_hi:[1,1]\n"
"v_pk_mul_f32 v[58:59], v[58:59], v[230:231] op_sel:[0,1] op_sel_hi:[1,1]\n"
"v_pk_mul_f32 v[60:61], v[60:61], v[230:231] op_sel:[0,1] op_sel_hi:[1,1]\n"
"v_pk_mul_f32 v[62:63], v[62:63], v[230:231] op_sel:[0,1] op_sel_hi:[1,1]\n"
"v_cvt_pk_bf16_f32 v28, v56, v57\n"
"v_cvt_pk_bf16_f32 v29, v58, v59\n"
"v_cvt_pk_bf16_f32 v30, v60, v61\n"
"v_cvt_pk_bf16_f32 v31, v62, v63\n"
"v_permlane16_swap_b32 v24, v28\n"
"v_permlane16_swap_b32 v25, v29\n"
"v_permlane16_swap_b32 v26, v30\n"
"v_permlane16_swap_b32 v27, v31\n"
"v_pk_mul_f32 v[64:65], v[64:65], v[230:231] op_sel_hi:[1,0]\n"
"v_pk_mul_f32 v[66:67], v[66:67], v[230:231] op_sel_hi:[1,0]\n"
"v_pk_mul_f32 v[68:69], v[68:69], v[230:231] op_sel_hi:[1,0]\n"
"v_pk_mul_f32 v[70:71], v[70:71], v[230:231] op_sel_hi:[1,0]\n"
"Lst10_%=:\n"
"s_cbranch_execz Lback0_%=\n"
"v_cvt_pk_bf16_f32 v32, v64, v65\n"
"v_cvt_pk_bf16_f32 v33, v66, v67\n"
"v_cvt_pk_bf16_f32 v34, v68, v69\n"
"v_cvt_pk_bf16_f32 v35, v70, v71\n"
"v_pk_mul_f32 v[72:73], v[72:73], v[230:231] op_sel_hi:[1,0]\n"
"v_pk_mul_f32 v[74:75], v[74:75], v[230:231] op_sel_hi:[1,0]\n"
"v_pk_mul_f32 v[76:77], v[76:77], v[230:231] op_sel_hi:[1,0]\n"
"v_pk_mul_f32 v[78:79], v[78:79], v[230:231] op_sel_hi:[1,0]\n"
"v_cvt_pk_bf16_f32 v36, v72, v73\n"
"v_cvt_pk_bf16_f32 v37, v74, v75\n"
"v_cvt_pk_bf16_f32 v38, v76, v77\n"
"v_cvt_pk_bf16_f32 v39, v78, v79\n"
"v_permlane16_swap_b32 v32, v36\n"
"v_permlane16_swap_b32 v33, v37\n"
"v_permlane16_swap_b32 v34, v38\n"
"v_permlane16_swap_b32 v35, v39\n"
"v_pk_mul_f32 v[80:81], v[80:81], v[230:231] op_sel:[0,1] op_sel_hi:[1,1]\n"
"v_pk_mul_f32 v[82:83], v[82:83], v[230:231] op_sel:[0,1] op_sel_hi:[1,1]\n"
"v_pk_mul_f32 v[84:85], v[84:85], v[230:231] op_sel:[0,1] op_sel_hi:[1,1]\n"
"v_pk_mul_f32 v[86:87], v[86:87], v[230:231] op_sel:[0,1] op_sel_hi:[1,1]\n"
"v_cvt_pk_bf16_f32 v40, v80, v81\n"
"v_cvt_pk_bf16_f32 v41, v82, v83\n"
"v_cvt_pk_bf16_f32 v42, v84, v85\n"
"v_cvt_pk_bf16_f32 v43, v86, v87\n"
"v_pk_mul_f32 v[88:89], v[88:89], v[230:231] op_sel:[0,1] op_sel_hi:[1,1]\n"
"v_pk_mul_f32 v[90:91], v[90:91], v[230:231] op_sel:[0,1] op_sel_hi:[1,1]\n"
"v_pk_mul_f32 v[92:93], v[92:93], v[230:231] op_sel:[0,1] op_sel_hi:[1,1]\n"
"v_pk_mul_f32 v[94:95], v[94:95], v[230:231] op_sel:[0,1] op_sel_hi:[1,1]\n"
"v_cvt_pk_bf16_f32 v44, v88, v89\n"
"v_cvt_pk_bf16_f32 v45, v90, v91\n"
"v_cvt_pk_bf16_f32 v46, v92, v93\n"
"v_cvt_pk_bf16_f32 v47, v94, v95\n"
"v_permlane16_swap_b32 v40, v44\n"
"v_permlane16_swap_b32 v41, v45\n"
"v_permlane16_swap_b32 v42, v46\n"
"v_permlane16_swap_b32 v43, v47\n"
"v_pk_mul_f32 v[96:97], v[96:97], v[230:231] op_sel_hi:[1,0]\n"
"v_pk_mul_f32 v[98:99], v[98:99], v[230:231] op_sel_hi:[1,0]\n"
"v_pk_mul_f32 v[100:101], v[100:101], v[230:231] op_sel_hi:[1,0]\n"
"v_pk_mul_f32 v[102:103], v[102:103], v[230:231] op_sel_hi:[1,0]\n"
"v_cvt_pk_bf16_f32 v48, v96, v97\n"
"v_cvt_pk_bf16_f32 v49, v98, v99\n"
"v_cvt_pk_bf16_f32 v50, v100, v101\n"
"v_cvt_pk_bf16_f32 v51, v102, v103\n"
"v_pk_mul_f32 v[104:105], v[104:105], v[230:231] op_sel_hi:[1,0]\n"
"v_pk_mul_f32 v[106:107], v[106:107], v[230:231] op_sel_hi:[1,0]\n"
"v_pk_mul_f32 v[108:109], v[108:109], v[230:231] op_sel_hi:[1,0]\n"
"v_pk_mul_f32 v[110:111], v[110:111], v[230:231] op_sel_hi:[1,0]\n"
"v_cvt_pk_bf16_f32 v52, v104, v105\n"
"v_cvt_pk_bf16_f32 v53, v106, v107\n"
"v_cvt_pk_bf16_f32 v54, v108, v109\n"
"v_cvt_pk_bf16_f32 v55, v110, v111\n"
"v_permlane16_swap_b32 v48, v52\n"
"v_permlane16_swap_b32 v49, v53\n"
"v_permlane16_swap_b32 v50, v54\n"
"v_permlane16_swap_b32 v51, v55\n"
"v_pk_mul_f32 v[112:113], v[112:113], v[230:231] op_sel:[0,1] op_sel_hi:[1,1]\n"
"v_pk_mul_f32 v[114:115], v[114:115], v[230:231] op_sel:[0,1] op_sel_hi:[1,1]\n"
"v_pk_mul_f32 v[116:117], v[116:117], v[230:231] op_sel:[0,1] op_sel_hi:[1,1]\n"
"v_pk_mul_f32 v[118:119], v[118:119], v[230:231] op_sel:[0,1] op_sel_hi:[1,1]\n"
"v_cvt_pk_bf16_f32 v56, v112, v113\n"
"v_cvt_pk_bf16_f32 v57, v114, v115\n"
"v_cvt_pk_bf16_f32 v58, v116, v117\n"
"v_cvt_pk_bf16_f32 v59, v118, v119\n"
"v_pk_mul_f32 v[120:121], v[120:121], v[230:231] op_sel:[0,1] op_sel_hi:[1,1]\n"
"v_pk_mul_f32 v[122:123], v[122:123], v[230:231] op_sel:[0,1] op_sel_hi:[1,1]\n"
"v_pk_mul_f32 v[124:125], v[124:125], v[230:231] op_sel:[0,1] op_sel_hi:[1,1]\n"
"v_pk_mul_f32 v[126:127], v[126:127], v[230:231] op_sel:[0,1] op_sel_hi:[1,1]\n"
"v_cvt_pk_bf16_f32 v60, v120, v121\n"
"v_cvt_pk_bf16_f32 v61, v122, v123\n"
"v_cvt_pk_bf16_f32 v62, v124, v125\n"
"v_cvt_pk_bf16_f32 v63, v126, v127\n"
"v_permlane16_swap_b32 v56, v60\n"
"v_permlane16_swap_b32 v57, v61\n"
"v_permlane16_swap_b32 v58, v62\n"
"v_permlane16_swap_b32 v59, v63\n"
"v_mfma_f32_16x16x32_bf16 v[128:131], a[128:131], v[0:3], 0\n"
"ds_write_b128 v216, v[0:3] offset:0\n"
"v_mfma_f32_16x16x32_bf16 v[132:135], a[136:139], v[0:3], 0\n"
"ds_write_b128 v216, v[4:7] offset:2048\n"
"v_mfma_f32_16x16x32_bf16 v[144:147], a[128:131], v[8:11], 0\n"
"ds_write_b128 v216, v[8:11] offset:1024\n"
"v_mfma_f32_16x16x32_bf16 v[148:151], a[136:139], v[8:11], 0\n"
"buffer_load_dwordx4 v[244:247], v195, s[40:43], s8 offen offset:0\n"
"v_mfma_f32_16x16x32_bf16 v[160:163], a[132:135], v[0:3], 0\n"
"ds_write_b128 v216, v[12:15] offset:3072\n"
"v_mfma_f32_16x16x32_bf16 v[164:167], a[140:143], v[0:3], 0\n"
"buffer_load_dwordx4 v[248:251], v195, s[40:43], s8 offen offset:128\n"
"v_mfma_f32_16x16x32_bf16 v[176:179], a[132:135], v[8:11], 0\n"
"ds_write_b128 v216, v[16:19] offset:4096\n"
"v_mfma_f32_16x16x32_bf16 v[180:183], a[140:143], v[8:11], 0\n"
"v_mfma_f32_16x16x32_bf16 v[136:139], a[128:131], v[4:7], 0\n"
"ds_write_b128 v216, v[20:23] offset:6144\n"
"v_mfma_f32_16x16x32_bf16 v[140:143], a[136:139], v[4:7], 0\n"
"buffer_load_dwordx2 v[236:237], v196, s[60:63], s9 offen offset:0 sc1\n"
"v_mfma_f32_16x16x32_bf16 v[152:155], a[128:131], v[12:15], 0\n"
"ds_write_b128 v216, v[24:27] offset:5120\n"
"v_mfma_f32_16x16x32_bf16 v[156:159], a[136:139], v[12:15], 0\n"
"v_mfma_f32_16x16x32_bf16 v[168:171], a[132:135], v[4:7], 0\n"
"ds_write_b128 v216, v[28:31] offset:7168\n"
"v_mfma_f32_16x16x32_bf16 v[172:175], a[140:143], v[4:7], 0\n"
"buffer_load_dwordx2 v[238:239], v196, s[60:63], s9 offen offset:2048 sc1\n"
"v_mfma_f32_16x16x32_bf16 v[184:187], a[132:135], v[12:15], 0\n"
"ds_write_b128 v216, v[32:35] offset:8192\n"
"ds_write_b128 v216, v[36:39] offset:10240\n"
"v_mfma_f32_16x16x32_bf16 v[188:191], a[140:143], v[12:15], 0\n"
"v_mfma_f32_16x16x32_bf16 v[128:131], a[144:147], v[16:19], v[128:131]\n"
"ds_write_b128 v216, v[40:43] offset:9216\n"
"v_mfma_f32_16x16x32_bf16 v[132:135], a[152:155], v[16:19], v[132:135]\n"
"buffer_load_dwordx2 v[242:243], v196, s[60:63], s10 offen offset:0 sc1\n"
"v_mfma_f32_16x16x32_bf16 v[144:147], a[144:147], v[24:27], v[144:147]\n"
"ds_write_b128 v216, v[44:47] offset:11264\n"
"v_mfma_f32_16x16x32_bf16 v[148:151], a[152:155], v[24:27], v[148:151]\n"
"v_mfma_f32_16x16x32_bf16 v[160:163], a[148:151], v[16:19], v[160:163]\n"
"ds_write_b128 v216, v[48:51] offset:12288\n"
"v_mfma_f32_16x16x32_bf16 v[164:167], a[156:159], v[16:19], v[164:167]\n"
"Lcp_12_%=:\n"
"v_mfma_f32_16x16x32_bf16 v[176:179], a[148:151], v[24:27], v[176:179]\n"
"ds_write_b128 v216, v[52:55] offset:14336\n"
"v_lshl_add_u32 v254, v192, 2, s7\n"
"v_mfma_f32_16x16x32_bf16 v[180:183], a[156:159], v[24:27], v[180:183]\n"
"v_add_u32 v254, Lcp_12_%=-Lpc0_%=+1024, v254\n"
"v_min_u32 v254, Lfin_%=-Lpc0_%=, v254\n"
"v_mfma_f32_16x16x32_bf16 v[136:139], a[144:147], v[20:23], v[136:139]\n"
"ds_write_b128 v216, v[56:59] offset:13312\n"
"v_mfma_f32_16x16x32_bf16 v[140:143], a[152:155], v[20:23], v[140:143]\n"
"global_load_dword v255, v254, s[18:19]\n"
"v_mfma_f32_16x16x32_bf16 v[152:155], a[144:147], v[28:31], v[152:155]\n"
"ds_write_b128 v216, v[60:63] offset:15360\n"
"v_mfma_f32_16x16x32_bf16 v[156:159], a[152:155], v[28:31], v[156:159]\n"
"v_mfma_f32_16x16x32_bf16 v[168:171], a[148:151], v[20:23], v[168:171]\n"
"s_mov_b32 s40, s20\n"
"v_mfma_f32_16x16x32_bf16 v[172:175], a[156:159], v[20:23], v[172:175]\n"
"s_and_b32 s41, s21, 0xffff\n"
"v_mfma_f32_16x16x32_bf16 v[184:187], a[148:151], v[28:31], v[184:187]\n"
"s_mov_b32 s42, 0x1c0000\n"
"s_waitcnt lgkmcnt(0)\n"
"v_mfma_f32_16x16x32_bf16 v[188:191], a[156:159], v[28:31], v[188:191]\n"
"s_barrier\n"
"v_mfma_f32_16x16x32_bf16 v[128:131], a[160:163], v[32:35], v[128:131]\n"
"ds_read_b128 a[0:3], v217 offset:0\n"
"ds_read_b128 a[4:7], v217 offset:1024\n"
"ds_read_b128 a[8:11], v217 offset:2048\n"
"v_mfma_f32_16x16x32_bf16 v[132:135], a[168:171], v[32:35], v[132:135]\n"
"s_mov_b32 s43, 0x20000\n"
"s_lshl_b32 s30, s31, 2\n"
"v_mfma_f32_16x16x32_bf16 v[144:147], a[160:163], v[40:43], v[144:147]\n"
"ds_read_b128 a[12:15], v217 offset:3072\n"
"ds_read_b128 a[16:19], v217 offset:4096\n"
"v_mfma_f32_16x16x32_bf16 v[148:151], a[168:171], v[40:43], v[148:151]\n"
"s_add_u32 s30, s30, s64\n"
"v_mfma_f32_16x16x32_bf16 v[160:163], a[164:167], v[32:35], v[160:163]\n"
"ds_read_b128 a[20:23], v217 offset:5120\n"
"ds_read_b128 a[24:27], v217 offset:6144\n"
"v_mfma_f32_16x16x32_bf16 v[164:167], a[172:175], v[32:35], v[164:167]\n"
"s_and_b32 s34, s6, 1\n"
"v_mfma_f32_16x16x32_bf16 v[176:179], a[164:167], v[40:43], v[176:179]\n"
"ds_read_b128 a[28:31], v217 offset:7168\n"
"ds_read_b128 a[32:35], v217 offset:8192\n"
"ds_read_b128 a[36:39], v217 offset:9216\n"
"v_mfma_f32_16x16x32_bf16 v[180:183], a[172:175], v[40:43], v[180:183]\n"
"s_mul_i32 s30, s30, 14336\n"
"s_mul_i32 s34, s34, 1792\n"
"v_mfma_f32_16x16x32_bf16 v[136:139], a[160:163], v[36:39], v[136:139]\n"
"ds_read_b128 a[40:43], v217 offset:10240\n"
"ds_read_b128 a[44:47], v217 offset:11264\n"
"v_mfma_f32_16x16x32_bf16 v[140:143], a[168:171], v[36:39], v[140:143]\n"
"s_add_u32 s30, s30, s34\n"
"v_mfma_f32_16x16x32_bf16 v[152:155], a[160:163], v[44:47], v[152:155]\n"
"ds_read_b128 a[48:51], v217 offset:12288\n"
"ds_read_b128 a[52:55], v217 offset:13312\n"
"v_mfma_f32_16x16x32_bf16 v[156:159], a[168:171], v[44:47], v[156:159]\n"
"v_min_u32 v252, 55, v192\n"
"v_lshlrev_b32 v252, 6, v252\n"
"v_mfma_f32_16x16x32_bf16 v[168:171], a[164:167], v[36:39], v[168:171]\n"
"ds_read_b128 a[56:59], v217 offset:14336\n"
"ds_read_b128 a[60:63], v217 offset:15360\n"
"v_mfma_f32_16x16x32_bf16 v[172:175], a[172:175], v[36:39], v[172:175]\n"
"v_add_u32 v253, 1792, v252\n"
"v_mfma_f32_16x16x32_bf16 v[184:187], a[164:167], v[44:47], v[184:187]\n"
"v_cmp_gt_u32 vcc, 1792, v252\n"
"s_waitcnt lgkmcnt(0)\n"
"v_mfma_f32_16x16x32_bf16 v[188:191], a[172:175], v[44:47], v[188:191]\n"
"v_mfma_f32_16x16x32_bf16 v[128:131], a[176:179], v[48:51], v[128:131]\n"
"ds_read_b64_tr_b16 a[128:129], v232 offset:0\n"
"ds_read_b64_tr_b16 a[130:131], v232 offset:512\n"
"ds_read_b64_tr_b16 a[132:133], v233 offset:0\n"
"v_mfma_f32_16x16x32_bf16 v[132:135], a[184:187], v[48:51], v[132:135]\n"
"v_cndmask_b32 v252, v253, v252, vcc\n"
"buffer_load_dword v255, v252, s[40:43], s30 offen\n"
"v_mfma_f32_16x16x32_bf16 v[144:147], a[176:179], v[56:59], v[144:147]\n"
"ds_read_b64_tr_b16 a[134:135], v233 offset:512\n"
"ds_read_b64_tr_b16 a[136:137], v232 offset:8\n"
"v_mfma_f32_16x16x32_bf16 v[148:151], a[184:187], v[56:59], v[148:151]\n"
"s_add_u32 s30, s30, 7168\n"
"v_mfma_f32_16x16x32_bf16 v[160:163], a[180:183], v[48:51], v[160:163]\n"
"ds_read_b64_tr_b16 a[138:139], v232 offset:520\n"
"ds_read_b64_tr_b16 a[140:141], v233 offset:8\n"
"v_mfma_f32_16x16x32_bf16 v[164:167], a[188:191], v[48:51], v[164:167]\n"
"buffer_load_dword v255, v252, s[40:43], s30 offen\n"
"v_mfma_f32_16x16x32_bf16 v[176:179], a[180:183], v[56:59], v[176:179]\n"
"ds_read_b64_tr_b16 a[142:143], v233 offset:520\n"
"ds_read_b128 a[64:67], v218 offset:0\n"
"v_mfma_f32_16x16x32_bf16 v[180:183], a[188:191], v[56:59], v[180:183]\n"
"ds_read_b128 a[68:71], v218 offset:1024\n"
"v_mfma_f32_16x16x32_bf16 v[136:139], a[176:179], v[52:55], v[136:139]\n"
"ds_read_b128 a[72:75], v218 offset:2048\n"
"v_mfma_f32_16x16x32_bf16 v[140:143], a[184:187], v[52:55], v[140:143]\n"
"ds_read_b128 a[76:79], v218 offset:3072\n"
"v_mfma_f32_16x16x32_bf16 v[152:155], a[176:179], v[60:63], v[152:155]\n"
"ds_read_b128 a[80:83], v218 offset:4096\n"
"v_mfma_f32_16x16x32_bf16 v[156:159], a[184:187], v[60:63], v[156:159]\n"
"ds_read_b128 a[84:87], v218 offset:5120\n"
"v_mfma_f32_16x16x32_bf16 v[168:171], a[180:183], v[52:55], v[168:171]\n"
"ds_read_b128 a[88:91], v218 offset:6144\n"
"v_mfma_f32_16x16x32_bf16 v[172:175], a[188:191], v[52:55], v[172:175]\n"
"ds_read_b128 a[92:95], v218 offset:7168\n"
"v_mfma_f32_16x16x32_bf16 v[184:187], a[180:183], v[60:63], v[184:187]\n"
"s_waitcnt lgkmcnt(0)\n"
"v_mfma_f32_16x16x32_bf16 v[188:191], a[188:191], v[60:63], v[188:191]\n"
"v_mfma_f32_16x16x32_bf16 v[128:131], a[128:131], a[0:3], v[128:131]\n"
"ds_read_b64_tr_b16 a[144:145], v232 offset:16384\n"
"ds_read_b64_tr_b16 a[146:147], v232 offset:16896\n"
"ds_read_b64_tr_b16 a[148:149], v233 offset:16384\n"
"v_mfma_f32_16x16x32_bf16 v[132:135], a[136:139], a[0:3], v[132:135]\n"
"v_bfe_u32 v224, v192, 3, 1\n"
"s_add_u32 s74, s68, 393216\n"
"v_mfma_f32_16x16x32_bf16 v[144:147], a[128:131], a[4:7], v[144:147]\n"
"ds_read_b64_tr_b16 a[150:151], v233 offset:16896\n"
"ds_read_b64_tr_b16 a[152:153], v232 offset:16392\n"
"v_lshlrev_b32 v224, 9, v224\n"
"v_mfma_f32_16x16x32_bf16 v[148:151], a[136:139], a[4:7], v[148:151]\n"
"s_and_b32 s74, s74, 0x7ffff\n"
"s_add_u32 s74, s74, s69\n"
"s_add_u32 m0, s67, 65536\n"
"v_mfma_f32_16x16x32_bf16 v[160:163], a[132:135], a[0:3], v[160:163]\n"
"buffer_load_dwordx4 v205, s[56:59], s74 offen offset:0 lds\n"
"ds_read_b64_tr_b16 a[154:155], v232 offset:16904\n"
"v_mfma_f32_16x16x32_bf16 v[164:167], a[140:143], a[0:3], v[164:167]\n"
"ds_read_b64_tr_b16 a[156:157], v233 offset:16392\n"
"v_bfe_u32 v240, v192, 4, 1\n"
"v_mfma_f32_16x16x32_bf16 v[176:179], a[132:135], a[4:7], v[176:179]\n"
"ds_read_b64_tr_b16 a[158:159], v233 offset:16904\n"
"ds_read_b128 a[96:99], v218 offset:8192\n"
"v_mfma_f32_16x16x32_bf16 v[180:183], a[140:143], a[4:7], v[180:183]\n"
"ds_read_b128 a[100:103], v218 offset:9216\n"
"v_lshl_add_u32 v224, v240, 8, v224\n"
"v_mfma_f32_16x16x32_bf16 v[136:139], a[128:131], a[8:11], v[136:139]\n"
"buffer_load_dwordx4 v206, s[56:59], s74 offen offset:1024 lds\n"
"ds_read_b128 a[104:107], v218 offset:10240\n"
"v_mfma_f32_16x16x32_bf16 v[140:143], a[136:139], a[8:11], v[140:143]\n"
"ds_read_b128 a[108:111], v218 offset:11264\n"
"v_and_b32 v240, 7, v192\n"
"v_mfma_f32_16x16x32_bf16 v[152:155], a[128:131], a[12:15], v[152:155]\n"
"buffer_load_dwordx4 v207, s[56:59], s74 offen offset:2048 lds\n"
"ds_read_b128 a[112:115], v218 offset:12288\n"
"v_mfma_f32_16x16x32_bf16 v[156:159], a[136:139], a[12:15], v[156:159]\n"
"ds_read_b128 a[116:119], v218 offset:13312\n"
"v_lshl_add_u32 v224, v240, 4, v224\n"
"v_mfma_f32_16x16x32_bf16 v[168:171], a[132:135], a[8:11], v[168:171]\n"
"ds_read_b128 a[120:123], v218 offset:14336\n"
"ds_read_b128 a[124:127], v218 offset:15360\n"
"v_mfma_f32_16x16x32_bf16 v[172:175], a[140:143], a[8:11], v[172:175]\n"
"v_lshrrev_b32 v240, 5, v192\n"
"v_mfma_f32_16x16x32_bf16 v[184:187], a[132:135], a[12:15], v[184:187]\n"
"buffer_load_dwordx4 v208, s[56:59], s74 offen offset:3072 lds\n"
"s_waitcnt lgkmcnt(0)\n"
"v_mfma_f32_16x16x32_bf16 v[188:191], a[140:143], a[12:15], v[188:191]\n"
"v_mfma_f32_16x16x32_bf16 v[128:131], a[144:147], a[16:19], v[128:131]\n"
"ds_read_b128 v[64:67], v219 offset:0\n"
"ds_read_b128 v[68:71], v219 offset:1024\n"
"ds_read_b128 v[72:75], v219 offset:2048\n"
"v_mfma_f32_16x16x32_bf16 v[132:135], a[152:155], a[16:19], v[132:135]\n"
"v_lshl_add_u32 v224, v240, 7, v224\n"
"s_add_u32 s74, s68, 425984\n"
"v_mfma_f32_16x16x32_bf16 v[144:147], a[144:147], a[20:23], v[144:147]\n"
"ds_read_b128 v[76:79], v219 offset:3072\n"
"ds_read_b128 v[80:83], v219 offset:4096\n"
"s_lshl_b32 s75, s66, 19\n"
"v_mfma_f32_16x16x32_bf16 v[148:151], a[152:155], a[20:23], v[148:151]\n"
"s_and_b32 s74, s74, 0x7ffff\n"
"s_add_u32 s74, s74, s69\n"
"s_add_u32 m0, s67, 81920\n"
"v_mfma_f32_16x16x32_bf16 v[160:163], a[148:151], a[16:19], v[160:163]\n"
"buffer_load_dwordx4 v205, s[56:59], s74 offen offset:0 lds\n"
"ds_read_b128 v[84:87], v219 offset:5120\n"
"v_mfma_f32_16x16x32_bf16 v[164:167], a[156:159], a[16:19], v[164:167]\n"
"ds_read_b128 v[88:91], v219 offset:6144\n"
"s_lshl_b32 s30, s65, 12\n"
"v_mfma_f32_16x16x32_bf16 v[176:179], a[148:151], a[20:23], v[176:179]\n"
"ds_read_b128 v[92:95], v219 offset:7168\n"
"ds_read_b128 v[96:99], v219 offset:8192\n"
"v_mfma_f32_16x16x32_bf16 v[180:183], a[156:159], a[20:23], v[180:183]\n"
"ds_read_b128 v[100:103], v219 offset:9216\n"
"s_add_u32 s75, s75, s30\n"
"v_mfma_f32_16x16x32_bf16 v[136:139], a[144:147], a[24:27], v[136:139]\n"
"buffer_load_dwordx4 v206, s[56:59], s74 offen offset:1024 lds\n"
"ds_read_b128 v[104:107], v219 offset:10240\n"
"v_mfma_f32_16x16x32_bf16 v[140:143], a[152:155], a[24:27], v[140:143]\n"
"ds_read_b128 v[108:111], v219 offset:11264\n"
"s_lshl_b32 s30, s64, 10\n"
"v_mfma_f32_16x16x32_bf16 v[152:155], a[144:147], a[28:31], v[152:155]\n"
"buffer_load_dwordx4 v207, s[56:59], s74 offen offset:2048 lds\n"
"ds_read_b128 v[112:115], v219 offset:12288\n"
"v_mfma_f32_16x16x32_bf16 v[156:159], a[152:155], a[28:31], v[156:159]\n"
"ds_read_b128 v[116:119], v219 offset:13312\n"
"s_add_u32 s75, s75, s30\n"
"v_mfma_f32_16x16x32_bf16 v[168:171], a[148:151], a[24:27], v[168:171]\n"
"ds_read_b128 v[120:123], v219 offset:14336\n"
"ds_read_b128 v[124:127], v219 offset:15360\n"
"v_mfma_f32_16x16x32_bf16 v[172:175], a[156:159], a[24:27], v[172:175]\n"
"s_add_u32 s75, s75, 0x400000\n"
"v_mfma_f32_16x16x32_bf16 v[184:187], a[148:151], a[28:31], v[184:187]\n"
"buffer_load_dwordx4 v208, s[56:59], s74 offen offset:3072 lds\n"
"s_waitcnt lgkmcnt(0)\n"
"v_mfma_f32_16x16x32_bf16 v[188:191], a[156:159], a[28:31], v[188:191]\n"
"s_barrier\n"
"v_mfma_f32_16x16x32_bf16 v[128:131], a[192:195], a[32:35], v[128:131]\n"
"ds_read_b64_tr_b16 a[128:129], v232 offset:32768\n"
"ds_read_b64_tr_b16 a[130:131], v232 offset:33280\n"
"s_mov_b32 s44, s20\n"
"v_mfma_f32_16x16x32_bf16 v[132:135], a[200:203], a[32:35], v[132:135]\n"
"s_and_b32 s45, s21, 0xffff\n"
"s_add_u32 s74, s68, 491520\n"
"v_mfma_f32_16x16x32_bf16 v[144:147], a[192:195], a[36:39], v[144:147]\n"
"ds_read_b64_tr_b16 a[132:133], v233 offset:32768\n"
"s_mov_b32 s46, 0x1c0000\n"
"s_and_b32 s74, s74, 0x7ffff\n"
"v_mfma_f32_16x16x32_bf16 v[148:151], a[200:203], a[36:39], v[148:151]\n"
"s_add_u32 s74, s74, s69\n"
"s_add_u32 m0, s67, 0\n"
"v_mfma_f32_16x16x32_bf16 v[160:163], a[196:199], a[32:35], v[160:163]\n"
"buffer_load_dwordx4 v205, s[56:59], s74 offen offset:0 lds\n"
"ds_read_b64_tr_b16 a[134:135], v233 offset:33280\n"
"v_mfma_f32_16x16x32_bf16 v[164:167], a[204:207], a[32:35], v[164:167]\n"
"v_subrev_u32 v216, 0x10000, v232\n"
"s_mov_b32 s47, 0x20000\n"
"v_mfma_f32_16x16x32_bf16 v[176:179], a[196:199], a[36:39], v[176:179]\n"
"ds_read_b64_tr_b16 a[136:137], v232 offset:32776\n"
"ds_read_b64_tr_b16 a[138:139], v232 offset:33288\n"
"v_mfma_f32_16x16x32_bf16 v[180:183], a[204:207], a[36:39], v[180:183]\n"
"v_subrev_u32 v217, 0x10000, v233\n"
"s_mul_i32 s33, s79, 7\n"
"v_mfma_f32_16x16x32_bf16 v[136:139], a[192:195], a[40:43], v[136:139]\n"
"buffer_load_dwordx4 v206, s[56:59], s74 offen offset:1024 lds\n"
"ds_read_b64_tr_b16 a[140:141], v233 offset:32776\n"
"v_mfma_f32_16x16x32_bf16 v[140:143], a[200:203], a[40:43], v[140:143]\n"
"s_add_u32 s33, s33, 1\n"
"v_mfma_f32_16x16x32_bf16 v[152:155], a[192:195], a[44:47], v[152:155]\n"
"buffer_load_dwordx4 v207, s[56:59], s74 offen offset:2048 lds\n"
"ds_read_b64_tr_b16 a[142:143], v233 offset:33288\n"
"v_mfma_f32_16x16x32_bf16 v[156:159], a[200:203], a[44:47], v[156:159]\n"
"s_lshr_b32 s33, s33, 1\n"
"v_mfma_f32_16x16x32_bf16 v[168:171], a[196:199], a[40:43], v[168:171]\n"
"buffer_load_dwordx4 v208, s[56:59], s74 offen offset:3072 lds\n"
"s_and_b32 s30, s79, 1\n"
"v_mfma_f32_16x16x32_bf16 v[172:175], a[204:207], a[40:43], v[172:175]\n"
"s_sub_u32 s30, 3, s30\n"
"v_mfma_f32_16x16x32_bf16 v[184:187], a[196:199], a[44:47], v[184:187]\n"
"s_min_u32 s30, s30, s64\n"
"s_waitcnt lgkmcnt(0)\n"
"v_mfma_f32_16x16x32_bf16 v[188:191], a[204:207], a[44:47], v[188:191]\n"
"v_mfma_f32_16x16x32_bf16 v[128:131], a[128:131], a[48:51], v[128:131]\n"
"ds_read_b64_tr_b16 a[144:145], v232 offset:49152\n"
"ds_read_b64_tr_b16 a[146:147], v232 offset:49664\n"
"v_mfma_f32_16x16x32_bf16 v[132:135], a[136:139], a[48:51], v[132:135]\n"
"s_add_u32 s33, s33, s30\n"
"s_add_u32 s74, s68, 0\n"
"v_mfma_f32_16x16x32_bf16 v[144:147], a[128:131], a[52:55], v[144:147]\n"
"ds_read_b64_tr_b16 a[148:149], v233 offset:49152\n"
"s_sub_u32 s34, s64, s30\n"
"s_and_b32 s74, s74, 0x7ffff\n"
"v_mfma_f32_16x16x32_bf16 v[148:151], a[136:139], a[52:55], v[148:151]\n"
"s_add_u32 s74, s74, s35\n"
"s_add_u32 m0, s67, 16384\n"
"v_mfma_f32_16x16x32_bf16 v[160:163], a[132:135], a[48:51], v[160:163]\n"
"buffer_load_dwordx4 v205, s[56:59], s74 offen offset:0 lds\n"
"ds_read_b64_tr_b16 a[150:151], v233 offset:49664\n"
"v_mfma_f32_16x16x32_bf16 v[164:167], a[140:143], a[48:51], v[164:167]\n"
"s_mul_i32 s34, s34, 117440512\n"
"v_mfma_f32_16x16x32_bf16 v[176:179], a[132:135], a[52:55], v[176:179]\n"
"buffer_load_dwordx4 v206, s[56:59], s74 offen offset:1024 lds\n"
"ds_read_b64_tr_b16 a[152:153], v232 offset:49160\n"
"v_mfma_f32_16x16x32_bf16 v[180:183], a[140:143], a[52:55], v[180:183]\n"
"ds_read_b64_tr_b16 a[154:155], v232 offset:49672\n"
"s_lshl_b32 s30, s33, 7\n"
"v_mfma_f32_16x16x32_bf16 v[136:139], a[128:131], a[56:59], v[136:139]\n"
"ds_read_b64_tr_b16 a[156:157], v233 offset:49160\n"
"s_add_u32 s30, s30, s34\n"
"v_mfma_f32_16x16x32_bf16 v[140:143], a[136:139], a[56:59], v[140:143]\n"
"v_lshrrev_b32 v240, 2, v192\n"
"v_mfma_f32_16x16x32_bf16 v[152:155], a[128:131], a[60:63], v[152:155]\n"
"buffer_load_dwordx4 v207, s[56:59], s74 offen offset:2048 lds\n"
"ds_read_b64_tr_b16 a[158:159], v233 offset:49672\n"
"v_mfma_f32_16x16x32_bf16 v[156:159], a[136:139], a[60:63], v[156:159]\n"
"v_lshrrev_b32 v241, 4, v192\n"
"v_mfma_f32_16x16x32_bf16 v[168:171], a[132:135], a[56:59], v[168:171]\n"
"buffer_load_dwordx4 v208, s[56:59], s74 offen offset:3072 lds\n"
"v_mfma_f32_16x16x32_bf16 v[172:175], a[140:143], a[56:59], v[172:175]\n"
"v_xor_b32 v240, v240, v241\n"
"v_mfma_f32_16x16x32_bf16 v[184:187], a[132:135], a[60:63], v[184:187]\n"
"v_and_b32 v240, 1, v240\n"
"s_waitcnt lgkmcnt(0)\n"
"v_mfma_f32_16x16x32_bf16 v[188:191], a[140:143], a[60:63], v[188:191]\n"
"v_mfma_f32_16x16x32_bf16 v[128:131], a[144:147], a[64:67], v[128:131]\n"
"v_lshlrev_b32 v240, 6, v240\n"
"v_mfma_f32_16x16x32_bf16 v[132:135], a[152:155], a[64:67], v[132:135]\n"
"s_add_u32 s74, s68, 32768\n"
"v_mfma_f32_16x16x32_bf16 v[144:147], a[144:147], a[68:71], v[144:147]\n"
"v_and_b32 v241, 3, v192\n"
"s_and_b32 s74, s74, 0x7ffff\n"
"v_mfma_f32_16x16x32_bf16 v[148:151], a[152:155], a[68:71], v[148:151]\n"
"s_add_u32 s74, s74, s35\n"
"s_add_u32 m0, s67, 32768\n"
"v_mfma_f32_16x16x32_bf16 v[160:163], a[148:151], a[64:67], v[160:163]\n"
"buffer_load_dwordx4 v205, s[56:59], s74 offen offset:0 lds\n"
"v_lshl_add_u32 v240, v241, 4, v240\n"
"v_mfma_f32_16x16x32_bf16 v[164:167], a[156:159], a[64:67], v[164:167]\n"
"v_lshrrev_b32 v241, 3, v192\n"
"v_mfma_f32_16x16x32_bf16 v[176:179], a[148:151], a[68:71], v[176:179]\n"
"buffer_load_dwordx4 v206, s[56:59], s74 offen offset:1024 lds\n"
"v_mfma_f32_16x16x32_bf16 v[180:183], a[156:159], a[68:71], v[180:183]\n"
"v_mul_u32_u24 v241, 3584, v241\n"
"v_mfma_f32_16x16x32_bf16 v[136:139], a[144:147], a[72:75], v[136:139]\n"
"v_add3_u32 v209, v240, v241, s30\n"
"v_mfma_f32_16x16x32_bf16 v[140:143], a[152:155], a[72:75], v[140:143]\n"
"v_mfma_f32_16x16x32_bf16 v[152:155], a[144:147], a[76:79], v[152:155]\n"
"buffer_load_dwordx4 v207, s[56:59], s74 offen offset:2048 lds\n"
"v_mfma_f32_16x16x32_bf16 v[156:159], a[152:155], a[76:79], v[156:159]\n"
"v_add_u32 v210, 27648, v209\n"
"v_mfma_f32_16x16x32_bf16 v[168:171], a[148:151], a[72:75], v[168:171]\n"
"buffer_load_dwordx4 v208, s[56:59], s74 offen offset:3072 lds\n"
"v_mfma_f32_16x16x32_bf16 v[172:175], a[156:159], a[72:75], v[172:175]\n"
"v_add_u32 v211, 55296, v209\n"
"v_mfma_f32_16x16x32_bf16 v[184:187], a[148:151], a[76:79], v[184:187]\n"
"v_add_u32 v212, 82944, v209\n"
"v_mfma_f32_16x16x32_bf16 v[188:191], a[156:159], a[76:79], v[188:191]\n"
"v_mfma_f32_16x16x32_bf16 v[128:131], a[208:211], a[80:83], v[128:131]\n"
"ds_read_b64_tr_b16 a[128:129], v234 offset:0\n"
"ds_read_b64_tr_b16 a[130:131], v234 offset:512\n"
"v_mfma_f32_16x16x32_bf16 v[132:135], a[216:219], a[80:83], v[132:135]\n"
"s_add_u32 s74, s68, 65536\n"
"v_mfma_f32_16x16x32_bf16 v[144:147], a[208:211], a[84:87], v[144:147]\n"
"ds_read_b64_tr_b16 a[132:133], v235 offset:0\n"
"s_and_b32 s74, s74, 0x7ffff\n"
"v_mfma_f32_16x16x32_bf16 v[148:151], a[216:219], a[84:87], v[148:151]\n"
"s_add_u32 s74, s74, s35\n"
"s_add_u32 m0, s67, 49152\n"
"v_mfma_f32_16x16x32_bf16 v[160:163], a[212:215], a[80:83], v[160:163]\n"
"buffer_load_dwordx4 v205, s[56:59], s74 offen offset:0 lds\n"
"v_mfma_f32_16x16x32_bf16 v[164:167], a[220:223], a[80:83], v[164:167]\n"
"ds_read_b64_tr_b16 a[134:135], v235 offset:512\n"
"v_mfma_f32_16x16x32_bf16 v[176:179], a[212:215], a[84:87], v[176:179]\n"
"ds_read_b64_tr_b16 a[136:137], v234 offset:8\n"
"v_mfma_f32_16x16x32_bf16 v[180:183], a[220:223], a[84:87], v[180:183]\n"
"ds_read_b64_tr_b16 a[138:139], v234 offset:520\n"
"v_mfma_f32_16x16x32_bf16 v[136:139], a[208:211], a[88:91], v[136:139]\n"
"buffer_load_dwordx4 v206, s[56:59], s74 offen offset:1024 lds\n"
"v_mfma_f32_16x16x32_bf16 v[140:143], a[216:219], a[88:91], v[140:143]\n"
"ds_read_b64_tr_b16 a[140:141], v235 offset:8\n"
"v_mfma_f32_16x16x32_bf16 v[152:155], a[208:211], a[92:95], v[152:155]\n"
"buffer_load_dwordx4 v207, s[56:59], s74 offen offset:2048 lds\n"
"v_mfma_f32_16x16x32_bf16 v[156:159], a[216:219], a[92:95], v[156:159]\n"
"ds_read_b64_tr_b16 a[142:143], v235 offset:520\n"
"v_mfma_f32_16x16x32_bf16 v[168:171], a[212:215], a[88:91], v[168:171]\n"
"buffer_load_dwordx4 v208, s[56:59], s74 offen offset:3072 lds\n"
"v_mfma_f32_16x16x32_bf16 v[172:175], a[220:223], a[88:91], v[172:175]\n"
"v_mfma_f32_16x16x32_bf16 v[184:187], a[212:215], a[92:95], v[184:187]\n"
"s_waitcnt lgkmcnt(0)\n"
"v_mfma_f32_16x16x32_bf16 v[188:191], a[220:223], a[92:95], v[188:191]\n"
"v_mfma_f32_16x16x32_bf16 v[128:131], a[128:131], a[96:99], v[128:131]\n"
"ds_read_b64_tr_b16 a[144:145], v234 offset:16384\n"
"ds_read_b64_tr_b16 a[146:147], v234 offset:16896\n"
"v_accvgpr_write_b32 a160, v64\n"
"v_mfma_f32_16x16x32_bf16 v[132:135], a[136:139], a[96:99], v[132:135]\n"
"v_accvgpr_write_b32 a161, v65\n"
"s_add_u32 s74, s68, 98304\n"
"v_mfma_f32_16x16x32_bf16 v[144:147], a[128:131], a[100:103], v[144:147]\n"
"ds_read_b64_tr_b16 a[148:149], v235 offset:16384\n"
"v_accvgpr_write_b32 a162, v66\n"
"s_and_b32 s74, s74, 0x7ffff\n"
"v_mfma_f32_16x16x32_bf16 v[148:151], a[136:139], a[100:103], v[148:151]\n"
"s_add_u32 s74, s74, s35\n"
"s_add_u32 m0, s67, 98304\n"
"v_mfma_f32_16x16x32_bf16 v[160:163], a[132:135], a[96:99], v[160:163]\n"
"buffer_load_dwordx4 v205, s[56:59], s74 offen offset:0 lds\n"
"ds_read_b64_tr_b16 a[150:151], v235 offset:16896\n"
"v_mfma_f32_16x16x32_bf16 v[164:167], a[140:143], a[96:99], v[164:167]\n"
"v_accvgpr_write_b32 a163, v67\n"
"v_accvgpr_write_b32 a164, v68\n"
"v_mfma_f32_16x16x32_bf16 v[176:179], a[132:135], a[100:103], v[176:179]\n"
"buffer_load_dwordx4 v206, s[56:59], s74 offen offset:1024 lds\n"
"ds_read_b64_tr_b16 a[152:153], v234 offset:16392\n"
"v_mfma_f32_16x16x32_bf16 v[180:183], a[140:143], a[100:103], v[180:183]\n"
"ds_read_b64_tr_b16 a[154:155], v234 offset:16904\n"
"v_accvgpr_write_b32 a165, v69\n"
"v_mfma_f32_16x16x32_bf16 v[136:139], a[128:131], a[104:107], v[136:139]\n"
"ds_read_b64_tr_b16 a[156:157], v235 offset:16392\n"
"v_accvgpr_write_b32 a166, v70\n"
"v_mfma_f32_16x16x32_bf16 v[140:143], a[136:139], a[104:107], v[140:143]\n"
"v_accvgpr_write_b32 a167, v71\n"
"v_mfma_f32_16x16x32_bf16 v[152:155], a[128:131], a[108:111], v[152:155]\n"
"buffer_load_dwordx4 v207, s[56:59], s74 offen offset:2048 lds\n"
"ds_read_b64_tr_b16 a[158:159], v235 offset:16904\n"
"v_mfma_f32_16x16x32_bf16 v[156:159], a[136:139], a[108:111], v[156:159]\n"
"v_accvgpr_write_b32 a168, v72\n"
"v_mfma_f32_16x16x32_bf16 v[168:171], a[132:135], a[104:107], v[168:171]\n"
"buffer_load_dwordx4 v208, s[56:59], s74 offen offset:3072 lds\n"
"v_mfma_f32_16x16x32_bf16 v[172:175], a[140:143], a[104:107], v[172:175]\n"
"v_accvgpr_write_b32 a169, v73\n"
"v_mfma_f32_16x16x32_bf16 v[184:187], a[132:135], a[108:111], v[184:187]\n"
"v_accvgpr_write_b32 a170, v74\n"
"v_accvgpr_write_b32 a171, v75\n"
"v_mfma_f32_16x16x32_bf16 v[188:191], a[140:143], a[108:111], v[188:191]\n"
"s_waitcnt vmcnt(24) lgkmcnt(0)\n"
"v_mfma_f32_16x16x32_bf16 v[128:131], a[144:147], a[112:115], v[128:131]\n"
"ds_read_b64_tr_b16 a[128:129], v232 offset:0\n"
"ds_read_b64_tr_b16 a[130:131], v232 offset:512\n"
"v_mfma_f32_16x16x32_bf16 v[132:135], a[152:155], a[112:115], v[132:135]\n"
"v_accvgpr_write_b32 a172, v76\n"
"s_add_u32 s74, s68, 163840\n"
"v_mfma_f32_16x16x32_bf16 v[144:147], a[144:147], a[116:119], v[144:147]\n"
"ds_read_b64_tr_b16 a[132:133], v233 offset:0\n"
"v_accvgpr_write_b32 a173, v77\n"
"v_accvgpr_write_b32 a174, v78\n"
"v_mfma_f32_16x16x32_bf16 v[148:151], a[152:155], a[116:119], v[148:151]\n"
"s_and_b32 s74, s74, 0x7ffff\n"
"s_add_u32 s74, s74, s35\n"
"s_add_u32 m0, s67, 114688\n"
"v_mfma_f32_16x16x32_bf16 v[160:163], a[148:151], a[112:115], v[160:163]\n"
"buffer_load_dwordx4 v205, s[56:59], s74 offen offset:0 lds\n"
"ds_read_b64_tr_b16 a[134:135], v233 offset:512\n"
"v_mfma_f32_16x16x32_bf16 v[164:167], a[156:159], a[112:115], v[164:167]\n"
"v_accvgpr_write_b32 a175, v79\n"
"v_mfma_f32_16x16x32_bf16 v[176:179], a[148:151], a[116:119], v[176:179]\n"
"ds_read_b64_tr_b16 a[136:137], v232 offset:8\n"
"ds_read_b64_tr_b16 a[138:139], v232 offset:520\n"
"v_mfma_f32_16x16x32_bf16 v[180:183], a[156:159], a[116:119], v[180:183]\n"
"v_accvgpr_write_b32 a176, v80\n"
"v_accvgpr_write_b32 a177, v81\n"
"v_mfma_f32_16x16x32_bf16 v[136:139], a[144:147], a[120:123], v[136:139]\n"
"buffer_load_dwordx4 v206, s[56:59], s74 offen offset:1024 lds\n"
"ds_read_b64_tr_b16 a[140:141], v233 offset:8\n"
"v_mfma_f32_16x16x32_bf16 v[140:143], a[152:155], a[120:123], v[140:143]\n"
"v_accvgpr_write_b32 a178, v82\n"
"v_mfma_f32_16x16x32_bf16 v[152:155], a[144:147], a[124:127], v[152:155]\n"
"buffer_load_dwordx4 v207, s[56:59], s74 offen offset:2048 lds\n"
"ds_read_b64_tr_b16 a[142:143], v233 offset:520\n"
"v_mfma_f32_16x16x32_bf16 v[156:159], a[152:155], a[124:127], v[156:159]\n"
"Lst11_%=:\n"
"s_cbranch_execz Lback0_%=\n"
"v_accvgpr_write_b32 a179, v83\n"
"v_mfma_f32_16x16x32_bf16 v[168:171], a[148:151], a[120:123], v[168:171]\n"
"v_accvgpr_write_b32 a180, v84\n"
"v_mfma_f32_16x16x32_bf16 v[172:175], a[156:159], a[120:123], v[172:175]\n"
"v_accvgpr_write_b32 a181, v85\n"
"v_mfma_f32_16x16x32_bf16 v[184:187], a[148:151], a[124:127], v[184:187]\n"
"buffer_load_dwordx4 v208, s[56:59], s74 offen offset:3072 lds\n"
"v_accvgpr_write_b32 a182, v86\n"
"v_mfma_f32_16x16x32_bf16 v[188:191], a[156:159], a[124:127], v[188:191]\n"
"s_waitcnt vmcnt(24) lgkmcnt(0)\n"
"v_mfma_f32_16x16x32_bf16 v[128:131], a[128:131], v[64:67], v[128:131]\n"
"ds_read_b64_tr_b16 a[144:145], v232 offset:16384\n"
"ds_read_b64_tr_b16 a[146:147], v232 offset:16896\n"
"v_accvgpr_write_b32 a183, v87\n"
"v_mfma_f32_16x16x32_bf16 v[132:135], a[136:139], v[64:67], v[132:135]\n"
"v_accvgpr_write_b32 a184, v88\n"
"s_add_u32 s74, s68, 196608\n"
"v_mfma_f32_16x16x32_bf16 v[144:147], a[128:131], v[68:71], v[144:147]\n"
"ds_read_b64_tr_b16 a[148:149], v233 offset:16384\n"
"v_accvgpr_write_b32 a185, v89\n"
"s_and_b32 s74, s74, 0x7ffff\n"
"v_mfma_f32_16x16x32_bf16 v[148:151], a[136:139], v[68:71], v[148:151]\n"
"s_add_u32 s74, s74, s35\n"
"s_add_u32 m0, s67, 131072\n"
"v_mfma_f32_16x16x32_bf16 v[160:163], a[132:135], v[64:67], v[160:163]\n"
"buffer_load_dwordx4 v205, s[56:59], s74 offen offset:0 lds\n"
"ds_read_b64_tr_b16 a[150:151], v233 offset:16896\n"
"v_mfma_f32_16x16x32_bf16 v[164:167], a[140:143], v[64:67], v[164:167]\n"
"v_accvgpr_write_b32 a186, v90\n"
"v_accvgpr_write_b32 a187, v91\n"
"v_mfma_f32_16x16x32_bf16 v[176:179], a[132:135], v[68:71], v[176:179]\n"
"ds_read_b64_tr_b16 a[152:153], v232 offset:16392\n"
"ds_read_b64_tr_b16 a[154:155], v232 offset:16904\n"
"v_mfma_f32_16x16x32_bf16 v[180:183], a[140:143], v[68:71], v[180:183]\n"
"v_accvgpr_write_b32 a188, v92\n"
"v_mfma_f32_16x16x32_bf16 v[136:139], a[128:131], v[72:75], v[136:139]\n"
"buffer_load_dwordx4 v206, s[56:59], s74 offen offset:1024 lds\n"
"ds_read_b64_tr_b16 a[156:157], v233 offset:16392\n"
"v_mfma_f32_16x16x32_bf16 v[140:143], a[136:139], v[72:75], v[140:143]\n"
"v_accvgpr_write_b32 a189, v93\n"
"v_lshlrev_b32 v194, 4, v192\n"
"v_mfma_f32_16x16x32_bf16 v[152:155], a[128:131], v[76:79], v[152:155]\n"
"buffer_load_dwordx4 v207, s[56:59], s74 offen offset:2048 lds\n"
"ds_read_b64_tr_b16 a[158:159], v233 offset:16904\n"
"v_accvgpr_write_b32 a190, v94\n"
"v_mfma_f32_16x16x32_bf16 v[156:159], a[136:139], v[76:79], v[156:159]\n"
"v_accvgpr_write_b32 a191, v95\n"
"v_and_b32 v193, 7, v192\n"
"v_mfma_f32_16x16x32_bf16 v[168:171], a[132:135], v[72:75], v[168:171]\n"
"buffer_load_dwordx4 v208, s[56:59], s74 offen offset:3072 lds\n"
"v_accvgpr_write_b32 a192, v96\n"
"v_mfma_f32_16x16x32_bf16 v[172:175], a[140:143], v[72:75], v[172:175]\n"
"v_lshlrev_b32 v193, 9, v193\n"
"v_mfma_f32_16x16x32_bf16 v[184:187], a[132:135], v[76:79], v[184:187]\n"
"v_accvgpr_write_b32 a193, v97\n"
"v_accvgpr_write_b32 a194, v98\n"
"s_lshl_b32 s30, s64, 7\n"
"v_mfma_f32_16x16x32_bf16 v[188:191], a[140:143], v[76:79], v[188:191]\n"
"s_waitcnt lgkmcnt(0)\n"
"v_mfma_f32_16x16x32_bf16 v[128:131], a[144:147], v[80:83], v[128:131]\n"
"v_accvgpr_write_b32 a195, v99\n"
"v_add_u32 v193, s30, v193\n"
"v_mfma_f32_16x16x32_bf16 v[132:135], a[152:155], v[80:83], v[132:135]\n"
"s_add_u32 s74, s68, 229376\n"
"v_mfma_f32_16x16x32_bf16 v[144:147], a[144:147], v[84:87], v[144:147]\n"
"v_accvgpr_write_b32 a196, v100\n"
"v_lshrrev_b32 v198, 3, v192\n"
"s_and_b32 s74, s74, 0x7ffff\n"
"v_mfma_f32_16x16x32_bf16 v[148:151], a[152:155], v[84:87], v[148:151]\n"
"s_add_u32 s74, s74, s35\n"
"s_add_u32 m0, s67, 147456\n"
"v_mfma_f32_16x16x32_bf16 v[160:163], a[148:151], v[80:83], v[160:163]\n"
"buffer_load_dwordx4 v205, s[56:59], s74 offen offset:0 lds\n"
"v_accvgpr_write_b32 a197, v101\n"
"v_mfma_f32_16x16x32_bf16 v[164:167], a[156:159], v[80:83], v[164:167]\n"
"v_accvgpr_write_b32 a198, v102\n"
"s_nop 0\n"
"v_mfma_f32_16x16x32_bf16 v[176:179], a[148:151], v[84:87], v[176:179]\n"
"buffer_load_dwordx4 v206, s[56:59], s74 offen offset:1024 lds\n"
"v_accvgpr_write_b32 a199, v103\n"
"v_mfma_f32_16x16x32_bf16 v[180:183], a[156:159], v[84:87], v[180:183]\n"
"v_and_b32 v198, 31, v192\n"
"v_mfma_f32_16x16x32_bf16 v[136:139], a[144:147], v[88:91], v[136:139]\n"
"v_accvgpr_write_b32 a200, v104\n"
"v_accvgpr_write_b32 a201, v105\n"
"v_mfma_f32_16x16x32_bf16 v[140:143], a[152:155], v[88:91], v[140:143]\n"
"v_lshlrev_b32 v198, 2, v198\n"
"v_mfma_f32_16x16x32_bf16 v[152:155], a[144:147], v[92:95], v[152:155]\n"
"buffer_load_dwordx4 v207, s[56:59], s74 offen offset:2048 lds\n"
"v_accvgpr_write_b32 a202, v106\n"
"v_mfma_f32_16x16x32_bf16 v[156:159], a[152:155], v[92:95], v[156:159]\n"
"v_mov_b32 v199, s36\n"
"v_mfma_f32_16x16x32_bf16 v[168:171], a[148:151], v[88:91], v[168:171]\n"
"v_accvgpr_write_b32 a203, v107\n"
"v_accvgpr_write_b32 a204, v108\n"
"v_mfma_f32_16x16x32_bf16 v[172:175], a[156:159], v[88:91], v[172:175]\n"
"s_cmp_eq_u32 s79, 7\n"
"s_cselect_b32 s48, 0, s79\n"
"v_mfma_f32_16x16x32_bf16 v[184:187], a[148:151], v[92:95], v[184:187]\n"
"buffer_load_dwordx4 v208, s[56:59], s74 offen offset:3072 lds\n"
"v_accvgpr_write_b32 a205, v109\n"
"s_add_u32 s49, s48, 1\n"
"v_mfma_f32_16x16x32_bf16 v[188:191], a[156:159], v[92:95], v[188:191]\n"
"s_waitcnt vmcnt(28)\n"
"v_mfma_f32_16x16x32_bf16 v[128:131], a[224:227], v[96:99], v[128:131]\n"
"ds_read_b64_tr_b16 a[128:129], v216 offset:0\n"
"ds_read_b64_tr_b16 a[130:131], v216 offset:512\n"
"v_accvgpr_write_b32 a206, v110\n"
"v_mfma_f32_16x16x32_bf16 v[132:135], a[232:235], v[96:99], v[132:135]\n"
"s_cmp_eq_u32 s49, 7\n"
"s_cselect_b32 s49, 0, s49\n"
"s_add_u32 s74, s68, 262144\n"
"v_mfma_f32_16x16x32_bf16 v[144:147], a[224:227], v[100:103], v[144:147]\n"
"ds_read_b64_tr_b16 a[132:133], v217 offset:0\n"
"v_accvgpr_write_b32 a207, v111\n"
"v_accvgpr_write_b32 a208, v112\n"
"s_add_u32 s50, s49, 1\n"
"v_mfma_f32_16x16x32_bf16 v[148:151], a[232:235], v[100:103], v[148:151]\n"
"s_and_b32 s74, s74, 0x7ffff\n"
"s_add_u32 s74, s74, s35\n"
"s_add_u32 m0, s67, 65536\n"
"v_mfma_f32_16x16x32_bf16 v[160:163], a[228:231], v[96:99], v[160:163]\n"
"buffer_load_dwordx4 v205, s[56:59], s74 offen offset:0 lds\n"
"ds_read_b64_tr_b16 a[134:135], v217 offset:512\n"
"v_accvgpr_write_b32 a209, v113\n"
"v_mfma_f32_16x16x32_bf16 v[164:167], a[236:239], v[96:99], v[164:167]\n"
"s_cmp_eq_u32 s50, 7\n"
"s_cselect_b32 s50, 0, s50\n"
"v_mfma_f32_16x16x32_bf16 v[176:179], a[228:231], v[100:103], v[176:179]\n"
"ds_read_b64_tr_b16 a[136:137], v216 offset:8\n"
"ds_read_b64_tr_b16 a[138:139], v216 offset:520\n"
"v_accvgpr_write_b32 a210, v114\n"
"v_mfma_f32_16x16x32_bf16 v[180:183], a[236:239], v[100:103], v[180:183]\n"
"v_accvgpr_write_b32 a211, v115\n"
"s_add_u32 s51, s50, 1\n"
"v_mfma_f32_16x16x32_bf16 v[136:139], a[224:227], v[104:107], v[136:139]\n"
"buffer_load_dwordx4 v206, s[56:59], s74 offen offset:1024 lds\n"
"ds_read_b64_tr_b16 a[140:141], v217 offset:8\n"
"v_accvgpr_write_b32 a212, v116\n"
"v_mfma_f32_16x16x32_bf16 v[140:143], a[232:235], v[104:107], v[140:143]\n"
"s_cmp_eq_u32 s51, 7\n"
"s_cselect_b32 s51, 0, s51\n"
"v_mfma_f32_16x16x32_bf16 v[152:155], a[224:227], v[108:111], v[152:155]\n"
"buffer_load_dwordx4 v207, s[56:59], s74 offen offset:2048 lds\n"
"ds_read_b64_tr_b16 a[142:143], v217 offset:520\n"
"v_accvgpr_write_b32 a213, v117\n"
"v_mfma_f32_16x16x32_bf16 v[156:159], a[232:235], v[108:111], v[156:159]\n"
"v_accvgpr_write_b32 a214, v118\n"
"s_add_u32 s52, s51, 1\n"
"v_mfma_f32_16x16x32_bf16 v[168:171], a[228:231], v[104:107], v[168:171]\n"
"buffer_load_dwordx4 v208, s[56:59], s74 offen offset:3072 lds\n"
"v_accvgpr_write_b32 a215, v119\n"
"v_mfma_f32_16x16x32_bf16 v[172:175], a[236:239], v[104:107], v[172:175]\n"
"s_cmp_eq_u32 s52, 7\n"
"s_cselect_b32 s52, 0, s52\n"
"v_mfma_f32_16x16x32_bf16 v[184:187], a[228:231], v[108:111], v[184:187]\n"
"v_accvgpr_write_b32 a216, v120\n"
"s_add_u32 s53, s52, 1\n"
"v_mfma_f32_16x16x32_bf16 v[188:191], a[236:239], v[108:111], v[188:191]\n"
"s_waitcnt vmcnt(28) lgkmcnt(0)\n"
"v_mfma_f32_16x16x32_bf16 v[128:131], a[128:131], v[112:115], v[128:131]\n"
"ds_read_b64_tr_b16 a[144:145], v216 offset:16384\n"
"ds_read_b64_tr_b16 a[146:147], v216 offset:16896\n"
"v_accvgpr_write_b32 a217, v121\n"
"v_accvgpr_write_b32 a218, v122\n"
"v_mfma_f32_16x16x32_bf16 v[132:135], a[136:139], v[112:115], v[132:135]\n"
"s_cmp_eq_u32 s53, 7\n"
"s_cselect_b32 s53, 0, s53\n"
"s_add_u32 s74, s68, 294912\n"
"v_mfma_f32_16x16x32_bf16 v[144:147], a[128:131], v[116:119], v[144:147]\n"
"ds_read_b64_tr_b16 a[148:149], v217 offset:16384\n"
"v_accvgpr_write_b32 a219, v123\n"
"s_add_u32 s54, s53, 1\n"
"v_mfma_f32_16x16x32_bf16 v[148:151], a[136:139], v[116:119], v[148:151]\n"
"s_and_b32 s74, s74, 0x7ffff\n"
"s_add_u32 s74, s74, s35\n"
"s_add_u32 m0, s67, 81920\n"
"v_mfma_f32_16x16x32_bf16 v[160:163], a[132:135], v[112:115], v[160:163]\n"
"buffer_load_dwordx4 v205, s[56:59], s74 offen offset:0 lds\n"
"ds_read_b64_tr_b16 a[150:151], v217 offset:16896\n"
"v_accvgpr_write_b32 a220, v124\n"
"v_mfma_f32_16x16x32_bf16 v[164:167], a[140:143], v[112:115], v[164:167]\n"
"v_accvgpr_write_b32 a221, v125\n"
"s_cmp_eq_u32 s54, 7\n"
"s_cselect_b32 s54, 0, s54\n"
"v_mfma_f32_16x16x32_bf16 v[176:179], a[132:135], v[116:119], v[176:179]\n"
"ds_read_b64_tr_b16 a[152:153], v216 offset:16392\n"
"ds_read_b64_tr_b16 a[154:155], v216 offset:16904\n"
"v_mfma_f32_16x16x32_bf16 v[180:183], a[140:143], v[116:119], v[180:183]\n"
"v_accvgpr_write_b32 a222, v126\n"
"s_lshl_b32 s55, s66, 3\n"
"v_mfma_f32_16x16x32_bf16 v[136:139], a[128:131], v[120:123], v[136:139]\n"
"buffer_load_dwordx4 v206, s[56:59], s74 offen offset:1024 lds\n"
"ds_read_b64_tr_b16 a[156:157], v217 offset:16392\n"
"v_mfma_f32_16x16x32_bf16 v[140:143], a[136:139], v[120:123], v[140:143]\n"
"v_accvgpr_write_b32 a223, v127\n"
"s_add_u32 s55, s55, s65\n"
"v_mfma_f32_16x16x32_bf16 v[152:155], a[128:131], v[124:127], v[152:155]\n"
"buffer_load_dwordx4 v207, s[56:59], s74 offen offset:2048 lds\n"
"ds_read_b64_tr_b16 a[158:159], v217 offset:16904\n"
"v_mfma_f32_16x16x32_bf16 v[156:159], a[136:139], v[124:127], v[156:159]\n"
"s_lshl_b32 s55, s55, 2\n"
"v_mfma_f32_16x16x32_bf16 v[168:171], a[132:135], v[120:123], v[168:171]\n"
"buffer_load_dwordx4 v208, s[56:59], s74 offen offset:3072 lds\n"
"v_mfma_f32_16x16x32_bf16 v[172:175], a[140:143], v[120:123], v[172:175]\n"
"s_add_u32 s55, s55, s64\n"
"v_mfma_f32_16x16x32_bf16 v[184:187], a[132:135], v[124:127], v[184:187]\n"
"s_lshl_b32 s55, s55, 7\n"
"s_waitcnt vmcnt(28) lgkmcnt(0)\n"
"v_mfma_f32_16x16x32_bf16 v[188:191], a[140:143], v[124:127], v[188:191]\n"
"v_mfma_f32_16x16x32_bf16 v[64:67], a[144:147], v[0:3], 0\n"
"ds_read_b64_tr_b16 a[128:129], v216 offset:32768\n"
"ds_read_b64_tr_b16 a[130:131], v216 offset:33280\n"
"v_mfma_f32_16x16x32_bf16 v[68:71], a[152:155], v[0:3], 0\n"
"s_add_u32 s55, s55, 1048576\n"
"s_add_u32 s74, s68, 327680\n"
"v_mfma_f32_16x16x32_bf16 v[80:83], a[144:147], v[8:11], 0\n"
"ds_read_b64_tr_b16 a[132:133], v217 offset:32768\n"
"s_mov_b32 s40, s24\n"
"s_and_b32 s74, s74, 0x7ffff\n"
"v_mfma_f32_16x16x32_bf16 v[84:87], a[152:155], v[8:11], 0\n"
"s_add_u32 s74, s74, s35\n"
"s_add_u32 m0, s67, 0\n"
"v_mfma_f32_16x16x32_bf16 v[96:99], a[148:151], v[0:3], 0\n"
"buffer_load_dwordx4 v205, s[56:59], s74 offen offset:0 lds\n"
"ds_read_b64_tr_b16 a[134:135], v217 offset:33280\n"
"v_mfma_f32_16x16x32_bf16 v[100:103], a[156:159], v[0:3], 0\n"
"s_and_b32 s41, s25, 0xffff\n"
"v_mfma_f32_16x16x32_bf16 v[112:115], a[148:151], v[8:11], 0\n"
"ds_read_b64_tr_b16 a[136:137], v216 offset:32776\n"
"ds_read_b64_tr_b16 a[138:139], v216 offset:33288\n"
"v_mfma_f32_16x16x32_bf16 v[116:119], a[156:159], v[8:11], 0\n"
"s_mov_b32 s42, 0x1c0000\n"
"v_mfma_f32_16x16x32_bf16 v[72:75], a[144:147], v[4:7], 0\n"
"buffer_load_dwordx4 v206, s[56:59], s74 offen offset:1024 lds\n"
"ds_read_b64_tr_b16 a[140:141], v217 offset:32776\n"
"v_cvt_pk_bf16_f32 v128, v128, v129\n"
"v_cvt_pk_bf16_f32 v129, v130, v131\n"
"v_mfma_f32_16x16x32_bf16 v[76:79], a[152:155], v[4:7], 0\n"
"v_cvt_pk_bf16_f32 v130, v132, v133\n"
"v_cvt_pk_bf16_f32 v131, v134, v135\n"
"s_add_u32 s76, s75, 0\n"
"s_mov_b32 s43, 0x20000\n"
"v_mfma_f32_16x16x32_bf16 v[88:91], a[144:147], v[12:15], 0\n"
"buffer_store_dwordx4 v[128:131], v224, s[60:63], s76 offen sc1\n"
"buffer_load_dwordx4 v207, s[56:59], s74 offen offset:2048 lds\n"
"v_mfma_f32_16x16x32_bf16 v[92:95], a[152:155], v[12:15], 0\n"
"ds_read_b64_tr_b16 a[142:143], v217 offset:33288\n"
"s_mul_i32 s11, s64, 458496\n"
"v_mfma_f32_16x16x32_bf16 v[104:107], a[148:151], v[4:7], 0\n"
"buffer_load_dwordx4 v208, s[56:59], s74 offen offset:3072 lds\n"
"v_mfma_f32_16x16x32_bf16 v[108:111], a[156:159], v[4:7], 0\n"
"s_lshl_b32 s30, s79, 7\n"
"v_mfma_f32_16x16x32_bf16 v[120:123], a[148:151], v[12:15], 0\n"
"s_add_u32 s11, s11, s30\n"
"s_waitcnt vmcnt(29) lgkmcnt(0)\n"
"v_mfma_f32_16x16x32_bf16 v[124:127], a[156:159], v[12:15], 0\n"
"v_mfma_f32_16x16x32_bf16 v[64:67], a[128:131], v[16:19], v[64:67]\n"
"ds_read_b64_tr_b16 a[144:145], v216 offset:49152\n"
"ds_read_b64_tr_b16 a[146:147], v216 offset:49664\n"
"v_mfma_f32_16x16x32_bf16 v[68:71], a[136:139], v[16:19], v[68:71]\n"
"s_lshr_b32 s30, s64, 1\n"
"s_add_u32 s74, s68, 360448\n"
"v_mfma_f32_16x16x32_bf16 v[80:83], a[128:131], v[24:27], v[80:83]\n"
"ds_read_b64_tr_b16 a[148:149], v217 offset:49152\n"
"s_lshl_b32 s30, s30, 6\n"
"s_and_b32 s74, s74, 0x7ffff\n"
"v_mfma_f32_16x16x32_bf16 v[84:87], a[136:139], v[24:27], v[84:87]\n"
"s_add_u32 s74, s74, s35\n"
"s_add_u32 m0, s67, 16384\n"
"v_mfma_f32_16x16x32_bf16 v[96:99], a[132:135], v[16:19], v[96:99]\n"
"buffer_load_dwordx4 v205, s[56:59], s74 offen offset:0 lds\n"
"ds_read_b64_tr_b16 a[150:151], v217 offset:49664\n"
"v_mfma_f32_16x16x32_bf16 v[100:103], a[140:143], v[16:19], v[100:103]\n"
"v_xor_b32 v200, s30, v232\n"
"v_mfma_f32_16x16x32_bf16 v[112:115], a[132:135], v[24:27], v[112:115]\n"
"ds_read_b64_tr_b16 a[152:153], v216 offset:49160\n"
"ds_read_b64_tr_b16 a[154:155], v216 offset:49672\n"
"v_mfma_f32_16x16x32_bf16 v[116:119], a[140:143], v[24:27], v[116:119]\n"
"v_xor_b32 v201, s30, v233\n"
"v_mfma_f32_16x16x32_bf16 v[72:75], a[128:131], v[20:23], v[72:75]\n"
"ds_read_b64_tr_b16 a[156:157], v217 offset:49160\n"
"v_mfma_f32_16x16x32_bf16 v[76:79], a[136:139], v[20:23], v[76:79]\n"
"v_xor_b32 v202, s30, v234\n"
"v_mfma_f32_16x16x32_bf16 v[88:91], a[128:131], v[28:31], v[88:91]\n"
"ds_read_b64_tr_b16 a[158:159], v217 offset:49672\n"
"v_mfma_f32_16x16x32_bf16 v[92:95], a[136:139], v[28:31], v[92:95]\n"
"v_xor_b32 v203, s30, v235\n"
"v_mfma_f32_16x16x32_bf16 v[104:107], a[132:135], v[20:23], v[104:107]\n"
"buffer_load_dwordx4 v206, s[56:59], s74 offen offset:1024 lds\n"
"v_mfma_f32_16x16x32_bf16 v[108:111], a[140:143], v[20:23], v[108:111]\n"
"s_lshl_b32 s12, s78, 12\n"
"v_mfma_f32_16x16x32_bf16 v[120:123], a[132:135], v[28:31], v[120:123]\n"
"s_add_u32 s12, s12, 1048576\n"
"s_waitcnt vmcnt(27) lgkmcnt(0)\n"
"v_mfma_f32_16x16x32_bf16 v[124:127], a[140:143], v[28:31], v[124:127]\n"
"v_mfma_f32_16x16x32_bf16 v[64:67], a[144:147], v[32:35], v[64:67]\n"
"ds_read_b64_tr_b16 a[128:129], v232 offset:32768\n"
"ds_read_b64_tr_b16 a[130:131], v232 offset:33280\n"
"v_cvt_pk_bf16_f32 v136, v136, v137\n"
"v_cvt_pk_bf16_f32 v137, v138, v139\n"
"v_mfma_f32_16x16x32_bf16 v[68:71], a[152:155], v[32:35], v[68:71]\n"
"v_cvt_pk_bf16_f32 v138, v140, v141\n"
"v_cvt_pk_bf16_f32 v139, v142, v143\n"
"s_add_u32 s76, s75, 32768\n"
"s_lshl_b32 s37, s78, 19\n"
"v_mfma_f32_16x16x32_bf16 v[80:83], a[144:147], v[40:43], v[80:83]\n"
"buffer_store_dwordx4 v[136:139], v224, s[60:63], s76 offen sc1\n"
"buffer_load_dwordx4 v207, s[56:59], s74 offen offset:2048 lds\n"
"v_mfma_f32_16x16x32_bf16 v[84:87], a[152:155], v[40:43], v[84:87]\n"
"ds_read_b64_tr_b16 a[132:133], v233 offset:32768\n"
"s_lshr_b32 s30, s79, 2\n"
"v_mfma_f32_16x16x32_bf16 v[96:99], a[148:151], v[32:35], v[96:99]\n"
"ds_read_b64_tr_b16 a[134:135], v233 offset:33280\n"
"v_mfma_f32_16x16x32_bf16 v[100:103], a[156:159], v[32:35], v[100:103]\n"
"s_lshl_b32 s30, s30, 18\n"
"v_mfma_f32_16x16x32_bf16 v[112:115], a[148:151], v[40:43], v[112:115]\n"
"ds_read_b64_tr_b16 a[136:137], v232 offset:32776\n"
"ds_read_b64_tr_b16 a[138:139], v232 offset:33288\n"
"v_mfma_f32_16x16x32_bf16 v[116:119], a[156:159], v[40:43], v[116:119]\n"
"s_add_u32 s37, s37, s30\n"
"v_mfma_f32_16x16x32_bf16 v[72:75], a[144:147], v[36:39], v[72:75]\n"
"buffer_load_dwordx4 v208, s[56:59], s74 offen offset:3072 lds\n"
"ds_read_b64_tr_b16 a[140:141], v233 offset:32776\n"
"v_mfma_f32_16x16x32_bf16 v[76:79], a[152:155], v[36:39], v[76:79]\n"
"s_lshl_b32 s30, s64, 10\n"
"v_mfma_f32_16x16x32_bf16 v[88:91], a[144:147], v[44:47], v[88:91]\n"
"ds_read_b64_tr_b16 a[142:143], v233 offset:33288\n"
"v_mfma_f32_16x16x32_bf16 v[92:95], a[152:155], v[44:47], v[92:95]\n"
"s_add_u32 s37, s37, s30\n"
"v_mfma_f32_16x16x32_bf16 v[104:107], a[148:151], v[36:39], v[104:107]\n"
"s_add_u32 s37, s37, 0x400000\n"
"v_mfma_f32_16x16x32_bf16 v[108:111], a[156:159], v[36:39], v[108:111]\n"
"v_mfma_f32_16x16x32_bf16 v[120:123], a[148:151], v[44:47], v[120:123]\n"
"s_and_b32 s30, s79, 1\n"
"s_waitcnt lgkmcnt(0)\n"
"v_mfma_f32_16x16x32_bf16 v[124:127], a[156:159], v[44:47], v[124:127]\n"
"v_mfma_f32_16x16x32_bf16 v[64:67], a[128:131], v[48:51], v[64:67]\n"
"s_lshl_b32 s30, s30, 9\n"
"v_mfma_f32_16x16x32_bf16 v[68:71], a[136:139], v[48:51], v[68:71]\n"
"s_add_u32 s74, s68, 393216\n"
"v_mfma_f32_16x16x32_bf16 v[80:83], a[128:131], v[56:59], v[80:83]\n"
"s_add_u32 s37, s37, s30\n"
"s_and_b32 s74, s74, 0x7ffff\n"
"v_mfma_f32_16x16x32_bf16 v[84:87], a[136:139], v[56:59], v[84:87]\n"
"s_add_u32 s74, s74, s35\n"
"s_add_u32 m0, s67, 32768\n"
"v_mfma_f32_16x16x32_bf16 v[96:99], a[132:135], v[48:51], v[96:99]\n"
"buffer_load_dwordx4 v205, s[56:59], s74 offen offset:0 lds\n"
"v_mfma_f32_16x16x32_bf16 v[100:103], a[140:143], v[48:51], v[100:103]\n"
"s_and_b32 s30, s79, 2\n"
"v_mfma_f32_16x16x32_bf16 v[112:115], a[132:135], v[56:59], v[112:115]\n"
"s_lshl_b32 s30, s30, 14\n"
"v_mfma_f32_16x16x32_bf16 v[116:119], a[140:143], v[56:59], v[116:119]\n"
"v_mfma_f32_16x16x32_bf16 v[72:75], a[128:131], v[52:55], v[72:75]\n"
"v_cvt_pk_bf16_f32 v144, v144, v145\n"
"v_cvt_pk_bf16_f32 v145, v146, v147\n"
"v_cvt_pk_bf16_f32 v146, v148, v149\n"
"v_mfma_f32_16x16x32_bf16 v[76:79], a[136:139], v[52:55], v[76:79]\n"
"v_cvt_pk_bf16_f32 v147, v150, v151\n"
"s_add_u32 s76, s75, 262144\n"
"s_add_u32 s37, s37, s30\n"
"v_mfma_f32_16x16x32_bf16 v[88:91], a[128:131], v[60:63], v[88:91]\n"
"buffer_store_dwordx4 v[144:147], v224, s[60:63], s76 offen sc1\n"
"buffer_load_dwordx4 v206, s[56:59], s74 offen offset:1024 lds\n"
"v_mfma_f32_16x16x32_bf16 v[92:95], a[136:139], v[60:63], v[92:95]\n"
"v_lshrrev_b32 v219, 5, v192\n"
"v_mfma_f32_16x16x32_bf16 v[104:107], a[132:135], v[52:55], v[104:107]\n"
"v_lshlrev_b32 v219, 16, v219\n"
"v_mfma_f32_16x16x32_bf16 v[108:111], a[140:143], v[52:55], v[108:111]\n"
"v_mfma_f32_16x16x32_bf16 v[120:123], a[132:135], v[60:63], v[120:123]\n"
"v_and_b32 v218, 31, v192\n"
"s_waitcnt vmcnt(29)\n"
"v_mfma_f32_16x16x32_bf16 v[124:127], a[140:143], v[60:63], v[124:127]\n"
"v_mfma_f32_16x16x32_bf16 v[64:67], a[240:243], a[0:3], v[64:67]\n"
"ds_read_b64_tr_b16 a[144:145], v232 offset:49152\n"
"ds_read_b64_tr_b16 a[146:147], v232 offset:49664\n"
"v_mfma_f32_16x16x32_bf16 v[68:71], a[248:251], a[0:3], v[68:71]\n"
"v_lshl_add_u32 v219, v218, 4, v219\n"
"v_mfma_f32_16x16x32_bf16 v[80:83], a[240:243], a[4:7], v[80:83]\n"
"buffer_load_dwordx4 v207, s[56:59], s74 offen offset:2048 lds\n"
"v_mfma_f32_16x16x32_bf16 v[84:87], a[248:251], a[4:7], v[84:87]\n"
"ds_read_b64_tr_b16 a[148:149], v233 offset:49152\n"
"v_mfma_f32_16x16x32_bf16 v[96:99], a[244:247], a[0:3], v[96:99]\n"
"ds_read_b64_tr_b16 a[150:151], v233 offset:49664\n"
"v_mfma_f32_16x16x32_bf16 v[100:103], a[252:255], a[0:3], v[100:103]\n"
"v_mfma_f32_16x16x32_bf16 v[112:115], a[244:247], a[4:7], v[112:115]\n"
"ds_read_b64_tr_b16 a[152:153], v232 offset:49160\n"
"v_mfma_f32_16x16x32_bf16 v[116:119], a[252:255], a[4:7], v[116:119]\n"
"ds_read_b64_tr_b16 a[154:155], v232 offset:49672\n"
"v_mfma_f32_16x16x32_bf16 v[72:75], a[240:243], a[8:11], v[72:75]\n"
"buffer_load_dwordx4 v208, s[56:59], s74 offen offset:3072 lds\n"
"v_mfma_f32_16x16x32_bf16 v[76:79], a[248:251], a[8:11], v[76:79]\n"
"ds_read_b64_tr_b16 a[156:157], v233 offset:49160\n"
"v_mfma_f32_16x16x32_bf16 v[88:91], a[240:243], a[12:15], v[88:91]\n"
"ds_read_b64_tr_b16 a[158:159], v233 offset:49672\n"
"v_mfma_f32_16x16x32_bf16 v[92:95], a[248:251], a[12:15], v[92:95]\n"
"v_mfma_f32_16x16x32_bf16 v[104:107], a[244:247], a[8:11], v[104:107]\n"
"v_mfma_f32_16x16x32_bf16 v[108:111], a[252:255], a[8:11], v[108:111]\n"
"v_mfma_f32_16x16x32_bf16 v[120:123], a[244:247], a[12:15], v[120:123]\n"
"s_waitcnt vmcnt(27) lgkmcnt(0)\n"
"v_mfma_f32_16x16x32_bf16 v[124:127], a[252:255], a[12:15], v[124:127]\n"
"v_mfma_f32_16x16x32_bf16 v[64:67], a[144:147], a[16:19], v[64:67]\n"
"ds_read_b64_tr_b16 a[128:129], v234 offset:0\n"
"ds_read_b64_tr_b16 a[130:131], v234 offset:512\n"
"v_cvt_pk_bf16_f32 v152, v152, v153\n"
"v_cvt_pk_bf16_f32 v153, v154, v155\n"
"v_mfma_f32_16x16x32_bf16 v[68:71], a[152:155], a[16:19], v[68:71]\n"
"v_cvt_pk_bf16_f32 v154, v156, v157\n"
"v_cvt_pk_bf16_f32 v155, v158, v159\n"
"s_add_u32 s76, s75, 294912\n"
"s_add_u32 s74, s68, 425984\n"
"v_mfma_f32_16x16x32_bf16 v[80:83], a[144:147], a[20:23], v[80:83]\n"
"buffer_store_dwordx4 v[152:155], v224, s[60:63], s76 offen sc1\n"
"ds_read_b64_tr_b16 a[132:133], v235 offset:0\n"
"s_and_b32 s74, s74, 0x7ffff\n"
"v_mfma_f32_16x16x32_bf16 v[84:87], a[152:155], a[20:23], v[84:87]\n"
"s_add_u32 s74, s74, s35\n"
"s_add_u32 m0, s67, 49152\n"
"v_mfma_f32_16x16x32_bf16 v[96:99], a[148:151], a[16:19], v[96:99]\n"
"buffer_load_dwordx4 v205, s[56:59], s74 offen offset:0 lds\n"
"v_mfma_f32_16x16x32_bf16 v[100:103], a[156:159], a[16:19], v[100:103]\n"
"ds_read_b64_tr_b16 a[134:135], v235 offset:512\n"
"v_mfma_f32_16x16x32_bf16 v[112:115], a[148:151], a[20:23], v[112:115]\n"
"ds_read_b64_tr_b16 a[136:137], v234 offset:8\n"
"v_mfma_f32_16x16x32_bf16 v[116:119], a[156:159], a[20:23], v[116:119]\n"
"ds_read_b64_tr_b16 a[138:139], v234 offset:520\n"
"v_mfma_f32_16x16x32_bf16 v[72:75], a[144:147], a[24:27], v[72:75]\n"
"ds_read_b64_tr_b16 a[140:141], v235 offset:8\n"
"v_mfma_f32_16x16x32_bf16 v[76:79], a[152:155], a[24:27], v[76:79]\n"
"v_mfma_f32_16x16x32_bf16 v[88:91], a[144:147], a[28:31], v[88:91]\n"
"buffer_load_dwordx4 v206, s[56:59], s74 offen offset:1024 lds\n"
"v_mfma_f32_16x16x32_bf16 v[92:95], a[152:155], a[28:31], v[92:95]\n"
"ds_read_b64_tr_b16 a[142:143], v235 offset:520\n"
"v_mfma_f32_16x16x32_bf16 v[104:107], a[148:151], a[24:27], v[104:107]\n"
"v_mfma_f32_16x16x32_bf16 v[108:111], a[156:159], a[24:27], v[108:111]\n"
"v_mfma_f32_16x16x32_bf16 v[120:123], a[148:151], a[28:31], v[120:123]\n"
"s_waitcnt vmcnt(26) lgkmcnt(0)\n"
"v_mfma_f32_16x16x32_bf16 v[124:127], a[156:159], a[28:31], v[124:127]\n"
"v_mfma_f32_16x16x32_bf16 v[64:67], a[128:131], a[32:35], v[64:67]\n"
"ds_read_b64_tr_b16 a[144:145], v234 offset:16384\n"
"v_mfma_f32_16x16x32_bf16 v[68:71], a[136:139], a[32:35], v[68:71]\n"
"ds_read_b64_tr_b16 a[146:147], v234 offset:16896\n"
"v_mfma_f32_16x16x32_bf16 v[80:83], a[128:131], a[36:39], v[80:83]\n"
"buffer_load_dwordx4 v207, s[56:59], s74 offen offset:2048 lds\n"
"v_mfma_f32_16x16x32_bf16 v[84:87], a[136:139], a[36:39], v[84:87]\n"
"ds_read_b64_tr_b16 a[148:149], v235 offset:16384\n"
"v_mfma_f32_16x16x32_bf16 v[96:99], a[132:135], a[32:35], v[96:99]\n"
"ds_read_b64_tr_b16 a[150:151], v235 offset:16896\n"
"v_mfma_f32_16x16x32_bf16 v[100:103], a[140:143], a[32:35], v[100:103]\n"
"v_mfma_f32_16x16x32_bf16 v[112:115], a[132:135], a[36:39], v[112:115]\n"
"ds_read_b64_tr_b16 a[152:153], v234 offset:16392\n"
"v_mfma_f32_16x16x32_bf16 v[116:119], a[140:143], a[36:39], v[116:119]\n"
"ds_read_b64_tr_b16 a[154:155], v234 offset:16904\n"
"v_mfma_f32_16x16x32_bf16 v[72:75], a[128:131], a[40:43], v[72:75]\n"
"buffer_load_dwordx4 v208, s[56:59], s74 offen offset:3072 lds\n"
"ds_read_b64_tr_b16 a[156:157], v235 offset:16392\n"
"v_cvt_pk_bf16_f32 v160, v160, v161\n"
"v_cvt_pk_bf16_f32 v161, v162, v163\n"
"v_mfma_f32_16x16x32_bf16 v[76:79], a[136:139], a[40:43], v[76:79]\n"
"v_cvt_pk_bf16_f32 v162, v164, v165\n"
"v_cvt_pk_bf16_f32 v163, v166, v167\n"
"s_add_u32 s76, s75, 65536\n"
"v_mfma_f32_16x16x32_bf16 v[88:91], a[128:131], a[44:47], v[88:91]\n"
"buffer_store_dwordx4 v[160:163], v224, s[60:63], s76 offen sc1\n"
"v_mfma_f32_16x16x32_bf16 v[92:95], a[136:139], a[44:47], v[92:95]\n"
"ds_read_b64_tr_b16 a[158:159], v235 offset:16904\n"
"v_mfma_f32_16x16x32_bf16 v[104:107], a[132:135], a[40:43], v[104:107]\n"
"v_mfma_f32_16x16x32_bf16 v[108:111], a[140:143], a[40:43], v[108:111]\n"
"v_mfma_f32_16x16x32_bf16 v[120:123], a[132:135], a[44:47], v[120:123]\n"
"s_waitcnt vmcnt(25) lgkmcnt(0)\n"
"v_mfma_f32_16x16x32_bf16 v[124:127], a[140:143], a[44:47], v[124:127]\n"
"v_mfma_f32_16x16x32_bf16 v[64:67], a[144:147], a[48:51], v[64:67]\n"
"ds_read_b64_tr_b16 a[128:129], v232 offset:0\n"
"ds_read_b64_tr_b16 a[130:131], v232 offset:512\n"
"v_mfma_f32_16x16x32_bf16 v[68:71], a[152:155], a[48:51], v[68:71]\n"
"s_add_u32 s74, s68, 458752\n"
"v_mfma_f32_16x16x32_bf16 v[80:83], a[144:147], a[52:55], v[80:83]\n"
"ds_read_b64_tr_b16 a[132:133], v233 offset:0\n"
"s_and_b32 s74, s74, 0x7ffff\n"
"v_mfma_f32_16x16x32_bf16 v[84:87], a[152:155], a[52:55], v[84:87]\n"
"s_add_u32 s74, s74, s35\n"
"s_add_u32 m0, s67, 98304\n"
"v_mfma_f32_16x16x32_bf16 v[96:99], a[148:151], a[48:51], v[96:99]\n"
"buffer_load_dwordx4 v205, s[56:59], s74 offen offset:0 lds\n"
"v_mfma_f32_16x16x32_bf16 v[100:103], a[156:159], a[48:51], v[100:103]\n"
"ds_read_b64_tr_b16 a[134:135], v233 offset:512\n"
"v_mfma_f32_16x16x32_bf16 v[112:115], a[148:151], a[52:55], v[112:115]\n"
"ds_read_b64_tr_b16 a[136:137], v232 offset:8\n"
"v_mfma_f32_16x16x32_bf16 v[116:119], a[156:159], a[52:55], v[116:119]\n"
"ds_read_b64_tr_b16 a[138:139], v232 offset:520\n"
"v_mfma_f32_16x16x32_bf16 v[72:75], a[144:147], a[56:59], v[72:75]\n"
"ds_read_b64_tr_b16 a[140:141], v233 offset:8\n"
"v_mfma_f32_16x16x32_bf16 v[76:79], a[152:155], a[56:59], v[76:79]\n"
"v_mfma_f32_16x16x32_bf16 v[88:91], a[144:147], a[60:63], v[88:91]\n"
"buffer_load_dwordx4 v206, s[56:59], s74 offen offset:1024 lds\n"
"v_mfma_f32_16x16x32_bf16 v[92:95], a[152:155], a[60:63], v[92:95]\n"
"ds_read_b64_tr_b16 a[142:143], v233 offset:520\n"
"v_mfma_f32_16x16x32_bf16 v[104:107], a[148:151], a[56:59], v[104:107]\n"
"v_mfma_f32_16x16x32_bf16 v[108:111], a[156:159], a[56:59], v[108:111]\n"
"v_mfma_f32_16x16x32_bf16 v[120:123], a[148:151], a[60:63], v[120:123]\n"
"s_waitcnt vmcnt(23) lgkmcnt(0)\n"
"v_mfma_f32_16x16x32_bf16 v[124:127], a[156:159], a[60:63], v[124:127]\n"
"v_mfma_f32_16x16x32_bf16 v[64:67], a[128:131], a[64:67], v[64:67]\n"
"ds_read_b64_tr_b16 a[144:145], v232 offset:16384\n"
"ds_read_b64_tr_b16 a[146:147], v232 offset:16896\n"
"v_cvt_pk_bf16_f32 v168, v168, v169\n"
"v_cvt_pk_bf16_f32 v169, v170, v171\n"
"v_mfma_f32_16x16x32_bf16 v[68:71], a[136:139], a[64:67], v[68:71]\n"
"v_cvt_pk_bf16_f32 v170, v172, v173\n"
"v_cvt_pk_bf16_f32 v171, v174, v175\n"
"s_add_u32 s76, s75, 98304\n"
"v_mfma_f32_16x16x32_bf16 v[80:83], a[128:131], a[68:71], v[80:83]\n"
"buffer_store_dwordx4 v[168:171], v224, s[60:63], s76 offen sc1\n"
"buffer_load_dwordx4 v207, s[56:59], s74 offen offset:2048 lds\n"
"v_mfma_f32_16x16x32_bf16 v[84:87], a[136:139], a[68:71], v[84:87]\n"
"ds_read_b64_tr_b16 a[148:149], v233 offset:16384\n"
"v_mfma_f32_16x16x32_bf16 v[96:99], a[132:135], a[64:67], v[96:99]\n"
"ds_read_b64_tr_b16 a[150:151], v233 offset:16896\n"
"v_mfma_f32_16x16x32_bf16 v[100:103], a[140:143], a[64:67], v[100:103]\n"
"v_mfma_f32_16x16x32_bf16 v[112:115], a[132:135], a[68:71], v[112:115]\n"
"ds_read_b64_tr_b16 a[152:153], v232 offset:16392\n"
"v_mfma_f32_16x16x32_bf16 v[116:119], a[140:143], a[68:71], v[116:119]\n"
"ds_read_b64_tr_b16 a[154:155], v232 offset:16904\n"
"v_mfma_f32_16x16x32_bf16 v[72:75], a[128:131], a[72:75], v[72:75]\n"
"buffer_load_dwordx4 v208, s[56:59], s74 offen offset:3072 lds\n"
"v_mfma_f32_16x16x32_bf16 v[76:79], a[136:139], a[72:75], v[76:79]\n"
"ds_read_b64_tr_b16 a[156:157], v233 offset:16392\n"
"v_mfma_f32_16x16x32_bf16 v[88:91], a[128:131], a[76:79], v[88:91]\n"
"ds_read_b64_tr_b16 a[158:159], v233 offset:16904\n"
"v_mfma_f32_16x16x32_bf16 v[92:95], a[136:139], a[76:79], v[92:95]\n"
"v_mfma_f32_16x16x32_bf16 v[104:107], a[132:135], a[72:75], v[104:107]\n"
"v_mfma_f32_16x16x32_bf16 v[108:111], a[140:143], a[72:75], v[108:111]\n"
"v_mfma_f32_16x16x32_bf16 v[120:123], a[132:135], a[76:79], v[120:123]\n"
"s_waitcnt vmcnt(21) lgkmcnt(0)\n"
"v_mfma_f32_16x16x32_bf16 v[124:127], a[140:143], a[76:79], v[124:127]\n"
"v_mfma_f32_16x16x32_bf16 v[64:67], a[144:147], a[80:83], v[64:67]\n"
"ds_read_b64_tr_b16 a[128:129], v216 offset:0\n"
"ds_read_b64_tr_b16 a[130:131], v216 offset:512\n"
"v_mfma_f32_16x16x32_bf16 v[68:71], a[152:155], a[80:83], v[68:71]\n"
"s_cmp_eq_u32 s64, 0\n"
"s_cselect_b32 s17, 0, 0x7000000\n"
"s_cselect_b64 s[20:21], -1, 0\n"
"s_add_u32 s74, s68, 491520\n"
"v_mfma_f32_16x16x32_bf16 v[80:83], a[144:147], a[84:87], v[80:83]\n"
"ds_read_b64_tr_b16 a[132:133], v217 offset:0\n"
"s_add_u32 s55, s55, s17\n"
"s_and_b32 s74, s74, 0x7ffff\n"
"v_mfma_f32_16x16x32_bf16 v[84:87], a[152:155], a[84:87], v[84:87]\n"
"s_add_u32 s74, s74, s35\n"
"s_add_u32 m0, s67, 114688\n"
"v_mfma_f32_16x16x32_bf16 v[96:99], a[148:151], a[80:83], v[96:99]\n"
"buffer_load_dwordx4 v205, s[56:59], s74 offen offset:0 lds\n"
"ds_read_b64_tr_b16 a[134:135], v217 offset:512\n"
"v_mfma_f32_16x16x32_bf16 v[100:103], a[156:159], a[80:83], v[100:103]\n"
"Lst12_%=:\n"
"s_cbranch_execz Lback0_%=\n"
"s_add_u32 s12, s12, s17\n"
"v_mfma_f32_16x16x32_bf16 v[112:115], a[148:151], a[84:87], v[112:115]\n"
"ds_read_b64_tr_b16 a[136:137], v216 offset:8\n"
"v_mfma_f32_16x16x32_bf16 v[116:119], a[156:159], a[84:87], v[116:119]\n"
"ds_read_b64_tr_b16 a[138:139], v216 offset:520\n"
"v_mfma_f32_16x16x32_bf16 v[72:75], a[144:147], a[88:91], v[72:75]\n"
"ds_read_b64_tr_b16 a[140:141], v217 offset:8\n"
"v_cvt_pk_bf16_f32 v176, v176, v177\n"
"v_cvt_pk_bf16_f32 v177, v178, v179\n"
"v_mfma_f32_16x16x32_bf16 v[76:79], a[152:155], a[88:91], v[76:79]\n"
"v_cvt_pk_bf16_f32 v178, v180, v181\n"
"v_cvt_pk_bf16_f32 v179, v182, v183\n"
"s_add_u32 s76, s75, 327680\n"
"v_mfma_f32_16x16x32_bf16 v[88:91], a[144:147], a[92:95], v[88:91]\n"
"buffer_store_dwordx4 v[176:179], v224, s[60:63], s76 offen sc1\n"
"buffer_load_dwordx4 v206, s[56:59], s74 offen offset:1024 lds\n"
"v_mfma_f32_16x16x32_bf16 v[92:95], a[152:155], a[92:95], v[92:95]\n"
"ds_read_b64_tr_b16 a[142:143], v217 offset:520\n"
"v_mfma_f32_16x16x32_bf16 v[104:107], a[148:151], a[88:91], v[104:107]\n"
"v_mfma_f32_16x16x32_bf16 v[108:111], a[156:159], a[88:91], v[108:111]\n"
"v_mfma_f32_16x16x32_bf16 v[120:123], a[148:151], a[92:95], v[120:123]\n"
"s_waitcnt vmcnt(19) lgkmcnt(0)\n"
"v_mfma_f32_16x16x32_bf16 v[124:127], a[156:159], a[92:95], v[124:127]\n"
"v_mfma_f32_16x16x32_bf16 v[64:67], a[128:131], a[96:99], v[64:67]\n"
"buffer_load_dwordx4 v207, s[56:59], s74 offen offset:2048 lds\n"
"ds_read_b64_tr_b16 a[144:145], v216 offset:16384\n"
"v_mfma_f32_16x16x32_bf16 v[68:71], a[136:139], a[96:99], v[68:71]\n"
"ds_read_b64_tr_b16 a[146:147], v216 offset:16896\n"
"v_mfma_f32_16x16x32_bf16 v[80:83], a[128:131], a[100:103], v[80:83]\n"
"ds_read_b64_tr_b16 a[148:149], v217 offset:16384\n"
"v_mfma_f32_16x16x32_bf16 v[84:87], a[136:139], a[100:103], v[84:87]\n"
"v_mfma_f32_16x16x32_bf16 v[96:99], a[132:135], a[96:99], v[96:99]\n"
"ds_read_b64_tr_b16 a[150:151], v217 offset:16896\n"
"v_mfma_f32_16x16x32_bf16 v[100:103], a[140:143], a[96:99], v[100:103]\n"
"v_mfma_f32_16x16x32_bf16 v[112:115], a[132:135], a[100:103], v[112:115]\n"
"buffer_load_dwordx4 v208, s[56:59], s74 offen offset:3072 lds\n"
"ds_read_b64_tr_b16 a[152:153], v216 offset:16392\n"
"v_mfma_f32_16x16x32_bf16 v[116:119], a[140:143], a[100:103], v[116:119]\n"
"ds_read_b64_tr_b16 a[154:155], v216 offset:16904\n"
"v_mfma_f32_16x16x32_bf16 v[72:75], a[128:131], a[104:107], v[72:75]\n"
"ds_read_b64_tr_b16 a[156:157], v217 offset:16392\n"
"v_mfma_f32_16x16x32_bf16 v[76:79], a[136:139], a[104:107], v[76:79]\n"
"v_mfma_f32_16x16x32_bf16 v[88:91], a[128:131], a[108:111], v[88:91]\n"
"ds_read_b64_tr_b16 a[158:159], v217 offset:16904\n"
"v_mfma_f32_16x16x32_bf16 v[92:95], a[136:139], a[108:111], v[92:95]\n"
"v_mfma_f32_16x16x32_bf16 v[104:107], a[132:135], a[104:107], v[104:107]\n"
"v_mfma_f32_16x16x32_bf16 v[108:111], a[140:143], a[104:107], v[108:111]\n"
"v_mfma_f32_16x16x32_bf16 v[120:123], a[132:135], a[108:111], v[120:123]\n"
"s_waitcnt vmcnt(16) lgkmcnt(0)\n"
"v_mfma_f32_16x16x32_bf16 v[124:127], a[140:143], a[108:111], v[124:127]\n"
"v_mfma_f32_16x16x32_bf16 v[64:67], a[144:147], a[112:115], v[64:67]\n"
"ds_read_b64_tr_b16 a[128:129], v216 offset:32768\n"
"ds_read_b64_tr_b16 a[130:131], v216 offset:33280\n"
"v_cvt_pk_bf16_f32 v184, v184, v185\n"
"v_cvt_pk_bf16_f32 v185, v186, v187\n"
"v_mfma_f32_16x16x32_bf16 v[68:71], a[152:155], a[112:115], v[68:71]\n"
"v_cvt_pk_bf16_f32 v186, v188, v189\n"
"v_cvt_pk_bf16_f32 v187, v190, v191\n"
"s_add_u32 s76, s75, 360448\n"
"v_mfma_f32_16x16x32_bf16 v[80:83], a[144:147], a[116:119], v[80:83]\n"
"buffer_store_dwordx4 v[184:187], v224, s[60:63], s76 offen sc1\n"
"v_mfma_f32_16x16x32_bf16 v[84:87], a[152:155], a[116:119], v[84:87]\n"
"ds_read_b64_tr_b16 a[132:133], v217 offset:32768\n"
"v_mfma_f32_16x16x32_bf16 v[96:99], a[148:151], a[112:115], v[96:99]\n"
"ds_read_b64_tr_b16 a[134:135], v217 offset:33280\n"
"v_mfma_f32_16x16x32_bf16 v[100:103], a[156:159], a[112:115], v[100:103]\n"
"v_mfma_f32_16x16x32_bf16 v[112:115], a[148:151], a[116:119], v[112:115]\n"
"ds_read_b64_tr_b16 a[136:137], v216 offset:32776\n"
"v_mfma_f32_16x16x32_bf16 v[116:119], a[156:159], a[116:119], v[116:119]\n"
"ds_read_b64_tr_b16 a[138:139], v216 offset:33288\n"
"v_mfma_f32_16x16x32_bf16 v[72:75], a[144:147], a[120:123], v[72:75]\n"
"ds_read_b64_tr_b16 a[140:141], v217 offset:32776\n"
"v_mfma_f32_16x16x32_bf16 v[76:79], a[152:155], a[120:123], v[76:79]\n"
"v_mfma_f32_16x16x32_bf16 v[88:91], a[144:147], a[124:127], v[88:91]\n"
"ds_read_b64_tr_b16 a[142:143], v217 offset:33288\n"
"s_add_u32 m0, s67, 131072\n"
"v_mfma_f32_16x16x32_bf16 v[92:95], a[152:155], a[124:127], v[92:95]\n"
"s_mov_b32 s74, 0\n"
"v_mfma_f32_16x16x32_bf16 v[104:107], a[148:151], a[120:123], v[104:107]\n"
"buffer_load_dwordx4 v209, s[44:47], s74 offen offset:0 lds\n"
"v_mfma_f32_16x16x32_bf16 v[108:111], a[156:159], a[120:123], v[108:111]\n"
"v_mfma_f32_16x16x32_bf16 v[120:123], a[148:151], a[124:127], v[120:123]\n"
"s_waitcnt vmcnt(13) lgkmcnt(0)\n"
"v_mfma_f32_16x16x32_bf16 v[124:127], a[156:159], a[124:127], v[124:127]\n"
"v_mfma_f32_16x16x32_bf16 v[64:67], a[128:131], a[160:163], v[64:67]\n"
"ds_read_b64_tr_b16 a[144:145], v216 offset:49152\n"
"v_mfma_f32_16x16x32_bf16 v[68:71], a[136:139], a[160:163], v[68:71]\n"
"ds_read_b64_tr_b16 a[146:147], v216 offset:49664\n"
"v_mfma_f32_16x16x32_bf16 v[80:83], a[128:131], a[164:167], v[80:83]\n"
"buffer_load_dwordx4 v210, s[44:47], s74 offen offset:1024 lds\n"
"v_mfma_f32_16x16x32_bf16 v[84:87], a[136:139], a[164:167], v[84:87]\n"
"ds_read_b64_tr_b16 a[148:149], v217 offset:49152\n"
"v_mfma_f32_16x16x32_bf16 v[96:99], a[132:135], a[160:163], v[96:99]\n"
"ds_read_b64_tr_b16 a[150:151], v217 offset:49664\n"
"v_mfma_f32_16x16x32_bf16 v[100:103], a[140:143], a[160:163], v[100:103]\n"
"v_mfma_f32_16x16x32_bf16 v[112:115], a[132:135], a[164:167], v[112:115]\n"
"ds_read_b64_tr_b16 a[152:153], v216 offset:49160\n"
"v_mfma_f32_16x16x32_bf16 v[116:119], a[140:143], a[164:167], v[116:119]\n"
"ds_read_b64_tr_b16 a[154:155], v216 offset:49672\n"
"v_mfma_f32_16x16x32_bf16 v[72:75], a[128:131], a[168:171], v[72:75]\n"
"ds_read_b64_tr_b16 a[156:157], v217 offset:49160\n"
"v_mfma_f32_16x16x32_bf16 v[76:79], a[136:139], a[168:171], v[76:79]\n"
"v_mfma_f32_16x16x32_bf16 v[88:91], a[128:131], a[172:175], v[88:91]\n"
"ds_read_b64_tr_b16 a[158:159], v217 offset:49672\n"
"v_mfma_f32_16x16x32_bf16 v[92:95], a[136:139], a[172:175], v[92:95]\n"
"v_mfma_f32_16x16x32_bf16 v[104:107], a[132:135], a[168:171], v[104:107]\n"
"buffer_load_dwordx4 v211, s[44:47], s74 offen offset:2048 lds\n"
"v_mfma_f32_16x16x32_bf16 v[108:111], a[140:143], a[168:171], v[108:111]\n"
"v_mfma_f32_16x16x32_bf16 v[120:123], a[132:135], a[172:175], v[120:123]\n"
"s_waitcnt vmcnt(4) lgkmcnt(0)\n"
"v_mfma_f32_16x16x32_bf16 v[124:127], a[140:143], a[172:175], v[124:127]\n"
"v_mfma_f32_16x16x32_bf16 v[64:67], a[144:147], a[176:179], v[64:67]\n"
"ds_read_b64_tr_b16 a[128:129], v232 offset:32768\n"
"ds_read_b64_tr_b16 a[130:131], v232 offset:33280\n"
"v_mfma_f32_16x16x32_bf16 v[68:71], a[152:155], a[176:179], v[68:71]\n"
"ds_read_b64_tr_b16 a[132:133], v233 offset:32768\n"
"v_mfma_f32_16x16x32_bf16 v[80:83], a[144:147], a[180:183], v[80:83]\n"
"buffer_load_dwordx4 v212, s[44:47], s74 offen offset:3072 lds\n"
"ds_read_b64_tr_b16 a[134:135], v233 offset:33280\n"
"v_mfma_f32_16x16x32_bf16 v[84:87], a[152:155], a[180:183], v[84:87]\n"
"ds_read_b64_tr_b16 a[136:137], v232 offset:32776\n"
"v_mfma_f32_16x16x32_bf16 v[96:99], a[148:151], a[176:179], v[96:99]\n"
"ds_read_b64_tr_b16 a[138:139], v232 offset:33288\n"
"v_mfma_f32_16x16x32_bf16 v[100:103], a[156:159], a[176:179], v[100:103]\n"
"ds_read_b64_tr_b16 a[140:141], v233 offset:32776\n"
"v_mfma_f32_16x16x32_bf16 v[112:115], a[148:151], a[180:183], v[112:115]\n"
"ds_read_b64_tr_b16 a[142:143], v233 offset:33288\n"
"ds_read_b64_tr_b16 a[224:225], v232 offset:49152\n"
"v_mfma_f32_16x16x32_bf16 v[116:119], a[156:159], a[180:183], v[116:119]\n"
"ds_read_b64_tr_b16 a[226:227], v232 offset:49664\n"
"v_mfma_f32_16x16x32_bf16 v[72:75], a[144:147], a[184:187], v[72:75]\n"
"ds_read_b64_tr_b16 a[228:229], v233 offset:49152\n"
"v_mfma_f32_16x16x32_bf16 v[76:79], a[152:155], a[184:187], v[76:79]\n"
"ds_read_b64_tr_b16 a[230:231], v233 offset:49664\n"
"v_mfma_f32_16x16x32_bf16 v[88:91], a[144:147], a[188:191], v[88:91]\n"
"ds_read_b64_tr_b16 a[232:233], v232 offset:49160\n"
"v_mfma_f32_16x16x32_bf16 v[92:95], a[152:155], a[188:191], v[92:95]\n"
"ds_read_b64_tr_b16 a[234:235], v232 offset:49672\n"
"v_mfma_f32_16x16x32_bf16 v[104:107], a[148:151], a[184:187], v[104:107]\n"
"ds_read_b64_tr_b16 a[236:237], v233 offset:49160\n"
"v_mfma_f32_16x16x32_bf16 v[108:111], a[156:159], a[184:187], v[108:111]\n"
"ds_read_b64_tr_b16 a[238:239], v233 offset:49672\n"
"v_mfma_f32_16x16x32_bf16 v[120:123], a[148:151], a[188:191], v[120:123]\n"
"s_add_u32 m0, s67, 147456\n"
"s_mov_b32 s74, 114688\n"
"v_mfma_f32_16x16x32_bf16 v[124:127], a[156:159], a[188:191], v[124:127]\n"
"s_waitcnt lgkmcnt(0)\n"
"v_mfma_f32_16x16x32_bf16 v[64:67], a[128:131], a[192:195], v[64:67]\n"
"buffer_load_dwordx4 v209, s[44:47], s74 offen offset:0 lds\n"
"v_mfma_f32_16x16x32_bf16 v[68:71], a[136:139], a[192:195], v[68:71]\n"
"v_mfma_f32_16x16x32_bf16 v[72:75], a[128:131], a[200:203], v[72:75]\n"
"v_mfma_f32_16x16x32_bf16 v[76:79], a[136:139], a[200:203], v[76:79]\n"
"v_mfma_f32_16x16x32_bf16 v[64:67], a[224:227], a[208:211], v[64:67]\n"
"v_mfma_f32_16x16x32_bf16 v[68:71], a[232:235], a[208:211], v[68:71]\n"
"v_mfma_f32_16x16x32_bf16 v[72:75], a[224:227], a[216:219], v[72:75]\n"
"buffer_load_dwordx4 v210, s[44:47], s74 offen offset:1024 lds\n"
"v_mfma_f32_16x16x32_bf16 v[76:79], a[232:235], a[216:219], v[76:79]\n"
"v_mfma_f32_16x16x32_bf16 v[80:83], a[128:131], a[196:199], v[80:83]\n"
"s_waitcnt vmcnt(6)\n"
"s_barrier\n"
"buffer_store_dword v199, v198, s[60:63], s55 offen sc1\n"
"v_mfma_f32_16x16x32_bf16 v[84:87], a[136:139], a[196:199], v[84:87]\n"
"s_add_u32 s55, s55, 131072\n"
"v_mfma_f32_16x16x32_bf16 v[88:91], a[128:131], a[204:207], v[88:91]\n"
"v_mfma_f32_16x16x32_bf16 v[92:95], a[136:139], a[204:207], v[92:95]\n"
"v_mfma_f32_16x16x32_bf16 v[80:83], a[224:227], a[212:215], v[80:83]\n"
"v_cvt_pk_bf16_f32 v64, v64, v65\n"
"v_cvt_pk_bf16_f32 v65, v66, v67\n"
"v_cvt_pk_bf16_f32 v66, v68, v69\n"
"v_mfma_f32_16x16x32_bf16 v[84:87], a[232:235], a[212:215], v[84:87]\n"
"v_cvt_pk_bf16_f32 v67, v70, v71\n"
"s_add_u32 s76, s75, 131072\n"
"v_mfma_f32_16x16x32_bf16 v[88:91], a[224:227], a[220:223], v[88:91]\n"
"buffer_store_dwordx4 v[64:67], v224, s[60:63], s76 offen sc1\n"
"v_cvt_pk_bf16_f32 v72, v72, v73\n"
"v_cvt_pk_bf16_f32 v73, v74, v75\n"
"v_mfma_f32_16x16x32_bf16 v[92:95], a[232:235], a[220:223], v[92:95]\n"
"v_cvt_pk_bf16_f32 v74, v76, v77\n"
"v_cvt_pk_bf16_f32 v75, v78, v79\n"
"s_add_u32 s76, s75, 163840\n"
"v_mfma_f32_16x16x32_bf16 v[96:99], a[132:135], a[192:195], v[96:99]\n"
"buffer_store_dwordx4 v[72:75], v224, s[60:63], s76 offen sc1\n"
"v_mfma_f32_16x16x32_bf16 v[100:103], a[140:143], a[192:195], v[100:103]\n"
"v_mfma_f32_16x16x32_bf16 v[104:107], a[132:135], a[200:203], v[104:107]\n"
"buffer_load_dwordx4 v211, s[44:47], s74 offen offset:2048 lds\n"
"v_mfma_f32_16x16x32_bf16 v[108:111], a[140:143], a[200:203], v[108:111]\n"
"v_mfma_f32_16x16x32_bf16 v[96:99], a[228:231], a[208:211], v[96:99]\n"
"v_cvt_pk_bf16_f32 v80, v80, v81\n"
"v_cvt_pk_bf16_f32 v81, v82, v83\n"
"v_cvt_pk_bf16_f32 v82, v84, v85\n"
"v_mfma_f32_16x16x32_bf16 v[100:103], a[236:239], a[208:211], v[100:103]\n"
"v_cvt_pk_bf16_f32 v83, v86, v87\n"
"s_add_u32 s76, s75, 393216\n"
"v_mfma_f32_16x16x32_bf16 v[104:107], a[228:231], a[216:219], v[104:107]\n"
"buffer_store_dwordx4 v[80:83], v224, s[60:63], s76 offen sc1\n"
"v_cvt_pk_bf16_f32 v88, v88, v89\n"
"v_cvt_pk_bf16_f32 v89, v90, v91\n"
"v_mfma_f32_16x16x32_bf16 v[108:111], a[236:239], a[216:219], v[108:111]\n"
"v_cvt_pk_bf16_f32 v90, v92, v93\n"
"v_cvt_pk_bf16_f32 v91, v94, v95\n"
"s_add_u32 s76, s75, 425984\n"
"v_mfma_f32_16x16x32_bf16 v[112:115], a[132:135], a[196:199], v[112:115]\n"
"buffer_store_dwordx4 v[88:91], v224, s[60:63], s76 offen sc1\n"
"v_mfma_f32_16x16x32_bf16 v[116:119], a[140:143], a[196:199], v[116:119]\n"
"v_mfma_f32_16x16x32_bf16 v[120:123], a[132:135], a[204:207], v[120:123]\n"
"buffer_load_dwordx4 v212, s[44:47], s74 offen offset:3072 lds\n"
"v_mfma_f32_16x16x32_bf16 v[124:127], a[140:143], a[204:207], v[124:127]\n"
"v_mfma_f32_16x16x32_bf16 v[112:115], a[228:231], a[212:215], v[112:115]\n"
"v_cvt_pk_bf16_f32 v96, v96, v97\n"
"v_cvt_pk_bf16_f32 v97, v98, v99\n"
"v_cvt_pk_bf16_f32 v98, v100, v101\n"
"v_mfma_f32_16x16x32_bf16 v[116:119], a[236:239], a[212:215], v[116:119]\n"
"v_cvt_pk_bf16_f32 v99, v102, v103\n"
"s_add_u32 s76, s75, 196608\n"
"v_mfma_f32_16x16x32_bf16 v[120:123], a[228:231], a[220:223], v[120:123]\n"
"buffer_store_dwordx4 v[96:99], v224, s[60:63], s76 offen sc1\n"
"v_cvt_pk_bf16_f32 v104, v104, v105\n"
"v_cvt_pk_bf16_f32 v105, v106, v107\n"
"v_cvt_pk_bf16_f32 v106, v108, v109\n"
"v_mfma_f32_16x16x32_bf16 v[124:127], a[236:239], a[220:223], v[124:127]\n"
"v_cvt_pk_bf16_f32 v107, v110, v111\n"
"s_add_u32 s76, s75, 229376\n"
"buffer_store_dwordx4 v[104:107], v224, s[60:63], s76 offen sc1\n"
"v_cvt_pk_bf16_f32 v112, v112, v113\n"
"v_cvt_pk_bf16_f32 v113, v114, v115\n"
"v_cvt_pk_bf16_f32 v114, v116, v117\n"
"v_cvt_pk_bf16_f32 v115, v118, v119\n"
"s_add_u32 s76, s75, 458752\n"
"buffer_store_dwordx4 v[112:115], v224, s[60:63], s76 offen sc1\n"
"v_cvt_pk_bf16_f32 v120, v120, v121\n"
"v_cvt_pk_bf16_f32 v121, v122, v123\n"
"v_cvt_pk_bf16_f32 v122, v124, v125\n"
"v_cvt_pk_bf16_f32 v123, v126, v127\n"
"s_add_u32 s76, s75, 491520\n"
"buffer_store_dwordx4 v[120:123], v224, s[60:63], s76 offen sc1\n"
"s_add_u32 m0, s67, 16384\n"
"s_mov_b32 s74, 229376\n"
"buffer_load_dwordx4 v209, s[44:47], s74 offen offset:0 lds\n"
"buffer_load_dwordx4 v210, s[44:47], s74 offen offset:1024 lds\n"
"buffer_load_dwordx4 v211, s[44:47], s74 offen offset:2048 lds\n"
"buffer_load_dwordx4 v212, s[44:47], s74 offen offset:3072 lds\n"
"v_min3_u32 v240, v237, v239, v243\n"
"v_max3_u32 v241, v237, v239, v243\n"
"v_xor_b32 v240, s36, v240\n"
"v_xor_b32 v241, s36, v241\n"
"v_or_b32 v240, v240, v241\n"
"v_cmp_ne_u32 vcc, 0, v240\n"
"s_nop 1\n"
"s_cbranch_vccnz Lf0s_13_%=\n"
"Lf0r_14_%=:\n"
"v_mov_b32_dpp v160, v236 quad_perm:[0,0,0,0] row_mask:0xf bank_mask:0xf\n"
"v_mov_b32_dpp v164, v236 quad_perm:[0,0,0,0] row_mask:0xf bank_mask:0xf\n"
"v_mov_b32_dpp v161, v236 quad_perm:[1,1,1,1] row_mask:0xf bank_mask:0xf\n"
"v_mov_b32_dpp v165, v236 quad_perm:[1,1,1,1] row_mask:0xf bank_mask:0xf\n"
"v_mov_b32_dpp v162, v236 quad_perm:[2,2,2,2] row_mask:0xf bank_mask:0xf\n"
"v_mov_b32_dpp v166, v236 quad_perm:[2,2,2,2] row_mask:0xf bank_mask:0xf\n"
"v_mov_b32_dpp v163, v236 quad_perm:[3,3,3,3] row_mask:0xf bank_mask:0xf\n"
"v_mov_b32_dpp v167, v236 quad_perm:[3,3,3,3] row_mask:0xf bank_mask:0xf\n"
"v_mov_b32_dpp v160, v164 row_shr:4 row_mask:0xf bank_mask:0xa\n"
"v_mov_b32_dpp v161, v165 row_shr:4 row_mask:0xf bank_mask:0xa\n"
"v_mov_b32_dpp v162, v166 row_shr:4 row_mask:0xf bank_mask:0xa\n"
"v_mov_b32_dpp v163, v167 row_shr:4 row_mask:0xf bank_mask:0xa\n"
"v_mov_b32_dpp v164, v164 row_shl:4 row_mask:0xf bank_mask:0x5\n"
"v_mov_b32_dpp v165, v165 row_shl:4 row_mask:0xf bank_mask:0x5\n"
"v_mov_b32_dpp v166, v166 row_shl:4 row_mask:0xf bank_mask:0x5\n"
"v_mov_b32_dpp v167, v167 row_shl:4 row_mask:0xf bank_mask:0x5\n"
"v_mov_b32_dpp v168, v238 quad_perm:[0,0,0,0] row_mask:0xf bank_mask:0xf\n"
"v_mov_b32_dpp v172, v238 quad_perm:[0,0,0,0] row_mask:0xf bank_mask:0xf\n"
"v_mov_b32_dpp v169, v238 quad_perm:[1,1,1,1] row_mask:0xf bank_mask:0xf\n"
"v_mov_b32_dpp v173, v238 quad_perm:[1,1,1,1] row_mask:0xf bank_mask:0xf\n"
"v_mov_b32_dpp v170, v238 quad_perm:[2,2,2,2] row_mask:0xf bank_mask:0xf\n"
"v_mov_b32_dpp v174, v238 quad_perm:[2,2,2,2] row_mask:0xf bank_mask:0xf\n"
"v_mov_b32_dpp v171, v238 quad_perm:[3,3,3,3] row_mask:0xf bank_mask:0xf\n"
"v_mov_b32_dpp v175, v238 quad_perm:[3,3,3,3] row_mask:0xf bank_mask:0xf\n"
"v_mov_b32_dpp v168, v172 row_shr:4 row_mask:0xf bank_mask:0xa\n"
"v_mov_b32_dpp v169, v173 row_shr:4 row_mask:0xf bank_mask:0xa\n"
"v_mov_b32_dpp v170, v174 row_shr:4 row_mask:0xf bank_mask:0xa\n"
"v_mov_b32_dpp v171, v175 row_shr:4 row_mask:0xf bank_mask:0xa\n"
"v_mov_b32_dpp v172, v172 row_shl:4 row_mask:0xf bank_mask:0x5\n"
"v_mov_b32_dpp v173, v173 row_shl:4 row_mask:0xf bank_mask:0x5\n"
"v_mov_b32_dpp v174, v174 row_shl:4 row_mask:0xf bank_mask:0x5\n"
"v_mov_b32_dpp v175, v175 row_shl:4 row_mask:0xf bank_mask:0x5\n"
"v_mov_b32_dpp v176, v242 quad_perm:[0,0,0,0] row_mask:0xf bank_mask:0xf\n"
"v_mov_b32_dpp v180, v242 quad_perm:[0,0,0,0] row_mask:0xf bank_mask:0xf\n"
"v_mov_b32_dpp v177, v242 quad_perm:[1,1,1,1] row_mask:0xf bank_mask:0xf\n"
"v_mov_b32_dpp v181, v242 quad_perm:[1,1,1,1] row_mask:0xf bank_mask:0xf\n"
"v_mov_b32_dpp v178, v242 quad_perm:[2,2,2,2] row_mask:0xf bank_mask:0xf\n"
"v_mov_b32_dpp v182, v242 quad_perm:[2,2,2,2] row_mask:0xf bank_mask:0xf\n"
"v_mov_b32_dpp v179, v242 quad_perm:[3,3,3,3] row_mask:0xf bank_mask:0xf\n"
"v_mov_b32_dpp v183, v242 quad_perm:[3,3,3,3] row_mask:0xf bank_mask:0xf\n"
"v_mov_b32_dpp v176, v180 row_shr:4 row_mask:0xf bank_mask:0xa\n"
"v_mov_b32_dpp v177, v181 row_shr:4 row_mask:0xf bank_mask:0xa\n"
"v_mov_b32_dpp v178, v182 row_shr:4 row_mask:0xf bank_mask:0xa\n"
"v_mov_b32_dpp v179, v183 row_shr:4 row_mask:0xf bank_mask:0xa\n"
"v_mov_b32_dpp v180, v180 row_shl:4 row_mask:0xf bank_mask:0x5\n"
"v_mov_b32_dpp v181, v181 row_shl:4 row_mask:0xf bank_mask:0x5\n"
"v_mov_b32_dpp v182, v182 row_shl:4 row_mask:0xf bank_mask:0x5\n"
"v_mov_b32_dpp v183, v183 row_shl:4 row_mask:0xf bank_mask:0x5\n"
"v_lshlrev_b32 v80, 16, v160\n"
"v_and_b32 v81, 0xffff0000, v160\n"
"v_lshlrev_b32 v82, 16, v161\n"
"v_and_b32 v83, 0xffff0000, v161\n"
"v_lshlrev_b32 v84, 16, v162\n"
"v_and_b32 v85, 0xffff0000, v162\n"
"v_lshlrev_b32 v86, 16, v163\n"
"v_and_b32 v87, 0xffff0000, v163\n"
"v_lshlrev_b32 v88, 16, v164\n"
"v_and_b32 v89, 0xffff0000, v164\n"
"v_lshlrev_b32 v90, 16, v165\n"
"v_and_b32 v91, 0xffff0000, v165\n"
"v_lshlrev_b32 v92, 16, v166\n"
"v_and_b32 v93, 0xffff0000, v166\n"
"v_lshlrev_b32 v94, 16, v167\n"
"v_and_b32 v95, 0xffff0000, v167\n"
"v_lshlrev_b32 v96, 16, v168\n"
"v_and_b32 v97, 0xffff0000, v168\n"
"v_lshlrev_b32 v98, 16, v169\n"
"v_and_b32 v99, 0xffff0000, v169\n"
"v_lshlrev_b32 v100, 16, v170\n"
"v_and_b32 v101, 0xffff0000, v170\n"
"v_lshlrev_b32 v102, 16, v171\n"
"v_and_b32 v103, 0xffff0000, v171\n"
"v_lshlrev_b32 v104, 16, v172\n"
"v_and_b32 v105, 0xffff0000, v172\n"
"v_lshlrev_b32 v106, 16, v173\n"
"v_and_b32 v107, 0xffff0000, v173\n"
"v_lshlrev_b32 v108, 16, v174\n"
"v_and_b32 v109, 0xffff0000, v174\n"
"v_lshlrev_b32 v110, 16, v175\n"
"v_and_b32 v111, 0xffff0000, v175\n"
"v_lshlrev_b32 v112, 16, v176\n"
"v_and_b32 v113, 0xffff0000, v176\n"
"v_lshlrev_b32 v114, 16, v177\n"
"v_and_b32 v115, 0xffff0000, v177\n"
"v_lshlrev_b32 v116, 16, v178\n"
"v_and_b32 v117, 0xffff0000, v178\n"
"v_lshlrev_b32 v118, 16, v179\n"
"v_and_b32 v119, 0xffff0000, v179\n"
"v_lshlrev_b32 v120, 16, v180\n"
"v_and_b32 v121, 0xffff0000, v180\n"
"v_lshlrev_b32 v122, 16, v181\n"
"v_and_b32 v123, 0xffff0000, v181\n"
"v_lshlrev_b32 v124, 16, v182\n"
"v_and_b32 v125, 0xffff0000, v182\n"
"v_lshlrev_b32 v126, 16, v183\n"
"v_and_b32 v127, 0xffff0000, v183\n"
"v_lshlrev_b32 v48, 16, v244\n"
"v_and_b32 v49, 0xffff0000, v244\n"
"v_lshlrev_b32 v50, 16, v245\n"
"v_and_b32 v51, 0xffff0000, v245\n"
"v_lshlrev_b32 v52, 16, v246\n"
"v_and_b32 v53, 0xffff0000, v246\n"
"v_lshlrev_b32 v54, 16, v247\n"
"v_and_b32 v55, 0xffff0000, v247\n"
"v_lshlrev_b32 v56, 16, v248\n"
"v_and_b32 v57, 0xffff0000, v248\n"
"v_lshlrev_b32 v58, 16, v249\n"
"v_and_b32 v59, 0xffff0000, v249\n"
"v_lshlrev_b32 v60, 16, v250\n"
"v_and_b32 v61, 0xffff0000, v250\n"
"v_lshlrev_b32 v62, 16, v251\n"
"v_and_b32 v63, 0xffff0000, v251\n"
"s_waitcnt vmcnt(17)\n"
"ds_read_b64_tr_b16 a[0:1], v234 offset:0\n"
"ds_read_b64_tr_b16 a[2:3], v234 offset:512\n"
"ds_read_b64_tr_b16 a[4:5], v234 offset:8\n"
"ds_read_b64_tr_b16 a[6:7], v234 offset:520\n"
"ds_read_b64_tr_b16 a[128:129], v235 offset:0\n"
"ds_read_b64_tr_b16 a[130:131], v235 offset:512\n"
"ds_read_b64_tr_b16 a[132:133], v235 offset:8\n"
"ds_read_b64_tr_b16 a[134:135], v235 offset:520\n"
"s_waitcnt vmcnt(8)\n"
"ds_read_b64_tr_b16 a[8:9], v234 offset:16384\n"
"ds_read_b64_tr_b16 a[10:11], v234 offset:16896\n"
"ds_read_b64_tr_b16 a[12:13], v234 offset:16392\n"
"ds_read_b64_tr_b16 a[14:15], v234 offset:16904\n"
"ds_read_b64_tr_b16 a[136:137], v235 offset:16384\n"
"ds_read_b64_tr_b16 a[138:139], v235 offset:16896\n"
"ds_read_b64_tr_b16 a[140:141], v235 offset:16392\n"
"ds_read_b64_tr_b16 a[142:143], v235 offset:16904\n"
"s_mov_b64 exec, 0\n"
"s_cmp_eq_u32 s64, 0\n"
"s_cbranch_scc1 Lst13_%=\n"
"s_cmp_eq_u32 s64, 1\n"
"s_cbranch_scc1 Lst14_%=\n"
"s_cmp_eq_u32 s64, 2\n"
"s_cbranch_scc1 Lst15_%=\n"
"s_branch Lst16_%=\n"
"Lback1_%=:\n"
"s_mov_b64 exec, -1\n"
"s_nop 1\n"
"buffer_load_dword v197, v193, s[60:63], s12 offen sc1\n"
"s_waitcnt vmcnt(5)\n"
"s_barrier\n"
"buffer_store_dword v199, v198, s[60:63], s55 offen sc1\n"
"s_waitcnt vmcnt(1)\n"
"v_cmp_ne_u32 vcc, s36, v197\n"
"s_nop 1\n"
"s_nop 0\n"
"s_and_b64 vcc, vcc, s[20:21]\n"
"s_nop 1\n"
"s_cbranch_vccnz Lp1as_15_%=\n"
"Lp1ar_16_%=:\n"
"s_barrier\n"
"s_add_u32 s30, s37, 0\n"
"buffer_load_dwordx4 v[128:131], v219, s[60:63], s30 offen sc1 nt\n"
"s_add_u32 m0, s67, 32768\n"
"s_mov_b32 s74, 344064\n"
"buffer_load_dwordx4 v209, s[44:47], s74 offen offset:0 lds\n"
"s_add_u32 s30, s37, 4096\n"
"buffer_load_dwordx4 v[132:135], v219, s[60:63], s30 offen sc1 nt\n"
"buffer_load_dwordx4 v210, s[44:47], s74 offen offset:1024 lds\n"
"s_add_u32 s30, s37, 8192\n"
"buffer_load_dwordx4 v[136:139], v219, s[60:63], s30 offen sc1 nt\n"
"buffer_load_dwordx4 v211, s[44:47], s74 offen offset:2048 lds\n"
"s_add_u32 s30, s37, 12288\n"
"buffer_load_dwordx4 v[140:143], v219, s[60:63], s30 offen sc1 nt\n"
"buffer_load_dwordx4 v212, s[44:47], s74 offen offset:3072 lds\n"
"s_add_u32 s30, s37, 16384\n"
"buffer_load_dwordx4 v[144:147], v219, s[60:63], s30 offen sc1 nt\n"
"s_add_u32 m0, s67, 49152\n"
"s_mov_b32 s74, 458752\n"
"buffer_load_dwordx4 v209, s[44:47], s74 offen offset:0 lds\n"
"s_add_u32 s30, s37, 20480\n"
"buffer_load_dwordx4 v[148:151], v219, s[60:63], s30 offen sc1 nt\n"
"buffer_load_dwordx4 v210, s[44:47], s74 offen offset:1024 lds\n"
"s_add_u32 s30, s37, 24576\n"
"buffer_load_dwordx4 v[152:155], v219, s[60:63], s30 offen sc1 nt\n"
"buffer_load_dwordx4 v211, s[44:47], s74 offen offset:2048 lds\n"
"s_add_u32 s30, s37, 28672\n"
"buffer_load_dwordx4 v[156:159], v219, s[60:63], s30 offen sc1 nt\n"
"buffer_load_dwordx4 v212, s[44:47], s74 offen offset:3072 lds\n"
"s_add_u32 m0, s67, 65536\n"
"s_mov_b32 s74, 573440\n"
"buffer_load_dwordx4 v209, s[44:47], s74 offen offset:0 lds\n"
"buffer_load_dwordx4 v210, s[44:47], s74 offen offset:1024 lds\n"
"buffer_load_dwordx4 v211, s[44:47], s74 offen offset:2048 lds\n"
"buffer_load_dwordx4 v212, s[44:47], s74 offen offset:3072 lds\n"
"s_add_u32 s12, s12, 131072\n"
"s_add_u32 m0, s67, 81920\n"
"s_mov_b32 s74, 688128\n"
"buffer_load_dwordx4 v209, s[44:47], s74 offen offset:0 lds\n"
"buffer_load_dwordx4 v210, s[44:47], s74 offen offset:1024 lds\n"
"buffer_load_dwordx4 v211, s[44:47], s74 offen offset:2048 lds\n"
"buffer_load_dwordx4 v212, s[44:47], s74 offen offset:3072 lds\n"
"buffer_load_dword v197, v193, s[60:63], s12 offen sc1\n"
"v_and_b32 v189, 7, v192\n"
"v_lshlrev_b32 v220, 4, v189\n"
"v_lshlrev_b32 v189, 2, v189\n"
"s_lshl_b32 s74, s64, 8\n"
"v_lshl_add_u32 v188, v192, 2, s74\n"
"v_bfe_u32 v196, v192, 3, 2\n"
"v_lshl_add_u32 v220, v196, 13, v220\n"
"v_lshrrev_b32 v196, 5, v192\n"
"v_lshl_add_u32 v220, v196, 7, v220\n"
"s_lshl_b32 s13, s78, 16\n"
"s_lshl_b32 s74, s64, 11\n"
"s_add_u32 s13, s13, s74\n"
"s_add_u32 s13, s13, 0x1600000\n"
"s_lshl_b32 s74, s79, 13\n"
"s_add_u32 s9, s13, s74\n"
"s_sub_u32 s8, s9, 0x200000\n"
"s_lshl_b32 s55, s78, 3\n"
"s_add_u32 s55, s55, s79\n"
"s_lshl_b32 s55, s55, 2\n"
"s_add_u32 s55, s55, s64\n"
"s_lshl_b32 s55, s55, 7\n"
"s_add_u32 s55, s55, 1310720\n"
"ds_read_b64_tr_b16 a[16:17], v216 offset:16384\n"
"ds_read_b64_tr_b16 a[18:19], v216 offset:16896\n"
"ds_read_b64_tr_b16 a[20:21], v216 offset:16392\n"
"ds_read_b64_tr_b16 a[22:23], v216 offset:16904\n"
"ds_read_b64_tr_b16 a[144:145], v217 offset:16384\n"
"ds_read_b64_tr_b16 a[146:147], v217 offset:16896\n"
"ds_read_b64_tr_b16 a[148:149], v217 offset:16392\n"
"ds_read_b64_tr_b16 a[150:151], v217 offset:16904\n"
"s_waitcnt vmcnt(17)\n"
"ds_read_b64_tr_b16 a[24:25], v216 offset:32768\n"
"ds_read_b64_tr_b16 a[26:27], v216 offset:33280\n"
"ds_read_b64_tr_b16 a[28:29], v216 offset:32776\n"
"ds_read_b64_tr_b16 a[30:31], v216 offset:33288\n"
"ds_read_b64_tr_b16 a[152:153], v217 offset:32768\n"
"ds_read_b64_tr_b16 a[154:155], v217 offset:33280\n"
"ds_read_b64_tr_b16 a[156:157], v217 offset:32776\n"
"ds_read_b64_tr_b16 a[158:159], v217 offset:33288\n"
"v_dot2_f32_bf16 v32, v128, s39, 0\n"
"v_dot2_f32_bf16 v33, v128, s38, 0\n"
"v_dot2_f32_bf16 v34, v129, s39, 0\n"
"v_dot2_f32_bf16 v35, v129, s38, 0\n"
"v_dot2_f32_bf16 v36, v130, s39, 0\n"
"v_dot2_f32_bf16 v37, v130, s38, 0\n"
"v_dot2_f32_bf16 v38, v131, s39, 0\n"
"v_dot2_f32_bf16 v39, v131, s38, 0\n"
"v_dot2_f32_bf16 v32, v132, s39, v32\n"
"v_dot2_f32_bf16 v33, v132, s38, v33\n"
"v_dot2_f32_bf16 v34, v133, s39, v34\n"
"v_dot2_f32_bf16 v35, v133, s38, v35\n"
"v_dot2_f32_bf16 v36, v134, s39, v36\n"
"v_dot2_f32_bf16 v37, v134, s38, v37\n"
"v_dot2_f32_bf16 v38, v135, s39, v38\n"
"v_dot2_f32_bf16 v39, v135, s38, v39\n"
"v_dot2_f32_bf16 v32, v136, s39, v32\n"
"v_dot2_f32_bf16 v33, v136, s38, v33\n"
"v_dot2_f32_bf16 v34, v137, s39, v34\n"
"v_dot2_f32_bf16 v35, v137, s38, v35\n"
"v_dot2_f32_bf16 v36, v138, s39, v36\n"
"v_dot2_f32_bf16 v37, v138, s38, v37\n"
"v_dot2_f32_bf16 v38, v139, s39, v38\n"
"v_dot2_f32_bf16 v39, v139, s38, v39\n"
"v_dot2_f32_bf16 v32, v140, s39, v32\n"
"v_dot2_f32_bf16 v33, v140, s38, v33\n"
"v_dot2_f32_bf16 v34, v141, s39, v34\n"
"v_dot2_f32_bf16 v35, v141, s38, v35\n"
"v_dot2_f32_bf16 v36, v142, s39, v36\n"
"v_dot2_f32_bf16 v37, v142, s38, v37\n"
"v_dot2_f32_bf16 v38, v143, s39, v38\n"
"v_dot2_f32_bf16 v39, v143, s38, v39\n"
"s_waitcnt vmcnt(16)\n"
"v_dot2_f32_bf16 v32, v144, s39, v32\n"
"v_dot2_f32_bf16 v33, v144, s38, v33\n"
"v_dot2_f32_bf16 v34, v145, s39, v34\n"
"v_dot2_f32_bf16 v35, v145, s38, v35\n"
"v_dot2_f32_bf16 v36, v146, s39, v36\n"
"v_dot2_f32_bf16 v37, v146, s38, v37\n"
"v_dot2_f32_bf16 v38, v147, s39, v38\n"
"v_dot2_f32_bf16 v39, v147, s38, v39\n"
"s_waitcnt vmcnt(14)\n"
"v_dot2_f32_bf16 v32, v148, s39, v32\n"
"v_dot2_f32_bf16 v33, v148, s38, v33\n"
"v_dot2_f32_bf16 v34, v149, s39, v34\n"
"v_dot2_f32_bf16 v35, v149, s38, v35\n"
"v_dot2_f32_bf16 v36, v150, s39, v36\n"
"v_dot2_f32_bf16 v37, v150, s38, v37\n"
"v_dot2_f32_bf16 v38, v151, s39, v38\n"
"v_dot2_f32_bf16 v39, v151, s38, v39\n"
"s_waitcnt vmcnt(12)\n"
"v_dot2_f32_bf16 v32, v152, s39, v32\n"
"v_dot2_f32_bf16 v33, v152, s38, v33\n"
"v_dot2_f32_bf16 v34, v153, s39, v34\n"
"v_dot2_f32_bf16 v35, v153, s38, v35\n"
"v_dot2_f32_bf16 v36, v154, s39, v36\n"
"v_dot2_f32_bf16 v37, v154, s38, v37\n"
"v_dot2_f32_bf16 v38, v155, s39, v38\n"
"v_dot2_f32_bf16 v39, v155, s38, v39\n"
"s_waitcnt vmcnt(10)\n"
"v_dot2_f32_bf16 v32, v156, s39, v32\n"
"v_dot2_f32_bf16 v33, v156, s38, v33\n"
"v_dot2_f32_bf16 v34, v157, s39, v34\n"
"v_dot2_f32_bf16 v35, v157, s38, v35\n"
"v_dot2_f32_bf16 v36, v158, s39, v36\n"
"v_dot2_f32_bf16 v37, v158, s38, v37\n"
"v_dot2_f32_bf16 v38, v159, s39, v38\n"
"v_dot2_f32_bf16 v39, v159, s38, v39\n"
"v_pk_mul_f32 v[160:161], v[32:33], s[70:71] op_sel_hi:[1,0]\n"
"v_pk_mul_f32 v[162:163], v[34:35], s[70:71] op_sel_hi:[1,0]\n"
"v_pk_mul_f32 v[164:165], v[36:37], s[70:71] op_sel_hi:[1,0]\n"
"v_pk_mul_f32 v[166:167], v[38:39], s[70:71] op_sel_hi:[1,0]\n"
"v_pk_fma_f32 v[32:33], v[32:33], s[70:71], v[160:161] op_sel:[0,1,0] op_sel_hi:[1,1,1]\n"
"v_pk_fma_f32 v[34:35], v[34:35], s[70:71], v[162:163] op_sel:[0,1,0] op_sel_hi:[1,1,1]\n"
"Lst13_%=:\n"
"s_cbranch_execz Lback1_%=\n"
"v_pk_fma_f32 v[36:37], v[36:37], s[70:71], v[164:165] op_sel:[0,1,0] op_sel_hi:[1,1,1]\n"
"v_pk_fma_f32 v[38:39], v[38:39], s[70:71], v[166:167] op_sel:[0,1,0] op_sel_hi:[1,1,1]\n"
"v_pk_mul_f32 v[32:33], v[32:33], v[80:81]\n"
"v_pk_mul_f32 v[34:35], v[34:35], v[82:83]\n"
"v_pk_mul_f32 v[36:37], v[36:37], v[84:85]\n"
"v_pk_mul_f32 v[38:39], v[38:39], v[86:87]\n"
"v_pk_mul_f32 v[160:161], v[32:33], s[70:71] op_sel_hi:[1,0]\n"
"v_pk_mul_f32 v[162:163], v[34:35], s[70:71] op_sel_hi:[1,0]\n"
"v_pk_mul_f32 v[164:165], v[36:37], s[70:71] op_sel_hi:[1,0]\n"
"v_pk_mul_f32 v[166:167], v[38:39], s[70:71] op_sel_hi:[1,0]\n"
"v_pk_fma_f32 v[32:33], v[32:33], s[70:71], v[160:161] op_sel:[0,1,0] op_sel_hi:[1,1,1]\n"
"v_pk_fma_f32 v[34:35], v[34:35], s[70:71], v[162:163] op_sel:[0,1,0] op_sel_hi:[1,1,1]\n"
"v_pk_fma_f32 v[36:37], v[36:37], s[70:71], v[164:165] op_sel:[0,1,0] op_sel_hi:[1,1,1]\n"
"v_pk_fma_f32 v[38:39], v[38:39], s[70:71], v[166:167] op_sel:[0,1,0] op_sel_hi:[1,1,1]\n"
"v_pk_add_f32 v[32:33], v[32:33], v[48:49]\n"
"v_pk_add_f32 v[34:35], v[34:35], v[50:51]\n"
"v_pk_add_f32 v[36:37], v[36:37], v[52:53]\n"
"v_pk_add_f32 v[38:39], v[38:39], v[54:55]\n"
"v_cvt_pk_bf16_f32 v244, v32, v33\n"
"v_cvt_pk_bf16_f32 v245, v34, v35\n"
"v_cvt_pk_bf16_f32 v246, v36, v37\n"
"v_cvt_pk_bf16_f32 v247, v38, v39\n"
"v_dot2_f32_bf16 v184, v244, s72, 0\n"
"v_dot2_f32_bf16 v185, v244, v244, 0\n"
"v_dot2_f32_bf16 v184, v245, s72, v184\n"
"v_dot2_f32_bf16 v185, v245, v245, v185\n"
"v_dot2_f32_bf16 v184, v246, s72, v184\n"
"v_dot2_f32_bf16 v185, v246, v246, v185\n"
"v_dot2_f32_bf16 v184, v247, s72, v184\n"
"v_dot2_f32_bf16 v185, v247, v247, v185\n"
"v_lshlrev_b32 v32, 16, v244\n"
"v_and_b32 v33, 0xffff0000, v244\n"
"v_lshlrev_b32 v34, 16, v245\n"
"v_and_b32 v35, 0xffff0000, v245\n"
"v_lshlrev_b32 v36, 16, v246\n"
"v_and_b32 v37, 0xffff0000, v246\n"
"v_lshlrev_b32 v38, 16, v247\n"
"v_and_b32 v39, 0xffff0000, v247\n"
"s_waitcnt vmcnt(0)\n"
"v_cmp_ne_u32 vcc, s36, v197\n"
"s_nop 1\n"
"s_nop 0\n"
"s_and_b64 vcc, vcc, s[20:21]\n"
"s_nop 1\n"
"s_cbranch_vccnz Lp1bs_17_%=\n"
"Lp1br_18_%=:\n"
"s_barrier\n"
"s_add_u32 s55, s55, s17\n"
"s_add_u32 s30, s37, 131072\n"
"buffer_load_dwordx4 v[0:3], v219, s[60:63], s30 offen sc1 nt\n"
"s_add_u32 m0, s67, 98304\n"
"s_mov_b32 s74, 802816\n"
"buffer_load_dwordx4 v209, s[44:47], s74 offen offset:0 lds\n"
"s_add_u32 s30, s37, 135168\n"
"buffer_load_dwordx4 v[4:7], v219, s[60:63], s30 offen sc1 nt\n"
"buffer_load_dwordx4 v210, s[44:47], s74 offen offset:1024 lds\n"
"s_add_u32 s30, s37, 139264\n"
"buffer_load_dwordx4 v[8:11], v219, s[60:63], s30 offen sc1 nt\n"
"buffer_load_dwordx4 v211, s[44:47], s74 offen offset:2048 lds\n"
"s_add_u32 s30, s37, 143360\n"
"buffer_load_dwordx4 v[12:15], v219, s[60:63], s30 offen sc1 nt\n"
"buffer_load_dwordx4 v212, s[44:47], s74 offen offset:3072 lds\n"
"s_add_u32 s30, s37, 147456\n"
"buffer_load_dwordx4 v[16:19], v219, s[60:63], s30 offen sc1 nt\n"
"s_add_u32 m0, s67, 114688\n"
"s_mov_b32 s74, 917504\n"
"buffer_load_dwordx4 v209, s[44:47], s74 offen offset:0 lds\n"
"s_add_u32 s30, s37, 151552\n"
"buffer_load_dwordx4 v[20:23], v219, s[60:63], s30 offen sc1 nt\n"
"buffer_load_dwordx4 v210, s[44:47], s74 offen offset:1024 lds\n"
"s_add_u32 s30, s37, 155648\n"
"buffer_load_dwordx4 v[24:27], v219, s[60:63], s30 offen sc1 nt\n"
"buffer_load_dwordx4 v211, s[44:47], s74 offen offset:2048 lds\n"
"s_add_u32 s30, s37, 159744\n"
"buffer_load_dwordx4 v[28:31], v219, s[60:63], s30 offen sc1 nt\n"
"buffer_load_dwordx4 v212, s[44:47], s74 offen offset:3072 lds\n"
"s_add_u32 s12, s12, 131072\n"
"ds_read_b64_tr_b16 a[32:33], v216 offset:49152\n"
"ds_read_b64_tr_b16 a[34:35], v216 offset:49664\n"
"ds_read_b64_tr_b16 a[36:37], v216 offset:49160\n"
"ds_read_b64_tr_b16 a[38:39], v216 offset:49672\n"
"ds_read_b64_tr_b16 a[160:161], v217 offset:49152\n"
"ds_read_b64_tr_b16 a[162:163], v217 offset:49664\n"
"ds_read_b64_tr_b16 a[164:165], v217 offset:49160\n"
"ds_read_b64_tr_b16 a[166:167], v217 offset:49672\n"
"ds_read_b64_tr_b16 a[40:41], v232 offset:0\n"
"ds_read_b64_tr_b16 a[42:43], v232 offset:512\n"
"ds_read_b64_tr_b16 a[44:45], v232 offset:8\n"
"ds_read_b64_tr_b16 a[46:47], v232 offset:520\n"
"ds_read_b64_tr_b16 a[168:169], v233 offset:0\n"
"ds_read_b64_tr_b16 a[170:171], v233 offset:512\n"
"ds_read_b64_tr_b16 a[172:173], v233 offset:8\n"
"ds_read_b64_tr_b16 a[174:175], v233 offset:520\n"
"ds_read_b64_tr_b16 a[48:49], v232 offset:16384\n"
"ds_read_b64_tr_b16 a[50:51], v232 offset:16896\n"
"ds_read_b64_tr_b16 a[52:53], v232 offset:16392\n"
"ds_read_b64_tr_b16 a[54:55], v232 offset:16904\n"
"ds_read_b64_tr_b16 a[176:177], v233 offset:16384\n"
"ds_read_b64_tr_b16 a[178:179], v233 offset:16896\n"
"ds_read_b64_tr_b16 a[180:181], v233 offset:16392\n"
"ds_read_b64_tr_b16 a[182:183], v233 offset:16904\n"
"s_waitcnt vmcnt(15)\n"
"v_dot2_f32_bf16 v40, v0, s39, 0\n"
"v_dot2_f32_bf16 v41, v0, s38, 0\n"
"v_dot2_f32_bf16 v42, v1, s39, 0\n"
"v_dot2_f32_bf16 v43, v1, s38, 0\n"
"v_dot2_f32_bf16 v44, v2, s39, 0\n"
"v_dot2_f32_bf16 v45, v2, s38, 0\n"
"v_dot2_f32_bf16 v46, v3, s39, 0\n"
"v_dot2_f32_bf16 v47, v3, s38, 0\n"
"s_waitcnt vmcnt(13)\n"
"v_dot2_f32_bf16 v40, v4, s39, v40\n"
"v_dot2_f32_bf16 v41, v4, s38, v41\n"
"v_dot2_f32_bf16 v42, v5, s39, v42\n"
"v_dot2_f32_bf16 v43, v5, s38, v43\n"
"v_dot2_f32_bf16 v44, v6, s39, v44\n"
"v_dot2_f32_bf16 v45, v6, s38, v45\n"
"v_dot2_f32_bf16 v46, v7, s39, v46\n"
"v_dot2_f32_bf16 v47, v7, s38, v47\n"
"s_waitcnt vmcnt(11)\n"
"v_dot2_f32_bf16 v40, v8, s39, v40\n"
"v_dot2_f32_bf16 v41, v8, s38, v41\n"
"v_dot2_f32_bf16 v42, v9, s39, v42\n"
"v_dot2_f32_bf16 v43, v9, s38, v43\n"
"v_dot2_f32_bf16 v44, v10, s39, v44\n"
"v_dot2_f32_bf16 v45, v10, s38, v45\n"
"v_dot2_f32_bf16 v46, v11, s39, v46\n"
"v_dot2_f32_bf16 v47, v11, s38, v47\n"
"s_waitcnt vmcnt(9)\n"
"v_dot2_f32_bf16 v40, v12, s39, v40\n"
"v_dot2_f32_bf16 v41, v12, s38, v41\n"
"v_dot2_f32_bf16 v42, v13, s39, v42\n"
"v_dot2_f32_bf16 v43, v13, s38, v43\n"
"v_dot2_f32_bf16 v44, v14, s39, v44\n"
"v_dot2_f32_bf16 v45, v14, s38, v45\n"
"v_dot2_f32_bf16 v46, v15, s39, v46\n"
"v_dot2_f32_bf16 v47, v15, s38, v47\n"
"s_waitcnt vmcnt(7)\n"
"v_dot2_f32_bf16 v40, v16, s39, v40\n"
"v_dot2_f32_bf16 v41, v16, s38, v41\n"
"v_dot2_f32_bf16 v42, v17, s39, v42\n"
"v_dot2_f32_bf16 v43, v17, s38, v43\n"
"v_dot2_f32_bf16 v44, v18, s39, v44\n"
"v_dot2_f32_bf16 v45, v18, s38, v45\n"
"v_dot2_f32_bf16 v46, v19, s39, v46\n"
"v_dot2_f32_bf16 v47, v19, s38, v47\n"
"s_waitcnt vmcnt(5)\n"
"v_dot2_f32_bf16 v40, v20, s39, v40\n"
"v_dot2_f32_bf16 v41, v20, s38, v41\n"
"v_dot2_f32_bf16 v42, v21, s39, v42\n"
"v_dot2_f32_bf16 v43, v21, s38, v43\n"
"v_dot2_f32_bf16 v44, v22, s39, v44\n"
"v_dot2_f32_bf16 v45, v22, s38, v45\n"
"v_dot2_f32_bf16 v46, v23, s39, v46\n"
"v_dot2_f32_bf16 v47, v23, s38, v47\n"
"s_waitcnt vmcnt(3)\n"
"v_dot2_f32_bf16 v40, v24, s39, v40\n"
"v_dot2_f32_bf16 v41, v24, s38, v41\n"
"v_dot2_f32_bf16 v42, v25, s39, v42\n"
"v_dot2_f32_bf16 v43, v25, s38, v43\n"
"v_dot2_f32_bf16 v44, v26, s39, v44\n"
"v_dot2_f32_bf16 v45, v26, s38, v45\n"
"v_dot2_f32_bf16 v46, v27, s39, v46\n"
"v_dot2_f32_bf16 v47, v27, s38, v47\n"
"s_waitcnt vmcnt(1)\n"
"v_dot2_f32_bf16 v40, v28, s39, v40\n"
"v_dot2_f32_bf16 v41, v28, s38, v41\n"
"v_dot2_f32_bf16 v42, v29, s39, v42\n"
"v_dot2_f32_bf16 v43, v29, s38, v43\n"
"v_dot2_f32_bf16 v44, v30, s39, v44\n"
"v_dot2_f32_bf16 v45, v30, s38, v45\n"
"v_dot2_f32_bf16 v46, v31, s39, v46\n"
"v_dot2_f32_bf16 v47, v31, s38, v47\n"
"v_pk_mul_f32 v[160:161], v[40:41], s[70:71] op_sel_hi:[1,0]\n"
"v_pk_mul_f32 v[162:163], v[42:43], s[70:71] op_sel_hi:[1,0]\n"
"v_pk_mul_f32 v[164:165], v[44:45], s[70:71] op_sel_hi:[1,0]\n"
"v_pk_mul_f32 v[166:167], v[46:47], s[70:71] op_sel_hi:[1,0]\n"
"v_pk_fma_f32 v[40:41], v[40:41], s[70:71], v[160:161] op_sel:[0,1,0] op_sel_hi:[1,1,1]\n"
"v_pk_fma_f32 v[42:43], v[42:43], s[70:71], v[162:163] op_sel:[0,1,0] op_sel_hi:[1,1,1]\n"
"v_pk_fma_f32 v[44:45], v[44:45], s[70:71], v[164:165] op_sel:[0,1,0] op_sel_hi:[1,1,1]\n"
"v_pk_fma_f32 v[46:47], v[46:47], s[70:71], v[166:167] op_sel:[0,1,0] op_sel_hi:[1,1,1]\n"
"v_pk_mul_f32 v[40:41], v[40:41], v[88:89]\n"
"v_pk_mul_f32 v[42:43], v[42:43], v[90:91]\n"
"v_pk_mul_f32 v[44:45], v[44:45], v[92:93]\n"
"v_pk_mul_f32 v[46:47], v[46:47], v[94:95]\n"
"v_pk_mul_f32 v[160:161], v[40:41], s[70:71] op_sel_hi:[1,0]\n"
"v_pk_mul_f32 v[162:163], v[42:43], s[70:71] op_sel_hi:[1,0]\n"
"v_pk_mul_f32 v[164:165], v[44:45], s[70:71] op_sel_hi:[1,0]\n"
"v_pk_mul_f32 v[166:167], v[46:47], s[70:71] op_sel_hi:[1,0]\n"
"v_pk_fma_f32 v[40:41], v[40:41], s[70:71], v[160:161] op_sel:[0,1,0] op_sel_hi:[1,1,1]\n"
"v_pk_fma_f32 v[42:43], v[42:43], s[70:71], v[162:163] op_sel:[0,1,0] op_sel_hi:[1,1,1]\n"
"v_pk_fma_f32 v[44:45], v[44:45], s[70:71], v[164:165] op_sel:[0,1,0] op_sel_hi:[1,1,1]\n"
"v_pk_fma_f32 v[46:47], v[46:47], s[70:71], v[166:167] op_sel:[0,1,0] op_sel_hi:[1,1,1]\n"
"v_pk_add_f32 v[40:41], v[40:41], v[56:57]\n"
"v_pk_add_f32 v[42:43], v[42:43], v[58:59]\n"
"v_pk_add_f32 v[44:45], v[44:45], v[60:61]\n"
"v_pk_add_f32 v[46:47], v[46:47], v[62:63]\n"
"v_cvt_pk_bf16_f32 v248, v40, v41\n"
"v_cvt_pk_bf16_f32 v249, v42, v43\n"
"v_cvt_pk_bf16_f32 v250, v44, v45\n"
"v_cvt_pk_bf16_f32 v251, v46, v47\n"
"v_dot2_f32_bf16 v184, v248, s72, v184\n"
"v_dot2_f32_bf16 v185, v248, v248, v185\n"
"v_dot2_f32_bf16 v184, v249, s72, v184\n"
"v_dot2_f32_bf16 v185, v249, v249, v185\n"
"v_dot2_f32_bf16 v184, v250, s72, v184\n"
"v_dot2_f32_bf16 v185, v250, v250, v185\n"
"v_dot2_f32_bf16 v184, v251, s72, v184\n"
"v_dot2_f32_bf16 v185, v251, v251, v185\n"
"v_lshlrev_b32 v40, 16, v248\n"
"v_and_b32 v41, 0xffff0000, v248\n"
"v_lshlrev_b32 v42, 16, v249\n"
"v_permlane16_swap_b32 v184, v185\n"
"v_add_f32 v184, v184, v185\n"
"v_and_b32 v43, 0xffff0000, v249\n"
"v_lshlrev_b32 v44, 16, v250\n"
"v_add_f32_dpp v184, v184, v184 row_ror:8 row_mask:0xf bank_mask:0xf\n"
"v_mov_b32 v185, v184\n"
"v_and_b32 v45, 0xffff0000, v250\n"
"v_lshlrev_b32 v46, 16, v251\n"
"v_permlane32_swap_b32 v184, v185\n"
"v_add_f32 v184, v184, v185\n"
"ds_write_b32 v188, v184\n"
"v_and_b32 v47, 0xffff0000, v251\n"
"s_waitcnt lgkmcnt(0)\n"
"s_barrier\n"
"ds_read2_b32 v[0:1], v189 offset0:0 offset1:16\n"
"ds_read2_b32 v[2:3], v189 offset0:64 offset1:80\n"
"ds_read2_b32 v[4:5], v189 offset0:128 offset1:144\n"
"ds_read2_b32 v[6:7], v189 offset0:192 offset1:208\n"
"s_add_u32 m0, s67, 131072\n"
"s_mov_b32 s74, 1032192\n"
"buffer_load_dwordx4 v209, s[44:47], s74 offen offset:0 lds\n"
"buffer_load_dwordx4 v210, s[44:47], s74 offen offset:1024 lds\n"
"buffer_load_dwordx4 v211, s[44:47], s74 offen offset:2048 lds\n"
"buffer_load_dwordx4 v212, s[44:47], s74 offen offset:3072 lds\n"
"s_add_u32 m0, s67, 147456\n"
"s_mov_b32 s74, 1146880\n"
"buffer_load_dwordx4 v209, s[44:47], s74 offen offset:0 lds\n"
"buffer_load_dwordx4 v210, s[44:47], s74 offen offset:1024 lds\n"
"buffer_load_dwordx4 v211, s[44:47], s74 offen offset:2048 lds\n"
"buffer_load_dwordx4 v212, s[44:47], s74 offen offset:3072 lds\n"
"s_waitcnt lgkmcnt(2)\n"
"v_pk_add_f32 v[0:1], v[0:1], v[2:3]\n"
"s_waitcnt lgkmcnt(0)\n"
"v_pk_add_f32 v[4:5], v[4:5], v[6:7]\n"
"v_pk_add_f32 v[0:1], v[0:1], v[4:5]\n"
"v_mul_f32 v0, 0x3b000000, v0\n"
"v_mul_f32 v1, 0x3b000000, v1\n"
"v_fma_f32 v1, -v0, v0, v1\n"
"v_max_f32 v1, 0, v1\n"
"v_cvt_pk_bf16_f32 v0, 0, v0\n"
"v_cvt_pk_bf16_f32 v1, 0, v1\n"
"v_add_f32 v1, 0x3727c5ac, v1\n"
"v_cvt_pk_bf16_f32 v1, 0, v1\n"
"v_sqrt_f32 v1, v1\n"
"s_nop 0\n"
"v_cvt_pk_bf16_f32 v1, 0, v1\n"
"v_rcp_f32 v1, v1\n"
"s_nop 0\n"
"v_pk_add_f32 v[32:33], v[32:33], v[0:1] op_sel_hi:[1,0] neg_lo:[0,1] neg_hi:[0,1]\n"
"v_pk_add_f32 v[34:35], v[34:35], v[0:1] op_sel_hi:[1,0] neg_lo:[0,1] neg_hi:[0,1]\n"
"v_pk_add_f32 v[36:37], v[36:37], v[0:1] op_sel_hi:[1,0] neg_lo:[0,1] neg_hi:[0,1]\n"
"v_pk_add_f32 v[38:39], v[38:39], v[0:1] op_sel_hi:[1,0] neg_lo:[0,1] neg_hi:[0,1]\n"
"v_pk_add_f32 v[40:41], v[40:41], v[0:1] op_sel_hi:[1,0] neg_lo:[0,1] neg_hi:[0,1]\n"
"v_pk_add_f32 v[42:43], v[42:43], v[0:1] op_sel_hi:[1,0] neg_lo:[0,1] neg_hi:[0,1]\n"
"v_pk_add_f32 v[44:45], v[44:45], v[0:1] op_sel_hi:[1,0] neg_lo:[0,1] neg_hi:[0,1]\n"
"v_pk_add_f32 v[46:47], v[46:47], v[0:1] op_sel_hi:[1,0] neg_lo:[0,1] neg_hi:[0,1]\n"
"v_pk_mul_f32 v[160:161], v[32:33], s[70:71] op_sel_hi:[1,0]\n"
"v_pk_mul_f32 v[162:163], v[34:35], s[70:71] op_sel_hi:[1,0]\n"
"v_pk_mul_f32 v[164:165], v[36:37], s[70:71] op_sel_hi:[1,0]\n"
"v_pk_mul_f32 v[166:167], v[38:39], s[70:71] op_sel_hi:[1,0]\n"
"v_pk_mul_f32 v[168:169], v[40:41], s[70:71] op_sel_hi:[1,0]\n"
"v_pk_mul_f32 v[170:171], v[42:43], s[70:71] op_sel_hi:[1,0]\n"
"v_pk_mul_f32 v[172:173], v[44:45], s[70:71] op_sel_hi:[1,0]\n"
"v_pk_mul_f32 v[174:175], v[46:47], s[70:71] op_sel_hi:[1,0]\n"
"v_pk_fma_f32 v[32:33], v[32:33], s[70:71], v[160:161] op_sel:[0,1,0] op_sel_hi:[1,1,1]\n"
"v_pk_fma_f32 v[34:35], v[34:35], s[70:71], v[162:163] op_sel:[0,1,0] op_sel_hi:[1,1,1]\n"
"v_pk_fma_f32 v[36:37], v[36:37], s[70:71], v[164:165] op_sel:[0,1,0] op_sel_hi:[1,1,1]\n"
"v_pk_fma_f32 v[38:39], v[38:39], s[70:71], v[166:167] op_sel:[0,1,0] op_sel_hi:[1,1,1]\n"
"v_pk_fma_f32 v[40:41], v[40:41], s[70:71], v[168:169] op_sel:[0,1,0] op_sel_hi:[1,1,1]\n"
"v_pk_fma_f32 v[42:43], v[42:43], s[70:71], v[170:171] op_sel:[0,1,0] op_sel_hi:[1,1,1]\n"
"v_pk_fma_f32 v[44:45], v[44:45], s[70:71], v[172:173] op_sel:[0,1,0] op_sel_hi:[1,1,1]\n"
"v_pk_fma_f32 v[46:47], v[46:47], s[70:71], v[174:175] op_sel:[0,1,0] op_sel_hi:[1,1,1]\n"
"v_pk_mul_f32 v[32:33], v[32:33], v[0:1] op_sel:[0,1] op_sel_hi:[1,1]\n"
"v_pk_mul_f32 v[34:35], v[34:35], v[0:1] op_sel:[0,1] op_sel_hi:[1,1]\n"
"v_pk_mul_f32 v[36:37], v[36:37], v[0:1] op_sel:[0,1] op_sel_hi:[1,1]\n"
"v_pk_mul_f32 v[38:39], v[38:39], v[0:1] op_sel:[0,1] op_sel_hi:[1,1]\n"
"v_pk_mul_f32 v[40:41], v[40:41], v[0:1] op_sel:[0,1] op_sel_hi:[1,1]\n"
"v_pk_mul_f32 v[42:43], v[42:43], v[0:1] op_sel:[0,1] op_sel_hi:[1,1]\n"
"v_pk_mul_f32 v[44:45], v[44:45], v[0:1] op_sel:[0,1] op_sel_hi:[1,1]\n"
"v_pk_mul_f32 v[46:47], v[46:47], v[0:1] op_sel:[0,1] op_sel_hi:[1,1]\n"
"v_pk_mul_f32 v[160:161], v[32:33], s[70:71] op_sel_hi:[1,0]\n"
"v_pk_mul_f32 v[162:163], v[34:35], s[70:71] op_sel_hi:[1,0]\n"
"v_pk_mul_f32 v[164:165], v[36:37], s[70:71] op_sel_hi:[1,0]\n"
"v_pk_mul_f32 v[166:167], v[38:39], s[70:71] op_sel_hi:[1,0]\n"
"v_pk_mul_f32 v[168:169], v[40:41], s[70:71] op_sel_hi:[1,0]\n"
"v_pk_mul_f32 v[170:171], v[42:43], s[70:71] op_sel_hi:[1,0]\n"
"v_pk_mul_f32 v[172:173], v[44:45], s[70:71] op_sel_hi:[1,0]\n"
"v_pk_mul_f32 v[174:175], v[46:47], s[70:71] op_sel_hi:[1,0]\n"
"v_pk_fma_f32 v[32:33], v[32:33], s[70:71], v[160:161] op_sel:[0,1,0] op_sel_hi:[1,1,1]\n"
"v_pk_fma_f32 v[34:35], v[34:35], s[70:71], v[162:163] op_sel:[0,1,0] op_sel_hi:[1,1,1]\n"
"v_pk_fma_f32 v[36:37], v[36:37], s[70:71], v[164:165] op_sel:[0,1,0] op_sel_hi:[1,1,1]\n"
"v_pk_fma_f32 v[38:39], v[38:39], s[70:71], v[166:167] op_sel:[0,1,0] op_sel_hi:[1,1,1]\n"
"v_pk_fma_f32 v[40:41], v[40:41], s[70:71], v[168:169] op_sel:[0,1,0] op_sel_hi:[1,1,1]\n"
"v_pk_fma_f32 v[42:43], v[42:43], s[70:71], v[170:171] op_sel:[0,1,0] op_sel_hi:[1,1,1]\n"
"v_pk_fma_f32 v[44:45], v[44:45], s[70:71], v[172:173] op_sel:[0,1,0] op_sel_hi:[1,1,1]\n"
"v_pk_fma_f32 v[46:47], v[46:47], s[70:71], v[174:175] op_sel:[0,1,0] op_sel_hi:[1,1,1]\n"
"v_pk_mul_f32 v[32:33], v[32:33], v[112:113]\n"
"v_pk_mul_f32 v[40:41], v[40:41], v[120:121]\n"
"v_pk_mul_f32 v[34:35], v[34:35], v[114:115]\n"
"v_pk_mul_f32 v[42:43], v[42:43], v[122:123]\n"
"v_pk_mul_f32 v[36:37], v[36:37], v[116:117]\n"
"v_pk_mul_f32 v[44:45], v[44:45], v[124:125]\n"
"v_pk_mul_f32 v[38:39], v[38:39], v[118:119]\n"
"v_pk_mul_f32 v[46:47], v[46:47], v[126:127]\n"
"v_pk_mul_f32 v[160:161], v[32:33], s[70:71] op_sel_hi:[1,0]\n"
"v_pk_mul_f32 v[168:169], v[40:41], s[70:71] op_sel_hi:[1,0]\n"
"v_pk_mul_f32 v[162:163], v[34:35], s[70:71] op_sel_hi:[1,0]\n"
"v_pk_mul_f32 v[170:171], v[42:43], s[70:71] op_sel_hi:[1,0]\n"
"v_pk_mul_f32 v[164:165], v[36:37], s[70:71] op_sel_hi:[1,0]\n"
"v_pk_mul_f32 v[172:173], v[44:45], s[70:71] op_sel_hi:[1,0]\n"
"v_pk_mul_f32 v[166:167], v[38:39], s[70:71] op_sel_hi:[1,0]\n"
"v_pk_mul_f32 v[174:175], v[46:47], s[70:71] op_sel_hi:[1,0]\n"
"v_pk_fma_f32 v[32:33], v[32:33], s[70:71], v[160:161] op_sel:[0,1,0] op_sel_hi:[1,1,1]\n"
"v_pk_fma_f32 v[40:41], v[40:41], s[70:71], v[168:169] op_sel:[0,1,0] op_sel_hi:[1,1,1]\n"
"v_pk_fma_f32 v[34:35], v[34:35], s[70:71], v[162:163] op_sel:[0,1,0] op_sel_hi:[1,1,1]\n"
"v_pk_fma_f32 v[42:43], v[42:43], s[70:71], v[170:171] op_sel:[0,1,0] op_sel_hi:[1,1,1]\n"
"v_pk_fma_f32 v[36:37], v[36:37], s[70:71], v[164:165] op_sel:[0,1,0] op_sel_hi:[1,1,1]\n"
"v_pk_fma_f32 v[44:45], v[44:45], s[70:71], v[172:173] op_sel:[0,1,0] op_sel_hi:[1,1,1]\n"
"v_pk_fma_f32 v[38:39], v[38:39], s[70:71], v[166:167] op_sel:[0,1,0] op_sel_hi:[1,1,1]\n"
"v_pk_fma_f32 v[46:47], v[46:47], s[70:71], v[174:175] op_sel:[0,1,0] op_sel_hi:[1,1,1]\n"
"v_pk_add_f32 v[32:33], v[32:33], v[96:97]\n"
"v_pk_add_f32 v[40:41], v[40:41], v[104:105]\n"
"v_pk_add_f32 v[34:35], v[34:35], v[98:99]\n"
"v_pk_add_f32 v[42:43], v[42:43], v[106:107]\n"
"v_pk_add_f32 v[36:37], v[36:37], v[100:101]\n"
"v_pk_add_f32 v[44:45], v[44:45], v[108:109]\n"
"v_pk_add_f32 v[38:39], v[38:39], v[102:103]\n"
"v_pk_add_f32 v[46:47], v[46:47], v[110:111]\n"
"v_cvt_pk_bf16_f32 v32, v32, v33\n"
"v_cvt_pk_bf16_f32 v40, v40, v41\n"
"v_cvt_pk_bf16_f32 v33, v34, v35\n"
"v_cvt_pk_bf16_f32 v41, v42, v43\n"
"v_cvt_pk_bf16_f32 v34, v36, v37\n"
"v_cvt_pk_bf16_f32 v42, v44, v45\n"
"v_cvt_pk_bf16_f32 v35, v38, v39\n"
"v_cvt_pk_bf16_f32 v43, v46, v47\n"
"buffer_store_dwordx4 v[32:35], v194, s[60:63], s9 offen sc1\n"
"buffer_store_dwordx4 v[40:43], v194, s[60:63], s9 offen offset:1024 sc1\n"
"s_mov_b32 s56, s22\n"
"s_and_b32 s57, s23, 0xffff\n"
"s_mov_b32 s58, 0xe00\n"
"s_mov_b32 s59, 0x20000\n"
"v_lshrrev_b32 v195, 5, v192\n"
"v_lshlrev_b32 v195, 4, v195\n"
"s_lshl_b32 s30, s33, 7\n"
"v_bfe_u32 v196, v192, 4, 1\n"
"v_lshl_add_u32 v195, v196, 5, v195\n"
"v_and_b32 v221, 15, v192\n"
"v_lshlrev_b32 v221, 4, v221\n"
"v_lshl_add_u32 v221, v195, 5, v221\n"
"buffer_load_dwordx4 v[80:83], v195, s[56:59], s30 offen offset:0\n"
"buffer_load_dwordx4 v[84:87], v195, s[56:59], s30 offen offset:64\n"
"ds_read_b64_tr_b16 a[56:57], v232 offset:32768\n"
"ds_read_b64_tr_b16 a[58:59], v232 offset:33280\n"
"ds_read_b64_tr_b16 a[60:61], v232 offset:32776\n"
"ds_read_b64_tr_b16 a[62:63], v232 offset:33288\n"
"ds_read_b64_tr_b16 a[184:185], v233 offset:32768\n"
"ds_read_b64_tr_b16 a[186:187], v233 offset:33280\n"
"ds_read_b64_tr_b16 a[188:189], v233 offset:32776\n"
"ds_read_b64_tr_b16 a[190:191], v233 offset:33288\n"
"s_waitcnt vmcnt(12)\n"
"ds_read_b64_tr_b16 a[64:65], v232 offset:49152\n"
"ds_read_b64_tr_b16 a[66:67], v232 offset:49664\n"
"ds_read_b64_tr_b16 a[68:69], v232 offset:49160\n"
"ds_read_b64_tr_b16 a[70:71], v232 offset:49672\n"
"ds_read_b64_tr_b16 a[192:193], v233 offset:49152\n"
"ds_read_b64_tr_b16 a[194:195], v233 offset:49664\n"
"ds_read_b64_tr_b16 a[196:197], v233 offset:49160\n"
"ds_read_b64_tr_b16 a[198:199], v233 offset:49672\n"
"s_waitcnt vmcnt(8)\n"
"ds_read_b64_tr_b16 a[72:73], v234 offset:0\n"
"ds_read_b64_tr_b16 a[74:75], v234 offset:512\n"
"ds_read_b64_tr_b16 a[76:77], v234 offset:8\n"
"ds_read_b64_tr_b16 a[78:79], v234 offset:520\n"
"ds_read_b64_tr_b16 a[200:201], v235 offset:0\n"
"ds_read_b64_tr_b16 a[202:203], v235 offset:512\n"
"ds_read_b64_tr_b16 a[204:205], v235 offset:8\n"
"ds_read_b64_tr_b16 a[206:207], v235 offset:520\n"
"s_waitcnt vmcnt(2)\n"
"s_barrier\n"
"buffer_store_dword v199, v198, s[60:63], s55 offen sc1\n"
"buffer_store_dwordx4 v[244:247], v194, s[60:63], s8 offen sc1\n"
"buffer_store_dwordx4 v[248:251], v194, s[60:63], s8 offen offset:1024 sc1\n"
"s_lshl_b32 s30, s31, 2\n"
"s_add_u32 s30, s30, s64\n"
"s_and_b32 s34, s6, 1\n"
"s_mul_i32 s30, s30, 14336\n"
"s_lshl_b32 s34, s34, 9\n"
"s_add_u32 s30, s30, s34\n"
"v_min_u32 v190, 55, v192\n"
"v_and_b32 v191, 7, v190\n"
"v_lshrrev_b32 v190, 3, v190\n"
"v_lshlrev_b32 v190, 10, v190\n"
"v_lshl_add_u32 v190, v191, 6, v190\n"
"buffer_load_dword v255, v190, s[40:43], s30 offen\n"
"s_add_u32 s30, s30, 7168\n"
"buffer_load_dword v255, v190, s[40:43], s30 offen\n"
"s_add_u32 m0, s67, 65536\n"
"s_mov_b32 s74, 1261568\n"
"buffer_load_dwordx4 v209, s[44:47], s74 offen offset:0 lds\n"
"s_sleep 1\n"
"buffer_load_dwordx4 v210, s[44:47], s74 offen offset:1024 lds\n"
"s_sleep 1\n"
"buffer_load_dwordx4 v211, s[44:47], s74 offen offset:2048 lds\n"
"s_sleep 1\n"
"buffer_load_dwordx4 v212, s[44:47], s74 offen offset:3072 lds\n"
"s_sleep 1\n"
"s_add_u32 m0, s67, 81920\n"
"s_mov_b32 s74, 1376256\n"
"buffer_load_dwordx4 v209, s[44:47], s74 offen offset:0 lds\n"
"s_sleep 1\n"
"buffer_load_dwordx4 v210, s[44:47], s74 offen offset:1024 lds\n"
"s_sleep 1\n"
"buffer_load_dwordx4 v211, s[44:47], s74 offen offset:2048 lds\n"
"s_sleep 1\n"
"buffer_load_dwordx4 v212, s[44:47], s74 offen offset:3072 lds\n"
"s_sleep 1\n"
"s_waitcnt lgkmcnt(15)\n"
"s_add_u32 m0, s67, 98304\n"
"s_mov_b32 s74, 1490944\n"
"buffer_load_dwordx4 v209, s[44:47], s74 offen offset:0 lds\n"
"s_sleep 1\n"
"buffer_load_dwordx4 v210, s[44:47], s74 offen offset:1024 lds\n"
"s_sleep 1\n"
"buffer_load_dwordx4 v211, s[44:47], s74 offen offset:2048 lds\n"
"s_sleep 1\n"
"buffer_load_dwordx4 v212, s[44:47], s74 offen offset:3072 lds\n"
"s_sleep 1\n"
"buffer_load_dword v197, v193, s[60:63], s12 offen sc1\n"
"s_waitcnt vmcnt(19)\n"
"v_lshlrev_b32 v112, 16, v80\n"
"v_and_b32 v113, 0xffff0000, v80\n"
"v_lshlrev_b32 v114, 16, v81\n"
"v_and_b32 v115, 0xffff0000, v81\n"
"v_lshlrev_b32 v116, 16, v82\n"
"v_and_b32 v117, 0xffff0000, v82\n"
"v_lshlrev_b32 v118, 16, v83\n"
"v_and_b32 v119, 0xffff0000, v83\n"
"s_waitcnt vmcnt(18)\n"
"v_lshlrev_b32 v120, 16, v84\n"
"v_and_b32 v121, 0xffff0000, v84\n"
"v_lshlrev_b32 v122, 16, v85\n"
"v_and_b32 v123, 0xffff0000, v85\n"
"v_lshlrev_b32 v124, 16, v86\n"
"v_and_b32 v125, 0xffff0000, v86\n"
"v_lshlrev_b32 v126, 16, v87\n"
"v_and_b32 v127, 0xffff0000, v87\n"
"s_waitcnt vmcnt(0)\n"
"v_cmp_ne_u32 vcc, s36, v197\n"
"s_nop 1\n"
"s_nop 0\n"
"s_and_b64 vcc, vcc, s[20:21]\n"
"s_nop 1\n"
"s_cbranch_vccnz Lp2s_19_%=\n"
"Lp2r_20_%=:\n"
"s_waitcnt lgkmcnt(0)\n"
"s_barrier\n"
"s_sub_u32 s12, s12, s17\n"
"s_lshl_b32 s74, s64, 10\n"
"s_sub_u32 s13, s13, s74\n"
"s_add_u32 s10, s13, 0\n"
"s_add_u32 m0, s67, 0\n"
"s_nop 0\n"
"buffer_load_dwordx4 v220, s[60:63], s10 offen offset:0 sc1 lds\n"
"s_add_u32 m0, s67, 768\n"
"s_nop 0\n"
"buffer_load_dwordx4 v220, s[60:63], s10 offen offset:256 sc1 lds\n"
"s_add_u32 m0, s67, 1536\n"
"s_nop 0\n"
"buffer_load_dwordx4 v220, s[60:63], s10 offen offset:512 sc1 lds\n"
"s_add_u32 m0, s67, 2304\n"
"s_nop 0\n"
"buffer_load_dwordx4 v220, s[60:63], s10 offen offset:768 sc1 lds\n"
"s_add_u32 s10, s13, 32768\n"
"s_add_u32 m0, s67, 32768\n"
"s_nop 0\n"
"buffer_load_dwordx4 v220, s[60:63], s10 offen offset:0 sc1 lds\n"
"s_add_u32 m0, s67, 33536\n"
"s_nop 0\n"
"buffer_load_dwordx4 v220, s[60:63], s10 offen offset:256 sc1 lds\n"
"s_add_u32 m0, s67, 34304\n"
"s_nop 0\n"
"buffer_load_dwordx4 v220, s[60:63], s10 offen offset:512 sc1 lds\n"
"s_add_u32 m0, s67, 35072\n"
"s_nop 0\n"
"buffer_load_dwordx4 v220, s[60:63], s10 offen offset:768 sc1 lds\n"
"ds_read_b64_tr_b16 a[80:81], v234 offset:16384\n"
"ds_read_b64_tr_b16 a[82:83], v234 offset:16896\n"
"ds_read_b64_tr_b16 a[84:85], v234 offset:16392\n"
"ds_read_b64_tr_b16 a[86:87], v234 offset:16904\n"
"ds_read_b64_tr_b16 a[208:209], v235 offset:16384\n"
"ds_read_b64_tr_b16 a[210:211], v235 offset:16896\n"
"ds_read_b64_tr_b16 a[212:213], v235 offset:16392\n"
"ds_read_b64_tr_b16 a[214:215], v235 offset:16904\n"
"ds_read_b64_tr_b16 a[88:89], v232 offset:0\n"
"ds_read_b64_tr_b16 a[90:91], v232 offset:512\n"
"ds_read_b64_tr_b16 a[92:93], v232 offset:8\n"
"ds_read_b64_tr_b16 a[94:95], v232 offset:520\n"
"ds_read_b64_tr_b16 a[216:217], v233 offset:0\n"
"ds_read_b64_tr_b16 a[218:219], v233 offset:512\n"
"ds_read_b64_tr_b16 a[220:221], v233 offset:8\n"
"ds_read_b64_tr_b16 a[222:223], v233 offset:520\n"
"ds_read_b64_tr_b16 a[96:97], v232 offset:16384\n"
"ds_read_b64_tr_b16 a[98:99], v232 offset:16896\n"
"ds_read_b64_tr_b16 a[100:101], v232 offset:16392\n"
"ds_read_b64_tr_b16 a[102:103], v232 offset:16904\n"
"ds_read_b64_tr_b16 a[224:225], v233 offset:16384\n"
"ds_read_b64_tr_b16 a[226:227], v233 offset:16896\n"
"ds_read_b64_tr_b16 a[228:229], v233 offset:16392\n"
"ds_read_b64_tr_b16 a[230:231], v233 offset:16904\n"
"ds_read_b64_tr_b16 a[104:105], v232 offset:32768\n"
"ds_read_b64_tr_b16 a[106:107], v232 offset:33280\n"
"ds_read_b64_tr_b16 a[108:109], v232 offset:32776\n"
"ds_read_b64_tr_b16 a[110:111], v232 offset:33288\n"
"ds_read_b64_tr_b16 a[232:233], v233 offset:32768\n"
"ds_read_b64_tr_b16 a[234:235], v233 offset:33280\n"
"ds_read_b64_tr_b16 a[236:237], v233 offset:32776\n"
"ds_read_b64_tr_b16 a[238:239], v233 offset:33288\n"
"s_add_u32 s10, s13, 4096\n"
"s_add_u32 m0, s67, 16384\n"
"s_nop 0\n"
"buffer_load_dwordx4 v220, s[60:63], s10 offen offset:0 sc1 lds\n"
"s_add_u32 m0, s67, 17152\n"
"s_nop 0\n"
"buffer_load_dwordx4 v220, s[60:63], s10 offen offset:256 sc1 lds\n"
"s_add_u32 m0, s67, 17920\n"
"s_nop 0\n"
"buffer_load_dwordx4 v220, s[60:63], s10 offen offset:512 sc1 lds\n"
"s_add_u32 m0, s67, 18688\n"
"s_nop 0\n"
"buffer_load_dwordx4 v220, s[60:63], s10 offen offset:768 sc1 lds\n"
"s_waitcnt vmcnt(4) lgkmcnt(0)\n"
"s_barrier\n"
"ds_read_b128 v[0:3], v221 offset:0\n"
"ds_read_b128 v[4:7], v221 offset:32768\n"
"ds_read_b128 v[8:11], v221 offset:256\n"
"ds_read_b128 v[12:15], v221 offset:33024\n"
"ds_read_b128 v[16:19], v221 offset:2048\n"
"ds_read_b128 v[20:23], v221 offset:34816\n"
"ds_read_b128 v[24:27], v221 offset:2304\n"
"ds_read_b128 v[28:31], v221 offset:35072\n"
"s_add_u32 s10, s13, 36864\n"
"s_add_u32 m0, s67, 49152\n"
"s_waitcnt lgkmcnt(7)\n"
"v_mfma_f32_16x16x32_bf16 v[128:131], a[0:3], v[0:3], v[112:115]\n"
"buffer_load_dwordx4 v220, s[60:63], s10 offen offset:0 sc1 lds\n"
"ds_read_b128 v[32:35], v221 offset:4096\n"
"v_mfma_f32_16x16x32_bf16 v[132:135], a[4:7], v[0:3], v[116:119]\n"
"s_add_u32 m0, s67, 49920\n"
"s_waitcnt lgkmcnt(7)\n"
"v_mfma_f32_16x16x32_bf16 v[144:147], a[0:3], v[4:7], v[112:115]\n"
"buffer_load_dwordx4 v220, s[60:63], s10 offen offset:256 sc1 lds\n"
"ds_read_b128 v[36:39], v221 offset:36864\n"
"v_mfma_f32_16x16x32_bf16 v[148:151], a[4:7], v[4:7], v[116:119]\n"
"s_add_u32 m0, s67, 50688\n"
"s_waitcnt lgkmcnt(7)\n"
"v_mfma_f32_16x16x32_bf16 v[136:139], a[0:3], v[8:11], v[112:115]\n"
"buffer_load_dwordx4 v220, s[60:63], s10 offen offset:512 sc1 lds\n"
"ds_read_b128 v[40:43], v221 offset:4352\n"
"v_mfma_f32_16x16x32_bf16 v[140:143], a[4:7], v[8:11], v[116:119]\n"
"Lst14_%=:\n"
"s_cbranch_execz Lback1_%=\n"
"s_add_u32 m0, s67, 51456\n"
"s_waitcnt lgkmcnt(7)\n"
"v_mfma_f32_16x16x32_bf16 v[152:155], a[0:3], v[12:15], v[112:115]\n"
"buffer_load_dwordx4 v220, s[60:63], s10 offen offset:768 sc1 lds\n"
"ds_read_b128 v[44:47], v221 offset:37120\n"
"v_mfma_f32_16x16x32_bf16 v[156:159], a[4:7], v[12:15], v[116:119]\n"
"Lcp_22_%=:\n"
"s_waitcnt lgkmcnt(7)\n"
"v_mfma_f32_16x16x32_bf16 v[128:131], a[8:11], v[16:19], v[128:131]\n"
"ds_read_b128 v[0:3], v221 offset:6144\n"
"s_mov_b32 s56, s26\n"
"s_and_b32 s57, s27, 0xffff\n"
"s_mov_b32 s58, 0x400\n"
"v_mfma_f32_16x16x32_bf16 v[132:135], a[12:15], v[16:19], v[132:135]\n"
"v_lshl_add_u32 v254, v192, 2, s7\n"
"v_add_u32 v254, Lcp_22_%=-Lpc0_%=+1024, v254\n"
"v_min_u32 v254, Lfin_%=-Lpc0_%=, v254\n"
"s_waitcnt lgkmcnt(7)\n"
"v_mfma_f32_16x16x32_bf16 v[144:147], a[8:11], v[20:23], v[144:147]\n"
"global_load_dword v255, v254, s[18:19]\n"
"ds_read_b128 v[4:7], v221 offset:38912\n"
"s_mov_b32 s59, 0x20000\n"
"v_mfma_f32_16x16x32_bf16 v[148:151], a[12:15], v[20:23], v[148:151]\n"
"v_and_b32 v196, 7, v192\n"
"s_add_u32 m0, s67, 131072\n"
"s_mov_b32 s74, 1605632\n"
"s_waitcnt lgkmcnt(7)\n"
"v_mfma_f32_16x16x32_bf16 v[136:139], a[8:11], v[24:27], v[136:139]\n"
"buffer_load_dwordx4 v209, s[44:47], s74 offen offset:0 lds\n"
"ds_read_b128 v[8:11], v221 offset:6400\n"
"v_mfma_f32_16x16x32_bf16 v[140:143], a[12:15], v[24:27], v[140:143]\n"
"v_lshlrev_b32 v196, 4, v196\n"
"v_lshl_add_u32 v196, v195, 3, v196\n"
"s_waitcnt lgkmcnt(7)\n"
"v_mfma_f32_16x16x32_bf16 v[152:155], a[8:11], v[28:31], v[152:155]\n"
"buffer_load_dwordx4 v210, s[44:47], s74 offen offset:1024 lds\n"
"ds_read_b128 v[12:15], v221 offset:39168\n"
"v_mfma_f32_16x16x32_bf16 v[156:159], a[12:15], v[28:31], v[156:159]\n"
"v_bfe_u32 v224, v192, 3, 1\n"
"v_lshl_add_u32 v196, v224, 13, v196\n"
"s_waitcnt lgkmcnt(7)\n"
"v_mfma_f32_16x16x32_bf16 v[128:131], a[16:19], v[32:35], v[128:131]\n"
"buffer_load_dwordx4 v211, s[44:47], s74 offen offset:2048 lds\n"
"ds_read_b128 v[16:19], v221 offset:8192\n"
"v_add_u32 v222, 16384, v196\n"
"v_mfma_f32_16x16x32_bf16 v[132:135], a[20:23], v[32:35], v[132:135]\n"
"s_lshl_b32 s8, s78, 16\n"
"s_lshl_b32 s9, s79, 10\n"
"s_waitcnt lgkmcnt(7)\n"
"v_mfma_f32_16x16x32_bf16 v[144:147], a[16:19], v[36:39], v[144:147]\n"
"buffer_load_dwordx4 v212, s[44:47], s74 offen offset:3072 lds\n"
"ds_read_b128 v[20:23], v221 offset:40960\n"
"v_mfma_f32_16x16x32_bf16 v[148:151], a[20:23], v[36:39], v[148:151]\n"
"s_add_u32 s8, s8, s9\n"
"s_lshr_b32 s9, s64, 1\n"
"s_waitcnt lgkmcnt(7)\n"
"v_mfma_f32_16x16x32_bf16 v[136:139], a[16:19], v[40:43], v[136:139]\n"
"ds_read_b128 v[24:27], v221 offset:8448\n"
"s_lshl_b32 s9, s9, 9\n"
"v_mfma_f32_16x16x32_bf16 v[140:143], a[20:23], v[40:43], v[140:143]\n"
"s_add_u32 s8, s8, s9\n"
"s_waitcnt lgkmcnt(7)\n"
"v_mfma_f32_16x16x32_bf16 v[152:155], a[16:19], v[44:47], v[152:155]\n"
"ds_read_b128 v[28:31], v221 offset:41216\n"
"s_and_b32 s9, s64, 1\n"
"v_mfma_f32_16x16x32_bf16 v[156:159], a[20:23], v[44:47], v[156:159]\n"
"s_lshl_b32 s9, s9, 15\n"
"s_waitcnt lgkmcnt(7)\n"
"v_mfma_f32_16x16x32_bf16 v[128:131], a[24:27], v[0:3], v[128:131]\n"
"ds_read_b128 v[32:35], v221 offset:10240\n"
"s_add_u32 s8, s8, s9\n"
"s_add_u32 s8, s8, 0x1400000\n"
"v_mfma_f32_16x16x32_bf16 v[132:135], a[28:31], v[0:3], v[132:135]\n"
"s_lshl_b32 s14, s79, 7\n"
"s_add_u32 m0, s67, 147456\n"
"s_mov_b32 s74, 1720320\n"
"s_waitcnt lgkmcnt(7)\n"
"v_mfma_f32_16x16x32_bf16 v[144:147], a[24:27], v[4:7], v[144:147]\n"
"buffer_load_dwordx4 v209, s[44:47], s74 offen offset:0 lds\n"
"ds_read_b128 v[36:39], v221 offset:43008\n"
"v_mfma_f32_16x16x32_bf16 v[148:151], a[28:31], v[4:7], v[148:151]\n"
"s_lshr_b32 s9, s64, 1\n"
"s_lshl_b32 s9, s9, 6\n"
"s_waitcnt lgkmcnt(7)\n"
"v_mfma_f32_16x16x32_bf16 v[136:139], a[24:27], v[8:11], v[136:139]\n"
"buffer_load_dwordx4 v210, s[44:47], s74 offen offset:1024 lds\n"
"ds_read_b128 v[40:43], v221 offset:10496\n"
"v_mfma_f32_16x16x32_bf16 v[140:143], a[28:31], v[8:11], v[140:143]\n"
"s_add_u32 s14, s14, s9\n"
"v_lshlrev_b32 v224, 1, v195\n"
"s_waitcnt lgkmcnt(7)\n"
"v_mfma_f32_16x16x32_bf16 v[152:155], a[24:27], v[12:15], v[152:155]\n"
"buffer_load_dwordx4 v211, s[44:47], s74 offen offset:2048 lds\n"
"ds_read_b128 v[44:47], v221 offset:43264\n"
"s_lshl_b32 s10, s14, 1\n"
"v_mfma_f32_16x16x32_bf16 v[156:159], a[28:31], v[12:15], v[156:159]\n"
"s_add_u32 s10, s10, 47104\n"
"s_waitcnt lgkmcnt(7)\n"
"v_mfma_f32_16x16x32_bf16 v[128:131], a[32:35], v[16:19], v[128:131]\n"
"buffer_load_dwordx4 v212, s[44:47], s74 offen offset:3072 lds\n"
"ds_read_b128 v[0:3], v221 offset:12288\n"
"v_mfma_f32_16x16x32_bf16 v[132:135], a[36:39], v[16:19], v[132:135]\n"
"s_mul_i32 s17, s78, 229376\n"
"s_add_u32 s17, s17, 0x1800000\n"
"s_waitcnt lgkmcnt(7)\n"
"v_mfma_f32_16x16x32_bf16 v[144:147], a[32:35], v[20:23], v[144:147]\n"
"ds_read_b128 v[4:7], v221 offset:45056\n"
"v_mfma_f32_16x16x32_bf16 v[148:151], a[36:39], v[20:23], v[148:151]\n"
"s_lshl_b32 s9, s33, 13\n"
"s_waitcnt lgkmcnt(7)\n"
"v_mfma_f32_16x16x32_bf16 v[136:139], a[32:35], v[24:27], v[136:139]\n"
"ds_read_b128 v[8:11], v221 offset:12544\n"
"v_mfma_f32_16x16x32_bf16 v[140:143], a[36:39], v[24:27], v[140:143]\n"
"s_add_u32 s16, s17, s9\n"
"s_waitcnt lgkmcnt(7)\n"
"v_mfma_f32_16x16x32_bf16 v[152:155], a[32:35], v[28:31], v[152:155]\n"
"ds_read_b128 v[12:15], v221 offset:45312\n"
"v_mfma_f32_16x16x32_bf16 v[156:159], a[36:39], v[28:31], v[156:159]\n"
"s_and_b32 s9, s79, 1\n"
"s_waitcnt lgkmcnt(7)\n"
"v_mfma_f32_16x16x32_bf16 v[128:131], a[40:43], v[32:35], v[128:131]\n"
"ds_read_b128 v[16:19], v221 offset:14336\n"
"s_add_u32 s9, s9, s64\n"
"s_lshl_b32 s74, s48, 16\n"
"v_mfma_f32_16x16x32_bf16 v[132:135], a[44:47], v[32:35], v[132:135]\n"
"s_add_u32 s74, s74, s11\n"
"s_add_u32 m0, s67, 65536\n"
"s_waitcnt lgkmcnt(7)\n"
"v_mfma_f32_16x16x32_bf16 v[144:147], a[40:43], v[36:39], v[144:147]\n"
"buffer_load_dwordx4 v205, s[40:43], s74 offen offset:0 lds\n"
"ds_read_b128 v[20:23], v221 offset:47104\n"
"v_mfma_f32_16x16x32_bf16 v[148:151], a[44:47], v[36:39], v[148:151]\n"
"s_lshr_b32 s9, s9, 2\n"
"s_waitcnt lgkmcnt(7)\n"
"v_mfma_f32_16x16x32_bf16 v[136:139], a[40:43], v[40:43], v[136:139]\n"
"buffer_load_dwordx4 v206, s[40:43], s74 offen offset:1024 lds\n"
"ds_read_b128 v[24:27], v221 offset:14592\n"
"v_mfma_f32_16x16x32_bf16 v[140:143], a[44:47], v[40:43], v[140:143]\n"
"s_mul_i32 s15, s9, 0x7000000\n"
"s_waitcnt lgkmcnt(7)\n"
"v_mfma_f32_16x16x32_bf16 v[152:155], a[40:43], v[44:47], v[152:155]\n"
"ds_read_b128 v[28:31], v221 offset:47360\n"
"s_add_u32 s16, s16, s15\n"
"v_mfma_f32_16x16x32_bf16 v[156:159], a[44:47], v[44:47], v[156:159]\n"
"s_waitcnt vmcnt(11)\n"
"s_barrier\n"
"s_waitcnt lgkmcnt(7)\n"
"v_mfma_f32_16x16x32_bf16 v[128:131], a[48:51], v[0:3], v[128:131]\n"
"buffer_load_dwordx4 v207, s[40:43], s74 offen offset:2048 lds\n"
"ds_read_b128 v[32:35], v221 offset:16384\n"
"v_mfma_f32_16x16x32_bf16 v[132:135], a[52:55], v[0:3], v[132:135]\n"
"v_add_u32 v253, 1, v192\n"
"s_waitcnt lgkmcnt(7)\n"
"v_mfma_f32_16x16x32_bf16 v[144:147], a[48:51], v[4:7], v[144:147]\n"
"buffer_load_dwordx4 v208, s[40:43], s74 offen offset:3072 lds\n"
"ds_read_b128 v[36:39], v221 offset:49152\n"
"v_mfma_f32_16x16x32_bf16 v[148:151], a[52:55], v[4:7], v[148:151]\n"
"v_lshrrev_b32 v253, 3, v253\n"
"s_waitcnt lgkmcnt(7)\n"
"v_mfma_f32_16x16x32_bf16 v[136:139], a[48:51], v[8:11], v[136:139]\n"
"ds_read_b128 v[40:43], v221 offset:16640\n"
"v_mfma_f32_16x16x32_bf16 v[140:143], a[52:55], v[8:11], v[140:143]\n"
"s_nop 0\n"
"s_waitcnt lgkmcnt(7)\n"
"v_mfma_f32_16x16x32_bf16 v[152:155], a[48:51], v[12:15], v[152:155]\n"
"ds_read_b128 v[44:47], v221 offset:49408\n"
"v_mfma_f32_16x16x32_bf16 v[156:159], a[52:55], v[12:15], v[156:159]\n"
"s_mul_i32 s9, s64, 896\n"
"s_waitcnt lgkmcnt(7)\n"
"v_mfma_f32_16x16x32_bf16 v[128:131], a[56:59], v[16:19], v[128:131]\n"
"ds_read_b128 v[0:3], v221 offset:18432\n"
"s_mul_i32 s17, s78, 229376\n"
"s_lshl_b32 s74, s49, 16\n"
"v_mfma_f32_16x16x32_bf16 v[132:135], a[60:63], v[16:19], v[132:135]\n"
"s_add_u32 s74, s74, s11\n"
"s_add_u32 m0, s67, 81920\n"
"s_waitcnt lgkmcnt(7)\n"
"v_mfma_f32_16x16x32_bf16 v[144:147], a[56:59], v[20:23], v[144:147]\n"
"buffer_load_dwordx4 v205, s[40:43], s74 offen offset:0 lds\n"
"ds_read_b128 v[4:7], v221 offset:51200\n"
"v_mfma_f32_16x16x32_bf16 v[148:151], a[60:63], v[20:23], v[148:151]\n"
"s_add_u32 s17, s17, 0x1800000\n"
"s_waitcnt lgkmcnt(7)\n"
"v_mfma_f32_16x16x32_bf16 v[136:139], a[56:59], v[24:27], v[136:139]\n"
"buffer_load_dwordx4 v206, s[40:43], s74 offen offset:1024 lds\n"
"ds_read_b128 v[8:11], v221 offset:18688\n"
"v_mfma_f32_16x16x32_bf16 v[140:143], a[60:63], v[24:27], v[140:143]\n"
"s_mul_i32 s9, s64, 57344\n"
"s_waitcnt lgkmcnt(7)\n"
"v_mfma_f32_16x16x32_bf16 v[152:155], a[56:59], v[28:31], v[152:155]\n"
"ds_read_b128 v[12:15], v221 offset:51456\n"
"s_add_u32 s17, s17, s9\n"
"v_mfma_f32_16x16x32_bf16 v[156:159], a[60:63], v[28:31], v[156:159]\n"
"s_waitcnt lgkmcnt(7)\n"
"v_mfma_f32_16x16x32_bf16 v[128:131], a[64:67], v[32:35], v[128:131]\n"
"buffer_load_dwordx4 v207, s[40:43], s74 offen offset:2048 lds\n"
"ds_read_b128 v[16:19], v221 offset:20480\n"
"v_mfma_f32_16x16x32_bf16 v[132:135], a[68:71], v[32:35], v[132:135]\n"
"s_and_b32 s9, s64, 1\n"
"s_waitcnt lgkmcnt(7)\n"
"v_mfma_f32_16x16x32_bf16 v[144:147], a[64:67], v[36:39], v[144:147]\n"
"buffer_load_dwordx4 v208, s[40:43], s74 offen offset:3072 lds\n"
"ds_read_b128 v[20:23], v221 offset:53248\n"
"v_mfma_f32_16x16x32_bf16 v[148:151], a[68:71], v[36:39], v[148:151]\n"
"s_lshl_b32 s9, s9, 10\n"
"s_waitcnt vmcnt(12)\n"
"s_waitcnt lgkmcnt(7)\n"
"v_mfma_f32_16x16x32_bf16 v[136:139], a[64:67], v[40:43], v[136:139]\n"
"ds_read_b128 v[24:27], v221 offset:20736\n"
"ds_read_b64_tr_b16 a[112:113], v234 offset:0\n"
"v_mfma_f32_16x16x32_bf16 v[140:143], a[68:71], v[40:43], v[140:143]\n"
"ds_read_b64_tr_b16 a[114:115], v234 offset:512\n"
"v_add_u32 v225, s9, v194\n"
"s_waitcnt lgkmcnt(9)\n"
"v_mfma_f32_16x16x32_bf16 v[152:155], a[64:67], v[44:47], v[152:155]\n"
"ds_read_b128 v[28:31], v221 offset:53504\n"
"ds_read_b64_tr_b16 a[116:117], v234 offset:8\n"
"v_mfma_f32_16x16x32_bf16 v[156:159], a[68:71], v[44:47], v[156:159]\n"
"v_xor_b32 v226, 1024, v225\n"
"s_waitcnt lgkmcnt(10)\n"
"v_mfma_f32_16x16x32_bf16 v[128:131], a[72:75], v[0:3], v[128:131]\n"
"ds_read_b128 v[32:35], v221 offset:22528\n"
"ds_read_b64_tr_b16 a[118:119], v234 offset:520\n"
"s_cmp_eq_u32 s64, 0\n"
"s_cselect_b32 s9, 0, 0x7000000\n"
"v_mfma_f32_16x16x32_bf16 v[132:135], a[76:79], v[0:3], v[132:135]\n"
"s_lshl_b32 s74, s50, 16\n"
"s_add_u32 s74, s74, s11\n"
"s_add_u32 m0, s67, 98304\n"
"s_waitcnt lgkmcnt(11)\n"
"v_mfma_f32_16x16x32_bf16 v[144:147], a[72:75], v[4:7], v[144:147]\n"
"buffer_load_dwordx4 v205, s[40:43], s74 offen offset:0 lds\n"
"ds_read_b128 v[36:39], v221 offset:55296\n"
"ds_read_b64_tr_b16 a[240:241], v235 offset:0\n"
"v_mfma_f32_16x16x32_bf16 v[148:151], a[76:79], v[4:7], v[148:151]\n"
"ds_read_b64_tr_b16 a[242:243], v235 offset:512\n"
"s_lshl_b32 s12, s78, 12\n"
"s_waitcnt lgkmcnt(13)\n"
"v_mfma_f32_16x16x32_bf16 v[136:139], a[72:75], v[8:11], v[136:139]\n"
"buffer_load_dwordx4 v206, s[40:43], s74 offen offset:1024 lds\n"
"ds_read_b128 v[40:43], v221 offset:22784\n"
"v_mfma_f32_16x16x32_bf16 v[140:143], a[76:79], v[8:11], v[140:143]\n"
"ds_read_b64_tr_b16 a[244:245], v235 offset:8\n"
"s_add_u32 s12, s12, s9\n"
"s_waitcnt lgkmcnt(14)\n"
"v_mfma_f32_16x16x32_bf16 v[152:155], a[72:75], v[12:15], v[152:155]\n"
"ds_read_b128 v[44:47], v221 offset:55552\n"
"ds_read_b64_tr_b16 a[246:247], v235 offset:520\n"
"v_mfma_f32_16x16x32_bf16 v[156:159], a[76:79], v[12:15], v[156:159]\n"
"s_add_u32 s12, s12, 1441792\n"
"s_waitcnt lgkmcnt(15)\n"
"v_mfma_f32_16x16x32_bf16 v[128:131], a[80:83], v[16:19], v[128:131]\n"
"buffer_load_dwordx4 v207, s[40:43], s74 offen offset:2048 lds\n"
"ds_read_b128 v[0:3], v221 offset:24576\n"
"v_mfma_f32_16x16x32_bf16 v[132:135], a[84:87], v[16:19], v[132:135]\n"
"s_add_u32 s55, s55, 131072\n"
"s_waitcnt vmcnt(11)\n"
"s_waitcnt lgkmcnt(15)\n"
"v_mfma_f32_16x16x32_bf16 v[144:147], a[80:83], v[20:23], v[144:147]\n"
"ds_read_b128 v[4:7], v221 offset:57344\n"
"ds_read_b64_tr_b16 a[120:121], v234 offset:16384\n"
"v_mfma_f32_16x16x32_bf16 v[148:151], a[84:87], v[20:23], v[148:151]\n"
"ds_read_b64_tr_b16 a[122:123], v234 offset:16896\n"
"s_waitcnt lgkmcnt(15)\n"
"v_mfma_f32_16x16x32_bf16 v[136:139], a[80:83], v[24:27], v[136:139]\n"
"buffer_load_dwordx4 v208, s[40:43], s74 offen offset:3072 lds\n"
"ds_read_b128 v[8:11], v221 offset:24832\n"
"v_mfma_f32_16x16x32_bf16 v[140:143], a[84:87], v[24:27], v[140:143]\n"
"ds_read_b64_tr_b16 a[124:125], v234 offset:16392\n"
"s_waitcnt lgkmcnt(15)\n"
"v_mfma_f32_16x16x32_bf16 v[152:155], a[80:83], v[28:31], v[152:155]\n"
"ds_read_b128 v[12:15], v221 offset:57600\n"
"v_mfma_f32_16x16x32_bf16 v[156:159], a[84:87], v[28:31], v[156:159]\n"
"ds_read_b64_tr_b16 a[126:127], v234 offset:16904\n"
"s_waitcnt lgkmcnt(15)\n"
"v_mfma_f32_16x16x32_bf16 v[128:131], a[88:91], v[32:35], v[128:131]\n"
"ds_read_b128 v[16:19], v221 offset:26624\n"
"ds_read_b64_tr_b16 a[248:249], v235 offset:16384\n"
"ds_read_b64_tr_b16 a[250:251], v235 offset:16896\n"
"v_mfma_f32_16x16x32_bf16 v[132:135], a[92:95], v[32:35], v[132:135]\n"
"s_lshl_b32 s74, s51, 16\n"
"s_add_u32 s74, s74, s11\n"
"s_add_u32 m0, s67, 114688\n"
"s_waitcnt lgkmcnt(15)\n"
"v_mfma_f32_16x16x32_bf16 v[144:147], a[88:91], v[36:39], v[144:147]\n"
"buffer_load_dwordx4 v205, s[40:43], s74 offen offset:0 lds\n"
"ds_read_b128 v[20:23], v221 offset:59392\n"
"v_mfma_f32_16x16x32_bf16 v[148:151], a[92:95], v[36:39], v[148:151]\n"
"ds_read_b64_tr_b16 a[252:253], v235 offset:16392\n"
"s_waitcnt lgkmcnt(15)\n"
"v_mfma_f32_16x16x32_bf16 v[136:139], a[88:91], v[40:43], v[136:139]\n"
"buffer_load_dwordx4 v206, s[40:43], s74 offen offset:1024 lds\n"
"ds_read_b128 v[24:27], v221 offset:26880\n"
"v_mfma_f32_16x16x32_bf16 v[140:143], a[92:95], v[40:43], v[140:143]\n"
"ds_read_b64_tr_b16 a[254:255], v235 offset:16904\n"
"s_waitcnt lgkmcnt(15)\n"
"v_mfma_f32_16x16x32_bf16 v[152:155], a[88:91], v[44:47], v[152:155]\n"
"ds_read_b128 v[28:31], v221 offset:59648\n"
"v_mfma_f32_16x16x32_bf16 v[156:159], a[92:95], v[44:47], v[156:159]\n"
"s_waitcnt lgkmcnt(15)\n"
"v_mfma_f32_16x16x32_bf16 v[128:131], a[96:99], v[0:3], v[128:131]\n"
"buffer_load_dwordx4 v207, s[40:43], s74 offen offset:2048 lds\n"
"v_mfma_f32_16x16x32_bf16 v[132:135], a[100:103], v[0:3], v[132:135]\n"
"ds_read_b128 v[32:35], v221 offset:28672\n"
"s_waitcnt lgkmcnt(15)\n"
"v_mfma_f32_16x16x32_bf16 v[144:147], a[96:99], v[4:7], v[144:147]\n"
"buffer_load_dwordx4 v208, s[40:43], s74 offen offset:3072 lds\n"
"v_mfma_f32_16x16x32_bf16 v[148:151], a[100:103], v[4:7], v[148:151]\n"
"ds_read_b128 v[36:39], v221 offset:61440\n"
"s_waitcnt lgkmcnt(13)\n"
"v_mfma_f32_16x16x32_bf16 v[136:139], a[96:99], v[8:11], v[136:139]\n"
"ds_read_b128 v[40:43], v221 offset:28928\n"
"v_mfma_f32_16x16x32_bf16 v[140:143], a[100:103], v[8:11], v[140:143]\n"
"s_waitcnt lgkmcnt(12)\n"
"v_mfma_f32_16x16x32_bf16 v[152:155], a[96:99], v[12:15], v[152:155]\n"
"ds_read_b128 v[44:47], v221 offset:61696\n"
"v_mfma_f32_16x16x32_bf16 v[156:159], a[100:103], v[12:15], v[156:159]\n"
"s_waitcnt lgkmcnt(11)\n"
"v_mfma_f32_16x16x32_bf16 v[128:131], a[104:107], v[16:19], v[128:131]\n"
"ds_read_b128 v[0:3], v221 offset:30720\n"
"s_lshl_b32 s74, s52, 16\n"
"v_mfma_f32_16x16x32_bf16 v[132:135], a[108:111], v[16:19], v[132:135]\n"
"s_add_u32 s74, s74, s11\n"
"s_add_u32 m0, s67, 131072\n"
"s_waitcnt lgkmcnt(9)\n"
"v_mfma_f32_16x16x32_bf16 v[144:147], a[104:107], v[20:23], v[144:147]\n"
"buffer_load_dwordx4 v205, s[40:43], s74 offen offset:0 lds\n"
"v_mfma_f32_16x16x32_bf16 v[148:151], a[108:111], v[20:23], v[148:151]\n"
"ds_read_b128 v[4:7], v221 offset:63488\n"
"s_waitcnt lgkmcnt(8)\n"
"v_mfma_f32_16x16x32_bf16 v[136:139], a[104:107], v[24:27], v[136:139]\n"
"buffer_load_dwordx4 v206, s[40:43], s74 offen offset:1024 lds\n"
"v_mfma_f32_16x16x32_bf16 v[140:143], a[108:111], v[24:27], v[140:143]\n"
"ds_read_b128 v[8:11], v221 offset:30976\n"
"s_waitcnt lgkmcnt(7)\n"
"v_mfma_f32_16x16x32_bf16 v[152:155], a[104:107], v[28:31], v[152:155]\n"
"ds_read_b128 v[12:15], v221 offset:63744\n"
"v_mfma_f32_16x16x32_bf16 v[156:159], a[108:111], v[28:31], v[156:159]\n"
"s_waitcnt lgkmcnt(7)\n"
"v_mfma_f32_16x16x32_bf16 v[128:131], a[112:115], v[32:35], v[128:131]\n"
"buffer_load_dwordx4 v207, s[40:43], s74 offen offset:2048 lds\n"
"v_mfma_f32_16x16x32_bf16 v[132:135], a[116:119], v[32:35], v[132:135]\n"
"ds_read_b128 v[16:19], v221 offset:0\n"
"s_waitcnt lgkmcnt(7)\n"
"v_mfma_f32_16x16x32_bf16 v[144:147], a[112:115], v[36:39], v[144:147]\n"
"buffer_load_dwordx4 v208, s[40:43], s74 offen offset:3072 lds\n"
"v_mfma_f32_16x16x32_bf16 v[148:151], a[116:119], v[36:39], v[148:151]\n"
"ds_read_b128 v[20:23], v221 offset:256\n"
"s_waitcnt lgkmcnt(7)\n"
"v_mfma_f32_16x16x32_bf16 v[136:139], a[112:115], v[40:43], v[136:139]\n"
"ds_read_b128 v[24:27], v221 offset:2048\n"
"v_mfma_f32_16x16x32_bf16 v[140:143], a[116:119], v[40:43], v[140:143]\n"
"s_waitcnt lgkmcnt(7)\n"
"v_mfma_f32_16x16x32_bf16 v[152:155], a[112:115], v[44:47], v[152:155]\n"
"ds_read_b128 v[28:31], v221 offset:2304\n"
"v_mfma_f32_16x16x32_bf16 v[156:159], a[116:119], v[44:47], v[156:159]\n"
"s_waitcnt lgkmcnt(7)\n"
"v_mfma_f32_16x16x32_bf16 v[128:131], a[120:123], v[0:3], v[128:131]\n"
"buffer_load_dwordx4 v[244:247], v195, s[56:59], s14 offen offset:0\n"
"ds_read_b128 v[32:35], v221 offset:4096\n"
"s_lshl_b32 s74, s53, 16\n"
"v_mfma_f32_16x16x32_bf16 v[132:135], a[124:127], v[0:3], v[132:135]\n"
"s_add_u32 s74, s74, s11\n"
"s_add_u32 m0, s67, 147456\n"
"s_waitcnt lgkmcnt(7)\n"
"v_mfma_f32_16x16x32_bf16 v[144:147], a[120:123], v[4:7], v[144:147]\n"
"buffer_load_dwordx4 v205, s[40:43], s74 offen offset:0 lds\n"
"v_mfma_f32_16x16x32_bf16 v[148:151], a[124:127], v[4:7], v[148:151]\n"
"ds_read_b128 v[36:39], v221 offset:4352\n"
"s_waitcnt lgkmcnt(7)\n"
"v_mfma_f32_16x16x32_bf16 v[136:139], a[120:123], v[8:11], v[136:139]\n"
"buffer_load_dwordx4 v206, s[40:43], s74 offen offset:1024 lds\n"
"buffer_load_dwordx4 v[80:83], v224, s[60:63], s10 offen offset:0 sc1\n"
"v_mfma_f32_16x16x32_bf16 v[140:143], a[124:127], v[8:11], v[140:143]\n"
"ds_read_b128 v[40:43], v221 offset:6144\n"
"s_waitcnt lgkmcnt(7)\n"
"v_mfma_f32_16x16x32_bf16 v[152:155], a[120:123], v[12:15], v[152:155]\n"
"ds_read_b128 v[44:47], v221 offset:6400\n"
"s_waitcnt vmcnt(20)\n"
"v_mfma_f32_16x16x32_bf16 v[156:159], a[124:127], v[12:15], v[156:159]\n"
"s_waitcnt lgkmcnt(7)\n"
"v_mfma_f32_16x16x32_bf16 v[160:163], a[128:131], v[16:19], v[120:123]\n"
"buffer_load_dwordx4 v207, s[40:43], s74 offen offset:2048 lds\n"
"ds_read_b128 v[0:3], v221 offset:8192\n"
"v_mfma_f32_16x16x32_bf16 v[164:167], a[132:135], v[16:19], v[124:127]\n"
"ds_read_b64_tr_b16 a[0:1], v200 offset:0\n"
"s_waitcnt lgkmcnt(8)\n"
"v_mfma_f32_16x16x32_bf16 v[168:171], a[128:131], v[20:23], v[120:123]\n"
"buffer_load_dwordx4 v208, s[40:43], s74 offen offset:3072 lds\n"
"buffer_load_dwordx4 v[84:87], v224, s[60:63], s10 offen offset:16 sc1\n"
"v_mfma_f32_16x16x32_bf16 v[172:175], a[132:135], v[20:23], v[124:127]\n"
"ds_read_b128 v[4:7], v221 offset:8448\n"
"ds_read_b64_tr_b16 a[2:3], v200 offset:512\n"
"s_waitcnt lgkmcnt(9)\n"
"v_mfma_f32_16x16x32_bf16 v[160:163], a[136:139], v[24:27], v[160:163]\n"
"ds_read_b128 v[8:11], v221 offset:10240\n"
"ds_read_b64_tr_b16 a[4:5], v201 offset:0\n"
"v_max_f32 v128, 0, v128\n"
"v_mfma_f32_16x16x32_bf16 v[164:167], a[140:143], v[24:27], v[164:167]\n"
"v_max_f32 v129, 0, v129\n"
"v_max_f32 v130, 0, v130\n"
"v_max_f32 v131, 0, v131\n"
"s_waitcnt lgkmcnt(10)\n"
"v_mfma_f32_16x16x32_bf16 v[168:171], a[136:139], v[28:31], v[168:171]\n"
"ds_read_b128 v[12:15], v221 offset:10496\n"
"ds_read_b64_tr_b16 a[6:7], v201 offset:512\n"
"v_max_f32 v132, 0, v132\n"
"v_mfma_f32_16x16x32_bf16 v[172:175], a[140:143], v[28:31], v[172:175]\n"
"v_max_f32 v133, 0, v133\n"
"v_max_f32 v134, 0, v134\n"
"s_waitcnt lgkmcnt(11)\n"
"v_mfma_f32_16x16x32_bf16 v[160:163], a[144:147], v[32:35], v[160:163]\n"
"ds_read_b128 v[16:19], v221 offset:12288\n"
"ds_read_b64_tr_b16 a[8:9], v200 offset:8\n"
"v_max_f32 v135, 0, v135\n"
"v_mfma_f32_16x16x32_bf16 v[164:167], a[148:151], v[32:35], v[164:167]\n"
"v_mul_f32 v128, v128, v128\n"
"v_mul_f32 v129, v129, v129\n"
"s_waitcnt lgkmcnt(12)\n"
"v_mfma_f32_16x16x32_bf16 v[168:171], a[144:147], v[36:39], v[168:171]\n"
"ds_read_b128 v[20:23], v221 offset:12544\n"
"ds_read_b64_tr_b16 a[10:11], v200 offset:520\n"
"v_mul_f32 v130, v130, v130\n"
"v_mfma_f32_16x16x32_bf16 v[172:175], a[148:151], v[36:39], v[172:175]\n"
"v_mul_f32 v131, v131, v131\n"
"v_mul_f32 v132, v132, v132\n"
"v_mul_f32 v133, v133, v133\n"
"s_waitcnt lgkmcnt(13)\n"
"v_mfma_f32_16x16x32_bf16 v[160:163], a[152:155], v[40:43], v[160:163]\n"
"ds_read_b128 v[24:27], v221 offset:14336\n"
"ds_read_b64_tr_b16 a[12:13], v201 offset:8\n"
"v_mul_f32 v134, v134, v134\n"
"v_mfma_f32_16x16x32_bf16 v[164:167], a[156:159], v[40:43], v[164:167]\n"
"v_mul_f32 v135, v135, v135\n"
"v_cvt_pk_bf16_f32 v128, v128, v129\n"
"s_waitcnt lgkmcnt(14)\n"
"v_mfma_f32_16x16x32_bf16 v[168:171], a[152:155], v[44:47], v[168:171]\n"
"ds_read_b128 v[28:31], v221 offset:14592\n"
"ds_read_b64_tr_b16 a[14:15], v201 offset:520\n"
"v_cvt_pk_bf16_f32 v129, v130, v131\n"
"v_mfma_f32_16x16x32_bf16 v[172:175], a[156:159], v[44:47], v[172:175]\n"
"v_cvt_pk_bf16_f32 v130, v132, v133\n"
"v_cvt_pk_bf16_f32 v131, v134, v135\n"
"s_waitcnt vmcnt(19)\n"
"s_waitcnt lgkmcnt(15)\n"
"v_mfma_f32_16x16x32_bf16 v[160:163], a[160:163], v[0:3], v[160:163]\n"
"buffer_store_dwordx4 v[128:131], v194, s[60:63], s16 offen offset:0 sc1\n"
"ds_read_b128 v[32:35], v221 offset:16384\n"
"ds_read_b64_tr_b16 a[16:17], v200 offset:16384\n"
"v_max_f32 v144, 0, v144\n"
"v_mfma_f32_16x16x32_bf16 v[164:167], a[164:167], v[0:3], v[164:167]\n"
"v_max_f32 v145, 0, v145\n"
"v_max_f32 v146, 0, v146\n"
"v_max_f32 v147, 0, v147\n"
"s_waitcnt lgkmcnt(15)\n"
"v_mfma_f32_16x16x32_bf16 v[168:171], a[160:163], v[4:7], v[168:171]\n"
"ds_read_b128 v[36:39], v221 offset:16640\n"
"ds_read_b64_tr_b16 a[18:19], v200 offset:16896\n"
"v_max_f32 v148, 0, v148\n"
"v_mfma_f32_16x16x32_bf16 v[172:175], a[164:167], v[4:7], v[172:175]\n"
"v_max_f32 v149, 0, v149\n"
"v_max_f32 v150, 0, v150\n"
"s_waitcnt lgkmcnt(15)\n"
"v_mfma_f32_16x16x32_bf16 v[160:163], a[168:171], v[8:11], v[160:163]\n"
"ds_read_b128 v[40:43], v221 offset:18432\n"
"ds_read_b64_tr_b16 a[20:21], v201 offset:16384\n"
"v_max_f32 v151, 0, v151\n"
"v_mfma_f32_16x16x32_bf16 v[164:167], a[172:175], v[8:11], v[164:167]\n"
"v_mul_f32 v144, v144, v144\n"
"v_mul_f32 v145, v145, v145\n"
"s_waitcnt lgkmcnt(15)\n"
"v_mfma_f32_16x16x32_bf16 v[168:171], a[168:171], v[12:15], v[168:171]\n"
"ds_read_b128 v[44:47], v221 offset:18688\n"
"ds_read_b64_tr_b16 a[22:23], v201 offset:16896\n"
"v_mul_f32 v146, v146, v146\n"
"v_mul_f32 v147, v147, v147\n"
"v_mfma_f32_16x16x32_bf16 v[172:175], a[172:175], v[12:15], v[172:175]\n"
"v_mul_f32 v148, v148, v148\n"
"v_mul_f32 v149, v149, v149\n"
"s_waitcnt lgkmcnt(8)\n"
"v_mfma_f32_16x16x32_bf16 v[160:163], a[176:179], v[16:19], v[160:163]\n"
"ds_read_b128 v[0:3], v221 offset:20480\n"
"ds_read_b64_tr_b16 a[24:25], v200 offset:16392\n"
"v_mul_f32 v150, v150, v150\n"
"v_mfma_f32_16x16x32_bf16 v[164:167], a[180:183], v[16:19], v[164:167]\n"
"v_mul_f32 v151, v151, v151\n"
"v_cvt_pk_bf16_f32 v144, v144, v145\n"
"v_mfma_f32_16x16x32_bf16 v[168:171], a[176:179], v[20:23], v[168:171]\n"
"ds_read_b128 v[4:7], v221 offset:20736\n"
"ds_read_b64_tr_b16 a[26:27], v200 offset:16904\n"
"v_cvt_pk_bf16_f32 v145, v146, v147\n"
"v_cvt_pk_bf16_f32 v146, v148, v149\n"
"v_mfma_f32_16x16x32_bf16 v[172:175], a[180:183], v[20:23], v[172:175]\n"
"v_cvt_pk_bf16_f32 v147, v150, v151\n"
"s_lshl_b32 s74, s54, 16\n"
"s_add_u32 s74, s74, s11\n"
"s_add_u32 m0, s67, 65536\n"
"v_mfma_f32_16x16x32_bf16 v[160:163], a[184:187], v[24:27], v[160:163]\n"
"buffer_store_dwordx4 v[144:147], v194, s[60:63], s16 offen offset:1024 sc1\n"
"buffer_load_dwordx4 v205, s[40:43], s74 offen offset:0 lds\n"
"ds_read_b128 v[8:11], v221 offset:22528\n"
"ds_read_b64_tr_b16 a[28:29], v201 offset:16392\n"
"v_mfma_f32_16x16x32_bf16 v[164:167], a[188:191], v[24:27], v[164:167]\n"
"v_max_f32 v136, 0, v136\n"
"v_max_f32 v137, 0, v137\n"
"v_max_f32 v138, 0, v138\n"
"v_max_f32 v139, 0, v139\n"
"v_mfma_f32_16x16x32_bf16 v[168:171], a[184:187], v[28:31], v[168:171]\n"
"ds_read_b128 v[12:15], v221 offset:22784\n"
"ds_read_b64_tr_b16 a[30:31], v201 offset:16904\n"
"v_max_f32 v140, 0, v140\n"
"v_max_f32 v141, 0, v141\n"
"v_mfma_f32_16x16x32_bf16 v[172:175], a[188:191], v[28:31], v[172:175]\n"
"v_max_f32 v142, 0, v142\n"
"s_waitcnt vmcnt(18)\n"
"s_waitcnt lgkmcnt(15)\n"
"v_mfma_f32_16x16x32_bf16 v[160:163], a[192:195], v[32:35], v[160:163]\n"
"buffer_load_dwordx4 v206, s[40:43], s74 offen offset:1024 lds\n"
"ds_read_b128 v[16:19], v221 offset:24576\n"
"ds_read_b64_tr_b16 a[32:33], v200 offset:32768\n"
"v_mfma_f32_16x16x32_bf16 v[164:167], a[196:199], v[32:35], v[164:167]\n"
"v_max_f32 v143, 0, v143\n"
"v_mul_f32 v136, v136, v136\n"
"v_mul_f32 v137, v137, v137\n"
"s_waitcnt lgkmcnt(15)\n"
"v_mfma_f32_16x16x32_bf16 v[168:171], a[192:195], v[36:39], v[168:171]\n"
"ds_read_b128 v[20:23], v221 offset:24832\n"
"ds_read_b64_tr_b16 a[34:35], v200 offset:33280\n"
"v_mul_f32 v138, v138, v138\n"
"v_mfma_f32_16x16x32_bf16 v[172:175], a[196:199], v[36:39], v[172:175]\n"
"v_mul_f32 v139, v139, v139\n"
"v_mul_f32 v140, v140, v140\n"
"v_mul_f32 v141, v141, v141\n"
"s_waitcnt lgkmcnt(15)\n"
"v_mfma_f32_16x16x32_bf16 v[160:163], a[200:203], v[40:43], v[160:163]\n"
"buffer_load_dwordx4 v207, s[40:43], s74 offen offset:2048 lds\n"
"ds_read_b128 v[24:27], v221 offset:26624\n"
"ds_read_b64_tr_b16 a[36:37], v201 offset:32768\n"
"v_mfma_f32_16x16x32_bf16 v[164:167], a[204:207], v[40:43], v[164:167]\n"
"v_mul_f32 v142, v142, v142\n"
"v_mul_f32 v143, v143, v143\n"
"v_cvt_pk_bf16_f32 v136, v136, v137\n"
"s_waitcnt lgkmcnt(15)\n"
"v_mfma_f32_16x16x32_bf16 v[168:171], a[200:203], v[44:47], v[168:171]\n"
"ds_read_b128 v[28:31], v221 offset:26880\n"
"ds_read_b64_tr_b16 a[38:39], v201 offset:33280\n"
"v_cvt_pk_bf16_f32 v137, v138, v139\n"
"v_mfma_f32_16x16x32_bf16 v[172:175], a[204:207], v[44:47], v[172:175]\n"
"v_cvt_pk_bf16_f32 v138, v140, v141\n"
"v_cvt_pk_bf16_f32 v139, v142, v143\n"
"s_waitcnt lgkmcnt(15)\n"
"v_mfma_f32_16x16x32_bf16 v[160:163], a[208:211], v[0:3], v[160:163]\n"
"buffer_store_dwordx4 v[136:139], v194, s[60:63], s16 offen offset:2048 sc1\n"
"buffer_load_dwordx4 v208, s[40:43], s74 offen offset:3072 lds\n"
"ds_read_b128 v[32:35], v221 offset:28672\n"
"ds_read_b64_tr_b16 a[40:41], v200 offset:32776\n"
"v_mfma_f32_16x16x32_bf16 v[164:167], a[212:215], v[0:3], v[164:167]\n"
"v_max_f32 v152, 0, v152\n"
"v_max_f32 v153, 0, v153\n"
"v_max_f32 v154, 0, v154\n"
"v_max_f32 v155, 0, v155\n"
"s_waitcnt lgkmcnt(15)\n"
"v_mfma_f32_16x16x32_bf16 v[168:171], a[208:211], v[4:7], v[168:171]\n"
"ds_read_b128 v[36:39], v221 offset:28928\n"
"ds_read_b64_tr_b16 a[42:43], v200 offset:33288\n"
"v_max_f32 v156, 0, v156\n"
"v_mfma_f32_16x16x32_bf16 v[172:175], a[212:215], v[4:7], v[172:175]\n"
"v_max_f32 v157, 0, v157\n"
"v_max_f32 v158, 0, v158\n"
"s_waitcnt lgkmcnt(15)\n"
"v_mfma_f32_16x16x32_bf16 v[160:163], a[216:219], v[8:11], v[160:163]\n"
"ds_read_b128 v[40:43], v221 offset:30720\n"
"ds_read_b64_tr_b16 a[44:45], v201 offset:32776\n"
"v_max_f32 v159, 0, v159\n"
"v_mfma_f32_16x16x32_bf16 v[164:167], a[220:223], v[8:11], v[164:167]\n"
"v_mul_f32 v152, v152, v152\n"
"v_mul_f32 v153, v153, v153\n"
"s_waitcnt lgkmcnt(15)\n"
"v_mfma_f32_16x16x32_bf16 v[168:171], a[216:219], v[12:15], v[168:171]\n"
"ds_read_b128 v[44:47], v221 offset:30976\n"
"ds_read_b64_tr_b16 a[46:47], v201 offset:33288\n"
"v_mul_f32 v154, v154, v154\n"
"v_mul_f32 v155, v155, v155\n"
"v_mfma_f32_16x16x32_bf16 v[172:175], a[220:223], v[12:15], v[172:175]\n"
"v_mul_f32 v156, v156, v156\n"
"v_mul_f32 v157, v157, v157\n"
"s_waitcnt vmcnt(18)\n"
"s_waitcnt lgkmcnt(15)\n"
"v_mfma_f32_16x16x32_bf16 v[160:163], a[224:227], v[16:19], v[160:163]\n"
"ds_read_b128 v[0:3], v221 offset:32768\n"
"ds_read_b64_tr_b16 a[48:49], v200 offset:49152\n"
"v_mul_f32 v158, v158, v158\n"
"v_mfma_f32_16x16x32_bf16 v[164:167], a[228:231], v[16:19], v[164:167]\n"
"v_mul_f32 v159, v159, v159\n"
"v_cvt_pk_bf16_f32 v152, v152, v153\n"
"s_lshl_b32 s74, s48, 16\n"
"s_waitcnt lgkmcnt(15)\n"
"v_mfma_f32_16x16x32_bf16 v[168:171], a[224:227], v[20:23], v[168:171]\n"
"ds_read_b128 v[4:7], v221 offset:33024\n"
"ds_read_b64_tr_b16 a[50:51], v200 offset:49664\n"
"v_cvt_pk_bf16_f32 v153, v154, v155\n"
"v_cvt_pk_bf16_f32 v154, v156, v157\n"
"v_mfma_f32_16x16x32_bf16 v[172:175], a[228:231], v[20:23], v[172:175]\n"
"v_cvt_pk_bf16_f32 v155, v158, v159\n"
"s_add_u32 s74, s74, s11\n"
"s_add_u32 s74, s74, 0x8000\n"
"s_add_u32 m0, s67, 81920\n"
"s_waitcnt lgkmcnt(15)\n"
"v_mfma_f32_16x16x32_bf16 v[160:163], a[232:235], v[24:27], v[160:163]\n"
"buffer_store_dwordx4 v[152:155], v194, s[60:63], s16 offen offset:3072 sc1\n"
"buffer_load_dwordx4 v205, s[40:43], s74 offen offset:0 lds\n"
"ds_read_b128 v[8:11], v221 offset:34816\n"
"v_mfma_f32_16x16x32_bf16 v[164:167], a[236:239], v[24:27], v[164:167]\n"
"ds_read_b64_tr_b16 a[52:53], v201 offset:49152\n"
"s_waitcnt vmcnt(9)\n"
"v_min3_u32 v248, v81, v83, v85\n"
"v_max3_u32 v249, v81, v83, v85\n"
"s_waitcnt lgkmcnt(15)\n"
"v_mfma_f32_16x16x32_bf16 v[168:171], a[232:235], v[28:31], v[168:171]\n"
"Lst15_%=:\n"
"s_cbranch_execz Lback1_%=\n"
"ds_read_b128 v[12:15], v221 offset:35072\n"
"ds_read_b64_tr_b16 a[54:55], v201 offset:49664\n"
"v_mfma_f32_16x16x32_bf16 v[172:175], a[236:239], v[28:31], v[172:175]\n"
"v_min3_u32 v248, v248, v87, v87\n"
"s_waitcnt lgkmcnt(15)\n"
"v_mfma_f32_16x16x32_bf16 v[160:163], a[240:243], v[32:35], v[160:163]\n"
"buffer_load_dwordx4 v206, s[40:43], s74 offen offset:1024 lds\n"
"ds_read_b128 v[16:19], v221 offset:36864\n"
"ds_read_b64_tr_b16 a[56:57], v200 offset:49160\n"
"v_mfma_f32_16x16x32_bf16 v[164:167], a[244:247], v[32:35], v[164:167]\n"
"v_max3_u32 v249, v249, v87, v87\n"
"s_add_u32 s16, s16, 4096\n"
"s_waitcnt lgkmcnt(15)\n"
"v_mfma_f32_16x16x32_bf16 v[168:171], a[240:243], v[36:39], v[168:171]\n"
"ds_read_b128 v[20:23], v221 offset:37120\n"
"ds_read_b64_tr_b16 a[58:59], v200 offset:49672\n"
"v_mfma_f32_16x16x32_bf16 v[172:175], a[244:247], v[36:39], v[172:175]\n"
"v_xor_b32 v248, s36, v248\n"
"s_waitcnt lgkmcnt(15)\n"
"v_mfma_f32_16x16x32_bf16 v[160:163], a[248:251], v[40:43], v[160:163]\n"
"buffer_load_dwordx4 v207, s[40:43], s74 offen offset:2048 lds\n"
"ds_read_b128 v[24:27], v221 offset:38912\n"
"v_mfma_f32_16x16x32_bf16 v[164:167], a[252:255], v[40:43], v[164:167]\n"
"ds_read_b64_tr_b16 a[60:61], v201 offset:49160\n"
"v_xor_b32 v249, s36, v249\n"
"s_waitcnt lgkmcnt(15)\n"
"v_mfma_f32_16x16x32_bf16 v[168:171], a[248:251], v[44:47], v[168:171]\n"
"ds_read_b128 v[28:31], v221 offset:39168\n"
"ds_read_b64_tr_b16 a[62:63], v201 offset:49672\n"
"v_or_b32 v248, v248, v249\n"
"v_mfma_f32_16x16x32_bf16 v[172:175], a[252:255], v[44:47], v[172:175]\n"
"s_waitcnt lgkmcnt(15)\n"
"v_mfma_f32_16x16x32_bf16 v[176:179], a[128:131], v[0:3], v[120:123]\n"
"buffer_load_dwordx4 v208, s[40:43], s74 offen offset:3072 lds\n"
"ds_read_b128 v[32:35], v221 offset:40960\n"
"v_mfma_f32_16x16x32_bf16 v[180:183], a[132:135], v[0:3], v[124:127]\n"
"ds_read_b64_tr_b16 a[64:65], v202 offset:0\n"
"v_cmp_ne_u32 vcc, 0, v248\n"
"s_nop 1\n"
"s_cbranch_vccnz Lgbs_23_%=\n"
"Lgbr_24_%=:\n"
"s_waitcnt lgkmcnt(15)\n"
"v_mfma_f32_16x16x32_bf16 v[184:187], a[128:131], v[4:7], v[120:123]\n"
"ds_read_b128 v[36:39], v221 offset:41216\n"
"ds_read_b64_tr_b16 a[66:67], v202 offset:512\n"
"v_mfma_f32_16x16x32_bf16 v[188:191], a[132:135], v[4:7], v[124:127]\n"
"v_mov_b32 v227, v80\n"
"s_waitcnt lgkmcnt(15)\n"
"v_mfma_f32_16x16x32_bf16 v[176:179], a[136:139], v[8:11], v[176:179]\n"
"ds_read_b128 v[40:43], v221 offset:43008\n"
"ds_read_b64_tr_b16 a[68:69], v203 offset:0\n"
"v_mfma_f32_16x16x32_bf16 v[180:183], a[140:143], v[8:11], v[180:183]\n"
"v_mov_b32 v228, v82\n"
"s_waitcnt lgkmcnt(15)\n"
"v_mfma_f32_16x16x32_bf16 v[184:187], a[136:139], v[12:15], v[184:187]\n"
"ds_read_b128 v[44:47], v221 offset:43264\n"
"ds_read_b64_tr_b16 a[70:71], v203 offset:512\n"
"v_mfma_f32_16x16x32_bf16 v[188:191], a[140:143], v[12:15], v[188:191]\n"
"v_mov_b32 v229, v84\n"
"s_waitcnt lgkmcnt(15)\n"
"v_mfma_f32_16x16x32_bf16 v[176:179], a[144:147], v[16:19], v[176:179]\n"
"ds_read_b128 v[0:3], v221 offset:45056\n"
"ds_read_b64_tr_b16 a[72:73], v202 offset:8\n"
"v_mfma_f32_16x16x32_bf16 v[180:183], a[148:151], v[16:19], v[180:183]\n"
"v_mov_b32 v230, v86\n"
"s_lshl_b32 s74, s49, 16\n"
"s_waitcnt lgkmcnt(15)\n"
"v_mfma_f32_16x16x32_bf16 v[184:187], a[144:147], v[20:23], v[184:187]\n"
"ds_read_b128 v[4:7], v221 offset:45312\n"
"ds_read_b64_tr_b16 a[74:75], v202 offset:520\n"
"v_max_f32 v160, 0, v160\n"
"v_max_f32 v161, 0, v161\n"
"v_max_f32 v162, 0, v162\n"
"v_mfma_f32_16x16x32_bf16 v[188:191], a[148:151], v[20:23], v[188:191]\n"
"v_max_f32 v163, 0, v163\n"
"s_add_u32 s74, s74, s11\n"
"s_add_u32 s74, s74, 0x8000\n"
"s_add_u32 m0, s67, 98304\n"
"s_waitcnt lgkmcnt(15)\n"
"v_mfma_f32_16x16x32_bf16 v[176:179], a[152:155], v[24:27], v[176:179]\n"
"buffer_load_dwordx4 v205, s[40:43], s74 offen offset:0 lds\n"
"ds_read_b128 v[8:11], v221 offset:47104\n"
"ds_read_b64_tr_b16 a[76:77], v203 offset:8\n"
"v_mfma_f32_16x16x32_bf16 v[180:183], a[156:159], v[24:27], v[180:183]\n"
"v_max_f32 v164, 0, v164\n"
"v_max_f32 v165, 0, v165\n"
"v_max_f32 v166, 0, v166\n"
"s_waitcnt lgkmcnt(15)\n"
"v_mfma_f32_16x16x32_bf16 v[184:187], a[152:155], v[28:31], v[184:187]\n"
"ds_read_b128 v[12:15], v221 offset:47360\n"
"ds_read_b64_tr_b16 a[78:79], v203 offset:520\n"
"v_max_f32 v167, 0, v167\n"
"v_mfma_f32_16x16x32_bf16 v[188:191], a[156:159], v[28:31], v[188:191]\n"
"v_mul_f32 v160, v160, v160\n"
"v_mul_f32 v161, v161, v161\n"
"s_waitcnt lgkmcnt(15)\n"
"v_mfma_f32_16x16x32_bf16 v[176:179], a[160:163], v[32:35], v[176:179]\n"
"buffer_load_dwordx4 v206, s[40:43], s74 offen offset:1024 lds\n"
"ds_read_b128 v[16:19], v221 offset:49152\n"
"ds_read_b64_tr_b16 a[80:81], v202 offset:16384\n"
"v_mul_f32 v162, v162, v162\n"
"v_mfma_f32_16x16x32_bf16 v[180:183], a[164:167], v[32:35], v[180:183]\n"
"v_mul_f32 v163, v163, v163\n"
"v_mul_f32 v164, v164, v164\n"
"v_mul_f32 v165, v165, v165\n"
"s_waitcnt lgkmcnt(15)\n"
"v_mfma_f32_16x16x32_bf16 v[184:187], a[160:163], v[36:39], v[184:187]\n"
"ds_read_b128 v[20:23], v221 offset:49408\n"
"ds_read_b64_tr_b16 a[82:83], v202 offset:16896\n"
"v_mul_f32 v166, v166, v166\n"
"v_mfma_f32_16x16x32_bf16 v[188:191], a[164:167], v[36:39], v[188:191]\n"
"v_mul_f32 v167, v167, v167\n"
"v_cvt_pk_bf16_f32 v160, v160, v161\n"
"s_waitcnt lgkmcnt(15)\n"
"v_mfma_f32_16x16x32_bf16 v[176:179], a[168:171], v[40:43], v[176:179]\n"
"buffer_load_dwordx4 v207, s[40:43], s74 offen offset:2048 lds\n"
"ds_read_b128 v[24:27], v221 offset:51200\n"
"ds_read_b64_tr_b16 a[84:85], v203 offset:16384\n"
"v_mfma_f32_16x16x32_bf16 v[180:183], a[172:175], v[40:43], v[180:183]\n"
"v_cvt_pk_bf16_f32 v161, v162, v163\n"
"v_cvt_pk_bf16_f32 v162, v164, v165\n"
"v_cvt_pk_bf16_f32 v163, v166, v167\n"
"s_waitcnt lgkmcnt(15)\n"
"v_mfma_f32_16x16x32_bf16 v[184:187], a[168:171], v[44:47], v[184:187]\n"
"buffer_store_dwordx4 v[160:163], v194, s[60:63], s16 offen offset:0 sc1\n"
"ds_read_b128 v[28:31], v221 offset:51456\n"
"ds_read_b64_tr_b16 a[86:87], v203 offset:16896\n"
"v_max_f32 v168, 0, v168\n"
"v_mfma_f32_16x16x32_bf16 v[188:191], a[172:175], v[44:47], v[188:191]\n"
"v_max_f32 v169, 0, v169\n"
"v_max_f32 v170, 0, v170\n"
"v_max_f32 v171, 0, v171\n"
"s_waitcnt lgkmcnt(15)\n"
"v_mfma_f32_16x16x32_bf16 v[176:179], a[176:179], v[0:3], v[176:179]\n"
"buffer_load_dwordx4 v208, s[40:43], s74 offen offset:3072 lds\n"
"ds_read_b128 v[32:35], v221 offset:53248\n"
"ds_read_b64_tr_b16 a[88:89], v202 offset:16392\n"
"v_mfma_f32_16x16x32_bf16 v[180:183], a[180:183], v[0:3], v[180:183]\n"
"v_max_f32 v172, 0, v172\n"
"v_max_f32 v173, 0, v173\n"
"v_max_f32 v174, 0, v174\n"
"s_waitcnt lgkmcnt(15)\n"
"v_mfma_f32_16x16x32_bf16 v[184:187], a[176:179], v[4:7], v[184:187]\n"
"ds_read_b128 v[36:39], v221 offset:53504\n"
"ds_read_b64_tr_b16 a[90:91], v202 offset:16904\n"
"v_max_f32 v175, 0, v175\n"
"v_mfma_f32_16x16x32_bf16 v[188:191], a[180:183], v[4:7], v[188:191]\n"
"v_mul_f32 v168, v168, v168\n"
"v_mul_f32 v169, v169, v169\n"
"s_waitcnt lgkmcnt(15)\n"
"v_mfma_f32_16x16x32_bf16 v[176:179], a[184:187], v[8:11], v[176:179]\n"
"ds_read_b128 v[40:43], v221 offset:55296\n"
"ds_read_b64_tr_b16 a[92:93], v203 offset:16392\n"
"v_mul_f32 v170, v170, v170\n"
"v_mfma_f32_16x16x32_bf16 v[180:183], a[188:191], v[8:11], v[180:183]\n"
"v_mul_f32 v171, v171, v171\n"
"v_mul_f32 v172, v172, v172\n"
"v_mul_f32 v173, v173, v173\n"
"s_waitcnt lgkmcnt(15)\n"
"v_mfma_f32_16x16x32_bf16 v[184:187], a[184:187], v[12:15], v[184:187]\n"
"ds_read_b128 v[44:47], v221 offset:55552\n"
"ds_read_b64_tr_b16 a[94:95], v203 offset:16904\n"
"v_mul_f32 v174, v174, v174\n"
"v_mul_f32 v175, v175, v175\n"
"v_mfma_f32_16x16x32_bf16 v[188:191], a[188:191], v[12:15], v[188:191]\n"
"v_cvt_pk_bf16_f32 v168, v168, v169\n"
"s_waitcnt lgkmcnt(15)\n"
"v_mfma_f32_16x16x32_bf16 v[176:179], a[192:195], v[16:19], v[176:179]\n"
"ds_read_b128 v[0:3], v221 offset:57344\n"
"v_cvt_pk_bf16_f32 v169, v170, v171\n"
"v_cvt_pk_bf16_f32 v170, v172, v173\n"
"v_mfma_f32_16x16x32_bf16 v[180:183], a[196:199], v[16:19], v[180:183]\n"
"v_cvt_pk_bf16_f32 v171, v174, v175\n"
"s_lshl_b32 s74, s50, 16\n"
"s_waitcnt lgkmcnt(14)\n"
"v_mfma_f32_16x16x32_bf16 v[184:187], a[192:195], v[20:23], v[184:187]\n"
"buffer_store_dwordx4 v[168:171], v194, s[60:63], s16 offen offset:2048 sc1\n"
"ds_read_b128 v[4:7], v221 offset:57600\n"
"s_add_u32 s74, s74, s11\n"
"v_mfma_f32_16x16x32_bf16 v[188:191], a[196:199], v[20:23], v[188:191]\n"
"s_add_u32 s74, s74, 0x8000\n"
"s_add_u32 m0, s67, 114688\n"
"s_waitcnt lgkmcnt(13)\n"
"v_mfma_f32_16x16x32_bf16 v[176:179], a[200:203], v[24:27], v[176:179]\n"
"buffer_load_dwordx4 v205, s[40:43], s74 offen offset:0 lds\n"
"v_mfma_f32_16x16x32_bf16 v[180:183], a[204:207], v[24:27], v[180:183]\n"
"ds_read_b128 v[8:11], v221 offset:59392\n"
"s_waitcnt lgkmcnt(12)\n"
"v_mfma_f32_16x16x32_bf16 v[184:187], a[200:203], v[28:31], v[184:187]\n"
"ds_read_b128 v[12:15], v221 offset:59648\n"
"v_mfma_f32_16x16x32_bf16 v[188:191], a[204:207], v[28:31], v[188:191]\n"
"s_waitcnt vmcnt(12)\n"
"s_waitcnt lgkmcnt(11)\n"
"v_mfma_f32_16x16x32_bf16 v[176:179], a[208:211], v[32:35], v[176:179]\n"
"buffer_load_dwordx4 v206, s[40:43], s74 offen offset:1024 lds\n"
"ds_read_b128 v[16:19], v221 offset:61440\n"
"v_mfma_f32_16x16x32_bf16 v[180:183], a[212:215], v[32:35], v[180:183]\n"
"ds_read_b64_tr_b16 a[96:97], v200 offset:0\n"
"s_waitcnt lgkmcnt(11)\n"
"v_mfma_f32_16x16x32_bf16 v[184:187], a[208:211], v[36:39], v[184:187]\n"
"ds_read_b128 v[20:23], v221 offset:61696\n"
"v_mfma_f32_16x16x32_bf16 v[188:191], a[212:215], v[36:39], v[188:191]\n"
"ds_read_b64_tr_b16 a[98:99], v200 offset:512\n"
"s_waitcnt lgkmcnt(11)\n"
"v_mfma_f32_16x16x32_bf16 v[176:179], a[216:219], v[40:43], v[176:179]\n"
"buffer_load_dwordx4 v207, s[40:43], s74 offen offset:2048 lds\n"
"ds_read_b128 v[24:27], v221 offset:63488\n"
"v_mfma_f32_16x16x32_bf16 v[180:183], a[220:223], v[40:43], v[180:183]\n"
"ds_read_b64_tr_b16 a[100:101], v201 offset:0\n"
"s_waitcnt lgkmcnt(11)\n"
"v_mfma_f32_16x16x32_bf16 v[184:187], a[216:219], v[44:47], v[184:187]\n"
"ds_read_b128 v[28:31], v221 offset:63744\n"
"v_mfma_f32_16x16x32_bf16 v[188:191], a[220:223], v[44:47], v[188:191]\n"
"ds_read_b64_tr_b16 a[102:103], v201 offset:512\n"
"s_waitcnt lgkmcnt(11)\n"
"v_mfma_f32_16x16x32_bf16 v[176:179], a[224:227], v[0:3], v[176:179]\n"
"buffer_load_dwordx4 v208, s[40:43], s74 offen offset:3072 lds\n"
"v_mfma_f32_16x16x32_bf16 v[180:183], a[228:231], v[0:3], v[180:183]\n"
"ds_read_b64_tr_b16 a[104:105], v200 offset:8\n"
"s_waitcnt lgkmcnt(11)\n"
"v_mfma_f32_16x16x32_bf16 v[184:187], a[224:227], v[4:7], v[184:187]\n"
"ds_read_b64_tr_b16 a[106:107], v200 offset:520\n"
"v_mfma_f32_16x16x32_bf16 v[188:191], a[228:231], v[4:7], v[188:191]\n"
"s_waitcnt lgkmcnt(11)\n"
"v_mfma_f32_16x16x32_bf16 v[176:179], a[232:235], v[8:11], v[176:179]\n"
"ds_read_b64_tr_b16 a[108:109], v201 offset:8\n"
"v_mfma_f32_16x16x32_bf16 v[180:183], a[236:239], v[8:11], v[180:183]\n"
"s_waitcnt lgkmcnt(11)\n"
"v_mfma_f32_16x16x32_bf16 v[184:187], a[232:235], v[12:15], v[184:187]\n"
"ds_read_b64_tr_b16 a[110:111], v201 offset:520\n"
"v_mfma_f32_16x16x32_bf16 v[188:191], a[236:239], v[12:15], v[188:191]\n"
"s_waitcnt lgkmcnt(11)\n"
"v_mfma_f32_16x16x32_bf16 v[176:179], a[240:243], v[16:19], v[176:179]\n"
"v_mfma_f32_16x16x32_bf16 v[180:183], a[244:247], v[16:19], v[180:183]\n"
"s_waitcnt lgkmcnt(9)\n"
"v_mfma_f32_16x16x32_bf16 v[184:187], a[240:243], v[20:23], v[184:187]\n"
"v_mfma_f32_16x16x32_bf16 v[188:191], a[244:247], v[20:23], v[188:191]\n"
"s_waitcnt lgkmcnt(7)\n"
"v_mfma_f32_16x16x32_bf16 v[176:179], a[248:251], v[24:27], v[176:179]\n"
"v_mfma_f32_16x16x32_bf16 v[180:183], a[252:255], v[24:27], v[180:183]\n"
"s_waitcnt lgkmcnt(5)\n"
"v_mfma_f32_16x16x32_bf16 v[184:187], a[248:251], v[28:31], v[184:187]\n"
"v_mfma_f32_16x16x32_bf16 v[188:191], a[252:255], v[28:31], v[188:191]\n"
"s_nop 3\n"
"v_max_f32 v176, 0, v176\n"
"v_max_f32 v177, 0, v177\n"
"v_max_f32 v178, 0, v178\n"
"v_max_f32 v179, 0, v179\n"
"v_max_f32 v180, 0, v180\n"
"v_max_f32 v181, 0, v181\n"
"v_max_f32 v182, 0, v182\n"
"v_max_f32 v183, 0, v183\n"
"v_pk_mul_f32 v[176:177], v[176:177], v[176:177]\n"
"v_pk_mul_f32 v[178:179], v[178:179], v[178:179]\n"
"v_pk_mul_f32 v[180:181], v[180:181], v[180:181]\n"
"v_pk_mul_f32 v[182:183], v[182:183], v[182:183]\n"
"v_cvt_pk_bf16_f32 v176, v176, v177\n"
"v_cvt_pk_bf16_f32 v177, v178, v179\n"
"v_cvt_pk_bf16_f32 v178, v180, v181\n"
"v_cvt_pk_bf16_f32 v179, v182, v183\n"
"buffer_store_dwordx4 v[176:179], v194, s[60:63], s16 offen offset:1024 sc1\n"
"v_max_f32 v184, 0, v184\n"
"v_max_f32 v185, 0, v185\n"
"v_max_f32 v186, 0, v186\n"
"v_max_f32 v187, 0, v187\n"
"v_max_f32 v188, 0, v188\n"
"v_max_f32 v189, 0, v189\n"
"v_max_f32 v190, 0, v190\n"
"v_max_f32 v191, 0, v191\n"
"v_pk_mul_f32 v[184:185], v[184:185], v[184:185]\n"
"v_pk_mul_f32 v[186:187], v[186:187], v[186:187]\n"
"v_pk_mul_f32 v[188:189], v[188:189], v[188:189]\n"
"v_pk_mul_f32 v[190:191], v[190:191], v[190:191]\n"
"v_cvt_pk_bf16_f32 v184, v184, v185\n"
"v_cvt_pk_bf16_f32 v185, v186, v187\n"
"v_cvt_pk_bf16_f32 v186, v188, v189\n"
"v_cvt_pk_bf16_f32 v187, v190, v191\n"
"buffer_store_dwordx4 v[184:187], v194, s[60:63], s16 offen offset:3072 sc1\n"
"s_waitcnt lgkmcnt(0)\n"
"s_waitcnt vmcnt(12)\n"
"ds_read_b64_tr_b16 a[128:129], v200 offset:16384\n"
"ds_read_b64_tr_b16 a[130:131], v200 offset:16896\n"
"ds_read_b64_tr_b16 a[132:133], v201 offset:16384\n"
"ds_read_b64_tr_b16 a[134:135], v201 offset:16896\n"
"ds_read_b64_tr_b16 a[136:137], v200 offset:16392\n"
"ds_read_b64_tr_b16 a[138:139], v200 offset:16904\n"
"ds_read_b64_tr_b16 a[140:141], v201 offset:16392\n"
"ds_read_b64_tr_b16 a[142:143], v201 offset:16904\n"
"s_waitcnt vmcnt(7)\n"
"ds_read_b64_tr_b16 a[144:145], v200 offset:32768\n"
"ds_read_b64_tr_b16 a[146:147], v200 offset:33280\n"
"ds_read_b64_tr_b16 a[148:149], v201 offset:32768\n"
"ds_read_b64_tr_b16 a[150:151], v201 offset:33280\n"
"ds_read_b64_tr_b16 a[152:153], v200 offset:32776\n"
"ds_read_b64_tr_b16 a[154:155], v200 offset:33288\n"
"ds_read_b64_tr_b16 a[156:157], v201 offset:32776\n"
"ds_read_b64_tr_b16 a[158:159], v201 offset:33288\n"
"s_waitcnt vmcnt(2)\n"
"ds_read_b64_tr_b16 a[160:161], v200 offset:49152\n"
"ds_read_b64_tr_b16 a[162:163], v200 offset:49664\n"
"ds_read_b64_tr_b16 a[164:165], v201 offset:49152\n"
"ds_read_b64_tr_b16 a[166:167], v201 offset:49664\n"
"ds_read_b64_tr_b16 a[168:169], v200 offset:49160\n"
"ds_read_b64_tr_b16 a[170:171], v200 offset:49672\n"
"ds_read_b64_tr_b16 a[172:173], v201 offset:49160\n"
"ds_read_b64_tr_b16 a[174:175], v201 offset:49672\n"
"s_waitcnt vmcnt(0) lgkmcnt(0)\n"
"s_barrier\n"
"buffer_store_dword v199, v198, s[60:63], s55 offen sc1\n"
"s_lshl_b32 s74, s51, 16\n"
"s_add_u32 s74, s74, s11\n"
"s_add_u32 s74, s74, 0x8000\n"
"s_add_u32 m0, s67, 131072\n"
"s_nop 0\n"
"buffer_load_dwordx4 v205, s[40:43], s74 offen offset:0 lds\n"
"buffer_load_dwordx4 v206, s[40:43], s74 offen offset:1024 lds\n"
"buffer_load_dwordx4 v207, s[40:43], s74 offen offset:2048 lds\n"
"buffer_load_dwordx4 v208, s[40:43], s74 offen offset:3072 lds\n"
"s_lshl_b32 s74, s52, 16\n"
"s_add_u32 s74, s74, s11\n"
"s_add_u32 s74, s74, 0x8000\n"
"s_add_u32 m0, s67, 147456\n"
"s_nop 0\n"
"buffer_load_dwordx4 v205, s[40:43], s74 offen offset:0 lds\n"
"buffer_load_dwordx4 v206, s[40:43], s74 offen offset:1024 lds\n"
"buffer_load_dwordx4 v207, s[40:43], s74 offen offset:2048 lds\n"
"buffer_load_dwordx4 v208, s[40:43], s74 offen offset:3072 lds\n"
"s_lshl_b32 s74, s53, 16\n"
"s_add_u32 s74, s74, s11\n"
"s_add_u32 s74, s74, 0x8000\n"
"s_add_u32 m0, s67, 65536\n"
"s_nop 0\n"
"buffer_load_dwordx4 v205, s[40:43], s74 offen offset:0 lds\n"
"buffer_load_dwordx4 v206, s[40:43], s74 offen offset:1024 lds\n"
"buffer_load_dwordx4 v207, s[40:43], s74 offen offset:2048 lds\n"
"buffer_load_dwordx4 v208, s[40:43], s74 offen offset:3072 lds\n"
"s_lshl_b32 s74, s54, 16\n"
"s_add_u32 s74, s74, s11\n"
"s_add_u32 s74, s74, 0x8000\n"
"s_add_u32 m0, s67, 81920\n"
"s_nop 0\n"
"buffer_load_dwordx4 v205, s[40:43], s74 offen offset:0 lds\n"
"buffer_load_dwordx4 v206, s[40:43], s74 offen offset:1024 lds\n"
"buffer_load_dwordx4 v207, s[40:43], s74 offen offset:2048 lds\n"
"buffer_load_dwordx4 v208, s[40:43], s74 offen offset:3072 lds\n"
"buffer_load_dword v197, v193, s[60:63], s12 offen sc1\n"
"s_waitcnt vmcnt(0)\n"
"v_cmp_ne_u32 vcc, s36, v197\n"
"s_nop 1\n"
"s_nop 0\n"
"s_and_b64 vcc, vcc, s[20:21]\n"
"s_nop 1\n"
"s_cbranch_vccnz Lp3as_25_%=\n"
"Lp3ar_26_%=:\n"
"s_barrier\n"
"s_lshl_b32 s9, s48, 13\n"
"s_add_u32 s9, s9, s17\n"
"buffer_load_dwordx4 v[48:51], v225, s[60:63], s9 offen offset:0 sc1\n"
"buffer_load_dwordx4 v[52:55], v226, s[60:63], s9 offen offset:0 sc1\n"
"buffer_load_dwordx4 v[56:59], v225, s[60:63], s9 offen offset:2048 sc1\n"
"buffer_load_dwordx4 v[60:63], v226, s[60:63], s9 offen offset:2048 sc1\n"
"s_lshl_b32 s9, s49, 13\n"
"s_add_u32 s9, s9, s17\n"
"buffer_load_dwordx4 v[64:67], v225, s[60:63], s9 offen offset:0 sc1\n"
"buffer_load_dwordx4 v[68:71], v226, s[60:63], s9 offen offset:0 sc1\n"
"buffer_load_dwordx4 v[72:75], v225, s[60:63], s9 offen offset:2048 sc1\n"
"buffer_load_dwordx4 v[76:79], v226, s[60:63], s9 offen offset:2048 sc1\n"
"s_lshl_b32 s9, s50, 13\n"
"s_add_u32 s9, s9, s17\n"
"buffer_load_dwordx4 v[96:99], v225, s[60:63], s9 offen offset:0 sc1\n"
"buffer_load_dwordx4 v[100:103], v226, s[60:63], s9 offen offset:0 sc1\n"
"buffer_load_dwordx4 v[104:107], v225, s[60:63], s9 offen offset:2048 sc1\n"
"buffer_load_dwordx4 v[108:111], v226, s[60:63], s9 offen offset:2048 sc1\n"
"s_waitcnt vmcnt(8)\n"
"v_mfma_f32_16x16x32_bf16 v[128:131], a[0:3], v[48:51], 0\n"
"s_lshl_b32 s9, s51, 13\n"
"s_add_u32 s9, s9, s17\n"
"v_mfma_f32_16x16x32_bf16 v[132:135], a[8:11], v[48:51], 0\n"
"buffer_load_dwordx4 v[80:83], v225, s[60:63], s9 offen offset:0 sc1\n"
"v_mfma_f32_16x16x32_bf16 v[144:147], a[0:3], v[52:55], 0\n"
"buffer_load_dwordx4 v[84:87], v226, s[60:63], s9 offen offset:0 sc1\n"
"v_mfma_f32_16x16x32_bf16 v[148:151], a[8:11], v[52:55], 0\n"
"v_mfma_f32_16x16x32_bf16 v[160:163], a[4:7], v[48:51], 0\n"
"buffer_load_dwordx4 v[88:91], v225, s[60:63], s9 offen offset:2048 sc1\n"
"ds_read_b64_tr_b16 a[176:177], v202 offset:0\n"
"v_mfma_f32_16x16x32_bf16 v[164:167], a[12:15], v[48:51], 0\n"
"ds_read_b64_tr_b16 a[178:179], v202 offset:512\n"
"s_add_u32 s30, s6, s64\n"
"v_mfma_f32_16x16x32_bf16 v[176:179], a[4:7], v[52:55], 0\n"
"buffer_load_dwordx4 v[92:95], v226, s[60:63], s9 offen offset:2048 sc1\n"
"ds_read_b64_tr_b16 a[180:181], v203 offset:0\n"
"v_mfma_f32_16x16x32_bf16 v[180:183], a[12:15], v[52:55], 0\n"
"s_cmp_eq_u32 s30, 0\n"
"s_cselect_b32 s15, 0, 0x7000000\n"
"s_cselect_b64 s[20:21], -1, 0\n"
"v_mfma_f32_16x16x32_bf16 v[136:139], a[0:3], v[56:59], 0\n"
"ds_read_b64_tr_b16 a[182:183], v203 offset:512\n"
"v_mfma_f32_16x16x32_bf16 v[140:143], a[8:11], v[56:59], 0\n"
"v_lshlrev_b32 v116, 9, v192\n"
"v_mfma_f32_16x16x32_bf16 v[152:155], a[0:3], v[60:63], 0\n"
"s_lshl_b32 s9, s52, 13\n"
"s_add_u32 s9, s9, s17\n"
"buffer_load_dwordx4 v[0:3], v225, s[60:63], s9 offen offset:0 sc1\n"
"v_mfma_f32_16x16x32_bf16 v[156:159], a[8:11], v[60:63], 0\n"
"ds_read_b64_tr_b16 a[184:185], v202 offset:8\n"
"ds_read_b64_tr_b16 a[186:187], v202 offset:520\n"
"v_mfma_f32_16x16x32_bf16 v[168:171], a[4:7], v[56:59], 0\n"
"buffer_load_dwordx4 v[4:7], v226, s[60:63], s9 offen offset:0 sc1\n"
"v_mfma_f32_16x16x32_bf16 v[172:175], a[12:15], v[56:59], 0\n"
"ds_read_b64_tr_b16 a[188:189], v203 offset:8\n"
"v_mfma_f32_16x16x32_bf16 v[184:187], a[4:7], v[60:63], 0\n"
"buffer_load_dwordx4 v[8:11], v225, s[60:63], s9 offen offset:2048 sc1\n"
"ds_read_b64_tr_b16 a[190:191], v203 offset:520\n"
"v_mfma_f32_16x16x32_bf16 v[188:191], a[12:15], v[60:63], 0\n"
"s_waitcnt vmcnt(11)\n"
"v_mfma_f32_16x16x32_bf16 v[128:131], a[16:19], v[64:67], v[128:131]\n"
"buffer_load_dwordx4 v[12:15], v226, s[60:63], s9 offen offset:2048 sc1\n"
"v_mfma_f32_16x16x32_bf16 v[132:135], a[24:27], v[64:67], v[132:135]\n"
"v_mfma_f32_16x16x32_bf16 v[144:147], a[16:19], v[68:71], v[144:147]\n"
"v_mfma_f32_16x16x32_bf16 v[148:151], a[24:27], v[68:71], v[148:151]\n"
"v_mfma_f32_16x16x32_bf16 v[160:163], a[20:23], v[64:67], v[160:163]\n"
"s_lshl_b32 s9, s53, 13\n"
"s_add_u32 s9, s9, s17\n"
"buffer_load_dwordx4 v[16:19], v225, s[60:63], s9 offen offset:0 sc1\n"
"v_mfma_f32_16x16x32_bf16 v[164:167], a[28:31], v[64:67], v[164:167]\n"
"ds_read_b64_tr_b16 a[192:193], v202 offset:16384\n"
"ds_read_b64_tr_b16 a[194:195], v202 offset:16896\n"
"v_mfma_f32_16x16x32_bf16 v[176:179], a[20:23], v[68:71], v[176:179]\n"
"buffer_load_dwordx4 v[20:23], v226, s[60:63], s9 offen offset:0 sc1\n"
"v_mfma_f32_16x16x32_bf16 v[180:183], a[28:31], v[68:71], v[180:183]\n"
"ds_read_b64_tr_b16 a[196:197], v203 offset:16384\n"
"v_mfma_f32_16x16x32_bf16 v[136:139], a[16:19], v[72:75], v[136:139]\n"
"buffer_load_dwordx4 v[24:27], v225, s[60:63], s9 offen offset:2048 sc1\n"
"v_mfma_f32_16x16x32_bf16 v[140:143], a[24:27], v[72:75], v[140:143]\n"
"ds_read_b64_tr_b16 a[198:199], v203 offset:16896\n"
"v_mfma_f32_16x16x32_bf16 v[152:155], a[16:19], v[76:79], v[152:155]\n"
"buffer_load_dwordx4 v[28:31], v226, s[60:63], s9 offen offset:2048 sc1\n"
"ds_read_b64_tr_b16 a[200:201], v202 offset:16392\n"
"v_mfma_f32_16x16x32_bf16 v[156:159], a[24:27], v[76:79], v[156:159]\n"
"ds_read_b64_tr_b16 a[202:203], v202 offset:16904\n"
"v_mfma_f32_16x16x32_bf16 v[168:171], a[20:23], v[72:75], v[168:171]\n"
"ds_read_b64_tr_b16 a[204:205], v203 offset:16392\n"
"v_mfma_f32_16x16x32_bf16 v[172:175], a[28:31], v[72:75], v[172:175]\n"
"v_mfma_f32_16x16x32_bf16 v[184:187], a[20:23], v[76:79], v[184:187]\n"
"s_lshl_b32 s9, s54, 13\n"
"s_add_u32 s9, s9, s17\n"
"buffer_load_dwordx4 v[32:35], v225, s[60:63], s9 offen offset:0 sc1\n"
"v_mfma_f32_16x16x32_bf16 v[188:191], a[28:31], v[76:79], v[188:191]\n"
"ds_read_b64_tr_b16 a[206:207], v203 offset:16904\n"
"s_waitcnt vmcnt(13)\n"
"v_mfma_f32_16x16x32_bf16 v[128:131], a[32:35], v[96:99], v[128:131]\n"
"buffer_load_dwordx4 v[36:39], v226, s[60:63], s9 offen offset:0 sc1\n"
"v_mfma_f32_16x16x32_bf16 v[132:135], a[40:43], v[96:99], v[132:135]\n"
"v_mfma_f32_16x16x32_bf16 v[144:147], a[32:35], v[100:103], v[144:147]\n"
"buffer_load_dwordx4 v[40:43], v225, s[60:63], s9 offen offset:2048 sc1\n"
"v_mfma_f32_16x16x32_bf16 v[148:151], a[40:43], v[100:103], v[148:151]\n"
"v_mfma_f32_16x16x32_bf16 v[160:163], a[36:39], v[96:99], v[160:163]\n"
"buffer_load_dwordx4 v[44:47], v226, s[60:63], s9 offen offset:2048 sc1\n"
"ds_read_b64_tr_b16 a[208:209], v200 offset:0\n"
"v_mfma_f32_16x16x32_bf16 v[164:167], a[44:47], v[96:99], v[164:167]\n"
"ds_read_b64_tr_b16 a[210:211], v200 offset:512\n"
"v_mfma_f32_16x16x32_bf16 v[176:179], a[36:39], v[100:103], v[176:179]\n"
"ds_read_b64_tr_b16 a[212:213], v201 offset:0\n"
"v_mfma_f32_16x16x32_bf16 v[180:183], a[44:47], v[100:103], v[180:183]\n"
"v_mfma_f32_16x16x32_bf16 v[136:139], a[32:35], v[104:107], v[136:139]\n"
"ds_read_b64_tr_b16 a[214:215], v201 offset:512\n"
"v_mfma_f32_16x16x32_bf16 v[140:143], a[40:43], v[104:107], v[140:143]\n"
"v_mfma_f32_16x16x32_bf16 v[152:155], a[32:35], v[108:111], v[152:155]\n"
"ds_read_b64_tr_b16 a[216:217], v200 offset:8\n"
"v_mfma_f32_16x16x32_bf16 v[156:159], a[40:43], v[108:111], v[156:159]\n"
"ds_read_b64_tr_b16 a[218:219], v200 offset:520\n"
"v_mfma_f32_16x16x32_bf16 v[168:171], a[36:39], v[104:107], v[168:171]\n"
"ds_read_b64_tr_b16 a[220:221], v201 offset:8\n"
"v_mfma_f32_16x16x32_bf16 v[172:175], a[44:47], v[104:107], v[172:175]\n"
"v_mfma_f32_16x16x32_bf16 v[184:187], a[36:39], v[108:111], v[184:187]\n"
"s_add_u32 s17, s17, 4096\n"
"s_add_u32 s12, s12, 131072\n"
"ds_read_b64_tr_b16 a[222:223], v201 offset:520\n"
"v_mfma_f32_16x16x32_bf16 v[188:191], a[44:47], v[108:111], v[188:191]\n"
"s_waitcnt vmcnt(12)\n"
"v_mfma_f32_16x16x32_bf16 v[128:131], a[48:51], v[80:83], v[128:131]\n"
"s_lshl_b32 s9, s48, 13\n"
"s_add_u32 s9, s9, s17\n"
"v_mfma_f32_16x16x32_bf16 v[132:135], a[56:59], v[80:83], v[132:135]\n"
"buffer_load_dwordx4 v[48:51], v225, s[60:63], s9 offen offset:0 sc1\n"
"v_mfma_f32_16x16x32_bf16 v[144:147], a[48:51], v[84:87], v[144:147]\n"
"buffer_load_dwordx4 v[52:55], v226, s[60:63], s9 offen offset:0 sc1\n"
"v_mfma_f32_16x16x32_bf16 v[148:151], a[56:59], v[84:87], v[148:151]\n"
"v_mfma_f32_16x16x32_bf16 v[160:163], a[52:55], v[80:83], v[160:163]\n"
"buffer_load_dwordx4 v[56:59], v225, s[60:63], s9 offen offset:2048 sc1\n"
"ds_read_b64_tr_b16 a[224:225], v200 offset:16384\n"
"v_mfma_f32_16x16x32_bf16 v[164:167], a[60:63], v[80:83], v[164:167]\n"
"ds_read_b64_tr_b16 a[226:227], v200 offset:16896\n"
"v_mfma_f32_16x16x32_bf16 v[176:179], a[52:55], v[84:87], v[176:179]\n"
"buffer_load_dwordx4 v[60:63], v226, s[60:63], s9 offen offset:2048 sc1\n"
"v_mfma_f32_16x16x32_bf16 v[180:183], a[60:63], v[84:87], v[180:183]\n"
"ds_read_b64_tr_b16 a[228:229], v201 offset:16384\n"
"v_mfma_f32_16x16x32_bf16 v[136:139], a[48:51], v[88:91], v[136:139]\n"
"ds_read_b64_tr_b16 a[230:231], v201 offset:16896\n"
"v_mfma_f32_16x16x32_bf16 v[140:143], a[56:59], v[88:91], v[140:143]\n"
"v_mfma_f32_16x16x32_bf16 v[152:155], a[48:51], v[92:95], v[152:155]\n"
"s_lshl_b32 s9, s49, 13\n"
"s_add_u32 s9, s9, s17\n"
"buffer_load_dwordx4 v[64:67], v225, s[60:63], s9 offen offset:0 sc1\n"
"v_mfma_f32_16x16x32_bf16 v[156:159], a[56:59], v[92:95], v[156:159]\n"
"ds_read_b64_tr_b16 a[232:233], v200 offset:16392\n"
"ds_read_b64_tr_b16 a[234:235], v200 offset:16904\n"
"v_mfma_f32_16x16x32_bf16 v[168:171], a[52:55], v[88:91], v[168:171]\n"
"buffer_load_dwordx4 v[68:71], v226, s[60:63], s9 offen offset:0 sc1\n"
"v_mfma_f32_16x16x32_bf16 v[172:175], a[60:63], v[88:91], v[172:175]\n"
"ds_read_b64_tr_b16 a[236:237], v201 offset:16392\n"
"v_mfma_f32_16x16x32_bf16 v[184:187], a[52:55], v[92:95], v[184:187]\n"
"buffer_load_dwordx4 v[72:75], v225, s[60:63], s9 offen offset:2048 sc1\n"
"ds_read_b64_tr_b16 a[238:239], v201 offset:16904\n"
"v_mfma_f32_16x16x32_bf16 v[188:191], a[60:63], v[92:95], v[188:191]\n"
"s_waitcnt vmcnt(15)\n"
"v_mfma_f32_16x16x32_bf16 v[128:131], a[64:67], v[0:3], v[128:131]\n"
"buffer_load_dwordx4 v[76:79], v226, s[60:63], s9 offen offset:2048 sc1\n"
"v_mfma_f32_16x16x32_bf16 v[132:135], a[72:75], v[0:3], v[132:135]\n"
"v_mfma_f32_16x16x32_bf16 v[144:147], a[64:67], v[4:7], v[144:147]\n"
"v_mfma_f32_16x16x32_bf16 v[148:151], a[72:75], v[4:7], v[148:151]\n"
"v_mfma_f32_16x16x32_bf16 v[160:163], a[68:71], v[0:3], v[160:163]\n"
"s_lshl_b32 s9, s50, 13\n"
"s_add_u32 s9, s9, s17\n"
"v_mfma_f32_16x16x32_bf16 v[164:167], a[76:79], v[0:3], v[164:167]\n"
"buffer_load_dwordx4 v[96:99], v225, s[60:63], s9 offen offset:0 sc1\n"
"v_mfma_f32_16x16x32_bf16 v[176:179], a[68:71], v[4:7], v[176:179]\n"
"buffer_load_dwordx4 v[100:103], v226, s[60:63], s9 offen offset:0 sc1\n"
"v_mfma_f32_16x16x32_bf16 v[180:183], a[76:79], v[4:7], v[180:183]\n"
"v_mfma_f32_16x16x32_bf16 v[136:139], a[64:67], v[8:11], v[136:139]\n"
"buffer_load_dwordx4 v[104:107], v225, s[60:63], s9 offen offset:2048 sc1\n"
"v_mfma_f32_16x16x32_bf16 v[140:143], a[72:75], v[8:11], v[140:143]\n"
"v_mfma_f32_16x16x32_bf16 v[152:155], a[64:67], v[12:15], v[152:155]\n"
"buffer_load_dwordx4 v[108:111], v226, s[60:63], s9 offen offset:2048 sc1\n"
"v_mfma_f32_16x16x32_bf16 v[156:159], a[72:75], v[12:15], v[156:159]\n"
"v_mfma_f32_16x16x32_bf16 v[168:171], a[68:71], v[8:11], v[168:171]\n"
"v_mfma_f32_16x16x32_bf16 v[172:175], a[76:79], v[8:11], v[172:175]\n"
"v_mfma_f32_16x16x32_bf16 v[184:187], a[68:71], v[12:15], v[184:187]\n"
"s_lshl_b32 s9, s51, 13\n"
"s_add_u32 s9, s9, s17\n"
"buffer_load_dwordx4 v[80:83], v225, s[60:63], s9 offen offset:0 sc1\n"
"v_mfma_f32_16x16x32_bf16 v[188:191], a[76:79], v[12:15], v[188:191]\n"
"s_waitcnt vmcnt(17)\n"
"v_mfma_f32_16x16x32_bf16 v[128:131], a[80:83], v[16:19], v[128:131]\n"
"buffer_load_dwordx4 v[84:87], v226, s[60:63], s9 offen offset:0 sc1\n"
"v_mfma_f32_16x16x32_bf16 v[132:135], a[88:91], v[16:19], v[132:135]\n"
"v_mfma_f32_16x16x32_bf16 v[144:147], a[80:83], v[20:23], v[144:147]\n"
"buffer_load_dwordx4 v[88:91], v225, s[60:63], s9 offen offset:2048 sc1\n"
"v_mfma_f32_16x16x32_bf16 v[148:151], a[88:91], v[20:23], v[148:151]\n"
"v_mfma_f32_16x16x32_bf16 v[160:163], a[84:87], v[16:19], v[160:163]\n"
"buffer_load_dwordx4 v[92:95], v226, s[60:63], s9 offen offset:2048 sc1\n"
"v_mfma_f32_16x16x32_bf16 v[164:167], a[92:95], v[16:19], v[164:167]\n"
"v_mfma_f32_16x16x32_bf16 v[176:179], a[84:87], v[20:23], v[176:179]\n"
"v_mfma_f32_16x16x32_bf16 v[180:183], a[92:95], v[20:23], v[180:183]\n"
"v_mfma_f32_16x16x32_bf16 v[136:139], a[80:83], v[24:27], v[136:139]\n"
"s_lshl_b32 s9, s52, 13\n"
"s_add_u32 s9, s9, s17\n"
"v_mfma_f32_16x16x32_bf16 v[140:143], a[88:91], v[24:27], v[140:143]\n"
"buffer_load_dwordx4 v[0:3], v225, s[60:63], s9 offen offset:0 sc1\n"
"v_mfma_f32_16x16x32_bf16 v[152:155], a[80:83], v[28:31], v[152:155]\n"
"buffer_load_dwordx4 v[4:7], v226, s[60:63], s9 offen offset:0 sc1\n"
"v_mfma_f32_16x16x32_bf16 v[156:159], a[88:91], v[28:31], v[156:159]\n"
"v_mfma_f32_16x16x32_bf16 v[168:171], a[84:87], v[24:27], v[168:171]\n"
"buffer_load_dwordx4 v[8:11], v225, s[60:63], s9 offen offset:2048 sc1\n"
"v_mfma_f32_16x16x32_bf16 v[172:175], a[92:95], v[24:27], v[172:175]\n"
"v_mfma_f32_16x16x32_bf16 v[184:187], a[84:87], v[28:31], v[184:187]\n"
"buffer_load_dwordx4 v[12:15], v226, s[60:63], s9 offen offset:2048 sc1\n"
"s_waitcnt vmcnt(20)\n"
"v_mfma_f32_16x16x32_bf16 v[188:191], a[92:95], v[28:31], v[188:191]\n"
"v_mfma_f32_16x16x32_bf16 v[128:131], a[96:99], v[32:35], v[128:131]\n"
"v_mfma_f32_16x16x32_bf16 v[132:135], a[104:107], v[32:35], v[132:135]\n"
"v_mfma_f32_16x16x32_bf16 v[144:147], a[96:99], v[36:39], v[144:147]\n"
"s_lshl_b32 s9, s53, 13\n"
"s_add_u32 s9, s9, s17\n"
"v_mfma_f32_16x16x32_bf16 v[148:151], a[104:107], v[36:39], v[148:151]\n"
"buffer_load_dwordx4 v[16:19], v225, s[60:63], s9 offen offset:0 sc1\n"
"v_mfma_f32_16x16x32_bf16 v[160:163], a[100:103], v[32:35], v[160:163]\n"
"buffer_load_dwordx4 v[20:23], v226, s[60:63], s9 offen offset:0 sc1\n"
"v_mfma_f32_16x16x32_bf16 v[164:167], a[108:111], v[32:35], v[164:167]\n"
"v_mfma_f32_16x16x32_bf16 v[176:179], a[100:103], v[36:39], v[176:179]\n"
"buffer_load_dwordx4 v[24:27], v225, s[60:63], s9 offen offset:2048 sc1\n"
"v_mfma_f32_16x16x32_bf16 v[180:183], a[108:111], v[36:39], v[180:183]\n"
"v_mfma_f32_16x16x32_bf16 v[136:139], a[96:99], v[40:43], v[136:139]\n"
"buffer_load_dwordx4 v[28:31], v226, s[60:63], s9 offen offset:2048 sc1\n"
"v_mfma_f32_16x16x32_bf16 v[140:143], a[104:107], v[40:43], v[140:143]\n"
"v_mfma_f32_16x16x32_bf16 v[152:155], a[96:99], v[44:47], v[152:155]\n"
"v_mfma_f32_16x16x32_bf16 v[156:159], a[104:107], v[44:47], v[156:159]\n"
"v_mfma_f32_16x16x32_bf16 v[168:171], a[100:103], v[40:43], v[168:171]\n"
"buffer_load_dwordx4 v[236:239], v196, s[60:63], s8 offen sc1\n"
"v_mfma_f32_16x16x32_bf16 v[172:175], a[108:111], v[40:43], v[172:175]\n"
"v_mfma_f32_16x16x32_bf16 v[184:187], a[100:103], v[44:47], v[184:187]\n"
"Lst16_%=:\n"
"s_cbranch_execz Lback1_%=\n"
"buffer_load_dwordx4 v[240:243], v222, s[60:63], s8 offen sc1\n"
"s_waitcnt vmcnt(22)\n"
"v_mfma_f32_16x16x32_bf16 v[188:191], a[108:111], v[44:47], v[188:191]\n"
"v_mfma_f32_16x16x32_bf16 v[128:131], a[128:131], v[48:51], v[128:131]\n"
"s_lshl_b32 s9, s54, 13\n"
"s_add_u32 s9, s9, s17\n"
"v_mfma_f32_16x16x32_bf16 v[132:135], a[136:139], v[48:51], v[132:135]\n"
"buffer_load_dwordx4 v[32:35], v225, s[60:63], s9 offen offset:0 sc1\n"
"v_mfma_f32_16x16x32_bf16 v[144:147], a[128:131], v[52:55], v[144:147]\n"
"buffer_load_dwordx4 v[36:39], v226, s[60:63], s9 offen offset:0 sc1\n"
"v_mfma_f32_16x16x32_bf16 v[148:151], a[136:139], v[52:55], v[148:151]\n"
"v_mfma_f32_16x16x32_bf16 v[160:163], a[132:135], v[48:51], v[160:163]\n"
"buffer_load_dwordx4 v[40:43], v225, s[60:63], s9 offen offset:2048 sc1\n"
"v_and_b32 v196, 15, v192\n"
"v_mfma_f32_16x16x32_bf16 v[164:167], a[140:143], v[48:51], v[164:167]\n"
"v_lshl_add_u32 v196, v196, 10, v195\n"
"v_mfma_f32_16x16x32_bf16 v[176:179], a[132:135], v[52:55], v[176:179]\n"
"buffer_load_dwordx4 v[44:47], v226, s[60:63], s9 offen offset:2048 sc1\n"
"v_mfma_f32_16x16x32_bf16 v[180:183], a[140:143], v[52:55], v[180:183]\n"
"v_add_u32 v222, 16384, v196\n"
"v_mfma_f32_16x16x32_bf16 v[136:139], a[128:131], v[56:59], v[136:139]\n"
"v_add_u32 v195, s67, v194\n"
"v_mfma_f32_16x16x32_bf16 v[140:143], a[136:139], v[56:59], v[140:143]\n"
"v_mfma_f32_16x16x32_bf16 v[152:155], a[128:131], v[60:63], v[152:155]\n"
"v_add_u32 v195, 0x10000, v195\n"
"v_mfma_f32_16x16x32_bf16 v[156:159], a[136:139], v[60:63], v[156:159]\n"
"v_mfma_f32_16x16x32_bf16 v[168:171], a[132:135], v[56:59], v[168:171]\n"
"v_xor_b32 v252, 4096, v195\n"
"v_mfma_f32_16x16x32_bf16 v[172:175], a[140:143], v[56:59], v[172:175]\n"
"v_mfma_f32_16x16x32_bf16 v[184:187], a[132:135], v[60:63], v[184:187]\n"
"v_xor_b32 v253, 8192, v195\n"
"s_waitcnt vmcnt(22)\n"
"v_mfma_f32_16x16x32_bf16 v[188:191], a[140:143], v[60:63], v[188:191]\n"
"v_mfma_f32_16x16x32_bf16 v[128:131], a[144:147], v[64:67], v[128:131]\n"
"v_xor_b32 v254, 12288, v195\n"
"v_mfma_f32_16x16x32_bf16 v[132:135], a[152:155], v[64:67], v[132:135]\n"
"v_mfma_f32_16x16x32_bf16 v[144:147], a[144:147], v[68:71], v[144:147]\n"
"s_mov_b32 s56, s28\n"
"v_mfma_f32_16x16x32_bf16 v[148:151], a[152:155], v[68:71], v[148:151]\n"
"s_and_b32 s57, s29, 0xffff\n"
"v_mfma_f32_16x16x32_bf16 v[160:163], a[148:151], v[64:67], v[160:163]\n"
"s_mov_b32 s58, 0x200000\n"
"v_mfma_f32_16x16x32_bf16 v[164:167], a[156:159], v[64:67], v[164:167]\n"
"v_mfma_f32_16x16x32_bf16 v[176:179], a[148:151], v[68:71], v[176:179]\n"
"s_mov_b32 s59, 0x20000\n"
"v_mfma_f32_16x16x32_bf16 v[180:183], a[156:159], v[68:71], v[180:183]\n"
"v_mfma_f32_16x16x32_bf16 v[136:139], a[144:147], v[72:75], v[136:139]\n"
"s_lshl_b32 s8, s78, 16\n"
"v_mfma_f32_16x16x32_bf16 v[140:143], a[152:155], v[72:75], v[140:143]\n"
"v_mfma_f32_16x16x32_bf16 v[152:155], a[144:147], v[76:79], v[152:155]\n"
"s_and_b32 s10, s64, 1\n"
"v_mfma_f32_16x16x32_bf16 v[156:159], a[152:155], v[76:79], v[156:159]\n"
"v_mfma_f32_16x16x32_bf16 v[168:171], a[148:151], v[72:75], v[168:171]\n"
"s_lshl_b32 s10, s10, 15\n"
"v_mfma_f32_16x16x32_bf16 v[172:175], a[156:159], v[72:75], v[172:175]\n"
"v_mfma_f32_16x16x32_bf16 v[184:187], a[148:151], v[76:79], v[184:187]\n"
"s_add_u32 s8, s8, s10\n"
"s_waitcnt vmcnt(18)\n"
"v_mfma_f32_16x16x32_bf16 v[188:191], a[156:159], v[76:79], v[188:191]\n"
"v_mfma_f32_16x16x32_bf16 v[128:131], a[160:163], v[96:99], v[128:131]\n"
"s_add_u32 s8, s8, s14\n"
"v_mfma_f32_16x16x32_bf16 v[132:135], a[168:171], v[96:99], v[132:135]\n"
"v_mfma_f32_16x16x32_bf16 v[144:147], a[160:163], v[100:103], v[144:147]\n"
"v_mfma_f32_16x16x32_bf16 v[148:151], a[168:171], v[100:103], v[148:151]\n"
"v_mfma_f32_16x16x32_bf16 v[160:163], a[164:167], v[96:99], v[160:163]\n"
"v_mfma_f32_16x16x32_bf16 v[164:167], a[172:175], v[96:99], v[164:167]\n"
"v_mfma_f32_16x16x32_bf16 v[176:179], a[164:167], v[100:103], v[176:179]\n"
"v_mfma_f32_16x16x32_bf16 v[180:183], a[172:175], v[100:103], v[180:183]\n"
"v_mfma_f32_16x16x32_bf16 v[136:139], a[160:163], v[104:107], v[136:139]\n"
"v_mfma_f32_16x16x32_bf16 v[140:143], a[168:171], v[104:107], v[140:143]\n"
"v_mfma_f32_16x16x32_bf16 v[152:155], a[160:163], v[108:111], v[152:155]\n"
"v_mfma_f32_16x16x32_bf16 v[156:159], a[168:171], v[108:111], v[156:159]\n"
"v_mfma_f32_16x16x32_bf16 v[168:171], a[164:167], v[104:107], v[168:171]\n"
"v_mfma_f32_16x16x32_bf16 v[172:175], a[172:175], v[104:107], v[172:175]\n"
"v_mfma_f32_16x16x32_bf16 v[184:187], a[164:167], v[108:111], v[184:187]\n"
"s_waitcnt vmcnt(14)\n"
"v_mfma_f32_16x16x32_bf16 v[188:191], a[172:175], v[108:111], v[188:191]\n"
"s_waitcnt lgkmcnt(15)\n"
"v_mfma_f32_16x16x32_bf16 v[128:131], a[176:179], v[80:83], v[128:131]\n"
"v_lshlrev_b32 v48, 16, v244\n"
"v_mfma_f32_16x16x32_bf16 v[132:135], a[184:187], v[80:83], v[132:135]\n"
"v_and_b32 v49, 0xffff0000, v244\n"
"v_mfma_f32_16x16x32_bf16 v[144:147], a[176:179], v[84:87], v[144:147]\n"
"v_lshlrev_b32 v50, 16, v245\n"
"v_mfma_f32_16x16x32_bf16 v[148:151], a[184:187], v[84:87], v[148:151]\n"
"v_and_b32 v51, 0xffff0000, v245\n"
"v_mfma_f32_16x16x32_bf16 v[160:163], a[180:183], v[80:83], v[160:163]\n"
"v_lshlrev_b32 v52, 16, v246\n"
"v_mfma_f32_16x16x32_bf16 v[164:167], a[188:191], v[80:83], v[164:167]\n"
"v_mfma_f32_16x16x32_bf16 v[176:179], a[180:183], v[84:87], v[176:179]\n"
"v_and_b32 v53, 0xffff0000, v246\n"
"v_mfma_f32_16x16x32_bf16 v[180:183], a[188:191], v[84:87], v[180:183]\n"
"v_lshlrev_b32 v54, 16, v247\n"
"v_mfma_f32_16x16x32_bf16 v[136:139], a[176:179], v[88:91], v[136:139]\n"
"s_add_u32 s30, s15, 1048576\n"
"buffer_load_dword v112, v116, s[60:63], s30 offen sc1\n"
"v_mfma_f32_16x16x32_bf16 v[140:143], a[184:187], v[88:91], v[140:143]\n"
"v_and_b32 v55, 0xffff0000, v247\n"
"v_mfma_f32_16x16x32_bf16 v[152:155], a[176:179], v[92:95], v[152:155]\n"
"s_add_u32 s30, s15, 1081344\n"
"buffer_load_dword v113, v116, s[60:63], s30 offen sc1\n"
"v_mfma_f32_16x16x32_bf16 v[156:159], a[184:187], v[92:95], v[156:159]\n"
"v_lshlrev_b32 v64, 16, v227\n"
"v_and_b32 v65, 0xffff0000, v227\n"
"v_mfma_f32_16x16x32_bf16 v[168:171], a[180:183], v[88:91], v[168:171]\n"
"s_add_u32 s30, s15, 1114112\n"
"buffer_load_dword v114, v116, s[60:63], s30 offen sc1\n"
"v_mfma_f32_16x16x32_bf16 v[172:175], a[188:191], v[88:91], v[172:175]\n"
"v_lshlrev_b32 v66, 16, v228\n"
"v_mfma_f32_16x16x32_bf16 v[184:187], a[180:183], v[92:95], v[184:187]\n"
"s_add_u32 s30, s15, 1146880\n"
"buffer_load_dword v115, v116, s[60:63], s30 offen sc1\n"
"v_and_b32 v67, 0xffff0000, v228\n"
"v_mfma_f32_16x16x32_bf16 v[188:191], a[188:191], v[92:95], v[188:191]\n"
"v_lshlrev_b32 v68, 16, v229\n"
"s_waitcnt vmcnt(14)\n"
"v_mfma_f32_16x16x32_bf16 v[128:131], a[192:195], v[0:3], v[128:131]\n"
"v_and_b32 v69, 0xffff0000, v229\n"
"v_mfma_f32_16x16x32_bf16 v[132:135], a[200:203], v[0:3], v[132:135]\n"
"v_mfma_f32_16x16x32_bf16 v[144:147], a[192:195], v[4:7], v[144:147]\n"
"v_lshlrev_b32 v70, 16, v230\n"
"v_mfma_f32_16x16x32_bf16 v[148:151], a[200:203], v[4:7], v[148:151]\n"
"v_and_b32 v71, 0xffff0000, v230\n"
"v_mfma_f32_16x16x32_bf16 v[160:163], a[196:199], v[0:3], v[160:163]\n"
"s_waitcnt vmcnt(9)\n"
"v_lshlrev_b32 v96, 16, v236\n"
"v_mfma_f32_16x16x32_bf16 v[164:167], a[204:207], v[0:3], v[164:167]\n"
"v_mfma_f32_16x16x32_bf16 v[176:179], a[196:199], v[4:7], v[176:179]\n"
"v_and_b32 v97, 0xffff0000, v236\n"
"v_mfma_f32_16x16x32_bf16 v[180:183], a[204:207], v[4:7], v[180:183]\n"
"v_lshlrev_b32 v98, 16, v237\n"
"v_mfma_f32_16x16x32_bf16 v[136:139], a[192:195], v[8:11], v[136:139]\n"
"v_and_b32 v99, 0xffff0000, v237\n"
"v_mfma_f32_16x16x32_bf16 v[140:143], a[200:203], v[8:11], v[140:143]\n"
"v_mfma_f32_16x16x32_bf16 v[152:155], a[192:195], v[12:15], v[152:155]\n"
"v_lshlrev_b32 v100, 16, v238\n"
"v_mfma_f32_16x16x32_bf16 v[156:159], a[200:203], v[12:15], v[156:159]\n"
"v_and_b32 v101, 0xffff0000, v238\n"
"v_mfma_f32_16x16x32_bf16 v[168:171], a[196:199], v[8:11], v[168:171]\n"
"v_lshlrev_b32 v102, 16, v239\n"
"v_mfma_f32_16x16x32_bf16 v[172:175], a[204:207], v[8:11], v[172:175]\n"
"v_mfma_f32_16x16x32_bf16 v[184:187], a[196:199], v[12:15], v[184:187]\n"
"v_and_b32 v103, 0xffff0000, v239\n"
"s_waitcnt vmcnt(8)\n"
"v_lshlrev_b32 v104, 16, v240\n"
"v_mfma_f32_16x16x32_bf16 v[188:191], a[204:207], v[12:15], v[188:191]\n"
"s_waitcnt vmcnt(4) lgkmcnt(0)\n"
"v_mfma_f32_16x16x32_bf16 v[144:147], a[208:211], v[20:23], v[144:147]\n"
"v_and_b32 v105, 0xffff0000, v240\n"
"v_mfma_f32_16x16x32_bf16 v[148:151], a[216:219], v[20:23], v[148:151]\n"
"v_mfma_f32_16x16x32_bf16 v[152:155], a[208:211], v[28:31], v[152:155]\n"
"v_lshlrev_b32 v106, 16, v241\n"
"v_mfma_f32_16x16x32_bf16 v[156:159], a[216:219], v[28:31], v[156:159]\n"
"v_and_b32 v107, 0xffff0000, v241\n"
"v_mfma_f32_16x16x32_bf16 v[144:147], a[224:227], v[36:39], v[144:147]\n"
"v_lshlrev_b32 v108, 16, v242\n"
"v_mfma_f32_16x16x32_bf16 v[148:151], a[232:235], v[36:39], v[148:151]\n"
"v_mfma_f32_16x16x32_bf16 v[152:155], a[224:227], v[44:47], v[152:155]\n"
"v_and_b32 v109, 0xffff0000, v242\n"
"v_mfma_f32_16x16x32_bf16 v[156:159], a[232:235], v[44:47], v[156:159]\n"
"v_lshlrev_b32 v110, 16, v243\n"
"v_mfma_f32_16x16x32_bf16 v[160:163], a[212:215], v[16:19], v[160:163]\n"
"v_and_b32 v111, 0xffff0000, v243\n"
"v_mfma_f32_16x16x32_bf16 v[164:167], a[220:223], v[16:19], v[164:167]\n"
"v_mfma_f32_16x16x32_bf16 v[168:171], a[212:215], v[24:27], v[168:171]\n"
"v_mfma_f32_16x16x32_bf16 v[172:175], a[220:223], v[24:27], v[172:175]\n"
"v_mfma_f32_16x16x32_bf16 v[160:163], a[228:231], v[32:35], v[160:163]\n"
"ds_write_b128 v195, v[144:147] offset:16384\n"
"v_mfma_f32_16x16x32_bf16 v[164:167], a[236:239], v[32:35], v[164:167]\n"
"v_mfma_f32_16x16x32_bf16 v[168:171], a[228:231], v[40:43], v[168:171]\n"
"ds_write_b128 v195, v[148:151] offset:17408\n"
"v_mfma_f32_16x16x32_bf16 v[172:175], a[236:239], v[40:43], v[172:175]\n"
"v_mfma_f32_16x16x32_bf16 v[176:179], a[212:215], v[20:23], v[176:179]\n"
"ds_write_b128 v195, v[152:155] offset:18432\n"
"v_mfma_f32_16x16x32_bf16 v[180:183], a[220:223], v[20:23], v[180:183]\n"
"v_mfma_f32_16x16x32_bf16 v[184:187], a[212:215], v[28:31], v[184:187]\n"
"ds_write_b128 v195, v[156:159] offset:19456\n"
"v_mfma_f32_16x16x32_bf16 v[188:191], a[220:223], v[28:31], v[188:191]\n"
"v_mfma_f32_16x16x32_bf16 v[176:179], a[228:231], v[36:39], v[176:179]\n"
"ds_write_b128 v195, v[160:163] offset:32768\n"
"v_mfma_f32_16x16x32_bf16 v[180:183], a[236:239], v[36:39], v[180:183]\n"
"v_mfma_f32_16x16x32_bf16 v[184:187], a[228:231], v[44:47], v[184:187]\n"
"ds_write_b128 v195, v[164:167] offset:33792\n"
"v_mfma_f32_16x16x32_bf16 v[188:191], a[236:239], v[44:47], v[188:191]\n"
"v_mfma_f32_16x16x32_bf16 v[128:131], a[208:211], v[16:19], v[128:131]\n"
"ds_write_b128 v195, v[168:171] offset:34816\n"
"v_mfma_f32_16x16x32_bf16 v[132:135], a[216:219], v[16:19], v[132:135]\n"
"v_mfma_f32_16x16x32_bf16 v[136:139], a[208:211], v[24:27], v[136:139]\n"
"ds_write_b128 v195, v[172:175] offset:35840\n"
"v_mfma_f32_16x16x32_bf16 v[140:143], a[216:219], v[24:27], v[140:143]\n"
"v_mfma_f32_16x16x32_bf16 v[128:131], a[224:227], v[32:35], v[128:131]\n"
"ds_write_b128 v195, v[176:179] offset:49152\n"
"v_mfma_f32_16x16x32_bf16 v[132:135], a[232:235], v[32:35], v[132:135]\n"
"ds_write_b128 v195, v[180:183] offset:50176\n"
"v_mfma_f32_16x16x32_bf16 v[136:139], a[224:227], v[40:43], v[136:139]\n"
"ds_write_b128 v195, v[184:187] offset:51200\n"
"v_mfma_f32_16x16x32_bf16 v[140:143], a[232:235], v[40:43], v[140:143]\n"
"ds_write_b128 v195, v[188:191] offset:52224\n"
"s_waitcnt lgkmcnt(0)\n"
"s_barrier\n"
"ds_read_b128 v[0:3], v252 offset:16384\n"
"ds_read_b128 v[4:7], v252 offset:17408\n"
"ds_read_b128 v[8:11], v252 offset:18432\n"
"ds_read_b128 v[12:15], v252 offset:19456\n"
"ds_read_b128 v[16:19], v253 offset:32768\n"
"ds_read_b128 v[20:23], v253 offset:33792\n"
"ds_read_b128 v[24:27], v253 offset:34816\n"
"ds_read_b128 v[28:31], v253 offset:35840\n"
"ds_read_b128 v[32:35], v254 offset:49152\n"
"ds_read_b128 v[36:39], v254 offset:50176\n"
"ds_read_b128 v[40:43], v254 offset:51200\n"
"ds_read_b128 v[44:47], v254 offset:52224\n"
"s_waitcnt vmcnt(1)\n"
"v_min3_u32 v118, v112, v113, v114\n"
"v_max3_u32 v119, v112, v113, v114\n"
"s_waitcnt vmcnt(0)\n"
"v_min3_u32 v118, v118, v115, v115\n"
"v_max3_u32 v119, v119, v115, v115\n"
"v_xor_b32 v118, s36, v118\n"
"v_xor_b32 v119, s36, v119\n"
"v_or_b32 v118, v118, v119\n"
"v_cmp_ne_u32 vcc, 0, v118\n"
"s_nop 1\n"
"s_nop 0\n"
"s_and_b64 vcc, vcc, s[20:21]\n"
"s_nop 1\n"
"s_cbranch_vccnz Lregs_27_%=\n"
"Lregr_28_%=:\n"
"buffer_store_dword v199, v198, s[60:63], s15 offen offset:0 sc1\n"
"s_waitcnt lgkmcnt(11)\n"
"v_pk_add_f32 v[128:129], v[128:129], v[0:1]\n"
"v_pk_add_f32 v[130:131], v[130:131], v[2:3]\n"
"s_waitcnt lgkmcnt(10)\n"
"v_pk_add_f32 v[132:133], v[132:133], v[4:5]\n"
"v_pk_add_f32 v[134:135], v[134:135], v[6:7]\n"
"s_waitcnt lgkmcnt(9)\n"
"v_pk_add_f32 v[136:137], v[136:137], v[8:9]\n"
"v_pk_add_f32 v[138:139], v[138:139], v[10:11]\n"
"s_waitcnt lgkmcnt(8)\n"
"v_pk_add_f32 v[140:141], v[140:141], v[12:13]\n"
"v_pk_add_f32 v[142:143], v[142:143], v[14:15]\n"
"s_waitcnt lgkmcnt(7)\n"
"v_pk_add_f32 v[128:129], v[128:129], v[16:17]\n"
"v_pk_add_f32 v[130:131], v[130:131], v[18:19]\n"
"s_waitcnt lgkmcnt(6)\n"
"v_pk_add_f32 v[132:133], v[132:133], v[20:21]\n"
"v_pk_add_f32 v[134:135], v[134:135], v[22:23]\n"
"s_waitcnt lgkmcnt(5)\n"
"v_pk_add_f32 v[136:137], v[136:137], v[24:25]\n"
"v_pk_add_f32 v[138:139], v[138:139], v[26:27]\n"
"s_waitcnt lgkmcnt(4)\n"
"v_pk_add_f32 v[140:141], v[140:141], v[28:29]\n"
"v_pk_add_f32 v[142:143], v[142:143], v[30:31]\n"
"s_waitcnt lgkmcnt(3)\n"
"v_pk_add_f32 v[128:129], v[128:129], v[32:33]\n"
"v_pk_add_f32 v[130:131], v[130:131], v[34:35]\n"
"s_waitcnt lgkmcnt(2)\n"
"v_pk_add_f32 v[132:133], v[132:133], v[36:37]\n"
"v_pk_add_f32 v[134:135], v[134:135], v[38:39]\n"
"s_waitcnt lgkmcnt(1)\n"
"v_pk_add_f32 v[136:137], v[136:137], v[40:41]\n"
"v_pk_add_f32 v[138:139], v[138:139], v[42:43]\n"
"s_waitcnt lgkmcnt(0)\n"
"v_pk_add_f32 v[140:141], v[140:141], v[44:45]\n"
"v_pk_add_f32 v[142:143], v[142:143], v[46:47]\n"
"v_pk_mul_f32 v[80:81], v[128:129], s[70:71] op_sel_hi:[1,0]\n"
"v_pk_mul_f32 v[82:83], v[130:131], s[70:71] op_sel_hi:[1,0]\n"
"v_pk_mul_f32 v[84:85], v[132:133], s[70:71] op_sel_hi:[1,0]\n"
"v_pk_mul_f32 v[86:87], v[134:135], s[70:71] op_sel_hi:[1,0]\n"
"v_pk_mul_f32 v[88:89], v[136:137], s[70:71] op_sel_hi:[1,0]\n"
"v_pk_mul_f32 v[90:91], v[138:139], s[70:71] op_sel_hi:[1,0]\n"
"v_pk_mul_f32 v[92:93], v[140:141], s[70:71] op_sel_hi:[1,0]\n"
"v_pk_mul_f32 v[94:95], v[142:143], s[70:71] op_sel_hi:[1,0]\n"
"v_pk_fma_f32 v[128:129], v[128:129], s[70:71], v[80:81] op_sel:[0,1,0] op_sel_hi:[1,1,1]\n"
"v_pk_fma_f32 v[130:131], v[130:131], s[70:71], v[82:83] op_sel:[0,1,0] op_sel_hi:[1,1,1]\n"
"v_pk_fma_f32 v[132:133], v[132:133], s[70:71], v[84:85] op_sel:[0,1,0] op_sel_hi:[1,1,1]\n"
"v_pk_fma_f32 v[134:135], v[134:135], s[70:71], v[86:87] op_sel:[0,1,0] op_sel_hi:[1,1,1]\n"
"v_pk_fma_f32 v[136:137], v[136:137], s[70:71], v[88:89] op_sel:[0,1,0] op_sel_hi:[1,1,1]\n"
"v_pk_fma_f32 v[138:139], v[138:139], s[70:71], v[90:91] op_sel:[0,1,0] op_sel_hi:[1,1,1]\n"
"v_pk_fma_f32 v[140:141], v[140:141], s[70:71], v[92:93] op_sel:[0,1,0] op_sel_hi:[1,1,1]\n"
"v_pk_fma_f32 v[142:143], v[142:143], s[70:71], v[94:95] op_sel:[0,1,0] op_sel_hi:[1,1,1]\n"
"v_pk_add_f32 v[128:129], v[128:129], v[48:49]\n"
"v_pk_add_f32 v[130:131], v[130:131], v[50:51]\n"
"v_pk_add_f32 v[132:133], v[132:133], v[52:53]\n"
"v_pk_add_f32 v[134:135], v[134:135], v[54:55]\n"
"v_pk_add_f32 v[136:137], v[136:137], v[48:49]\n"
"v_pk_add_f32 v[138:139], v[138:139], v[50:51]\n"
"v_pk_add_f32 v[140:141], v[140:141], v[52:53]\n"
"v_pk_add_f32 v[142:143], v[142:143], v[54:55]\n"
"v_pk_mul_f32 v[80:81], v[128:129], s[70:71] op_sel_hi:[1,0]\n"
"v_pk_mul_f32 v[82:83], v[130:131], s[70:71] op_sel_hi:[1,0]\n"
"v_pk_mul_f32 v[84:85], v[132:133], s[70:71] op_sel_hi:[1,0]\n"
"v_pk_mul_f32 v[86:87], v[134:135], s[70:71] op_sel_hi:[1,0]\n"
"v_pk_mul_f32 v[88:89], v[136:137], s[70:71] op_sel_hi:[1,0]\n"
"v_pk_mul_f32 v[90:91], v[138:139], s[70:71] op_sel_hi:[1,0]\n"
"v_pk_mul_f32 v[92:93], v[140:141], s[70:71] op_sel_hi:[1,0]\n"
"v_pk_mul_f32 v[94:95], v[142:143], s[70:71] op_sel_hi:[1,0]\n"
"v_pk_fma_f32 v[128:129], v[128:129], s[70:71], v[80:81] op_sel:[0,1,0] op_sel_hi:[1,1,1]\n"
"v_pk_fma_f32 v[130:131], v[130:131], s[70:71], v[82:83] op_sel:[0,1,0] op_sel_hi:[1,1,1]\n"
"v_pk_fma_f32 v[132:133], v[132:133], s[70:71], v[84:85] op_sel:[0,1,0] op_sel_hi:[1,1,1]\n"
"v_pk_fma_f32 v[134:135], v[134:135], s[70:71], v[86:87] op_sel:[0,1,0] op_sel_hi:[1,1,1]\n"
"v_pk_fma_f32 v[136:137], v[136:137], s[70:71], v[88:89] op_sel:[0,1,0] op_sel_hi:[1,1,1]\n"
"v_pk_fma_f32 v[138:139], v[138:139], s[70:71], v[90:91] op_sel:[0,1,0] op_sel_hi:[1,1,1]\n"
"v_pk_fma_f32 v[140:141], v[140:141], s[70:71], v[92:93] op_sel:[0,1,0] op_sel_hi:[1,1,1]\n"
"v_pk_fma_f32 v[142:143], v[142:143], s[70:71], v[94:95] op_sel:[0,1,0] op_sel_hi:[1,1,1]\n"
"v_pk_mul_f32 v[128:129], v[128:129], v[64:65]\n"
"v_pk_mul_f32 v[130:131], v[130:131], v[66:67]\n"
"v_pk_mul_f32 v[132:133], v[132:133], v[68:69]\n"
"v_pk_mul_f32 v[134:135], v[134:135], v[70:71]\n"
"v_pk_mul_f32 v[136:137], v[136:137], v[64:65]\n"
"v_pk_mul_f32 v[138:139], v[138:139], v[66:67]\n"
"v_pk_mul_f32 v[140:141], v[140:141], v[68:69]\n"
"v_pk_mul_f32 v[142:143], v[142:143], v[70:71]\n"
"v_pk_mul_f32 v[80:81], v[128:129], s[70:71] op_sel_hi:[1,0]\n"
"v_pk_mul_f32 v[82:83], v[130:131], s[70:71] op_sel_hi:[1,0]\n"
"v_pk_mul_f32 v[84:85], v[132:133], s[70:71] op_sel_hi:[1,0]\n"
"v_pk_mul_f32 v[86:87], v[134:135], s[70:71] op_sel_hi:[1,0]\n"
"v_pk_mul_f32 v[88:89], v[136:137], s[70:71] op_sel_hi:[1,0]\n"
"v_pk_mul_f32 v[90:91], v[138:139], s[70:71] op_sel_hi:[1,0]\n"
"v_pk_mul_f32 v[92:93], v[140:141], s[70:71] op_sel_hi:[1,0]\n"
"v_pk_mul_f32 v[94:95], v[142:143], s[70:71] op_sel_hi:[1,0]\n"
"v_pk_fma_f32 v[128:129], v[128:129], s[70:71], v[80:81] op_sel:[0,1,0] op_sel_hi:[1,1,1]\n"
"v_pk_fma_f32 v[130:131], v[130:131], s[70:71], v[82:83] op_sel:[0,1,0] op_sel_hi:[1,1,1]\n"
"v_pk_fma_f32 v[132:133], v[132:133], s[70:71], v[84:85] op_sel:[0,1,0] op_sel_hi:[1,1,1]\n"
"v_pk_fma_f32 v[134:135], v[134:135], s[70:71], v[86:87] op_sel:[0,1,0] op_sel_hi:[1,1,1]\n"
"v_pk_fma_f32 v[136:137], v[136:137], s[70:71], v[88:89] op_sel:[0,1,0] op_sel_hi:[1,1,1]\n"
"v_pk_fma_f32 v[138:139], v[138:139], s[70:71], v[90:91] op_sel:[0,1,0] op_sel_hi:[1,1,1]\n"
"v_pk_fma_f32 v[140:141], v[140:141], s[70:71], v[92:93] op_sel:[0,1,0] op_sel_hi:[1,1,1]\n"
"v_pk_fma_f32 v[142:143], v[142:143], s[70:71], v[94:95] op_sel:[0,1,0] op_sel_hi:[1,1,1]\n"
"v_pk_add_f32 v[128:129], v[128:129], v[96:97]\n"
"v_pk_add_f32 v[130:131], v[130:131], v[98:99]\n"
"v_pk_add_f32 v[132:133], v[132:133], v[100:101]\n"
"v_pk_add_f32 v[134:135], v[134:135], v[102:103]\n"
"v_pk_add_f32 v[136:137], v[136:137], v[104:105]\n"
"v_pk_add_f32 v[138:139], v[138:139], v[106:107]\n"
"v_pk_add_f32 v[140:141], v[140:141], v[108:109]\n"
"v_pk_add_f32 v[142:143], v[142:143], v[110:111]\n"
"v_cvt_pk_bf16_f32 v128, v128, v129\n"
"v_cvt_pk_bf16_f32 v129, v130, v131\n"
"v_cvt_pk_bf16_f32 v130, v132, v133\n"
"v_cvt_pk_bf16_f32 v131, v134, v135\n"
"v_cvt_pk_bf16_f32 v132, v136, v137\n"
"v_cvt_pk_bf16_f32 v133, v138, v139\n"
"v_cvt_pk_bf16_f32 v134, v140, v141\n"
"v_cvt_pk_bf16_f32 v135, v142, v143\n"
"buffer_store_dwordx4 v[128:131], v196, s[56:59], s8 offen offset:0\n"
"buffer_store_dwordx4 v[132:135], v222, s[56:59], s8 offen offset:0\n"
"s_endpgm\n"
"s_branch Lend_29_%=\n"
"Lg0s_1_%=:\n"
"s_waitcnt vmcnt(0) lgkmcnt(0)\n"
"s_mov_b32 s34, 0\n"
"s_nop 7\n"
"Lg0l_30_%=:\n"
"s_sleep 6\n"
"s_add_u32 s35, s37, 0\n"
"buffer_load_dwordx4 v[128:131], v226, s[60:63], s35 offen offset:0 sc1\n"
"buffer_load_dwordx4 v[132:135], v226, s[60:63], s35 offen offset:1024 sc1\n"
"s_add_u32 s35, s37, 24576\n"
"buffer_load_dwordx4 v[136:139], v226, s[60:63], s35 offen offset:0 sc1\n"
"buffer_load_dwordx4 v[140:143], v226, s[60:63], s35 offen offset:1024 sc1\n"
"s_add_u32 s35, s37, 49152\n"
"buffer_load_dwordx4 v[144:147], v226, s[60:63], s35 offen offset:0 sc1\n"
"buffer_load_dwordx4 v[148:151], v226, s[60:63], s35 offen offset:1024 sc1\n"
"s_add_u32 s35, s37, 73728\n"
"buffer_load_dwordx4 v[152:155], v226, s[60:63], s35 offen offset:0 sc1\n"
"buffer_load_dwordx4 v[156:159], v226, s[60:63], s35 offen offset:1024 sc1\n"
"buffer_load_dwordx4 v[216:219], v227, s[60:63], s33 offen sc1\n"
"s_waitcnt vmcnt(7)\n"
"v_min3_u32 v240, v129, v131, v133\n"
"v_max3_u32 v241, v129, v131, v133\n"
"s_waitcnt vmcnt(6)\n"
"v_min3_u32 v240, v240, v135, v137\n"
"v_max3_u32 v241, v241, v135, v137\n"
"s_waitcnt vmcnt(5)\n"
"v_min3_u32 v240, v240, v139, v141\n"
"v_max3_u32 v241, v241, v139, v141\n"
"s_waitcnt vmcnt(4)\n"
"v_min3_u32 v240, v240, v143, v145\n"
"v_max3_u32 v241, v241, v143, v145\n"
"s_waitcnt vmcnt(3)\n"
"v_min3_u32 v240, v240, v147, v149\n"
"v_max3_u32 v241, v241, v147, v149\n"
"s_waitcnt vmcnt(2)\n"
"v_min3_u32 v240, v240, v151, v153\n"
"v_max3_u32 v241, v241, v151, v153\n"
"s_waitcnt vmcnt(1)\n"
"v_min3_u32 v240, v240, v155, v157\n"
"v_max3_u32 v241, v241, v155, v157\n"
"s_waitcnt vmcnt(0)\n"
"v_min3_u32 v240, v240, v159, v217\n"
"v_max3_u32 v241, v241, v159, v217\n"
"v_min3_u32 v240, v240, v219, v219\n"
"v_max3_u32 v241, v241, v219, v219\n"
"v_xor_b32 v240, s36, v240\n"
"v_xor_b32 v241, s36, v241\n"
"v_or_b32 v240, v240, v241\n"
"v_cmp_ne_u32 vcc, 0, v240\n"
"s_add_u32 s34, s34, 1\n"
"s_nop 7\n"
"s_cbranch_vccz Lg0r_2_%=\n"
"s_cmp_lt_u32 s34, 262144\n"
"s_cbranch_scc1 Lg0l_30_%=\n"
"s_branch Lg0r_2_%=\n"
"Lslow_9_%=:\n"
"s_waitcnt vmcnt(0) lgkmcnt(0)\n"
"s_nop 15\n"
"v_and_b32 v240, 31, v192\n"
"v_lshlrev_b32 v240, 8, v240\n"
"v_lshrrev_b32 v241, 5, v192\n"
"v_lshl_add_u32 v240, v241, 4, v240\n"
"s_lshl_b32 s30, s64, 14\n"
"v_add_u32 v244, s30, v224\n"
"v_add_u32 v245, s30, v225\n"
"v_add_u32 v246, s30, v226\n"
"v_add_u32 v247, s30, v227\n"
"v_mov_b32 v228, 0xff800000\n"
"v_mov_b32 v229, 0xff800000\n"
"s_mov_b32 s34, 0\n"
"s_nop 15\n"
"Lp1_31_%=:\n"
"buffer_load_dwordx4 a[64:67], v240, s[48:51], s34 offen offset:0\n"
"buffer_load_dwordx4 a[68:71], v240, s[48:51], s34 offen offset:32\n"
"buffer_load_dwordx4 a[72:75], v240, s[48:51], s34 offen offset:64\n"
"buffer_load_dwordx4 a[76:79], v240, s[48:51], s34 offen offset:96\n"
"buffer_load_dwordx4 a[80:83], v240, s[48:51], s34 offen offset:128\n"
"buffer_load_dwordx4 a[84:87], v240, s[48:51], s34 offen offset:160\n"
"buffer_load_dwordx4 a[88:91], v240, s[48:51], s34 offen offset:192\n"
"buffer_load_dwordx4 a[92:95], v240, s[48:51], s34 offen offset:224\n"
"s_waitcnt vmcnt(7)\n"
"v_mfma_f32_32x32x16_bf16 v[128:143], a[64:67], a[0:3], 0\n"
"s_waitcnt vmcnt(6)\n"
"v_mfma_f32_32x32x16_bf16 v[128:143], a[68:71], a[4:7], v[128:143]\n"
"s_waitcnt vmcnt(5)\n"
"v_mfma_f32_32x32x16_bf16 v[128:143], a[72:75], a[8:11], v[128:143]\n"
"s_waitcnt vmcnt(4)\n"
"v_mfma_f32_32x32x16_bf16 v[128:143], a[76:79], a[12:15], v[128:143]\n"
"s_waitcnt vmcnt(3)\n"
"v_mfma_f32_32x32x16_bf16 v[128:143], a[80:83], a[16:19], v[128:143]\n"
"s_waitcnt vmcnt(2)\n"
"v_mfma_f32_32x32x16_bf16 v[128:143], a[84:87], a[20:23], v[128:143]\n"
"s_waitcnt vmcnt(1)\n"
"v_mfma_f32_32x32x16_bf16 v[128:143], a[88:91], a[24:27], v[128:143]\n"
"s_waitcnt vmcnt(0)\n"
"v_mfma_f32_32x32x16_bf16 v[128:143], a[92:95], a[28:31], v[128:143]\n"
"v_mfma_f32_32x32x16_bf16 v[144:159], a[64:67], a[32:35], 0\n"
"v_mfma_f32_32x32x16_bf16 v[144:159], a[68:71], a[36:39], v[144:159]\n"
"v_mfma_f32_32x32x16_bf16 v[144:159], a[72:75], a[40:43], v[144:159]\n"
"v_mfma_f32_32x32x16_bf16 v[144:159], a[76:79], a[44:47], v[144:159]\n"
"v_mfma_f32_32x32x16_bf16 v[144:159], a[80:83], a[48:51], v[144:159]\n"
"v_mfma_f32_32x32x16_bf16 v[144:159], a[84:87], a[52:55], v[144:159]\n"
"v_mfma_f32_32x32x16_bf16 v[144:159], a[88:91], a[56:59], v[144:159]\n"
"v_mfma_f32_32x32x16_bf16 v[144:159], a[92:95], a[60:63], v[144:159]\n"
"s_nop 3\n"
"v_max3_f32 v228, v228, v128, v129\n"
"v_max3_f32 v228, v228, v130, v131\n"
"v_max3_f32 v228, v228, v132, v133\n"
"v_max3_f32 v228, v228, v134, v135\n"
"v_max3_f32 v228, v228, v136, v137\n"
"v_max3_f32 v228, v228, v138, v139\n"
"v_max3_f32 v228, v228, v140, v141\n"
"v_max3_f32 v228, v228, v142, v143\n"
"v_max3_f32 v229, v229, v144, v145\n"
"v_max3_f32 v229, v229, v146, v147\n"
"v_max3_f32 v229, v229, v148, v149\n"
"v_max3_f32 v229, v229, v150, v151\n"
"v_max3_f32 v229, v229, v152, v153\n"
"v_max3_f32 v229, v229, v154, v155\n"
"v_max3_f32 v229, v229, v156, v157\n"
"v_max3_f32 v229, v229, v158, v159\n"
"s_add_u32 s34, s34, 8192\n"
"s_cmp_lt_u32 s34, 131072\n"
"s_nop 15\n"
"s_cbranch_scc1 Lp1_31_%=\n"
"v_mov_b32 v242, v228\n"
"v_mov_b32 v243, v229\n"
"s_nop 0\n"
"v_permlane32_swap_b32 v228, v242\n"
"v_permlane32_swap_b32 v229, v243\n"
"v_max_f32 v228, v228, v242\n"
"v_max_f32 v229, v229, v243\n"
"v_mul_f32 v228, s73, v228\n"
"v_mul_f32 v229, s73, v229\n"
"v_sub_f32 v228, 0, v228\n"
"v_sub_f32 v229, 0, v229\n"
"v_mov_b32 v0, 0\n"
"v_mov_b32 v1, 0\n"
"v_mov_b32 v2, 0\n"
"v_mov_b32 v3, 0\n"
"v_mov_b32 v4, 0\n"
"v_mov_b32 v5, 0\n"
"v_mov_b32 v6, 0\n"
"v_mov_b32 v7, 0\n"
"v_mov_b32 v8, 0\n"
"v_mov_b32 v9, 0\n"
"v_mov_b32 v10, 0\n"
"v_mov_b32 v11, 0\n"
"v_mov_b32 v12, 0\n"
"v_mov_b32 v13, 0\n"
"v_mov_b32 v14, 0\n"
"v_mov_b32 v15, 0\n"
"v_mov_b32 v16, 0\n"
"v_mov_b32 v17, 0\n"
"v_mov_b32 v18, 0\n"
"v_mov_b32 v19, 0\n"
"v_mov_b32 v20, 0\n"
"v_mov_b32 v21, 0\n"
"v_mov_b32 v22, 0\n"
"v_mov_b32 v23, 0\n"
"v_mov_b32 v24, 0\n"
"v_mov_b32 v25, 0\n"
"v_mov_b32 v26, 0\n"
"v_mov_b32 v27, 0\n"
"v_mov_b32 v28, 0\n"
"v_mov_b32 v29, 0\n"
"v_mov_b32 v30, 0\n"
"v_mov_b32 v31, 0\n"
"v_mov_b32 v32, 0\n"
"v_mov_b32 v33, 0\n"
"v_mov_b32 v34, 0\n"
"v_mov_b32 v35, 0\n"
"v_mov_b32 v36, 0\n"
"v_mov_b32 v37, 0\n"
"v_mov_b32 v38, 0\n"
"v_mov_b32 v39, 0\n"
"v_mov_b32 v40, 0\n"
"v_mov_b32 v41, 0\n"
"v_mov_b32 v42, 0\n"
"v_mov_b32 v43, 0\n"
"v_mov_b32 v44, 0\n"
"v_mov_b32 v45, 0\n"
"v_mov_b32 v46, 0\n"
"v_mov_b32 v47, 0\n"
"v_mov_b32 v48, 0\n"
"v_mov_b32 v49, 0\n"
"v_mov_b32 v50, 0\n"
"v_mov_b32 v51, 0\n"
"v_mov_b32 v52, 0\n"
"v_mov_b32 v53, 0\n"
"v_mov_b32 v54, 0\n"
"v_mov_b32 v55, 0\n"
"v_mov_b32 v56, 0\n"
"v_mov_b32 v57, 0\n"
"v_mov_b32 v58, 0\n"
"v_mov_b32 v59, 0\n"
"v_mov_b32 v60, 0\n"
"v_mov_b32 v61, 0\n"
"v_mov_b32 v62, 0\n"
"v_mov_b32 v63, 0\n"
"v_mov_b32 v64, 0\n"
"v_mov_b32 v65, 0\n"
"v_mov_b32 v66, 0\n"
"v_mov_b32 v67, 0\n"
"v_mov_b32 v68, 0\n"
"v_mov_b32 v69, 0\n"
"v_mov_b32 v70, 0\n"
"v_mov_b32 v71, 0\n"
"v_mov_b32 v72, 0\n"
"v_mov_b32 v73, 0\n"
"v_mov_b32 v74, 0\n"
"v_mov_b32 v75, 0\n"
"v_mov_b32 v76, 0\n"
"v_mov_b32 v77, 0\n"
"v_mov_b32 v78, 0\n"
"v_mov_b32 v79, 0\n"
"v_mov_b32 v80, 0\n"
"v_mov_b32 v81, 0\n"
"v_mov_b32 v82, 0\n"
"v_mov_b32 v83, 0\n"
"v_mov_b32 v84, 0\n"
"v_mov_b32 v85, 0\n"
"v_mov_b32 v86, 0\n"
"v_mov_b32 v87, 0\n"
"v_mov_b32 v88, 0\n"
"v_mov_b32 v89, 0\n"
"v_mov_b32 v90, 0\n"
"v_mov_b32 v91, 0\n"
"v_mov_b32 v92, 0\n"
"v_mov_b32 v93, 0\n"
"v_mov_b32 v94, 0\n"
"v_mov_b32 v95, 0\n"
"v_mov_b32 v96, 0\n"
"v_mov_b32 v97, 0\n"
"v_mov_b32 v98, 0\n"
"v_mov_b32 v99, 0\n"
"v_mov_b32 v100, 0\n"
"v_mov_b32 v101, 0\n"
"v_mov_b32 v102, 0\n"
"v_mov_b32 v103, 0\n"
"v_mov_b32 v104, 0\n"
"v_mov_b32 v105, 0\n"
"v_mov_b32 v106, 0\n"
"v_mov_b32 v107, 0\n"
"v_mov_b32 v108, 0\n"
"v_mov_b32 v109, 0\n"
"v_mov_b32 v110, 0\n"
"v_mov_b32 v111, 0\n"
"v_mov_b32 v112, 0\n"
"v_mov_b32 v113, 0\n"
"v_mov_b32 v114, 0\n"
"v_mov_b32 v115, 0\n"
"v_mov_b32 v116, 0\n"
"v_mov_b32 v117, 0\n"
"v_mov_b32 v118, 0\n"
"v_mov_b32 v119, 0\n"
"v_mov_b32 v120, 0\n"
"v_mov_b32 v121, 0\n"
"v_mov_b32 v122, 0\n"
"v_mov_b32 v123, 0\n"
"v_mov_b32 v124, 0\n"
"v_mov_b32 v125, 0\n"
"v_mov_b32 v126, 0\n"
"v_mov_b32 v127, 0\n"
"v_mov_b32 v230, 0\n"
"v_mov_b32 v231, 0\n"
"s_mov_b32 s34, 0\n"
"s_nop 15\n"
"Lp2_32_%=:\n"
"buffer_load_dwordx4 a[64:67], v240, s[48:51], s34 offen offset:0\n"
"buffer_load_dwordx4 a[68:71], v240, s[48:51], s34 offen offset:32\n"
"buffer_load_dwordx4 a[72:75], v240, s[48:51], s34 offen offset:64\n"
"buffer_load_dwordx4 a[76:79], v240, s[48:51], s34 offen offset:96\n"
"buffer_load_dwordx4 a[80:83], v240, s[48:51], s34 offen offset:128\n"
"buffer_load_dwordx4 a[84:87], v240, s[48:51], s34 offen offset:160\n"
"buffer_load_dwordx4 a[88:91], v240, s[48:51], s34 offen offset:192\n"
"buffer_load_dwordx4 a[92:95], v240, s[48:51], s34 offen offset:224\n"
"s_add_u32 m0, s30, 0\n"
"s_add_u32 s74, s34, 0\n"
"buffer_load_dwordx4 v213, s[52:55], s74 offen offset:0 lds\n"
"buffer_load_dwordx4 v213, s[52:55], s74 offen offset:1024 lds\n"
"buffer_load_dwordx4 v213, s[52:55], s74 offen offset:2048 lds\n"
"buffer_load_dwordx4 v213, s[52:55], s74 offen offset:3072 lds\n"
"s_add_u32 m0, s30, 4096\n"
"s_add_u32 s74, s34, 4096\n"
"buffer_load_dwordx4 v213, s[52:55], s74 offen offset:0 lds\n"
"buffer_load_dwordx4 v213, s[52:55], s74 offen offset:1024 lds\n"
"buffer_load_dwordx4 v213, s[52:55], s74 offen offset:2048 lds\n"
"buffer_load_dwordx4 v213, s[52:55], s74 offen offset:3072 lds\n"
"s_waitcnt vmcnt(15)\n"
"v_mfma_f32_32x32x16_bf16 v[128:143], a[64:67], a[0:3], 0\n"
"s_waitcnt vmcnt(14)\n"
"v_mfma_f32_32x32x16_bf16 v[128:143], a[68:71], a[4:7], v[128:143]\n"
"s_waitcnt vmcnt(13)\n"
"v_mfma_f32_32x32x16_bf16 v[128:143], a[72:75], a[8:11], v[128:143]\n"
"s_waitcnt vmcnt(12)\n"
"v_mfma_f32_32x32x16_bf16 v[128:143], a[76:79], a[12:15], v[128:143]\n"
"s_waitcnt vmcnt(11)\n"
"v_mfma_f32_32x32x16_bf16 v[128:143], a[80:83], a[16:19], v[128:143]\n"
"s_waitcnt vmcnt(10)\n"
"v_mfma_f32_32x32x16_bf16 v[128:143], a[84:87], a[20:23], v[128:143]\n"
"s_waitcnt vmcnt(9)\n"
"v_mfma_f32_32x32x16_bf16 v[128:143], a[88:91], a[24:27], v[128:143]\n"
"s_waitcnt vmcnt(8)\n"
"v_mfma_f32_32x32x16_bf16 v[128:143], a[92:95], a[28:31], v[128:143]\n"
"v_mfma_f32_32x32x16_bf16 v[144:159], a[64:67], a[32:35], 0\n"
"v_mfma_f32_32x32x16_bf16 v[144:159], a[68:71], a[36:39], v[144:159]\n"
"v_mfma_f32_32x32x16_bf16 v[144:159], a[72:75], a[40:43], v[144:159]\n"
"v_mfma_f32_32x32x16_bf16 v[144:159], a[76:79], a[44:47], v[144:159]\n"
"v_mfma_f32_32x32x16_bf16 v[144:159], a[80:83], a[48:51], v[144:159]\n"
"v_mfma_f32_32x32x16_bf16 v[144:159], a[84:87], a[52:55], v[144:159]\n"
"v_mfma_f32_32x32x16_bf16 v[144:159], a[88:91], a[56:59], v[144:159]\n"
"v_mfma_f32_32x32x16_bf16 v[144:159], a[92:95], a[60:63], v[144:159]\n"
"s_waitcnt vmcnt(0)\n"
"ds_read_b64_tr_b16 a[96:97], v244 offset:0\n"
"ds_read_b64_tr_b16 a[98:99], v244 offset:2048\n"
"ds_read_b64_tr_b16 a[100:101], v245 offset:0\n"
"ds_read_b64_tr_b16 a[102:103], v245 offset:2048\n"
"ds_read_b64_tr_b16 a[104:105], v246 offset:0\n"
"ds_read_b64_tr_b16 a[106:107], v246 offset:2048\n"
"ds_read_b64_tr_b16 a[108:109], v247 offset:0\n"
"ds_read_b64_tr_b16 a[110:111], v247 offset:2048\n"
"ds_read_b64_tr_b16 a[112:113], v244 offset:4096\n"
"ds_read_b64_tr_b16 a[114:115], v244 offset:6144\n"
"ds_read_b64_tr_b16 a[116:117], v245 offset:4096\n"
"ds_read_b64_tr_b16 a[118:119], v245 offset:6144\n"
"ds_read_b64_tr_b16 a[120:121], v246 offset:4096\n"
"ds_read_b64_tr_b16 a[122:123], v246 offset:6144\n"
"ds_read_b64_tr_b16 a[124:125], v247 offset:4096\n"
"ds_read_b64_tr_b16 a[126:127], v247 offset:6144\n"
"v_cvt_pk_bf16_f32 v128, 0, v128\n"
"v_cvt_pk_bf16_f32 v129, 0, v129\n"
"v_cvt_pk_bf16_f32 v130, 0, v130\n"
"v_cvt_pk_bf16_f32 v131, 0, v131\n"
"v_cvt_pk_bf16_f32 v132, 0, v132\n"
"v_cvt_pk_bf16_f32 v133, 0, v133\n"
"v_cvt_pk_bf16_f32 v134, 0, v134\n"
"v_cvt_pk_bf16_f32 v135, 0, v135\n"
"v_cvt_pk_bf16_f32 v136, 0, v136\n"
"v_cvt_pk_bf16_f32 v137, 0, v137\n"
"v_cvt_pk_bf16_f32 v138, 0, v138\n"
"v_cvt_pk_bf16_f32 v139, 0, v139\n"
"v_cvt_pk_bf16_f32 v140, 0, v140\n"
"v_cvt_pk_bf16_f32 v141, 0, v141\n"
"v_cvt_pk_bf16_f32 v142, 0, v142\n"
"v_cvt_pk_bf16_f32 v143, 0, v143\n"
"v_cvt_pk_bf16_f32 v144, 0, v144\n"
"v_cvt_pk_bf16_f32 v145, 0, v145\n"
"v_cvt_pk_bf16_f32 v146, 0, v146\n"
"v_cvt_pk_bf16_f32 v147, 0, v147\n"
"v_cvt_pk_bf16_f32 v148, 0, v148\n"
"v_cvt_pk_bf16_f32 v149, 0, v149\n"
"v_cvt_pk_bf16_f32 v150, 0, v150\n"
"v_cvt_pk_bf16_f32 v151, 0, v151\n"
"v_cvt_pk_bf16_f32 v152, 0, v152\n"
"v_cvt_pk_bf16_f32 v153, 0, v153\n"
"v_cvt_pk_bf16_f32 v154, 0, v154\n"
"v_cvt_pk_bf16_f32 v155, 0, v155\n"
"v_cvt_pk_bf16_f32 v156, 0, v156\n"
"v_cvt_pk_bf16_f32 v157, 0, v157\n"
"v_cvt_pk_bf16_f32 v158, 0, v158\n"
"v_cvt_pk_bf16_f32 v159, 0, v159\n"
"v_fma_f32 v128, v128, s73, v228\n"
"v_fma_f32 v129, v129, s73, v228\n"
"v_fma_f32 v130, v130, s73, v228\n"
"v_fma_f32 v131, v131, s73, v228\n"
"v_fma_f32 v132, v132, s73, v228\n"
"v_fma_f32 v133, v133, s73, v228\n"
"v_fma_f32 v134, v134, s73, v228\n"
"v_fma_f32 v135, v135, s73, v228\n"
"v_fma_f32 v136, v136, s73, v228\n"
"v_fma_f32 v137, v137, s73, v228\n"
"v_fma_f32 v138, v138, s73, v228\n"
"v_fma_f32 v139, v139, s73, v228\n"
"v_fma_f32 v140, v140, s73, v228\n"
"v_fma_f32 v141, v141, s73, v228\n"
"v_fma_f32 v142, v142, s73, v228\n"
"v_fma_f32 v143, v143, s73, v228\n"
"v_fma_f32 v144, v144, s73, v229\n"
"v_fma_f32 v145, v145, s73, v229\n"
"v_fma_f32 v146, v146, s73, v229\n"
"v_fma_f32 v147, v147, s73, v229\n"
"v_fma_f32 v148, v148, s73, v229\n"
"v_fma_f32 v149, v149, s73, v229\n"
"v_fma_f32 v150, v150, s73, v229\n"
"v_fma_f32 v151, v151, s73, v229\n"
"v_fma_f32 v152, v152, s73, v229\n"
"v_fma_f32 v153, v153, s73, v229\n"
"v_fma_f32 v154, v154, s73, v229\n"
"v_fma_f32 v155, v155, s73, v229\n"
"v_fma_f32 v156, v156, s73, v229\n"
"v_fma_f32 v157, v157, s73, v229\n"
"v_fma_f32 v158, v158, s73, v229\n"
"v_fma_f32 v159, v159, s73, v229\n"
"v_exp_f32 v128, v128\n"
"v_exp_f32 v129, v129\n"
"v_exp_f32 v130, v130\n"
"v_exp_f32 v131, v131\n"
"v_exp_f32 v132, v132\n"
"v_exp_f32 v133, v133\n"
"v_exp_f32 v134, v134\n"
"v_exp_f32 v135, v135\n"
"v_exp_f32 v136, v136\n"
"v_exp_f32 v137, v137\n"
"v_exp_f32 v138, v138\n"
"v_exp_f32 v139, v139\n"
"v_exp_f32 v140, v140\n"
"v_exp_f32 v141, v141\n"
"v_exp_f32 v142, v142\n"
"v_exp_f32 v143, v143\n"
"v_exp_f32 v144, v144\n"
"v_exp_f32 v145, v145\n"
"v_exp_f32 v146, v146\n"
"v_exp_f32 v147, v147\n"
"v_exp_f32 v148, v148\n"
"v_exp_f32 v149, v149\n"
"v_exp_f32 v150, v150\n"
"v_exp_f32 v151, v151\n"
"v_exp_f32 v152, v152\n"
"v_exp_f32 v153, v153\n"
"v_exp_f32 v154, v154\n"
"v_exp_f32 v155, v155\n"
"v_exp_f32 v156, v156\n"
"v_exp_f32 v157, v157\n"
"v_exp_f32 v158, v158\n"
"v_exp_f32 v159, v159\n"
"v_add_f32 v230, v230, v128\n"
"v_add_f32 v230, v230, v129\n"
"v_add_f32 v230, v230, v130\n"
"v_add_f32 v230, v230, v131\n"
"v_add_f32 v230, v230, v132\n"
"v_add_f32 v230, v230, v133\n"
"v_add_f32 v230, v230, v134\n"
"v_add_f32 v230, v230, v135\n"
"v_add_f32 v230, v230, v136\n"
"v_add_f32 v230, v230, v137\n"
"v_add_f32 v230, v230, v138\n"
"v_add_f32 v230, v230, v139\n"
"v_add_f32 v230, v230, v140\n"
"v_add_f32 v230, v230, v141\n"
"v_add_f32 v230, v230, v142\n"
"v_add_f32 v230, v230, v143\n"
"v_add_f32 v231, v231, v144\n"
"v_add_f32 v231, v231, v145\n"
"v_add_f32 v231, v231, v146\n"
"v_add_f32 v231, v231, v147\n"
"v_add_f32 v231, v231, v148\n"
"v_add_f32 v231, v231, v149\n"
"v_add_f32 v231, v231, v150\n"
"v_add_f32 v231, v231, v151\n"
"v_add_f32 v231, v231, v152\n"
"v_add_f32 v231, v231, v153\n"
"v_add_f32 v231, v231, v154\n"
"v_add_f32 v231, v231, v155\n"
"v_add_f32 v231, v231, v156\n"
"v_add_f32 v231, v231, v157\n"
"v_add_f32 v231, v231, v158\n"
"v_add_f32 v231, v231, v159\n"
"v_cvt_pk_bf16_f32 v128, v128, v129\n"
"v_cvt_pk_bf16_f32 v129, v130, v131\n"
"v_cvt_pk_bf16_f32 v130, v132, v133\n"
"v_cvt_pk_bf16_f32 v131, v134, v135\n"
"v_cvt_pk_bf16_f32 v136, v136, v137\n"
"v_cvt_pk_bf16_f32 v137, v138, v139\n"
"v_cvt_pk_bf16_f32 v138, v140, v141\n"
"v_cvt_pk_bf16_f32 v139, v142, v143\n"
"v_cvt_pk_bf16_f32 v144, v144, v145\n"
"v_cvt_pk_bf16_f32 v145, v146, v147\n"
"v_cvt_pk_bf16_f32 v146, v148, v149\n"
"v_cvt_pk_bf16_f32 v147, v150, v151\n"
"v_cvt_pk_bf16_f32 v152, v152, v153\n"
"v_cvt_pk_bf16_f32 v153, v154, v155\n"
"v_cvt_pk_bf16_f32 v154, v156, v157\n"
"v_cvt_pk_bf16_f32 v155, v158, v159\n"
"s_waitcnt lgkmcnt(14)\n"
"v_mfma_f32_32x32x16_bf16 v[0:15], a[96:99], v[128:131], v[0:15]\n"
"s_waitcnt lgkmcnt(12)\n"
"v_mfma_f32_32x32x16_bf16 v[32:47], a[100:103], v[128:131], v[32:47]\n"
"s_waitcnt lgkmcnt(10)\n"
"v_mfma_f32_32x32x16_bf16 v[64:79], a[104:107], v[128:131], v[64:79]\n"
"s_waitcnt lgkmcnt(8)\n"
"v_mfma_f32_32x32x16_bf16 v[96:111], a[108:111], v[128:131], v[96:111]\n"
"v_mfma_f32_32x32x16_bf16 v[16:31], a[96:99], v[144:147], v[16:31]\n"
"v_mfma_f32_32x32x16_bf16 v[48:63], a[100:103], v[144:147], v[48:63]\n"
"v_mfma_f32_32x32x16_bf16 v[80:95], a[104:107], v[144:147], v[80:95]\n"
"v_mfma_f32_32x32x16_bf16 v[112:127], a[108:111], v[144:147], v[112:127]\n"
"s_waitcnt lgkmcnt(6)\n"
"v_mfma_f32_32x32x16_bf16 v[0:15], a[112:115], v[136:139], v[0:15]\n"
"s_waitcnt lgkmcnt(4)\n"
"v_mfma_f32_32x32x16_bf16 v[32:47], a[116:119], v[136:139], v[32:47]\n"
"s_waitcnt lgkmcnt(2)\n"
"v_mfma_f32_32x32x16_bf16 v[64:79], a[120:123], v[136:139], v[64:79]\n"
"s_waitcnt lgkmcnt(0)\n"
"v_mfma_f32_32x32x16_bf16 v[96:111], a[124:127], v[136:139], v[96:111]\n"
"v_mfma_f32_32x32x16_bf16 v[16:31], a[112:115], v[152:155], v[16:31]\n"
"v_mfma_f32_32x32x16_bf16 v[48:63], a[116:119], v[152:155], v[48:63]\n"
"v_mfma_f32_32x32x16_bf16 v[80:95], a[120:123], v[152:155], v[80:95]\n"
"v_mfma_f32_32x32x16_bf16 v[112:127], a[124:127], v[152:155], v[112:127]\n"
"s_add_u32 s34, s34, 8192\n"
"s_cmp_lt_u32 s34, 131072\n"
"s_nop 15\n"
"s_cbranch_scc1 Lp2_32_%=\n"
"v_mov_b32 v240, v230\n"
"v_mov_b32 v241, v231\n"
"s_nop 0\n"
"v_permlane32_swap_b32 v230, v240\n"
"v_permlane32_swap_b32 v231, v241\n"
"v_add_f32 v230, v230, v240\n"
"v_add_f32 v231, v231, v241\n"
"v_rcp_f32 v230, v230\n"
"v_rcp_f32 v231, v231\n"
"s_nop 15\n"
"s_branch Ljoin_10_%=\n"
"Lf0s_13_%=:\n"
"s_waitcnt vmcnt(0) lgkmcnt(0)\n"
"s_mov_b32 s34, 0\n"
"s_nop 7\n"
"Lf0l_33_%=:\n"
"s_sleep 6\n"
"buffer_load_dwordx2 v[236:237], v196, s[60:63], s9 offen offset:0 sc1\n"
"buffer_load_dwordx2 v[238:239], v196, s[60:63], s9 offen offset:2048 sc1\n"
"buffer_load_dwordx2 v[242:243], v196, s[60:63], s10 offen offset:0 sc1\n"
"s_waitcnt vmcnt(0)\n"
"v_min3_u32 v240, v237, v239, v243\n"
"v_max3_u32 v241, v237, v239, v243\n"
"v_xor_b32 v240, s36, v240\n"
"v_xor_b32 v241, s36, v241\n"
"v_or_b32 v240, v240, v241\n"
"v_cmp_ne_u32 vcc, 0, v240\n"
"s_add_u32 s34, s34, 1\n"
"s_nop 7\n"
"s_cbranch_vccz Lf0r_14_%=\n"
"s_cmp_lt_u32 s34, 262144\n"
"s_cbranch_scc1 Lf0l_33_%=\n"
"s_branch Lf0r_14_%=\n"
"Lp1as_15_%=:\n"
"s_waitcnt vmcnt(0) lgkmcnt(0)\n"
"s_mov_b32 s34, 0\n"
"s_nop 7\n"
"Lp1al_34_%=:\n"
"s_sleep 6\n"
"buffer_load_dword v197, v193, s[60:63], s12 offen sc1\n"
"s_waitcnt vmcnt(0)\n"
"v_cmp_ne_u32 vcc, s36, v197\n"
"s_nop 1\n"
"s_nop 0\n"
"s_and_b64 vcc, vcc, s[20:21]\n"
"s_add_u32 s34, s34, 1\n"
"s_nop 7\n"
"s_cbranch_vccz Lp1ar_16_%=\n"
"s_cmp_lt_u32 s34, 262144\n"
"s_cbranch_scc1 Lp1al_34_%=\n"
"s_branch Lp1ar_16_%=\n"
"Lp1bs_17_%=:\n"
"s_waitcnt vmcnt(0) lgkmcnt(0)\n"
"s_mov_b32 s34, 0\n"
"s_nop 7\n"
"Lp1bl_35_%=:\n"
"s_sleep 6\n"
"buffer_load_dword v197, v193, s[60:63], s12 offen sc1\n"
"s_waitcnt vmcnt(0)\n"
"v_cmp_ne_u32 vcc, s36, v197\n"
"s_nop 1\n"
"s_nop 0\n"
"s_and_b64 vcc, vcc, s[20:21]\n"
"s_add_u32 s34, s34, 1\n"
"s_nop 7\n"
"s_cbranch_vccz Lp1br_18_%=\n"
"s_cmp_lt_u32 s34, 262144\n"
"s_cbranch_scc1 Lp1bl_35_%=\n"
"s_branch Lp1br_18_%=\n"
"Lp2s_19_%=:\n"
"s_waitcnt vmcnt(0) lgkmcnt(0)\n"
"s_mov_b32 s34, 0\n"
"s_nop 7\n"
"Lp2l_36_%=:\n"
"s_sleep 6\n"
"buffer_load_dword v197, v193, s[60:63], s12 offen sc1\n"
"s_waitcnt vmcnt(0)\n"
"v_cmp_ne_u32 vcc, s36, v197\n"
"s_nop 1\n"
"s_nop 0\n"
"s_and_b64 vcc, vcc, s[20:21]\n"
"s_add_u32 s34, s34, 1\n"
"s_nop 7\n"
"s_cbranch_vccz Lp2r_20_%=\n"
"s_cmp_lt_u32 s34, 262144\n"
"s_cbranch_scc1 Lp2l_36_%=\n"
"s_branch Lp2r_20_%=\n"
"Lgbs_23_%=:\n"
"s_waitcnt vmcnt(0) lgkmcnt(0)\n"
"s_mov_b32 s34, 0\n"
"s_nop 7\n"
"Lgbl_37_%=:\n"
"s_sleep 6\n"
"buffer_load_dwordx4 v[80:83], v224, s[60:63], s10 offen offset:0 sc1\n"
"buffer_load_dwordx4 v[84:87], v224, s[60:63], s10 offen offset:16 sc1\n"
"s_waitcnt vmcnt(0)\n"
"v_min3_u32 v248, v81, v83, v85\n"
"v_max3_u32 v249, v81, v83, v85\n"
"v_min3_u32 v248, v248, v87, v87\n"
"v_max3_u32 v249, v249, v87, v87\n"
"v_xor_b32 v248, s36, v248\n"
"v_xor_b32 v249, s36, v249\n"
"v_or_b32 v248, v248, v249\n"
"v_cmp_ne_u32 vcc, 0, v248\n"
"s_add_u32 s34, s34, 1\n"
"s_nop 7\n"
"s_cbranch_vccz Lgbr_24_%=\n"
"s_cmp_lt_u32 s34, 262144\n"
"s_cbranch_scc1 Lgbl_37_%=\n"
"s_branch Lgbr_24_%=\n"
"Lp3as_25_%=:\n"
"s_waitcnt vmcnt(0) lgkmcnt(0)\n"
"s_mov_b32 s34, 0\n"
"s_nop 7\n"
"Lp3al_38_%=:\n"
"s_sleep 6\n"
"buffer_load_dword v197, v193, s[60:63], s12 offen sc1\n"
"s_waitcnt vmcnt(0)\n"
"v_cmp_ne_u32 vcc, s36, v197\n"
"s_nop 1\n"
"s_nop 0\n"
"s_and_b64 vcc, vcc, s[20:21]\n"
"s_add_u32 s34, s34, 1\n"
"s_nop 7\n"
"s_cbranch_vccz Lp3ar_26_%=\n"
"s_cmp_lt_u32 s34, 262144\n"
"s_cbranch_scc1 Lp3al_38_%=\n"
"s_branch Lp3ar_26_%=\n"
"Lregs_27_%=:\n"
"s_waitcnt vmcnt(0) lgkmcnt(0)\n"
"s_mov_b32 s34, 0\n"
"s_nop 7\n"
"Lregl_39_%=:\n"
"s_sleep 6\n"
"s_add_u32 s30, s15, 1048576\n"
"buffer_load_dword v112, v116, s[60:63], s30 offen sc1\n"
"s_add_u32 s30, s15, 1081344\n"
"buffer_load_dword v113, v116, s[60:63], s30 offen sc1\n"
"s_add_u32 s30, s15, 1114112\n"
"buffer_load_dword v114, v116, s[60:63], s30 offen sc1\n"
"s_add_u32 s30, s15, 1146880\n"
"buffer_load_dword v115, v116, s[60:63], s30 offen sc1\n"
"s_waitcnt vmcnt(1)\n"
"v_min3_u32 v118, v112, v113, v114\n"
"v_max3_u32 v119, v112, v113, v114\n"
"s_waitcnt vmcnt(0)\n"
"v_min3_u32 v118, v118, v115, v115\n"
"v_max3_u32 v119, v119, v115, v115\n"
"v_xor_b32 v118, s36, v118\n"
"v_xor_b32 v119, s36, v119\n"
"v_or_b32 v118, v118, v119\n"
"v_cmp_ne_u32 vcc, 0, v118\n"
"s_nop 1\n"
"s_nop 0\n"
"s_and_b64 vcc, vcc, s[20:21]\n"
"s_add_u32 s34, s34, 1\n"
"s_nop 7\n"
"s_cbranch_vccz Lregr_28_%=\n"
"s_cmp_lt_u32 s34, 262144\n"
"s_cbranch_scc1 Lregl_39_%=\n"
"s_branch Lregr_28_%=\n"
"Lend_29_%=:\n"
"Lfin_%=:\n"
  : "+{v0}"(tid), "+{s6}"(bid), "+{s[4:5]}"(kp) : : "v1","v2","v3","v4","v5","v6","v7","v8","v9","v10","v11","v12","v13","v14","v15","v16","v17","v18","v19","v20","v21","v22","v23","v24","v25","v26","v27","v28","v29","v30","v31","v32","v33","v34","v35","v36","v37","v38","v39","v40","v41","v42","v43","v44","v45","v46","v47","v48","v49","v50","v51","v52","v53","v54","v55","v56","v57","v58","v59","v60","v61","v62","v63","v64","v65","v66","v67","v68","v69","v70","v71","v72","v73","v74","v75","v76","v77","v78","v79","v80","v81","v82","v83","v84","v85","v86","v87","v88","v89","v90","v91","v92","v93","v94","v95","v96","v97","v98","v99","v100","v101","v102","v103","v104","v105","v106","v107","v108","v109","v110","v111","v112","v113","v114","v115","v116","v117","v118","v119","v120","v121","v122","v123","v124","v125","v126","v127","v128","v129","v130","v131","v132","v133","v134","v135","v136","v137","v138","v139","v140","v141","v142","v143","v144","v145","v146","v147","v148","v149","v150","v151","v152","v153","v154","v155","v156","v157","v158","v159","v160","v161","v162","v163","v164","v165","v166","v167","v168","v169","v170","v171","v172","v173","v174","v175","v176","v177","v178","v179","v180","v181","v182","v183","v184","v185","v186","v187","v188","v189","v190","v191","v192","v193","v194","v195","v196","v197","v198","v199","v200","v201","v202","v203","v204","v205","v206","v207","v208","v209","v210","v211","v212","v213","v214","v215","v216","v217","v218","v219","v220","v221","v222","v223","v224","v225","v226","v227","v228","v229","v230","v231","v232","v233","v234","v235","v236","v237","v238","v239","v240","v241","v242","v243","v244","v245","v246","v247","v248","v249","v250","v251","v252","v253","v254","v255","a0","a1","a2","a3","a4","a5","a6","a7","a8","a9","a10","a11","a12","a13","a14","a15","a16","a17","a18","a19","a20","a21","a22","a23","a24","a25","a26","a27","a28","a29","a30","a31","a32","a33","a34","a35","a36","a37","a38","a39","a40","a41","a42","a43","a44","a45","a46","a47","a48","a49","a50","a51","a52","a53","a54","a55","a56","a57","a58","a59","a60","a61","a62","a63","a64","a65","a66","a67","a68","a69","a70","a71","a72","a73","a74","a75","a76","a77","a78","a79","a80","a81","a82","a83","a84","a85","a86","a87","a88","a89","a90","a91","a92","a93","a94","a95","a96","a97","a98","a99","a100","a101","a102","a103","a104","a105","a106","a107","a108","a109","a110","a111","a112","a113","a114","a115","a116","a117","a118","a119","a120","a121","a122","a123","a124","a125","a126","a127","a128","a129","a130","a131","a132","a133","a134","a135","a136","a137","a138","a139","a140","a141","a142","a143","a144","a145","a146","a147","a148","a149","a150","a151","a152","a153","a154","a155","a156","a157","a158","a159","a160","a161","a162","a163","a164","a165","a166","a167","a168","a169","a170","a171","a172","a173","a174","a175","a176","a177","a178","a179","a180","a181","a182","a183","a184","a185","a186","a187","a188","a189","a190","a191","a192","a193","a194","a195","a196","a197","a198","a199","a200","a201","a202","a203","a204","a205","a206","a207","a208","a209","a210","a211","a212","a213","a214","a215","a216","a217","a218","a219","a220","a221","a222","a223","a224","a225","a226","a227","a228","a229","a230","a231","a232","a233","a234","a235","a236","a237","a238","a239","a240","a241","a242","a243","a244","a245","a246","a247","a248","a249","a250","a251","a252","a253","a254","a255","s7","s8","s9","s10","s11","s12","s13","s14","s15","s16","s17","s18","s19","s20","s21","s22","s23","s24","s25","s26","s27","s28","s29","s30","s31","s33","s34","s35","s36","s37","s38","s39","s40","s41","s42","s43","s44","s45","s46","s47","s48","s49","s50","s51","s52","s53","s54","s55","s56","s57","s58","s59","s60","s61","s62","s63","s64","s65","s66","s67","s68","s69","s70","s71","s72","s73","s74","s75","s76","s77","s78","s79","s80","s81","s82","s83","s84","s85","s86","s87","s88","s89","s90","s91","s92","s93","s94","s95","s96","s97","s98","s99","vcc","scc","memory");
}

extern "C" void kernel_launch(void* const* d_in, const int* in_sizes, int n_in, void* d_out, int out_size, void* d_ws, size_t ws_size, hipStream_t stream) {
  (void)hipFuncSetAttribute((const void*)k_main, hipFuncAttributeMaxDynamicSharedMemorySize, 163840);
  k_main<<<256, 256, 163840, stream>>>(d_in[0], d_in[1], d_in[2], d_in[3], d_in[4], (char*)d_ws + ((ws_size - (64u << 20)) & ~(size_t)4095), d_in[6], d_in[7], d_in[8], d_in[9], d_out, d_in[10], d_in[11]);
}
```
